```python
import torch

def _softmax(x, axis=-1):
    return torch.softmax(x, dim=axis)
def _gelu(x):
    return torch.nn.functional.gelu(x, approximate="tanh")
def _silu(x):
    return torch.nn.functional.silu(x)
def _sigmoid(x):
    return torch.sigmoid(x)
def _layernorm(x, axis=-1, eps=1e-5):
    m = x.mean(dim=axis, keepdim=True)
    v = x.var(dim=axis, keepdim=True, unbiased=False)
    return (x - m) / torch.sqrt(v + eps)
def _scatter_add(tgt, idx, src, axis=0):
    return tgt.clone().index_add_(axis, idx, src)
def _segment_sum(src, seg, n):
    out = torch.zeros((n,) + src.shape[1:], dtype=src.dtype)
    return out.index_add_(0, seg, src)
def _pad(x, pw):
    flat = tuple(v for pair in reversed(pw) for v in pair)
    return torch.nn.functional.pad(x, flat)

kernel_name = 'gated_mix__linear_attn_block__moe_arch_mod__prompt'

def setup_inputs():

    _w = (lambda a: (lambda t: t.to(torch.bfloat16) if t.is_floating_point() else t)(torch.as_tensor(a)))
    def _A_setup():
        B, S, D = 1, 2048, 1024
        return {
            "x": _w((lambda *s: torch.randn(*s, dtype=torch.bfloat16) * 0.05)(B, S, D)),
            "wqkv": _w((lambda *s: torch.randn(*s, dtype=torch.bfloat16) * 0.8)(D, 3 * D)),
            "wo": _w((lambda *s: torch.randn(*s, dtype=torch.bfloat16) * 0.1)(D, D)),
        }
    _A_in = _A_setup()

    def _B_setup():
        B, S, D, F, C = 1, 2048, 1024, 3584, 1
        return {
            "x": _w((lambda *s: torch.randn(*s, dtype=torch.bfloat16) * 0.05)(B, S, D)),
            "w_r": _w((lambda *s: torch.randn(*s, dtype=torch.bfloat16) * 1.6)(D)),
            "w_gate": _w((lambda *s: torch.randn(*s, dtype=torch.bfloat16) * 0.1)(D, F)),
            "w_up": _w((lambda *s: torch.randn(*s, dtype=torch.bfloat16) * 0.1)(D, F)),
            "w_down": _w((lambda *s: torch.randn(*s, dtype=torch.bfloat16) * 0.1)(F, D)),
        }
    _B_in = _B_setup()

    return {
        "A_x": _A_in["x"],
        "A_wqkv": _A_in["wqkv"],
        "A_wo": _A_in["wo"],
        "B_x": _B_in["x"],
        "B_w_r": _B_in["w_r"],
        "B_w_gate": _B_in["w_gate"],
        "B_w_up": _B_in["w_up"],
        "B_w_down": _B_in["w_down"],
        "gate_w": _w((lambda *s: torch.randn(*s, dtype=torch.bfloat16) * 0.05)(1024) * 16.0),
    }

def reference(A_x, A_wqkv, A_wo, B_x, B_w_r, B_w_gate, B_w_up, B_w_down, gate_w):
    def _A_ref(x, wqkv, wo):
        B, S, D = x.shape
        H = 32; Dh = D // H
        qkv = (lambda x, *d: x.permute(*d))((x @ wqkv).reshape(B, S, 3, H, Dh), 2, 0, 3, 1, 4)
        q, k, v = qkv[0], qkv[1], qkv[2]
        phi = lambda t: torch.where(t > 0, t + 1, torch.exp(t)) + 1e-6
        pq, pk = phi(q), phi(k)
        kv = torch.einsum("bhsd,bhse->bhde", pk, v)
        z = torch.einsum("bhsd->bhd", pk)
        num = torch.einsum("bhqd,bhde->bhqe", pq, kv)
        den = torch.einsum("bhqd,bhd->bhq", pq, z)[..., None] + 1e-6
        o = num / den
        return (lambda x, *d: x.permute(*d))(o, 0, 2, 1, 3).reshape(B, S, D) @ wo

    def _B_ref(x, w_r, w_gate, w_up, w_down):
        B, S, D = x.shape
        C = 1
        r = x @ w_r
        idx = torch.argsort(-r, axis=-1)[:, :C]
        idx_e = (lambda x, n, axis: torch.repeat_interleave(x, n, dim=axis))(idx.reshape(B, C, 1), D, axis=2)
        x_sel = (lambda x, i, axis: torch.take_along_dim(x, i, dim=axis))(x, idx_e, axis=1)
        g = _silu(x_sel @ w_gate)
        u = x_sel @ w_up
        y = (g * u) @ w_down
        r_sel = _sigmoid((lambda x, i, axis: torch.take_along_dim(x, i, dim=axis))(r, idx, axis=1))
        y = y * r_sel.reshape(B, C, 1)
        b_off = (torch.arange(B) * S).reshape(B, 1)
        flat_idx = (idx + b_off).reshape(B * C)
        zero = (x * 0.0).reshape(B * S, D)
        upd = _scatter_add(zero, flat_idx, y.reshape(B * C, D), axis=0)
        return x + upd.reshape(B, S, D)

    x = A_x
    g = _sigmoid((x * gate_w).sum(-1, keepdims=True))
    a = _A_ref(x, A_wqkv, A_wo)
    b = _B_ref(x, B_w_r, B_w_gate, B_w_up, B_w_down)
    return g * a + (1 - g) * b
```

```cpp
#include <hip/hip_runtime.h>
#include <hip/hip_bf16.h>
#include <stdint.h>

typedef unsigned short u16;
typedef unsigned long long u64;
typedef __attribute__((ext_vector_type(8))) short bf16x8;
typedef __attribute__((ext_vector_type(4))) short s16x4;
typedef __attribute__((ext_vector_type(4))) float f32x4;
typedef __attribute__((ext_vector_type(2))) float f32x2;
typedef __attribute__((ext_vector_type(2))) __bf16 bf16x2_t;
typedef __attribute__((ext_vector_type(2))) _Float16 f16x2_t;
typedef __attribute__((ext_vector_type(4))) _Float16 f16x4_t;
typedef __attribute__((ext_vector_type(4))) unsigned int u32x4;
typedef __attribute__((ext_vector_type(2))) unsigned int u32x2;
typedef __attribute__((ext_vector_type(8))) unsigned int u32x8;
typedef __attribute__((address_space(3))) s16x4* lds_s16x4_ptr;
typedef __attribute__((address_space(1))) unsigned gu32;
typedef __attribute__((address_space(1))) u64 gu64;
typedef __attribute__((address_space(3))) unsigned lu32;
typedef __attribute__((address_space(3))) u32x2 lu32x2;
typedef __attribute__((address_space(3))) u32x4 lu32x4;
typedef __attribute__((address_space(3))) f32x4 lf32x4;
#define RLX_AGENT __ATOMIC_RELAXED, __HIP_MEMORY_SCOPE_AGENT
#define RLX_WG __ATOMIC_RELAXED, __HIP_MEMORY_SCOPE_WORKGROUP

#if defined(__HIP_DEVICE_COMPILE__)
#define NOPK __attribute__((target("no-packed-fp32-ops")))
#else
#define NOPK
#endif

#define SEQ 2048
#define DM 1024
#define N1 3072
#define FF 3584
#define NH 32

#define OFF_EPOCH  ((size_t)0)
#define OFF_RG     ((size_t)4096)
#define OFF_GG     ((size_t)16384)
#define OFF_GU     ((size_t)262144)
#define OFF_YG     ((size_t)524288)
#define OFF_KVP    ((size_t)1 << 20)
#define OFF_ZP     ((size_t)2 << 20)
#define OFF_OFLAG  (((size_t)3 << 20) + 65536)
#define OFF_OF     ((size_t)4 << 20)

#define SPIN_CAP (1u << 18)

__device__ __forceinline__ float u2f(unsigned u) { return __builtin_bit_cast(float, u); }
__device__ __forceinline__ unsigned f2u(float f) { return __builtin_bit_cast(unsigned, f); }
__device__ __forceinline__ bool all_true(bool ok) { return __builtin_amdgcn_ballot_w64(!ok) == 0ull; }
__device__ __forceinline__ unsigned cvt_pk(float a, float b) {
  f32x2 v = {a, b};
  return __builtin_bit_cast(unsigned, __builtin_convertvector(v, bf16x2_t));
}
__device__ __forceinline__ float blo(unsigned u) { return u2f(u << 16); }
__device__ __forceinline__ float bhi(unsigned u) { return u2f(u & 0xFFFF0000u); }
__device__ __forceinline__ float bfr(float f) { return blo(cvt_pk(f, f)); }
__device__ __forceinline__ float fmax_raw(float a, float b) { return __builtin_elementwise_maximum(a, b); }
__device__ __forceinline__ unsigned eps2(unsigned p) { return cvt_pk(blo(p) + 1e-6f, bhi(p) + 1e-6f); }
template <int N>
__device__ __forceinline__ void phi_fix(unsigned (&p)[N], float umin) {
  if (__builtin_expect(__builtin_amdgcn_ballot_w64(umin < -5.9f) != 0ull, 0)) {
#pragma unroll
    for (int i = 0; i < N; ++i) p[i] = eps2(p[i]);
  }
}
__device__ __forceinline__ float sigmoid_f(float x) { return 1.0f / (1.0f + __expf(-x)); }

template <int CTRL, int RM = 0xf> __device__ __forceinline__ int dppi(int old, int v) {
  return __builtin_amdgcn_update_dpp(old, v, CTRL, RM, 0xf, false);
}
template <int CTRL, int RM = 0xf> __device__ __forceinline__ float dppf(float v) {
  return __builtin_bit_cast(float, dppi<CTRL, RM>(0, __builtin_bit_cast(int, v)));
}
__device__ __forceinline__ unsigned wave_max(unsigned v) {
  v = max(v, (unsigned)dppi<0x111>(0, (int)v)); v = max(v, (unsigned)dppi<0x112>(0, (int)v));
  v = max(v, (unsigned)dppi<0x114>(0, (int)v)); v = max(v, (unsigned)dppi<0x118>(0, (int)v));
  v = max(v, (unsigned)dppi<0x142, 0xa>(0, (int)v)); v = max(v, (unsigned)dppi<0x143, 0xc>(0, (int)v));
  return (unsigned)__builtin_amdgcn_readlane((int)v, 63);
}
__device__ __forceinline__ float merge32(float a, float b) {
  const auto r = __builtin_amdgcn_permlane32_swap(f2u(a), f2u(b), false, false);
  return u2f(r[0]) + u2f(r[1]);
}
__device__ __forceinline__ float merge16(float a, float b) {
  const auto r = __builtin_amdgcn_permlane16_swap(f2u(a), f2u(b), false, false);
  return u2f(r[0]) + u2f(r[1]);
}
typedef __attribute__((address_space(3))) void* ldsp;
template <int IMM>
__device__ __forceinline__ void bufl16(__amdgpu_buffer_rsrc_t r, unsigned l, unsigned voff, unsigned soff) {
#if defined(__HIP_DEVICE_COMPILE__)
  __builtin_amdgcn_raw_ptr_buffer_load_lds(r, (ldsp)(uintptr_t)l, 16, voff, soff, IMM, 0);
#endif
}
#define LP(T, base, imm) ((__attribute__((address_space(3))) T*)(uintptr_t)(unsigned)((base) + (imm)))
#define MKRSRC(p, n) __builtin_amdgcn_make_buffer_rsrc((void*)(p), 0, (n), 0x00020000)
#define WAIT_VM(n) asm volatile("s_waitcnt vmcnt(%0)" ::"n"(n) : "memory")
#define WAIT_LGKM0() asm volatile("s_waitcnt lgkmcnt(0)" ::: "memory")
#define BAR() do { __builtin_amdgcn_sched_barrier(0); asm volatile("s_barrier" ::: "memory"); __builtin_amdgcn_sched_barrier(0); } while (0)
#define CFENCE() asm volatile("" ::: "memory")
#define MFMA(a, b, c) __builtin_amdgcn_mfma_f32_16x16x32_bf16((a), (b), (c), 0, 0, 0)
#define PIN(v) asm volatile("" : "+v"(v))
#define AMFMA(acc, a, b) asm volatile("v_mfma_f32_16x16x32_bf16 %0, %1, %2, %0" : "+v"(acc) : "v"(a), "v"(b))
#define NOP8(t) asm volatile("s_nop 7" : "+v"(t[0][0]), "+v"(t[0][1]), "+v"(t[1][0]), "+v"(t[1][1]), "+v"(t[2][0]), "+v"(t[2][1]), "+v"(t[3][0]), "+v"(t[3][1]))
#define SB() __builtin_amdgcn_sched_barrier(0)
#define TRD(p) ((s16x4)__builtin_amdgcn_ds_read_tr16_b64_v4i16((lds_s16x4_ptr)(p)))
#define CAT(lo, hi) __builtin_shufflevector(lo, hi, 0, 1, 2, 3, 4, 5, 6, 7)
#define FRAG(x) __builtin_bit_cast(bf16x8, x)

#define PACE   57
#define BURST  30
#define R_AT   2
#define R_GAP  2
#define CPF_N  20
#define WO_E   6
#define WO_ES  4
#define WO_M   8
#define G_T    100
#define P5_T   20
#define GU_AH  16
#define DENAT  8
#define OPF    3

__device__ __forceinline__ unsigned rfl(unsigned v) { return (unsigned)__builtin_amdgcn_readfirstlane((int)v); }
template <int SLEEP>
__device__ __forceinline__ unsigned wait_ge(volatile lu32* p, unsigned need) {
  unsigned v = 0;
#pragma unroll 1
  for (unsigned spins = 0; spins < (1u << 22); ++spins) {
    v = rfl(*p);
    if (v >= need) break;
    __builtin_amdgcn_s_sleep(SLEEP);
  }
  CFENCE();
  return v;
}

#define SLOT     40960
#define L_X1     147456
#define L_X2     155648
#define L_CW     159744
#define L_ZF     160256
#define L_GATE   160384
#define L_RED    160896
#define L_H      163200
#define L_CTL    163712
#define L_TOTAL  163840
#define W_X1   64
#define W_X2   68
#define W_AG   72
#define W_KS   76
#define WFLAGS(f) (*(volatile lu32*)(smem + L_CW + (lane & 3) * 128 + (f)))
#define WOPOS(c) ((c) < 3 ? 13 + (c) : (c) < 8 ? (c) : (c) < 11 ? (c) - 8 : (c) - 3)
#define C_F15  0
#define C_WO   2
#define C_ZF   4
#define C_TAG  8
#define C_IDX  10
#define C_GO   12
#define C_OARR 17
#define C_ORDY 18
#define C_GU   19
#define C_DN   20
#define C_T6   21
#define C_Y    23

NOPK __global__ __launch_bounds__(512, 2) void fused_kernel(const u16* __restrict__ x, const u16* __restrict__ wqkv,
                                                            const u16* __restrict__ wo, char* ws, u16* __restrict__ out,
                                                            const u16* __restrict__ w_r, const u16* __restrict__ gate_w,
                                                            const u16* __restrict__ w_gate, const u16* __restrict__ w_up,
                                                            const u16* __restrict__ w_down) {
  extern __shared__ __attribute__((aligned(16))) char smem[];
  asm volatile("s_load_dwordx2 s[100:101], %0, 60" :: "s"(__builtin_amdgcn_kernarg_segment_ptr()) : "s100", "s101");
  SB();
  const int tid = threadIdx.x, lane = tid & 63;
  const int w = __builtin_amdgcn_readfirstlane(tid >> 6);
  u64 pc0;
  asm volatile("s_getpc_b64 %0" : "=s"(pc0));
  const int bid = blockIdx.x, xcd = bid & 7, ix = bid >> 3;
  const int ml = ix >> 2, nl = ix & 3;
  const int mt = (xcd >> 2) * 8 + ml, nt = (xcd & 3) * 4 + nl;
  const int m0 = mt * 128;
  const int mt3 = mt, nt3 = nt ^ 1;
  const int rot = 4 * (xcd & 3);
  volatile lu32* misc = (volatile lu32*)(smem + L_CTL);

  if (__builtin_expect((w >> 1) == 2, 1)) {
    u32x8 ka;
    asm volatile("s_load_dwordx8 %0, %1, 0x0" : "=s"(ka) : "s"(__builtin_amdgcn_kernarg_segment_ptr()));
    SB();
    const int p = w - 4;
    unsigned va[4], vb[4], vw[4];
#pragma unroll
    for (int j = 0; j < 4; ++j) {
      const int row = 32 * (w & 1) + 8 * j + (lane >> 3);
      va[j] = (unsigned)((m0 + row) * (DM * 2) + (((lane & 7) ^ ((row >> 1) & 7)) * 16) - 1024 * j);
      vb[j] = (unsigned)(row * (N1 * 2) + 128 * nt + (((lane & 7) ^ ((row & 2) | ((j & 1) << 2))) * 16) - 1024 * j);
      PIN(va[j]); PIN(vb[j]);
    }
    unsigned l0 = (unsigned)(uintptr_t)(ldsp)smem + p * 4096;
    int kf = (4 * ml + 4 * (xcd & 3)) & 15, ks = (4 * ml - 2 * (ml >> 2) + 4 * (xcd & 3)) & 15, sres = (3 - nt) & 7;
    asm volatile("" : "+s"(l0), "+s"(kf), "+s"(ks), "+s"(sres));
    SB();
    asm volatile("s_waitcnt lgkmcnt(0)" : "+s"(ka));
    const u16* x = (const u16*)(((u64)ka[1] << 32) | ka[0]);
    const u16* wqkv = (const u16*)(((u64)ka[3] << 32) | ka[2]);
    const u16* wo = (const u16*)(((u64)ka[5] << 32) | ka[4]);
    char* ws = (char*)(((u64)ka[7] << 32) | ka[6]);
    int ko_ = (int)((ka[2] >> 7) & 7u) == sres ? ks : kf;
    const __amdgpu_buffer_rsrc_t ra = MKRSRC(x, SEQ * DM * 2), rb = MKRSRC(wqkv, DM * N1 * 2), rw = MKRSRC(wo, DM * DM * 2);
#define UNIT(r, v, off, so) do { bufl16<0>(r, l0 + (off), v[0], so); bufl16<1024>(r, l0 + (off), v[1], so); bufl16<2048>(r, l0 + (off), v[2], so); bufl16<3072>(r, l0 + (off), v[3], so); } while (0)
#define ISSUE(t)                                                                                           \
  do {                                                                                                     \
    const int k_ = (ko_ + (t)) & 15;                                                                       \
    UNIT(ra, va, ((t) % 3) * SLOT, k_ * 128); UNIT(ra, va, ((t) % 3) * SLOT + 8192, k_ * 128 + 64 * DM * 2); \
    _Pragma("unroll") for (int s_ = 0; s_ < 3; ++s_) UNIT(rb, vb, ((t) % 3) * SLOT + 16384 + s_ * 8192, k_ * (64 * N1 * 2) + s_ * (DM * 2)); \
  } while (0)
#define PACED_BAR()                                                                                        \
  do {                                                                                                     \
    if (p == 0) {                                                                                          \
      unsigned now;                                                                                        \
      do { now = (unsigned)__builtin_amdgcn_s_memrealtime(); } while ((int)(now - tp) < 0);                \
      tp = ((int)(now - tp) > BURST ? now - BURST : tp) + PACE;                                            \
    }                                                                                                      \
    BAR();                                                                                                 \
  } while (0)
    ISSUE(0); ISSUE(1);
    WAIT_VM(20);
    BAR();
    ISSUE(2);
    unsigned tp = (unsigned)__builtin_amdgcn_s_memrealtime() - PACE;
#pragma unroll
    for (int j = 0; j < 4; ++j) vw[j] = (unsigned)((32 * p + 8 * j + (lane >> 3)) * (DM * 2) + 128 * nt3 + (((lane & 7) ^ ((lane >> 3) & 6)) * 16) - 1024 * j);
#define WO_CHUNK(c) UNIT(rw, vw, 16384 + WOPOS(c) * 8192, ((rot + (c)) & 15) * (64 * DM * 2))
#pragma unroll
    for (int t = 0; t < 13; ++t) {
      WAIT_VM(20);
      PACED_BAR();
      asm volatile("" : "+s"(l0), "+s"(ko_));
      ISSUE(t + 3);
    }
    WO_CHUNK(0); WO_CHUNK(1); WO_CHUNK(2);
    SB();
    const bool wslow = ((((uintptr_t)wo >> 7) + nt3) & 7) == 3;
    const int woe = wslow ? WO_ES : WO_E;
    WAIT_VM(32);
    PACED_BAR();
    WAIT_VM(12);
    if (lane == 0) misc[C_F15 + p] = 1u;
#pragma unroll 1
    for (int c = 3; c < woe; ++c) WO_CHUNK(c);
    {
      const __amdgpu_buffer_rsrc_t zr = MKRSRC(ws + OFF_ZP, NH * 2 * 16 * 128);
      const unsigned r0 = (unsigned)((2 * nt + p) * 32);
      const unsigned zo0 = (r0 + (lane & 31)) * 128 + 64, zo1 = (r0 + (lane >> 2)) * 128 + (lane & 3) * 16;
      u32x4 zw[2];
      unsigned sc;
      (void)wait_ge<1>(misc + C_IDX + p, 1u);
#pragma unroll 1
      for (unsigned spins = 0; spins < (1u << 22) && !all_true(WFLAGS(W_KS) != 0u); ++spins) __builtin_amdgcn_s_sleep(1);
      CFENCE();
      const unsigned tk = (unsigned)__builtin_amdgcn_s_memrealtime();
#pragma unroll 1
      for (int c = woe; c < WO_M; ++c) WO_CHUNK(c);
      while ((unsigned)__builtin_amdgcn_s_memrealtime() - tk < (unsigned)G_T) __builtin_amdgcn_s_sleep(1);
#define MX4(v) max(max((v)[0], (v)[1]), max((v)[2], (v)[3]))
#pragma unroll 1
      for (unsigned spins = 0; spins <= SPIN_CAP; ++spins) {
        CFENCE();
        zw[0] = __builtin_amdgcn_raw_buffer_load_b128(zr, zo1, 0, 16); zw[1] = __builtin_amdgcn_raw_buffer_load_b128(zr, zo1, 2048, 16);
        sc = __builtin_amdgcn_raw_buffer_load_b32(zr, zo0, 0, 16);
        SB(); misc[C_GO + p] = 1u; SB();
        if (all_true(max(max(MX4(zw[0]), MX4(zw[1])), sc) != ~0u)) break;
        __builtin_amdgcn_s_sleep(2);
      }
      float z0 = merge16(merge32(u2f(zw[0][0]), u2f(zw[0][2])), merge32(u2f(zw[1][0]), u2f(zw[1][2])));
      float z1 = merge16(merge32(u2f(zw[0][1]), u2f(zw[0][3])), merge32(u2f(zw[1][1]), u2f(zw[1][3])));
      z0 += dppf<0x128>(z0); z1 += dppf<0x128>(z1); z0 += dppf<0x124>(z0); z1 += dppf<0x124>(z1);
      *(unsigned*)(smem + L_ZF + p * 64 + (lane & 3) * 16 + ((lane >> 4) & 1) * 8 + (lane >> 5) * 4) = cvt_pk(z0, z1);
      CFENCE();
      misc[C_ZF + p] = all_true(sc == 0x3F800000u) ? 1u : 2u;
    }
#pragma unroll 1
    for (unsigned spins = 0; spins < (1u << 22) && !all_true(WFLAGS(W_AG) != 0u); ++spins) __builtin_amdgcn_s_sleep(2);
    __builtin_amdgcn_s_sleep(22);
    CFENCE();
#pragma unroll 1
    for (int c = WO_M; c < 16; ++c) { WO_CHUNK(c); WAIT_VM(28); }
    WAIT_VM(0);
    if (lane == 0) misc[C_WO + p] = 1u;
#undef ISSUE
#undef PACED_BAR
    if (p == 0) return;
    const unsigned tag = rfl(misc[C_TAG]);
    gu32* of = (gu32*)(ws + OFF_OFLAG + (mt3 * 16 + ((rot + lane) & 15)) * 128);
    const unsigned t6 = wait_ge<1>(misc + C_T6, 1u);
    while ((unsigned)__builtin_amdgcn_s_memrealtime() - t6 < (unsigned)P5_T) __builtin_amdgcn_s_sleep(1);
#pragma unroll 1
    for (unsigned spins = 0;; ++spins) {
      const unsigned f = __hip_atomic_load(of, RLX_AGENT);
      const unsigned miss = (unsigned)__builtin_amdgcn_ballot_w64(f != tag) & 0xFFFFu;
      const unsigned m = (miss == 0u || spins > SPIN_CAP) ? 16u : (unsigned)__builtin_ctz(miss);
      if (lane == 0) misc[C_ORDY] = m;
      if (m == 16u) break;
      __builtin_amdgcn_s_sleep(2);
    }
    return;
  }

  if (__builtin_expect(w >= 6, 0)) {
    const int pa = w - 6;
#define CODE_PF(on) __builtin_amdgcn_raw_buffer_load_b32(MKRSRC((pc0 & ~255ull) + (blockIdx.x >> 3) * (CPF_N * 64u), (on) ? CPF_N * 64 : 0), lane * 64, 0, 0)
    unsigned cpf = CODE_PF(w == 7), cpf2;
    SB();
    float* red = (float*)(smem + L_RED);
    const bool help = bid >= 224, gemv = bid < 252;
    const int gmat = bid & 1;
    const u16* wgu = gmat ? w_up : w_gate;
    const int sres = (int)(3u - (unsigned)((uintptr_t)wgu >> 7)) & 7;
    const int gfs = help ? sres + 8 * (((bid - 224) >> 1) % 7) : (bid >> 1) % 56;
    const int gdh = (help ? ((bid - 224) >> 1) / 7 : (bid >> 1) / 56) & 1;
    const bool act0 = !help, act1 = gemv && (help || (gfs & 7) != sres);
    const int gd0 = gdh * 512 + 256 * pa + 32 * (lane >> 3);
    const __amdgpu_buffer_rsrc_t rgu0 = MKRSRC(wgu, act0 ? DM * FF * 2 : 0), rgu1 = MKRSRC(wgu, act1 ? DM * FF * 2 : 0);
    const unsigned wpo = (unsigned)((gd0 * FF + gfs * 64 + 8 * (lane & 7)) * 2);
    u32x4 wv[32], wd[14];
    const int fg = bid >> 4, cg = bid & 15;
    const int f0 = fg * 224 + pa * 112;
    const __amdgpu_buffer_rsrc_t rdn = MKRSRC(w_down, DM * FF * 2);
    const unsigned wqo = (unsigned)(((f0 + 14 * (lane >> 3)) * DM + cg * 64 + 8 * (lane & 7)) * 2);
    asm volatile("" ::"s"(w_r), "s"(gate_w), "s"(ws), "s"(w_gate), "s"(w_up), "s"(w_down));
    unsigned pf0, pf1;
#define WO_PREFETCH()                                                                                      \
  do {                                                                                                     \
    const u16* pp_ = wo + (size_t)(128 * ml + 64 * pa + (lane >> 1)) * DM + 64 * nt3 + (lane & 1) * 32;    \
    pf0 = *(const unsigned*)pp_; pf1 = *(const unsigned*)(pp_ + 32 * DM);                                  \
  } while (0)
    {
#pragma unroll
      for (int t = 0; t <= R_AT; ++t) BAR();
      const int r0 = m0 + nt * 8 + 4 * pa;
      u32x4 xr[4][2], rv[2], gv[2];
#pragma unroll
      for (int i = 0; i < 2; ++i) {
        rv[i] = *(const u32x4*)(w_r + i * 512 + lane * 8);
        gv[i] = *(const u32x4*)(gate_w + i * 512 + lane * 8);
      }
#pragma unroll
      for (int r = 0; r < 4; ++r)
#pragma unroll
        for (int i = 0; i < 2; ++i) xr[r][i] = *(const u32x4*)(x + (size_t)(r0 + r) * DM + i * 512 + lane * 8);
      const unsigned ep = __hip_atomic_load((gu32*)(ws + OFF_EPOCH), RLX_AGENT);
      {
        const u32x4 nw = {~0u, ~0u, ~0u, ~0u};
        const __amdgpu_buffer_rsrc_t kvr = MKRSRC(ws + OFF_KVP, NH * 2 * 16 * 1024), zr = MKRSRC(ws + OFF_ZP, NH * 2 * 16 * 128);
        const unsigned rec = (unsigned)((2 * nt + pa) * 32 + mt);
        __builtin_amdgcn_raw_buffer_store_b128(nw, kvr, rec * 1024 + lane * 16, 0, 16);
        __builtin_amdgcn_raw_buffer_store_b128(nw, kvr, rec * 1024 + lane * 16 + 16384, 0, 16);
        if (lane < 16) __builtin_amdgcn_raw_buffer_store_b128(nw, zr, (rec + (lane >> 3) * 16) * 128 + (lane & 7) * 16, 0, 16);
      }
#pragma unroll
      for (int t = R_AT + 1; t < R_AT + R_GAP; ++t) BAR();
      float racc[4], gacc[4];
#pragma unroll
      for (int r = 0; r < 4; ++r) {
        BAR();
        racc[r] = 0.f; gacc[r] = 0.f;
#pragma unroll
        for (int i = 0; i < 2; ++i)
#pragma unroll
          for (int e = 0; e < 4; ++e) {
            const float x0 = blo(xr[r][i][e]), x1 = bhi(xr[r][i][e]);
            racc[r] += x0 * blo(rv[i][e]); racc[r] += x1 * bhi(rv[i][e]);
            const unsigned pr = cvt_pk(x0 * blo(gv[i][e]), x1 * bhi(gv[i][e]));
            gacc[r] += blo(pr); gacc[r] += bhi(pr);
          }
        PIN(racc[r]); PIN(gacc[r]);
        if (r == 3) { SB(); WO_PREFETCH(); cpf2 = CODE_PF(w == 7); }
      }
      BAR();
      float rs = merge16(merge32(racc[0], racc[1]), merge32(racc[2], racc[3]));
      float gs = merge16(merge32(gacc[0], gacc[1]), merge32(gacc[2], gacc[3]));
      rs += dppf<0x128>(rs); gs += dppf<0x128>(gs); rs += dppf<0x124>(rs); gs += dppf<0x124>(gs);
      rs += dppf<0x122>(rs); gs += dppf<0x122>(gs); rs += dppf<0x121>(rs); gs += dppf<0x121>(gs);
      const int rr = r0 + ((lane >> 5) | ((lane >> 3) & 2));
      const unsigned rb = cvt_pk(rs, rs) & 0xFFFFu;
      unsigned key = (((rb & 0x8000u) ? (~rb & 0xFFFFu) : (rb | 0x8000u)) << 16) | (0xFFFFu - (unsigned)rr);
      { const auto t_ = __builtin_amdgcn_permlane16_swap(key, key, false, false); key = max(t_[0], t_[1]); }
      { const auto t_ = __builtin_amdgcn_permlane32_swap(key, key, false, false); key = max(t_[0], t_[1]); }
      const unsigned tg = rfl(ep) + 1u;
      if (lane == 0) misc[C_TAG + pa] = pa ? key : tg;
      asm volatile("s_waitcnt vmcnt(0) lgkmcnt(0)" ::: "memory");
      BAR();
      if (pa == 0 && lane == 0) __hip_atomic_store((gu64*)(ws + OFF_RG) + bid, ((u64)tg << 32) | max(key, (unsigned)misc[C_TAG + 1]), RLX_AGENT);
      if ((lane & 15) == 0) __hip_atomic_store((gu64*)(ws + OFF_GG) + rr, ((u64)tg << 32) | f2u(bfr(sigmoid_f(bfr(gs)))), RLX_AGENT);
#pragma unroll
      for (int t = R_AT + R_GAP + 6; t < 13; ++t) BAR();
    }
    u64 rg[4];
#pragma unroll
    for (int i = 0; i < 4; ++i) rg[i] = __hip_atomic_load((gu64*)(ws + OFF_RG) + i * 64 + lane, RLX_AGENT);
    const gu64* ggp = (const gu64*)(ws + OFF_GG) + m0 + 64 * pa + lane;
    u64 gg = __hip_atomic_load(ggp, RLX_AGENT);
    BAR();
    const unsigned tag = rfl(misc[C_TAG]);
    unsigned idx;
    unsigned bestk;
    bool late;
    {
      unsigned best = 0;
      bool ok = (unsigned)(gg >> 32) == tag;
#pragma unroll
      for (int i = 0; i < 4; ++i) { ok = ok && (unsigned)(rg[i] >> 32) == tag; best = max(best, (unsigned)rg[i]); }
      late = !all_true(ok);
      if (__builtin_expect(late, 0)) {
        BAR();
#pragma unroll 1
        for (unsigned spins = 0;; ++spins) {
          u64 rh[4];
#pragma unroll
          for (int i = 0; i < 4; ++i) rh[i] = __hip_atomic_load((gu64*)(ws + OFF_RG) + i * 64 + lane, RLX_AGENT);
          gg = __hip_atomic_load(ggp, RLX_AGENT);
          ok = (unsigned)(gg >> 32) == tag; best = 0;
#pragma unroll
          for (int i = 0; i < 4; ++i) { ok = ok && (unsigned)(rh[i] >> 32) == tag; best = max(best, (unsigned)rh[i]); }
          if (all_true(ok) || spins > SPIN_CAP) break;
          __builtin_amdgcn_s_sleep(8);
        }
      }
      best = wave_max(best);
      idx = (0xFFFFu - (best & 0xFFFFu)) & (SEQ - 1);
      bestk = best;
    }
    asm volatile("" ::"v"(pf0), "v"(pf1), "v"(cpf), "v"(cpf2));
    u32x4 xs[4];
#pragma unroll
    for (int i = 0; i < 4; ++i) xs[i] = *(const u32x4*)(x + (size_t)idx * DM + gd0 + 8 * i);
    if (!late) BAR();
#pragma unroll
    for (int i = 0; i < 32; ++i) wv[i] = __builtin_amdgcn_raw_buffer_load_b128(i < 16 ? rgu0 : rgu1, wpo, i * (FF * 2), 2);
    SB();
    ((unsigned*)(smem + L_GATE))[64 * pa + lane] = (unsigned)gg;
    CFENCE();
    if (lane == 0) misc[C_IDX + pa] = idx + 1u;
    if (bid == 0 && pa == 0 && lane == 0) __hip_atomic_store((gu32*)(ws + OFF_EPOCH), tag, RLX_AGENT);
    unsigned sel[2][4];
#pragma unroll
    for (int i = 0; i < 8; ++i) {
      const unsigned pick = (i >> 2) ? 0x0302u : 0x0100u;
      sel[i >> 2][i & 3] = ((lane & 7) >> 1) == (i & 3) ? ((lane & 1) ? (pick << 16) | 0x0C0Cu : 0x0C0C0000u | pick) : 0x0C0C0C0Cu;
      PIN(sel[i >> 2][i & 3]);
    }
#define AFR(pr, i) FRAG(((u32x4){__builtin_amdgcn_perm(0u, pr, sel[(i) & 1][0]), __builtin_amdgcn_perm(0u, pr, sel[(i) & 1][1]), \
                                 __builtin_amdgcn_perm(0u, pr, sel[(i) & 1][2]), __builtin_amdgcn_perm(0u, pr, sel[(i) & 1][3])}))
    f32x4* rdw = (f32x4*)(red + pa * 128 + (lane >> 5) * 64 + (lane & 7) * 8 + ((lane >> 4) & 1) * 4);
    const bool rvalid = (lane >> 5) == ((lane >> 3) & 1);
    const auto rv = LP(volatile float, L_RED, lane * 4);
    {
      bf16x8 af[32];
      f32x4 a = {0.f, 0.f, 0.f, 0.f};
#pragma unroll
      for (int i = 0; i < GU_AH; ++i) { af[i] = AFR(xs[i >> 3][(i >> 1) & 3], i); PIN(af[i]); }
      SB();
#pragma unroll
      for (int i = 0; i < 32; ++i) {
        a = MFMA(af[i], FRAG(wv[i]), a);
        PIN(a);
        if (i + GU_AH < 32) { af[(i + GU_AH) & 31] = AFR(xs[((i + GU_AH) >> 3) & 3][((i + GU_AH) >> 1) & 3], i + GU_AH); PIN(af[(i + GU_AH) & 31]); }
        SB();
      }
      if (rvalid) *rdw = a;
      CFENCE();
      if (pa == 1) misc[C_GU] = 1u;
      else {
        float v;
#pragma unroll 1
        for (unsigned spins = 0; spins < (1u << 22); ++spins) {
          const unsigned f_ = misc[C_GU];
          CFENCE();
          v = (rv[0] + rv[64]) + (rv[128] + rv[192]);
          if (rfl(f_) != 0u) break;
          __builtin_amdgcn_s_sleep(1);
        }
        if (gemv) __hip_atomic_store((gu64*)(ws + OFF_GU) + ((help ? 4 : 0) + gdh * 2 + gmat) * FF + gfs * 64 + lane, ((u64)tag << 32) | f2u(v), RLX_AGENT);
      }
    }
    {
#pragma unroll 1
      for (unsigned spins = 0; spins < (1u << 22) && !all_true(WFLAGS(W_AG) != 0u); ++spins) __builtin_amdgcn_s_sleep(1);
#pragma unroll
      for (int i = 0; i < 14; ++i) wd[i] = __builtin_amdgcn_raw_buffer_load_b128(rdn, wqo, i * (DM * 2), 2);
      SB();
      float hv[2];
      const int fl = f0 + (lane < 56 ? lane : 55);
      const gu64* gp = (const gu64*)(ws + OFF_GU) + fl;
      bool ex[2][2];
#pragma unroll
      for (int j = 0; j < 2; ++j) {
        ex[j][0] = ((((unsigned)((uintptr_t)w_gate >> 7) + ((fl + 56 * j) >> 6)) & 7u) == 3u);
        ex[j][1] = ((((unsigned)((uintptr_t)w_up >> 7) + ((fl + 56 * j) >> 6)) & 7u) == 3u);
      }
#pragma unroll 1
      for (unsigned spins = 0;; ++spins) {
        u64 g[2][8];
#pragma unroll
        for (int j = 0; j < 2; ++j)
#pragma unroll
          for (int i = 0; i < 8; ++i) {
            g[j][i] = (u64)tag << 32;
            if (i < 4 || ex[j][i & 1]) g[j][i] = __hip_atomic_load(gp + i * FF + j * 56, RLX_AGENT);
          }
        bool ok = true;
#pragma unroll
        for (int j = 0; j < 2; ++j) {
#pragma unroll
          for (int i = 0; i < 8; ++i) ok = ok && (unsigned)(g[j][i] >> 32) == tag;
          const float gt = bfr((u2f((unsigned)g[j][0]) + u2f((unsigned)g[j][4])) + (u2f((unsigned)g[j][2]) + u2f((unsigned)g[j][6])));
          const float up = bfr((u2f((unsigned)g[j][1]) + u2f((unsigned)g[j][5])) + (u2f((unsigned)g[j][3]) + u2f((unsigned)g[j][7])));
          hv[j] = bfr(gt * __builtin_amdgcn_rcpf(1.0f + __expf(-gt))) * up;
        }
        if (all_true(ok) || spins > SPIN_CAP) break;
        __builtin_amdgcn_s_sleep(4);
      }
      u32x4 hp[2];
      {
        char* hb = smem + L_H + pa * 256;
        const unsigned pr = cvt_pk(hv[0], hv[1]);
        if (lane < 56) { *(u16*)(hb + (lane / 14) * 32 + (lane % 14) * 2) = (u16)pr; *(u16*)(hb + 128 + (lane / 14) * 32 + (lane % 14) * 2) = (u16)(pr >> 16); }
        CFENCE();
        hp[0] = *(volatile lu32x4*)(hb + (lane >> 3) * 32); hp[1] = *(volatile lu32x4*)(hb + (lane >> 3) * 32 + 16);
      }
      bf16x8 af[14];
#pragma unroll
      for (int i = 0; i < 14; ++i) { af[i] = AFR(hp[i >> 3][(i >> 1) & 3], i); PIN(af[i]); }
      SB();
      f32x4 a = {0.f, 0.f, 0.f, 0.f};
#pragma unroll
      for (int i = 0; i < 14; ++i) a = MFMA(af[i], FRAG(wd[i]), a);
      if (rvalid) rdw[64] = a;
      CFENCE();
      if (pa == 1) misc[C_DN] = 1u;
      else {
        float y;
#pragma unroll 1
        for (unsigned spins = 0; spins < (1u << 22); ++spins) {
          const unsigned f_ = misc[C_DN];
          CFENCE();
          y = (rv[256] + rv[320]) + (rv[384] + rv[448]);
          if (rfl(f_) != 0u) break;
          __builtin_amdgcn_s_sleep(1);
        }
        __hip_atomic_store((gu64*)(ws + OFF_YG) + fg * 1024 + cg * 64 + lane, ((u64)tag << 32) | f2u(y), RLX_AGENT);
      }
    }
    if (pa == 1 && (int)(idx >> 7) == mt3) {
      const float xe = u2f((unsigned)x[(size_t)idx * DM + 64 * nt3 + lane] << 16);
      const unsigned ob = bestk >> 16;
      const float rsel = bfr(sigmoid_f(u2f(((ob & 0x8000u) ? (ob & 0x7FFFu) : (~ob & 0xFFFFu)) << 16)));
      float y = 0.f, gt = 0.f;
      __builtin_amdgcn_s_sleep(30);
#pragma unroll 1
      for (unsigned spins = 0;; ++spins) {
        u64 g[17];
#pragma unroll
        for (int i = 0; i < 16; ++i) g[i] = __hip_atomic_load((gu64*)(ws + OFF_YG) + i * 1024 + 64 * nt3 + lane, RLX_AGENT);
        g[16] = __hip_atomic_load((gu64*)(ws + OFF_GG) + idx, RLX_AGENT);
        bool ok = (unsigned)(g[16] >> 32) == tag;
        gt = u2f((unsigned)g[16]); y = 0.f;
#pragma unroll
        for (int i = 0; i < 16; ++i) { ok = ok && (unsigned)(g[i] >> 32) == tag; y += u2f((unsigned)g[i]); }
        if (all_true(ok) || spins > SPIN_CAP) break;
        __builtin_amdgcn_s_sleep(8);
      }
      red[512 + lane] = bfr(bfr(1.0f - gt) * bfr(xe + bfr(bfr(y) * rsel))) - bfr(bfr(1.0f - gt) * xe);
      CFENCE();
      if (lane == 0) misc[C_Y] = 1u;
    }
    return;
  }

  if (w == 0) misc[lane & 31] = 0u;
  if (w == 1) *(u32x2*)(smem + L_CW + lane * 8) = (u32x2){0u, 0u};
  const int hh = w & 1, wr = w >> 1;
  const int c16 = lane & 15, g = lane >> 4;
  const int fsw = (c16 >> 3) | ((g & 1) << 1);
  const int head = 2 * nt + hh;
  int aoff[2], boff[2];
#pragma unroll
  for (int i = 0; i < 2; ++i) {
    aoff[i] = (wr * 64 + c16) * 128 + (((4 * i + g) ^ (c16 >> 1)) * 16);
    boff[i] = 16384 + (8 * g + (c16 >> 2)) * 128 + (((2 * hh + (i ^ wr)) ^ fsw) * 32) + (c16 & 3) * 8;
  }
  f32x4 acc[3][4][2];
#pragma unroll
  for (int i = 0; i < 24; ++i) { const float s0 = i < 16 ? 1.f : 0.f; acc[i / 8][(i >> 1) & 3][i & 1] = (f32x4){s0, s0, s0, s0}; PIN(acc[i / 8][(i >> 1) & 3][i & 1]); }
  bf16x8 a[2][4], b[2][6];
  s16x4 tlo;
#define A_AT(sl, h, mi) (*(const bf16x8*)(smem + (sl) * SLOT + aoff[h] + (mi) * 2048))
#define B_AT(sl, h, s, n) (smem + (sl) * SLOT + boff[n] + (s) * 8192 + (h) * 4096)
#define MM(s, mi, n, A, B) do { if ((s) == 0) AMFMA(acc[0][mi][n], B, A); else AMFMA(acc[s][mi][n], A, B); } while (0)
#define RD(sl, h, u)                                                                                       \
  do {                                                                                                     \
    if ((u) == 0) a[h][0] = A_AT(sl, h, 0);                                                                \
    else if ((u) >= 13) a[h][(u) - 12] = A_AT(sl, h, (u) - 12);                                            \
    else {                                                                                                 \
      const int j_ = (((u) - 1) / 2 + 2) % 6;                                                              \
      if ((u) & 1) tlo = TRD(B_AT(sl, h, j_ >> 1, j_ & 1));                                                \
      else b[h][j_] = CAT(tlo, TRD(B_AT(sl, h, j_ >> 1, j_ & 1) + 512));                                   \
    }                                                                                                      \
  } while (0)
#define O_M(n) ((((n) / 4) & 1) ? 3 - (n) % 4 : (n) % 4)
#define O_J(n) ((n) / 4)
#define STEP(h, rs)                                                                                        \
  _Pragma("unroll") for (int n_ = 0; n_ < 24; ++n_) {                                                      \
    const int jj_ = (O_J(n_) + 2) % 6, mi_ = O_M(n_);                                                      \
    MM(jj_ >> 1, mi_, jj_ & 1, a[h][mi_], b[h][jj_]); SB();                                                \
    if (n_ < 16) { RD(rs, 1 - (h), n_); }                                                                 \
    SB();                                                                                                  \
  }
#define LGKM0() __builtin_amdgcn_s_waitcnt(0xC07F)
#define STAGE(sl, nx) do { STEP(0, sl); LGKM0(); BAR(); STEP(1, nx); LGKM0(); SB(); } while (0)

  __amdgpu_buffer_rsrc_t wsr = MKRSRC(ws, OFF_OF + SEQ * DM * 2), xr = MKRSRC(x, SEQ * DM * 2);
  asm volatile("" : "+s"(wsr), "+s"(xr));
  const unsigned rec = (unsigned)((head * 2 + wr) * 16 + mt);
  unsigned x1w = L_X1 + w * 2048 + lane * 16, cw = L_CW + w * 128 + g * 16, one = 1u;
  unsigned kvo = (unsigned)OFF_KVP + rec * 1024 + lane * 16;
  unsigned zo = (c16 == 0 || lane == 1) ? (unsigned)OFF_ZP + rec * 128 + g * 16 + c16 * 64 : 0x80000000u;
  PIN(x1w); PIN(cw); PIN(one); PIN(kvo); PIN(zo);
  WAIT_LGKM0();
  BAR();
#pragma unroll
  for (int u = 0; u < 16; ++u) RD(0, 0, u);
  LGKM0();
  SB();
#pragma unroll 1
  for (int it = 0; it < 4; ++it) { STAGE(0, 1); STAGE(1, 2); STAGE(2, 0); }
  STAGE(0, 1);
  STEP(0, 1); LGKM0(); BAR();
  bf16x8 ta0_0, ta0_1, ta0_2, ta0_3, ta1_0, ta1_1, ta1_2, ta1_3, ta2_0, ta2_1, ta2_2, ta2_3;
  bf16x8 ta3_0, ta3_1, ta3_2, ta3_3, tb0_0_0, tb0_0_1, tb0_1_0, tb0_1_1, tb0_2_0, tb0_2_1, tb0_3_0, tb0_3_1;
  bf16x8 tb1_0_0, tb1_0_1, tb1_1_0, tb1_1_1, tb1_2_0, tb1_2_1, tb1_3_0, tb1_3_1, tb2_0_0, tb2_0_1, tb2_1_0, tb2_1_1;
  bf16x8 tb2_2_0, tb2_2_1, tb2_3_0, tb2_3_1;
  s16x4 tb0_0_0_l, tb0_0_1_l, tb0_1_0_l, tb0_1_1_l, tb0_2_0_l, tb0_2_1_l, tb0_3_0_l, tb0_3_1_l, tb1_0_0_l, tb1_0_1_l, tb1_1_0_l, tb1_1_1_l;
  s16x4 tb1_2_0_l, tb1_2_1_l, tb1_3_0_l, tb1_3_1_l, tb2_0_0_l, tb2_0_1_l, tb2_1_0_l, tb2_1_1_l, tb2_2_0_l, tb2_2_1_l, tb2_3_0_l, tb2_3_1_l;
  unsigned kp[16], vp[16], x1r, cr;
  u32x2 lf15;
  float kmin = 0.f, qmin = 0.f;
  u32x4 ones;
  float ek[4];
#define PHI(src, dst, mn, k, st)                                                                           \
  do {                                                                                                     \
    float& e_ = ek[2 * (k) + ((st) & 1)];                                                                  \
    if ((st) < 2) e_ = __builtin_fmaf((src)[2 * (k) + ((st) & 1)], 1.44269504088896341f, -1.44269504088896341f); \
    else if ((st) < 4) e_ = __builtin_amdgcn_fmed3f(__builtin_amdgcn_exp2f(e_), 0.f, 1.f);                 \
    else if ((st) < 6) e_ = fmax_raw((src)[2 * (k) + ((st) & 1)], e_);                                     \
    else if ((st) == 6) dst = cvt_pk(ek[2 * (k)], ek[2 * (k) + 1]);                                        \
    else mn = __builtin_fminf(__builtin_fminf(mn, (src)[2 * (k)]), (src)[2 * (k) + 1]);                     \
    SB();                                                                                                  \
  } while (0)
#define PK(mi, n, k, st) PHI(acc[1][mi][n], kp[8 * (n) + 2 * (mi) + (k)], kmin, k, st)
#define CV(mi, n, k) do { vp[8 * (n) + 2 * (mi) + (k)] = cvt_pk(acc[2][mi][n][2 * (k)], acc[2][mi][n][2 * (k) + 1]); PIN(vp[8 * (n) + 2 * (mi) + (k)]); SB(); } while (0)
#define F15_RD lf15 = *(volatile lu32x2*)(smem + L_CTL + 4 * C_F15)
#define F15_CHK do { if (__builtin_expect(!all_true((lf15[0] & lf15[1]) != 0u), 0)) { (void)wait_ge<1>(misc + C_F15, 1u); (void)wait_ge<1>(misc + C_F15 + 1, 1u); } CFENCE(); } while (0)
#define PRE(i) do { if ((i) == 30) { x1r = x1w ^ 4096u; PIN(x1r); } if ((i) == 31) { cr = cw ^ 256u; PIN(cr); } \
                    if ((i) == 32) { ones = (u32x4){0x3F803F80u, 0x3F803F80u, 0x3F803F80u, 0x3F803F80u}; PIN(ones); } } while (0)
  MM(1, 0, 0, a[1][0], b[1][2]); SB(); tb1_0_1_l = TRD(B_AT(2, 0, 1, 1)); PRE(0); SB();
  MM(1, 0, 1, a[1][0], b[1][3]); SB(); tb1_0_1 = CAT(tb1_0_1_l, TRD(B_AT(2, 0, 1, 1) + 512)); PRE(1); SB();
  MM(2, 0, 0, a[1][0], b[1][4]); SB(); ta0_0 = A_AT(2, 0, 0); PRE(2); SB();
  MM(2, 0, 1, a[1][0], b[1][5]); SB(); ta0_1 = A_AT(2, 0, 1); PRE(3); SB();
  MM(0, 0, 0, a[1][0], b[1][0]); SB(); ta0_2 = A_AT(2, 0, 2); PRE(4); SB();
  MM(0, 0, 1, a[1][0], b[1][1]); SB(); ta0_3 = A_AT(2, 0, 3); PRE(5); SB();
  MM(1, 1, 0, a[1][1], b[1][2]); SB(); tb1_0_0_l = TRD(B_AT(2, 0, 1, 0)); PRE(6); SB();
  MM(1, 1, 1, a[1][1], b[1][3]); SB(); tb1_0_0 = CAT(tb1_0_0_l, TRD(B_AT(2, 0, 1, 0) + 512)); PRE(7); SB();
  MM(2, 1, 0, a[1][1], b[1][4]); SB(); tb1_1_1_l = TRD(B_AT(2, 1, 1, 1)); PRE(8); SB();
  MM(2, 1, 1, a[1][1], b[1][5]); SB(); tb1_1_1 = CAT(tb1_1_1_l, TRD(B_AT(2, 1, 1, 1) + 512)); PRE(9); SB();
  MM(0, 1, 0, a[1][1], b[1][0]); SB(); ta1_0 = A_AT(2, 1, 0); PRE(10); SB();
  MM(0, 1, 1, a[1][1], b[1][1]); SB(); ta1_1 = A_AT(2, 1, 1); PRE(11); SB();
  MM(1, 2, 0, a[1][2], b[1][2]); SB(); ta1_2 = A_AT(2, 1, 2); PRE(12); SB();
  MM(1, 2, 1, a[1][2], b[1][3]); SB(); ta1_3 = A_AT(2, 1, 3); PRE(13); SB();
  MM(2, 2, 0, a[1][2], b[1][4]); SB(); tb1_1_0_l = TRD(B_AT(2, 1, 1, 0)); PRE(14); SB();
  MM(2, 2, 1, a[1][2], b[1][5]); SB(); tb1_1_0 = CAT(tb1_1_0_l, TRD(B_AT(2, 1, 1, 0) + 512)); PRE(15); SB();
  MM(0, 2, 0, a[1][2], b[1][0]); SB(); PRE(16); SB();
  MM(0, 2, 1, a[1][2], b[1][1]); SB(); PRE(17); SB();
  MM(1, 3, 0, a[1][3], b[1][2]); SB(); PRE(18); SB();
  MM(1, 3, 1, a[1][3], b[1][3]); SB(); PRE(19); SB();
  MM(2, 3, 0, a[1][3], b[1][4]); SB(); F15_RD; PRE(20); SB();
  MM(2, 3, 1, a[1][3], b[1][5]); SB(); PRE(21); SB();
  MM(0, 3, 0, a[1][3], b[1][0]); SB(); tb2_0_1_l = TRD(B_AT(2, 0, 2, 1)); PRE(22); SB();
  MM(0, 3, 1, a[1][3], b[1][1]); SB(); tb2_0_1 = CAT(tb2_0_1_l, TRD(B_AT(2, 0, 2, 1) + 512)); PRE(23); SB();
  MM(1, 0, 1, ta0_0, tb1_0_1); SB(); PRE(24); SB();
  MM(1, 1, 1, ta0_1, tb1_0_1); SB(); PRE(25); SB();
  MM(1, 2, 1, ta0_2, tb1_0_1); SB(); F15_CHK; tb2_1_1_l = TRD(B_AT(2, 1, 2, 1)); PRE(26); SB();
  MM(1, 3, 1, ta0_3, tb1_0_1); SB(); tb2_1_1 = CAT(tb2_1_1_l, TRD(B_AT(2, 1, 2, 1) + 512)); PRE(27); SB();
  MM(1, 0, 0, ta0_0, tb1_0_0); SB(); tb1_2_1_l = TRD(B_AT(0, 0, 1, 1)); PRE(28); SB();
  MM(1, 1, 0, ta0_1, tb1_0_0); SB(); tb1_2_1 = CAT(tb1_2_1_l, TRD(B_AT(0, 0, 1, 1) + 512)); PRE(29); SB();
  MM(1, 2, 0, ta0_2, tb1_0_0); SB(); ta2_0 = A_AT(0, 0, 0); PRE(30); SB();
  MM(1, 3, 0, ta0_3, tb1_0_0); SB(); ta2_1 = A_AT(0, 0, 1); PRE(31); SB();
  MM(1, 0, 1, ta1_0, tb1_1_1); SB(); ta2_2 = A_AT(0, 0, 2); PRE(32); SB();
  MM(1, 1, 1, ta1_1, tb1_1_1); SB(); ta2_3 = A_AT(0, 0, 3); PRE(33); SB();
  MM(1, 2, 1, ta1_2, tb1_1_1); SB(); tb1_3_1_l = TRD(B_AT(0, 1, 1, 1)); PRE(34); SB();
  MM(1, 3, 1, ta1_3, tb1_1_1); SB(); tb1_3_1 = CAT(tb1_3_1_l, TRD(B_AT(0, 1, 1, 1) + 512)); PRE(35); SB();
  MM(1, 0, 0, ta1_0, tb1_1_0); SB(); ta3_0 = A_AT(0, 1, 0); PRE(36); SB();
  MM(1, 1, 0, ta1_1, tb1_1_0); SB(); ta3_1 = A_AT(0, 1, 1); PRE(37); SB();
  MM(1, 2, 0, ta1_2, tb1_1_0); SB(); ta3_2 = A_AT(0, 1, 2); PRE(38); SB();
  MM(1, 3, 0, ta1_3, tb1_1_0); SB(); ta3_3 = A_AT(0, 1, 3); PRE(39); SB();
  MM(1, 0, 1, ta2_0, tb1_2_1); SB(); tb1_2_0_l = TRD(B_AT(0, 0, 1, 0)); PRE(40); SB();
  MM(1, 1, 1, ta2_1, tb1_2_1); SB(); tb1_2_0 = CAT(tb1_2_0_l, TRD(B_AT(0, 0, 1, 0) + 512)); PRE(41); SB();
  MM(1, 2, 1, ta2_2, tb1_2_1); SB(); tb1_3_0_l = TRD(B_AT(0, 1, 1, 0)); PRE(42); SB();
  MM(1, 3, 1, ta2_3, tb1_2_1); SB(); tb1_3_0 = CAT(tb1_3_0_l, TRD(B_AT(0, 1, 1, 0) + 512)); PRE(43); SB();
  MM(1, 0, 1, ta3_0, tb1_3_1); SB(); tb2_2_1_l = TRD(B_AT(0, 0, 2, 1)); PRE(44); SB();
  MM(1, 1, 1, ta3_1, tb1_3_1); SB(); tb2_2_1 = CAT(tb2_2_1_l, TRD(B_AT(0, 0, 2, 1) + 512)); PRE(45); SB();
  MM(1, 2, 1, ta3_2, tb1_3_1); SB(); tb2_3_1_l = TRD(B_AT(0, 1, 2, 1)); PRE(46); SB();
  MM(1, 3, 1, ta3_3, tb1_3_1); SB(); tb2_3_1 = CAT(tb2_3_1_l, TRD(B_AT(0, 1, 2, 1) + 512)); PRE(47); SB();
  MM(1, 0, 0, ta2_0, tb1_2_0); SB(); tb2_0_0_l = TRD(B_AT(2, 0, 2, 0)); PK(0, 1, 0, 0); PK(0, 1, 0, 1); PK(0, 1, 1, 0); PK(0, 1, 1, 1); PK(0, 1, 0, 2); SB();
  MM(1, 1, 0, ta2_1, tb1_2_0); SB(); tb2_0_0 = CAT(tb2_0_0_l, TRD(B_AT(2, 0, 2, 0) + 512)); PK(0, 1, 0, 3); PK(0, 1, 0, 7); PK(0, 1, 1, 2); SB();
  MM(1, 2, 0, ta2_2, tb1_2_0); SB(); tb2_1_0_l = TRD(B_AT(2, 1, 2, 0)); PK(0, 1, 1, 3); PK(0, 1, 1, 7); PK(0, 1, 0, 4); PK(0, 1, 0, 5); SB();
  MM(1, 3, 0, ta2_3, tb1_2_0); SB(); tb2_1_0 = CAT(tb2_1_0_l, TRD(B_AT(2, 1, 2, 0) + 512)); PK(0, 1, 1, 4); PK(0, 1, 1, 5); PK(0, 1, 0, 6); PK(0, 1, 1, 6); PK(1, 1, 0, 0); SB();
  MM(1, 0, 0, ta3_0, tb1_3_0); SB(); tb2_2_0_l = TRD(B_AT(0, 0, 2, 0)); PK(1, 1, 0, 1); PK(1, 1, 1, 0); PK(1, 1, 1, 1); PK(1, 1, 0, 2); SB();
  MM(1, 1, 0, ta3_1, tb1_3_0); SB(); tb2_2_0 = CAT(tb2_2_0_l, TRD(B_AT(0, 0, 2, 0) + 512)); PK(1, 1, 0, 3); PK(1, 1, 0, 7); PK(1, 1, 1, 2); SB();
  MM(1, 2, 0, ta3_2, tb1_3_0); SB(); tb2_3_0_l = TRD(B_AT(0, 1, 2, 0)); PK(1, 1, 1, 3); PK(1, 1, 1, 7); PK(1, 1, 0, 4); PK(1, 1, 0, 5); SB();
  MM(1, 3, 0, ta3_3, tb1_3_0); SB(); tb2_3_0 = CAT(tb2_3_0_l, TRD(B_AT(0, 1, 2, 0) + 512)); PK(1, 1, 1, 4); PK(1, 1, 1, 5); PK(1, 1, 0, 6); PK(1, 1, 1, 6); SB();
  MM(2, 0, 1, ta0_0, tb2_0_1); SB(); PK(2, 1, 0, 0); PK(2, 1, 0, 1); PK(2, 1, 1, 0); PK(2, 1, 1, 1); SB();
  MM(2, 1, 1, ta0_1, tb2_0_1); SB(); PK(2, 1, 0, 2); PK(2, 1, 0, 3); SB();
  MM(2, 2, 1, ta0_2, tb2_0_1); SB(); PK(2, 1, 0, 7); PK(2, 1, 1, 2); PK(2, 1, 1, 3); SB();
  MM(2, 3, 1, ta0_3, tb2_0_1); SB(); PK(2, 1, 1, 7); PK(2, 1, 0, 4); PK(2, 1, 0, 5); PK(2, 1, 1, 4); SB();
  MM(2, 0, 1, ta1_0, tb2_1_1); SB(); PK(2, 1, 1, 5); PK(2, 1, 0, 6); PK(2, 1, 1, 6); PK(3, 1, 0, 0); SB();
  MM(2, 1, 1, ta1_1, tb2_1_1); SB(); PK(3, 1, 0, 1); PK(3, 1, 1, 0); PK(3, 1, 1, 1); PK(3, 1, 0, 2); SB();
  MM(2, 2, 1, ta1_2, tb2_1_1); SB(); PK(3, 1, 0, 3); PK(3, 1, 0, 7); PK(3, 1, 1, 2); SB();
  MM(2, 3, 1, ta1_3, tb2_1_1); SB(); PK(3, 1, 1, 3); PK(3, 1, 1, 7); PK(3, 1, 0, 4); SB();
  MM(2, 0, 1, ta2_0, tb2_2_1); SB(); PK(3, 1, 0, 5); PK(3, 1, 1, 4); PK(3, 1, 1, 5); PK(3, 1, 0, 6); SB();
  MM(2, 1, 1, ta2_1, tb2_2_1); SB(); PK(3, 1, 1, 6); PK(0, 0, 0, 0); PK(0, 0, 0, 1); PK(0, 0, 1, 0); SB();
  MM(2, 2, 1, ta2_2, tb2_2_1); SB(); PK(0, 0, 1, 1); PK(0, 0, 0, 2); PK(0, 0, 0, 3); SB();
  MM(2, 3, 1, ta2_3, tb2_2_1); SB(); PK(0, 0, 0, 7); PK(0, 0, 1, 2); PK(0, 0, 1, 3); SB();
  MM(2, 0, 1, ta3_0, tb2_3_1); SB(); PK(0, 0, 1, 7); PK(0, 0, 0, 4); PK(0, 0, 0, 5); PK(0, 0, 1, 4); SB();
  MM(2, 1, 1, ta3_1, tb2_3_1); SB(); PK(0, 0, 1, 5); PK(0, 0, 0, 6); PK(0, 0, 1, 6); PK(1, 0, 0, 0); SB();
  MM(2, 2, 1, ta3_2, tb2_3_1); SB(); PK(1, 0, 0, 1); PK(1, 0, 1, 0); PK(1, 0, 1, 1); PK(1, 0, 0, 2); SB();
  MM(2, 3, 1, ta3_3, tb2_3_1); SB(); PK(1, 0, 0, 3); PK(1, 0, 0, 7); PK(1, 0, 1, 2); SB();
  MM(2, 0, 0, ta0_0, tb2_0_0); SB(); PK(1, 0, 1, 3); PK(1, 0, 1, 7); PK(1, 0, 0, 4); SB();
  MM(2, 1, 0, ta0_1, tb2_0_0); SB(); PK(1, 0, 0, 5); PK(1, 0, 1, 4); PK(1, 0, 1, 5); PK(1, 0, 0, 6); SB();
  MM(2, 2, 0, ta0_2, tb2_0_0); SB(); PK(1, 0, 1, 6); PK(2, 0, 0, 0); PK(2, 0, 0, 1); PK(2, 0, 1, 0); SB();
  MM(2, 3, 0, ta0_3, tb2_0_0); SB(); PK(2, 0, 1, 1); PK(2, 0, 0, 2); PK(2, 0, 0, 3); SB();
  MM(2, 0, 0, ta1_0, tb2_1_0); SB(); PK(2, 0, 0, 7); PK(2, 0, 1, 2); PK(2, 0, 1, 3); SB();
  MM(2, 1, 0, ta1_1, tb2_1_0); SB(); PK(2, 0, 1, 7); PK(2, 0, 0, 4); PK(2, 0, 0, 5); PK(2, 0, 1, 4); SB();
  MM(2, 2, 0, ta1_2, tb2_1_0); SB(); PK(2, 0, 1, 5); PK(2, 0, 0, 6); PK(2, 0, 1, 6); PK(3, 0, 0, 0); SB();
  MM(2, 3, 0, ta1_3, tb2_1_0); SB(); PK(3, 0, 0, 1); PK(3, 0, 1, 0); PK(3, 0, 1, 1); SB();
  MM(2, 0, 0, ta2_0, tb2_2_0); SB(); PK(3, 0, 0, 2); PK(3, 0, 0, 3); SB();
  MM(2, 1, 0, ta2_1, tb2_2_0); SB(); PK(3, 0, 0, 7); PK(3, 0, 1, 2); SB();
  MM(2, 2, 0, ta2_2, tb2_2_0); SB(); PK(3, 0, 1, 3); PK(3, 0, 1, 7); SB();
  MM(2, 3, 0, ta2_3, tb2_2_0); SB(); PK(3, 0, 0, 4); PK(3, 0, 0, 5); PK(3, 0, 1, 4); SB();
  MM(2, 0, 0, ta3_0, tb2_3_0); SB(); PK(3, 0, 1, 5); PK(3, 0, 0, 6); PK(3, 0, 1, 6); SB();
  MM(2, 1, 0, ta3_1, tb2_3_0); SB(); CV(0, 1, 0); CV(0, 1, 1); CV(1, 1, 0); SB();
  MM(2, 2, 0, ta3_2, tb2_3_0); SB(); CV(1, 1, 1); CV(2, 1, 0); CV(2, 1, 1); CV(3, 1, 0); SB();
  MM(2, 3, 0, ta3_3, tb2_3_0); SB(); CV(3, 1, 1); SB();
  const f32x4 zero = {0.f, 0.f, 0.f, 0.f};
#define TAKE(flag, READS)                                                                                  \
  _Pragma("unroll 1") for (unsigned spins = 0; spins < (1u << 22); ++spins) {                              \
    const unsigned f_ = (flag);                                                                            \
    CFENCE();                                                                                              \
    READS;                                                                                                 \
    if (__builtin_expect(rfl(f_) != 0u, 1)) break;                                                         \
  }
  unsigned lidx, ltag;
  {
    phi_fix(kp, kmin);
#define KF(j, n) FRAG(((u32x4){kp[8 * (n) + 4 * (j)], kp[8 * (n) + 4 * (j) + 1], kp[8 * (n) + 4 * (j) + 2], kp[8 * (n) + 4 * (j) + 3]}))
#define VF(j, n) FRAG(((u32x4){vp[8 * (n) + 4 * (j)], vp[8 * (n) + 4 * (j) + 1], vp[8 * (n) + 4 * (j) + 2], vp[8 * (n) + 4 * (j) + 3]}))
    f32x4 s1 = MFMA(KF(0, 1), VF(0, 1), zero), s0 = MFMA(KF(0, 0), VF(0, 1), zero), zs = MFMA(KF(0, 1), FRAG(ones), zero);
    s1 = MFMA(KF(1, 1), VF(1, 1), s1); s0 = MFMA(KF(1, 0), VF(1, 1), s0); zs = MFMA(KF(1, 1), FRAG(ones), zs);
    f32x4 z = MFMA(KF(1, 0), FRAG(ones), MFMA(KF(0, 0), FRAG(ones), zero));
    SB();
    CV(0, 0, 0); CV(0, 0, 1); CV(1, 0, 0); CV(1, 0, 1);
    *LP(f32x4, x1w, 0) = s1; *LP(f32x4, x1w, 1024) = s0; *LP(f32x4, cw, 0) = zs;
    CFENCE();
    *LP(volatile unsigned, cw, W_X1) = one;
    SB();
    CV(2, 0, 0); CV(2, 0, 1); CV(3, 0, 0); CV(3, 0, 1);
    f32x4 kv0 = MFMA(KF(0, 0), VF(0, 0), zero), kv1 = MFMA(KF(0, 1), VF(0, 0), zero);
    kv0 = MFMA(KF(1, 0), VF(1, 0), kv0); kv1 = MFMA(KF(1, 1), VF(1, 0), kv1);
    SB();
    f32x4 p0, p1, pz;
#define X1_RD { p0 = *LP(volatile f32x4, x1r, 0); p1 = *LP(volatile f32x4, x1r, 1024); pz = *LP(volatile f32x4, cr, 0); }
    const unsigned f1 = *LP(volatile unsigned, cr, W_X1);
    CFENCE();
    X1_RD
    SB();
    tb0_0_0_l = TRD(B_AT(2, 0, 0, 0)); tb0_0_0 = CAT(tb0_0_0_l, TRD(B_AT(2, 0, 0, 0) + 512));
    tb0_0_1_l = TRD(B_AT(2, 0, 0, 1)); tb0_0_1 = CAT(tb0_0_1_l, TRD(B_AT(2, 0, 0, 1) + 512));
    tb0_1_0_l = TRD(B_AT(2, 1, 0, 0)); tb0_1_0 = CAT(tb0_1_0_l, TRD(B_AT(2, 1, 0, 0) + 512));
    tb0_1_1_l = TRD(B_AT(2, 1, 0, 1)); tb0_1_1 = CAT(tb0_1_1_l, TRD(B_AT(2, 1, 0, 1) + 512));
    tb0_2_0_l = TRD(B_AT(0, 0, 0, 0)); tb0_2_0 = CAT(tb0_2_0_l, TRD(B_AT(0, 0, 0, 0) + 512));
    tb0_2_1_l = TRD(B_AT(0, 0, 0, 1)); tb0_2_1 = CAT(tb0_2_1_l, TRD(B_AT(0, 0, 0, 1) + 512));
    tb0_3_0_l = TRD(B_AT(0, 1, 0, 0)); tb0_3_0 = CAT(tb0_3_0_l, TRD(B_AT(0, 1, 0, 0) + 512));
    tb0_3_1_l = TRD(B_AT(0, 1, 0, 1)); tb0_3_1 = CAT(tb0_3_1_l, TRD(B_AT(0, 1, 0, 1) + 512));
    SB();
    if (__builtin_expect(!all_true(f1 != 0u), 0)) { TAKE(*LP(volatile unsigned, cr, W_X1), X1_RD); LGKM0(); }
    const f32x4 m0_ = kv0 + p0, m1_ = kv1 + p1;
    float am = fmax_raw(fabsf(m0_[0]), fabsf(m1_[0]));
#pragma unroll
    for (int e = 1; e < 4; ++e) am = fmax_raw(fmax_raw(am, fabsf(m0_[e])), fabsf(m1_[e]));
#define PKH(a_, b_) __builtin_bit_cast(unsigned, __builtin_convertvector((f32x2){(a_), (b_)}, f16x2_t))
    u32x4 hv = {PKH(m0_[0], m0_[1]), PKH(m0_[2], m0_[3]), PKH(m1_[0], m1_[1]), PKH(m1_[2], m1_[3])};
#define Z_STORE(scl) do { unsigned z0_ = zv[0]; asm("v_writelane_b32 %0, %1, 1" : "+v"(z0_) : "s"(scl)); zv[0] = z0_; __builtin_amdgcn_raw_buffer_store_b128(zv, wsr, zo, 0, 16); } while (0)
    PIN(hv);
    if (__builtin_expect(all_true(f2u(am) - 0x3C800000u <= 0x47000000u - 0x3C800000u), 1)) {
      __builtin_amdgcn_raw_buffer_store_b128(hv, wsr, kvo, 0, 16);
      SB();
      u32x4 zv = __builtin_bit_cast(u32x4, z + pz);
      Z_STORE(0x3F800000u);
    } else {
      unsigned ex = max(wave_max(f2u(am) & 0x7FFFFFFFu) >> 23, 15u);
      if (ex >= 121u && ex < 142u) ex = 141u;
      const float inv = u2f((268u - ex) << 23);
      u32x4 hs = {PKH(m0_[0] * inv, m0_[1] * inv), PKH(m0_[2] * inv, m0_[3] * inv), PKH(m1_[0] * inv, m1_[1] * inv), PKH(m1_[2] * inv, m1_[3] * inv)};
      u32x4 zv = __builtin_bit_cast(u32x4, z + pz);
#pragma unroll
      for (int e = 0; e < 4; ++e) { hs[e] = min(hs[e], ~1u); zv[e] = min(zv[e], ~1u); }
      CFENCE();
      __builtin_amdgcn_raw_buffer_store_b128(hs, wsr, kvo, 0, 16);
      Z_STORE((ex - 14u) << 23);
      CFENCE();
    }
  }
  *LP(volatile unsigned, cw, W_KS) = one;
  unsigned eoff, ko, oo, x2w, zfa, zfb, zfp, gta;
  u32x2 eye;
  u32x4 xv[4], t[16];
  float gq[4];
  unsigned pq[16];
#define PQ(mi, n, k, st) PHI(acc[0][mi][n], pq[4 * (mi) + 2 * (n) + (k)], qmin, k, st)
#define NOPQ() asm volatile("s_nop 4" : "+v"(acc[0][3][0]), "+v"(acc[0][3][1]))
#define PRQ(i)                                                                                             \
  do {                                                                                                     \
    if ((i) == 0) { lidx = misc[C_IDX + wr]; ltag = misc[C_TAG]; }                                         \
    if ((i) == 1) { eoff = (unsigned)(((m0 + 32 * w + c16) * DM + 64 * nt3 + 16 * (g & 1) + 8 * (g >> 1)) * 2); PIN(eoff); } \
    if ((i) >= 3 && (i) < 7) xv[((i) - 3) & 3] = __builtin_amdgcn_raw_buffer_load_b128(xr, eoff + (((i) - 3) & 1) * 64, (((i) - 3) >> 1) * (32 * DM), 0); \
    if ((i) == 2) { zfp = L_CTL + 4 * (C_ZF + hh); PIN(zfp); }                                             \
    if ((i) == 3) { ko = kvo - mt * 1024; PIN(ko); }                                                       \
    if ((i) == 4) { gta = L_GATE + wr * 256 + c16 * 4; PIN(gta); }                                         \
    if ((i) == 5) { zfa = L_ZF + hh * 64 + wr * 8 + g * 16; PIN(zfa); }                                    \
    if ((i) == 6) { zfb = zfa ^ 8u; PIN(zfb); }                                                            \
    if ((i) == 7) { const unsigned o_ = 0x3C00u << (16 * (lane & 1)); eye = (u32x2){(lane & 2) ? 0u : o_, (lane & 2) ? o_ : 0u}; PIN(eye); } \
    if ((i) == 8) { x2w = L_X2 + w * 1024 + lane * 16; PIN(x2w); }                                         \
    if ((i) == 9) { oo = (unsigned)OFF_OF + (unsigned)(((mt * 8 + wr * 4) * NH + head) * 1024) + lane * 16; PIN(oo); } \
  } while (0)
#define SHOT_GATE                                                                                          \
  do {                                                                                                     \
    if (__builtin_expect(!all_true(lidx != 0u), 0)) (void)wait_ge<1>(misc + C_IDX + wr, 1u);        \
    (void)wait_ge<1>(misc + C_GO + hh, 1u);                                                                \
    CFENCE();                                                                                              \
    _Pragma("unroll") for (int i = 0; i < 4; ++i) gq[i] = *LP(volatile float, gta, i * 64);                \
  } while (0)
#define T_LD(i) t[i] = __builtin_amdgcn_raw_buffer_load_b128(wsr, ko + ((i) & 3) * 1024, ((i) >> 2) * 4096, 16)
  SB();
  MM(0, 0, 0, ta0_0, tb0_0_0); SB(); PRQ(0); SB();
  MM(0, 0, 1, ta0_0, tb0_0_1); SB(); PRQ(1); SB();
  MM(0, 0, 0, ta1_0, tb0_1_0); SB(); PRQ(2); SB();
  MM(0, 0, 1, ta1_0, tb0_1_1); SB(); PRQ(3); SB();
  MM(0, 0, 0, ta2_0, tb0_2_0); SB(); PRQ(4); SB();
  MM(0, 0, 1, ta2_0, tb0_2_1); SB(); PRQ(5); SB();
  MM(0, 0, 0, ta3_0, tb0_3_0); SB(); PRQ(6); SB();
  MM(0, 0, 1, ta3_0, tb0_3_1); SB(); PRQ(7); SB();
  MM(0, 1, 0, ta0_1, tb0_0_0); SB(); PRQ(8); SB();
  MM(0, 1, 1, ta0_1, tb0_0_1); SB(); PRQ(9); SB();
  MM(0, 1, 0, ta1_1, tb0_1_0); SB(); PQ(0, 0, 0, 0); PQ(0, 0, 0, 1); PQ(0, 0, 1, 0); SB();
  MM(0, 1, 1, ta1_1, tb0_1_1); SB(); PQ(0, 0, 1, 1); PQ(0, 0, 0, 2); SB();
  MM(0, 1, 0, ta2_1, tb0_2_0); SB(); PQ(0, 0, 0, 3); PQ(0, 0, 0, 7); SB();
  MM(0, 1, 1, ta2_1, tb0_2_1); SB(); PQ(0, 0, 1, 2); PQ(0, 0, 1, 3); SB();
  MM(0, 1, 0, ta3_1, tb0_3_0); SB(); PQ(0, 0, 1, 7); PQ(0, 0, 0, 4); PQ(0, 0, 0, 5); SB();
  MM(0, 1, 1, ta3_1, tb0_3_1); SB(); PQ(0, 0, 1, 4); PQ(0, 0, 1, 5); PQ(0, 0, 0, 6); SB();
  MM(0, 2, 0, ta0_2, tb0_0_0); SB(); PQ(0, 0, 1, 6); PQ(0, 1, 0, 0); PQ(0, 1, 0, 1); SB();
  MM(0, 2, 1, ta0_2, tb0_0_1); SB(); PQ(0, 1, 1, 0); PQ(0, 1, 1, 1); PQ(0, 1, 0, 2); SB();
  MM(0, 2, 0, ta1_2, tb0_1_0); SB(); PQ(0, 1, 0, 3); PQ(0, 1, 0, 7); SB();
  MM(0, 2, 1, ta1_2, tb0_1_1); SB(); PQ(0, 1, 1, 2); PQ(0, 1, 1, 3); SB();
  MM(0, 2, 0, ta2_2, tb0_2_0); SB(); PQ(0, 1, 1, 7); PQ(0, 1, 0, 4); PQ(0, 1, 0, 5); SB();
  MM(0, 2, 1, ta2_2, tb0_2_1); SB(); PQ(0, 1, 1, 4); PQ(0, 1, 1, 5); PQ(0, 1, 0, 6); SB();
  MM(0, 2, 0, ta3_2, tb0_3_0); SB(); PQ(0, 1, 1, 6); PQ(1, 0, 0, 0); PQ(1, 0, 0, 1); SB();
  MM(0, 2, 1, ta3_2, tb0_3_1); SB(); PQ(1, 0, 1, 0); PQ(1, 0, 1, 1); PQ(1, 0, 0, 2); SB();
  MM(0, 3, 0, ta0_3, tb0_0_0); SB(); PQ(1, 0, 0, 3); PQ(1, 0, 0, 7); SB();
  MM(0, 3, 1, ta0_3, tb0_0_1); SB(); PQ(1, 0, 1, 2); PQ(1, 0, 1, 3); SB();
  MM(0, 3, 0, ta1_3, tb0_1_0); SB(); PQ(1, 0, 1, 7); PQ(1, 0, 0, 4); PQ(1, 0, 0, 5); SB();
  MM(0, 3, 1, ta1_3, tb0_1_1); SB(); PQ(1, 0, 1, 4); PQ(1, 0, 1, 5); PQ(1, 0, 0, 6); SB();
  MM(0, 3, 0, ta2_3, tb0_2_0); SB(); PQ(1, 0, 1, 6); PQ(1, 1, 0, 0); PQ(1, 1, 0, 1); SB();
  MM(0, 3, 1, ta2_3, tb0_2_1); SB(); PQ(1, 1, 1, 0); PQ(1, 1, 1, 1); PQ(1, 1, 0, 2); SB();
  MM(0, 3, 0, ta3_3, tb0_3_0); SB(); PQ(1, 1, 0, 3); PQ(1, 1, 0, 7); SB();
  MM(0, 3, 1, ta3_3, tb0_3_1); SB(); PQ(1, 1, 1, 2); PQ(1, 1, 1, 3); SB();
  PQ(1, 1, 1, 7); SB();
  PQ(1, 1, 0, 4); SB();
  PQ(1, 1, 0, 5); SB();
  PQ(1, 1, 1, 4); SB();
  PQ(1, 1, 1, 5); SB();
  PQ(1, 1, 0, 6); SB();
  PQ(1, 1, 1, 6); SB();
  PQ(2, 0, 0, 0); SB();
  PQ(2, 0, 0, 1); SB();
  PQ(2, 0, 1, 0); SB();
  PQ(2, 0, 1, 1); SB();
  PQ(2, 0, 0, 2); SB();
  PQ(2, 0, 0, 3); SB();
  PQ(2, 0, 0, 7); SB();
  PQ(2, 0, 1, 2); SB();
  PQ(2, 0, 1, 3); SB();
  PQ(2, 0, 1, 7); SB();
  PQ(2, 0, 0, 4); SB();
  PQ(2, 0, 0, 5); SB();
  PQ(2, 0, 1, 4); SB();
  PQ(2, 0, 1, 5); SB();
  PQ(2, 0, 0, 6); SB();
  PQ(2, 0, 1, 6); SB();
  PQ(2, 1, 0, 0); SB();
  PQ(2, 1, 0, 1); SB();
  PQ(2, 1, 1, 0); SB();
  PQ(2, 1, 1, 1); SB();
  PQ(2, 1, 0, 2); SB();
  PQ(2, 1, 0, 3); SB();
  PQ(2, 1, 0, 7); SB();
  PQ(2, 1, 1, 2); SB();
  PQ(2, 1, 1, 3); SB();
  PQ(2, 1, 1, 7); SB();
  PQ(2, 1, 0, 4); SB();
  PQ(2, 1, 0, 5); SB();
  PQ(2, 1, 1, 4); SB();
  PQ(2, 1, 1, 5); SB();
  PQ(2, 1, 0, 6); SB();
  PQ(2, 1, 1, 6); SB();
  NOPQ();
  PQ(3, 0, 0, 0); SB();
  PQ(3, 0, 0, 1); SB();
  PQ(3, 0, 1, 0); SB();
  PQ(3, 0, 1, 1); SB();
  PQ(3, 0, 0, 2); SB();
  PQ(3, 0, 0, 3); SB();
  PQ(3, 0, 0, 7); SB();
  PQ(3, 0, 1, 2); SB();
  PQ(3, 0, 1, 3); SB();
  PQ(3, 0, 1, 7); SB();
  PQ(3, 0, 0, 4); SB();
  PQ(3, 0, 0, 5); SB();
  PQ(3, 0, 1, 4); SB();
  PQ(3, 0, 1, 5); SB();
  PQ(3, 0, 0, 6); SB();
  PQ(3, 0, 1, 6); SB();
  PQ(3, 1, 0, 0); SB();
  PQ(3, 1, 0, 1); SB();
  PQ(3, 1, 1, 0); SB();
  PQ(3, 1, 1, 1); SB();
  PQ(3, 1, 0, 2); SB();
  PQ(3, 1, 0, 3); SB();
  PQ(3, 1, 0, 7); SB();
  PQ(3, 1, 1, 2); SB();
  PQ(3, 1, 1, 3); SB();
  PQ(3, 1, 1, 7); SB();
  PQ(3, 1, 0, 4); SB();
  PQ(3, 1, 0, 5); SB();
  PQ(3, 1, 1, 4); SB();
  PQ(3, 1, 1, 5); SB();
  PQ(3, 1, 0, 6); SB();
  PQ(3, 1, 1, 6); SB();
  SHOT_GATE;
  T_LD(0);
  T_LD(1);
  T_LD(2);
  T_LD(3);
  T_LD(4);
  T_LD(5);
  T_LD(6);
  T_LD(7);
  T_LD(8);
  T_LD(9);
  T_LD(10);
  T_LD(11);
  T_LD(12);
  T_LD(13);
  T_LD(14);
  T_LD(15);
  *LP(volatile unsigned, cw, W_AG) = one;
  phi_fix(pq, qmin);
  const unsigned tag = rfl(ltag);
  {
#define PF(mi) FRAG(((u32x4){pq[4 * (mi)], pq[4 * (mi) + 1], pq[4 * (mi) + 2], pq[4 * (mi) + 3]}))
#define H4(v, k) __builtin_bit_cast(f16x4_t, (u32x2){(v)[2 * (k)], (v)[2 * (k) + 1]})
    f32x4 s0 = zero, s1 = zero;
#define KV_MM(i0, i1)                                                                                      \
  _Pragma("unroll") for (int i = (i0); i < (i1); ++i) {                                                    \
    s0 = __builtin_amdgcn_mfma_f32_4x4x4f16(__builtin_bit_cast(f16x4_t, eye), H4(t[i], 0), s0, 0, 0, 0);   \
    s1 = __builtin_amdgcn_mfma_f32_4x4x4f16(__builtin_bit_cast(f16x4_t, eye), H4(t[i], 1), s1, 0, 0, 0);   \
  }
    float rc[4];
    u32x2 z0, z1;
#define Z_RD { z0 = *LP(volatile u32x2, zfa, 0); z1 = *LP(volatile u32x2, zfb, 0); }
    f32x4 d[4];
#define DEN1() do { const u32x4 zf = {z0[0], z0[1], z1[0], z1[1]}; _Pragma("unroll") for (int mi = 0; mi < 4; ++mi) d[mi] = MFMA(FRAG(zf), PF(mi), zero); } while (0)
#define DEN2()                                                                                             \
  do {                                                                                                     \
    _Pragma("unroll") for (int mi = 0; mi < 4; ++mi) rc[mi] = __builtin_amdgcn_rcpf(d[mi][0] + 1e-6f);     \
    _Pragma("unroll") for (int mi = 0; mi < 4; ++mi) { rc[mi] *= gq[mi]; PIN(rc[mi]); }                    \
  } while (0)
    KV_MM(0, DENAT - 4);
    SB();
    unsigned zfl = *LP(volatile unsigned, zfp, 0);
    CFENCE();
    Z_RD
    SB();
    KV_MM(DENAT - 4, DENAT);
    SB();
    DEN1();
    SB();
    KV_MM(DENAT, DENAT + 2);
    SB();
    DEN2();
    const u64 zbad = __builtin_amdgcn_ballot_w64(zfl != 1u);
    SB();
    KV_MM(DENAT + 2, 16);
    if (__builtin_expect((__builtin_amdgcn_ballot_w64(__builtin_isunordered(s0[0], s1[0])) | zbad) != 0ull, 0)) {
      unsigned todo = 15u;
#pragma unroll 1
      for (unsigned spins = 0; spins <= SPIN_CAP; ++spins) {
        unsigned miss = 0;
#pragma unroll
        for (int q = 0; q < 4; ++q)
          if (todo & (1u << q)) { if (!all_true(max(max(MX4(t[4 * q]), MX4(t[4 * q + 1])), max(MX4(t[4 * q + 2]), MX4(t[4 * q + 3]))) != ~0u)) miss |= 1u << q; }
        todo = miss;
        if (todo == 0u) break;
        __builtin_amdgcn_s_sleep(2);
        CFENCE();
#pragma unroll
        for (int q = 0; q < 4; ++q)
          if (todo & (1u << q)) { T_LD(4 * q); T_LD(4 * q + 1); T_LD(4 * q + 2); T_LD(4 * q + 3); }
      }
      s0 = zero; s1 = zero;
      KV_MM(0, 16);
      zfl = rfl(zfl);
      if (zfl == 0u) { TAKE(zfl = misc[C_ZF + hh], Z_RD); zfl = rfl(zfl); DEN1(); DEN2(); }
      if (zfl != 1u) {
        const unsigned sc = __builtin_amdgcn_raw_buffer_load_b32(wsr, (unsigned)OFF_ZP + (rec - mt + c16) * 128 + 64, 0, 16);
        s0 = zero; s1 = zero;
#pragma unroll
        for (int i = 0; i < 16; ++i) {
          const float s_ = u2f((unsigned)__builtin_amdgcn_readlane((int)sc, i));
#pragma unroll
          for (int e = 0; e < 4; ++e) {
            const unsigned tw = t[i][e];
            const f16x2_t h = __builtin_bit_cast(f16x2_t, tw);
            f32x4& d = e < 2 ? s0 : s1;
            d[2 * (e & 1)] = __builtin_fmaf((float)h[0], s_, d[2 * (e & 1)]); d[2 * (e & 1) + 1] = __builtin_fmaf((float)h[1], s_, d[2 * (e & 1) + 1]);
          }
        }
      }
    }
    const u32x4 kown = {cvt_pk(s0[0], s0[1]), cvt_pk(s0[2], s0[3]), cvt_pk(s1[0], s1[1]), cvt_pk(s1[2], s1[3])};
    *LP(u32x2, x2w, 8) = (u32x2){kown[0], kown[1]}; *LP(u32x2, x2w, 0) = (u32x2){kown[2], kown[3]};
    CFENCE();
    *LP(volatile unsigned, cw, W_X2) = one;
    SB();
    f32x4 o[4];
    u32x2 ov[4][2];
    u32x4 koth;
#pragma unroll
    for (int mi = 0; mi < 4; ++mi) o[mi] = MFMA(FRAG(kown), PF(mi), zero);
    const unsigned x2r = x2w ^ 2048u;
    SB();
#define O_CVT(mi, h) do { ov[mi][h] = (u32x2){cvt_pk(o[mi][0] * rc[mi], o[mi][1] * rc[mi]), cvt_pk(o[mi][2] * rc[mi], o[mi][3] * rc[mi])}; if ((h) == 0) PIN(ov[mi][0]); } while (0)
    O_CVT(0, 0); SB();
    const unsigned f2 = *LP(volatile unsigned, cr, W_X2);
    CFENCE();
    koth = *LP(volatile u32x4, x2r, 0);
    SB();
    O_CVT(1, 0); O_CVT(2, 0); O_CVT(3, 0);
    SB();
    if (__builtin_expect(!all_true(f2 != 0u), 0)) TAKE(*LP(volatile unsigned, cr, W_X2), koth = *LP(volatile u32x4, x2r, 0));
#pragma unroll
    for (int mi = 0; mi < 4; ++mi) o[mi] = MFMA(FRAG(koth), PF(mi), zero);
    SB();
    unsigned oom[4];
#pragma unroll
    for (int mi = 0; mi < 4; ++mi) { oom[mi] = oo + mi * (NH * 1024); PIN(oom[mi]); }
    SB();
#pragma unroll
    for (int mi = 0; mi < 4; ++mi) { O_CVT(mi, 1); __builtin_amdgcn_raw_buffer_store_b128((u32x4){ov[mi][0][0], ov[mi][0][1], ov[mi][1][0], ov[mi][1][1]}, wsr, oom[mi], 0, 16); SB(); }
  }
  SB();
  f32x4 oc[4][2];
#pragma unroll
  for (int i = 0; i < 4; ++i) {
    const int tt = i >> 1, n = 2 * (i & 1);
    const float omg = bfr(1.0f - (tt ? (hh ? gq[3] : gq[1]) : (hh ? gq[2] : gq[0])));
#pragma unroll
    for (int k = 0; k < 2; ++k) {
      const auto sw = __builtin_amdgcn_permlane16_swap(cvt_pk(omg * blo(xv[i][k]), omg * bhi(xv[i][k])),
                                                       cvt_pk(omg * blo(xv[i][2 + k]), omg * bhi(xv[i][2 + k])), false, false);
      oc[n][tt][2 * k] = blo(sw[0]); oc[n][tt][2 * k + 1] = bhi(sw[0]);
      oc[n + 1][tt][2 * k] = blo(sw[1]); oc[n + 1][tt][2 * k + 1] = bhi(sw[1]);
    }
    PIN(oc[n][tt]); PIN(oc[n + 1][tt]);
  }
  int wof[2][2][4];
#pragma unroll
  for (int n = 0; n < 16; ++n) {
    wof[n >> 3][(n >> 2) & 1][n & 3] = 16384 + ((n >> 2) & 1) * 65536 + (((n >> 3) ^ wr) * 16 + 4 * g + (c16 >> 2)) * 128 + (((n & 3) ^ fsw) * 32) + (c16 & 3) * 8;
    PIN(wof[n >> 3][(n >> 2) & 1][n & 3]);
  }
  const unsigned obase = (unsigned)OFF_OF + (unsigned)(((mt3 * 8 + 2 * w) * NH) * 1024 + lane * 16);
  bf16x8 wf[2][4];
#define W_AT(hd, n, r) (smem + wof[r][WOPOS((hd) >> 1) >> 3][n] + (WOPOS((hd) >> 1) & 7) * 8192 + ((hd) & 1) * 4096)
  const unsigned lw = min(misc[C_WO], misc[C_WO + 1]);
  WAIT_VM(0);
  if (lane == 0) {
    const unsigned old = __hip_atomic_fetch_add((lu32*)(smem + L_CTL) + C_OARR, 1u, RLX_WG);
    if (old == 3u) {
      __hip_atomic_store((gu32*)(ws + OFF_OFLAG + (mt * 16 + nt) * 128), tag, RLX_AGENT);
      misc[C_T6] = (unsigned)__builtin_amdgcn_s_memrealtime() | 1u;
    }
  }
  CFENCE();

  if (__builtin_expect(rfl(lw) == 0u, 0)) { (void)wait_ge<1>(misc + C_WO, 1u); (void)wait_ge<1>(misc + C_WO + 1, 1u); }
  CFENCE();
#pragma unroll
  for (int n = 0; n < 4; ++n) wf[0][n] = CAT(TRD(W_AT(0, n, 0)), TRD(W_AT(0, n, 1)));
  u32x4 of[OPF + 1][4];
  unsigned rdy = 0;
#define O_READY(c) do { if (__builtin_expect(rdy <= (unsigned)(c), 0)) rdy = wait_ge<1>(misc + C_ORDY, (unsigned)(c) + 1u); CFENCE(); } while (0)
#define O_LD1(c, k4) of[(c) % (OPF + 1)][k4] = __builtin_amdgcn_raw_buffer_load_b128(wsr, obase + ((k4) >> 1) * 1024 + ((k4) & 1) * (NH * 1024), ((rot + (c)) & 15) * 2048, 16)
#pragma unroll
  for (int c = 0; c < OPF; ++c) { O_READY(c); O_LD1(c, 0); O_LD1(c, 1); O_LD1(c, 2); O_LD1(c, 3); }
  unsigned li = 0;
#define P3_MM(c, j)                                                                                        \
  do {                                                                                                     \
    constexpr int hd = 2 * (c) + ((j) >> 3), n = ((j) >> 1) & 3;                                           \
    AMFMA(oc[n][(j) & 1], wf[(j) >> 3][n], of[(c) % (OPF + 1)][2 * ((j) >> 3) + ((j) & 1)]); SB();         \
    if (hd + 1 < 32) {                                                                                     \
      if (((j) & 1) == 0) tlo = TRD(W_AT((hd + 1) & 31, n, 0)); else wf[1 - ((j) >> 3)][n] = CAT(tlo, TRD(W_AT((hd + 1) & 31, n, 1))); \
    }                                                                                                      \
    if ((c) + OPF < 16) {                                                                                  \
      if ((j) == 0) O_READY((c) + OPF);                                                                    \
      if (((j) & 3) == 1) O_LD1((c) + OPF, (j) >> 2);                                                      \
    }                                                                                                      \
    if ((c) == 15 && (j) == 8) li = misc[C_IDX];                                                           \
    SB();                                                                                                  \
  } while (0)
#define P3_4(c, j) P3_MM(c, j); P3_MM(c, (j) + 1); P3_MM(c, (j) + 2); P3_MM(c, (j) + 3)
#define P3_C(c) P3_4(c, 0); P3_4(c, 4); P3_4(c, 8); P3_4(c, 12)
  P3_C(0); P3_C(1); P3_C(2); P3_C(3); P3_C(4); P3_C(5); P3_C(6); P3_C(7);
  P3_C(8); P3_C(9); P3_C(10); P3_C(11); P3_C(12); P3_C(13); P3_C(14); P3_C(15);
  NOP8(oc);
  li = rfl(li);
  if (__builtin_expect(li == 0u, 0)) li = wait_ge<1>(misc + C_IDX, 1u);
  const int idx = (int)li - 1;
  if ((idx >> 5) == mt3 * 4 + w) {
    f32x4 dl[4];
    TAKE(misc[C_Y], { _Pragma("unroll") for (int n = 0; n < 4; ++n) dl[n] = *(volatile lf32x4*)(smem + L_RED + 2048 + n * 64 + g * 16); });
    const bool mine = c16 == (idx & 15);
    if (idx & 16) {
#pragma unroll
      for (int n = 0; n < 4; ++n) oc[n][1] += mine ? dl[n] : zero;
    } else {
#pragma unroll
      for (int n = 0; n < 4; ++n) oc[n][0] += mine ? dl[n] : zero;
    }
  }
#pragma unroll
  for (int i = 0; i < 4; ++i) {
    const int tt = i >> 1, n = 2 * (i & 1);
    u32x4 o;
#pragma unroll
    for (int k = 0; k < 2; ++k) {
      const auto sw = __builtin_amdgcn_permlane16_swap(cvt_pk(oc[n][tt][2 * k], oc[n][tt][2 * k + 1]),
                                                       cvt_pk(oc[n + 1][tt][2 * k], oc[n + 1][tt][2 * k + 1]), false, false);
      o[k] = sw[0]; o[2 + k] = sw[1];
    }
    __builtin_amdgcn_raw_buffer_store_b128(o, MKRSRC(out, SEQ * DM * 2), eoff + tt * (32 * DM) + (i & 1) * 64, 0, 17);
  }
}

extern "C" void kernel_launch(void* const* d_in, const int* in_sizes, int n_in,
                              void* d_out, int out_size, void* d_ws, size_t ws_size,
                              hipStream_t stream) {
  char* ws = (char*)d_ws;
  (void)hipFuncSetAttribute((const void*)fused_kernel, hipFuncAttributeMaxDynamicSharedMemorySize, L_TOTAL);
  fused_kernel<<<256, 512, L_TOTAL, stream>>>((const u16*)d_in[0], (const u16*)d_in[1], (const u16*)d_in[2], ws, (u16*)d_out,
                                              (const u16*)d_in[4], (const u16*)d_in[8], (const u16*)d_in[5], (const u16*)d_in[6],
                                              (const u16*)d_in[7]);
}
```

```cpp
#include <hip/hip_runtime.h>
#include <hip/hip_bf16.h>
#include <stdint.h>

typedef unsigned short u16;
typedef unsigned long long u64;
typedef __attribute__((ext_vector_type(8))) short bf16x8;
typedef __attribute__((ext_vector_type(4))) short s16x4;
typedef __attribute__((ext_vector_type(4))) float f32x4;
typedef __attribute__((ext_vector_type(2))) float f32x2;
typedef __attribute__((ext_vector_type(2))) __bf16 bf16x2_t;
typedef __attribute__((ext_vector_type(2))) _Float16 f16x2_t;
typedef __attribute__((ext_vector_type(4))) _Float16 f16x4_t;
typedef __attribute__((ext_vector_type(4))) unsigned int u32x4;
typedef __attribute__((ext_vector_type(2))) unsigned int u32x2;
typedef __attribute__((ext_vector_type(8))) unsigned int u32x8;
typedef __attribute__((address_space(3))) s16x4* lds_s16x4_ptr;
typedef __attribute__((address_space(1))) unsigned gu32;
typedef __attribute__((address_space(1))) u64 gu64;
typedef __attribute__((address_space(3))) unsigned lu32;
typedef __attribute__((address_space(3))) u32x2 lu32x2;
typedef __attribute__((address_space(3))) u32x4 lu32x4;
typedef __attribute__((address_space(3))) f32x4 lf32x4;
#define RLX_AGENT __ATOMIC_RELAXED, __HIP_MEMORY_SCOPE_AGENT
#define RLX_WG __ATOMIC_RELAXED, __HIP_MEMORY_SCOPE_WORKGROUP

#if defined(__HIP_DEVICE_COMPILE__)
#define NOPK __attribute__((target("no-packed-fp32-ops")))
#else
#define NOPK
#endif

#define SEQ 2048
#define DM 1024
#define N1 3072
#define FF 3584
#define NH 32

#define OFF_EPOCH  ((size_t)0)
#define OFF_RG     ((size_t)4096)
#define OFF_GG     ((size_t)16384)
#define OFF_GU     ((size_t)262144)
#define OFF_YG     ((size_t)524288)
#define OFF_KVP    ((size_t)1 << 20)
#define OFF_ZP     ((size_t)2 << 20)
#define OFF_OFLAG  (((size_t)3 << 20) + 65536)
#define OFF_OF     ((size_t)4 << 20)

#define SPIN_CAP (1u << 18)

__device__ __forceinline__ float u2f(unsigned u) { return __builtin_bit_cast(float, u); }
__device__ __forceinline__ unsigned f2u(float f) { return __builtin_bit_cast(unsigned, f); }
__device__ __forceinline__ bool all_true(bool ok) { return __builtin_amdgcn_ballot_w64(!ok) == 0ull; }
__device__ __forceinline__ unsigned cvt_pk(float a, float b) {
  f32x2 v = {a, b};
  return __builtin_bit_cast(unsigned, __builtin_convertvector(v, bf16x2_t));
}
__device__ __forceinline__ float blo(unsigned u) { return u2f(u << 16); }
__device__ __forceinline__ float bhi(unsigned u) { return u2f(u & 0xFFFF0000u); }
__device__ __forceinline__ float bfr(float f) { return blo(cvt_pk(f, f)); }
__device__ __forceinline__ float fmax_raw(float a, float b) { return __builtin_elementwise_maximum(a, b); }
__device__ __forceinline__ unsigned eps2(unsigned p) { return cvt_pk(blo(p) + 1e-6f, bhi(p) + 1e-6f); }
template <int N>
__device__ __forceinline__ void phi_fix(unsigned (&p)[N], float umin) {
  if (__builtin_expect(__builtin_amdgcn_ballot_w64(umin < -5.9f) != 0ull, 0)) {
#pragma unroll
    for (int i = 0; i < N; ++i) p[i] = eps2(p[i]);
  }
}
__device__ __forceinline__ float sigmoid_f(float x) { return 1.0f / (1.0f + __expf(-x)); }

template <int CTRL, int RM = 0xf> __device__ __forceinline__ int dppi(int old, int v) {
  return __builtin_amdgcn_update_dpp(old, v, CTRL, RM, 0xf, false);
}
template <int CTRL, int RM = 0xf> __device__ __forceinline__ float dppf(float v) {
  return __builtin_bit_cast(float, dppi<CTRL, RM>(0, __builtin_bit_cast(int, v)));
}
__device__ __forceinline__ unsigned wave_max(unsigned v) {
  v = max(v, (unsigned)dppi<0x111>(0, (int)v)); v = max(v, (unsigned)dppi<0x112>(0, (int)v));
  v = max(v, (unsigned)dppi<0x114>(0, (int)v)); v = max(v, (unsigned)dppi<0x118>(0, (int)v));
  v = max(v, (unsigned)dppi<0x142, 0xa>(0, (int)v)); v = max(v, (unsigned)dppi<0x143, 0xc>(0, (int)v));
  return (unsigned)__builtin_amdgcn_readlane((int)v, 63);
}
__device__ __forceinline__ float merge32(float a, float b) {
  const auto r = __builtin_amdgcn_permlane32_swap(f2u(a), f2u(b), false, false);
  return u2f(r[0]) + u2f(r[1]);
}
__device__ __forceinline__ float merge16(float a, float b) {
  const auto r = __builtin_amdgcn_permlane16_swap(f2u(a), f2u(b), false, false);
  return u2f(r[0]) + u2f(r[1]);
}
typedef __attribute__((address_space(3))) void* ldsp;
template <int IMM>
__device__ __forceinline__ void bufl16(__amdgpu_buffer_rsrc_t r, unsigned l, unsigned voff, unsigned soff) {
#if defined(__HIP_DEVICE_COMPILE__)
  __builtin_amdgcn_raw_ptr_buffer_load_lds(r, (ldsp)(uintptr_t)l, 16, voff, soff, IMM, 0);
#endif
}
#define LP(T, base, imm) ((__attribute__((address_space(3))) T*)(uintptr_t)(unsigned)((base) + (imm)))
#define MKRSRC(p, n) __builtin_amdgcn_make_buffer_rsrc((void*)(p), 0, (n), 0x00020000)
#define WAIT_VM(n) asm volatile("s_waitcnt vmcnt(%0)" ::"n"(n) : "memory")
#define WAIT_LGKM0() asm volatile("s_waitcnt lgkmcnt(0)" ::: "memory")
#define BAR() do { __builtin_amdgcn_sched_barrier(0); asm volatile("s_barrier" ::: "memory"); __builtin_amdgcn_sched_barrier(0); } while (0)
#define CFENCE() asm volatile("" ::: "memory")
#define MFMA(a, b, c) __builtin_amdgcn_mfma_f32_16x16x32_bf16((a), (b), (c), 0, 0, 0)
#define PIN(v) asm volatile("" : "+v"(v))
#define AMFMA(acc, a, b) asm volatile("v_mfma_f32_16x16x32_bf16 %0, %1, %2, %0" : "+v"(acc) : "v"(a), "v"(b))
#define NOP8(t) asm volatile("s_nop 7" : "+v"(t[0][0]), "+v"(t[0][1]), "+v"(t[1][0]), "+v"(t[1][1]), "+v"(t[2][0]), "+v"(t[2][1]), "+v"(t[3][0]), "+v"(t[3][1]))
#define SB() __builtin_amdgcn_sched_barrier(0)
#define TRD(p) ((s16x4)__builtin_amdgcn_ds_read_tr16_b64_v4i16((lds_s16x4_ptr)(p)))
#define CAT(lo, hi) __builtin_shufflevector(lo, hi, 0, 1, 2, 3, 4, 5, 6, 7)
#define FRAG(x) __builtin_bit_cast(bf16x8, x)

#define PACE   57
#define BURST  30
#define R_AT   2
#define R_GAP  2
#define CPF_N  20
#define WO_E   6
#define WO_ES  4
#define WO_M   11
#define G_T    80
#define P5_T   20
#define GU_AH  16
#define DENAT  8
#define OPF    3

__device__ __forceinline__ unsigned rfl(unsigned v) { return (unsigned)__builtin_amdgcn_readfirstlane((int)v); }
template <int SLEEP>
__device__ __forceinline__ unsigned wait_ge(volatile lu32* p, unsigned need) {
  unsigned v = 0;
#pragma unroll 1
  for (unsigned spins = 0; spins < (1u << 22); ++spins) {
    v = rfl(*p);
    if (v >= need) break;
    __builtin_amdgcn_s_sleep(SLEEP);
  }
  CFENCE();
  return v;
}

#define SLOT     40960
#define L_X1     147456
#define L_X2     155648
#define L_CW     159744
#define L_ZF     160256
#define L_GATE   160384
#define L_RED    160896
#define L_H      163200
#define L_CTL    163712
#define L_TOTAL  163840
#define W_X1   64
#define W_X2   68
#define W_AG   72
#define W_KS   76
#define WFLAGS(f) (*(volatile lu32*)(smem + L_CW + (lane & 3) * 128 + (f)))
#define WOPOS(c) ((c) < 3 ? 13 + (c) : (c) < 8 ? (c) : (c) < 11 ? (c) - 8 : (c) - 3)
#define C_F15  0
#define C_WO   2
#define C_ZF   4
#define C_TAG  8
#define C_IDX  10
#define C_GO   12
#define C_OARR 17
#define C_ORDY 18
#define C_GU   19
#define C_DN   20
#define C_T6   21
#define C_Y    23

NOPK __global__ __launch_bounds__(512, 2) void fused_kernel(const u16* __restrict__ x, const u16* __restrict__ wqkv,
                                                            const u16* __restrict__ wo, char* ws, u16* __restrict__ out,
                                                            const u16* __restrict__ w_r, const u16* __restrict__ gate_w,
                                                            const u16* __restrict__ w_gate, const u16* __restrict__ w_up,
                                                            const u16* __restrict__ w_down) {
  extern __shared__ __attribute__((aligned(16))) char smem[];
  asm volatile("s_load_dwordx2 s[100:101], %0, 60" :: "s"(__builtin_amdgcn_kernarg_segment_ptr()) : "s100", "s101");
  SB();
  const int tid = threadIdx.x, lane = tid & 63;
  const int w = __builtin_amdgcn_readfirstlane(tid >> 6);
  u64 pc0;
  asm volatile("s_getpc_b64 %0" : "=s"(pc0));
  const int bid = blockIdx.x, xcd = bid & 7, ix = bid >> 3;
  const int ml = ix >> 2, nl = ix & 3;
  const int mt = (xcd >> 2) * 8 + ml, nt = (xcd & 3) * 4 + nl;
  const int m0 = mt * 128;
  const int mt3 = mt, nt3 = nt ^ 1;
  const int rot = 4 * (xcd & 3);
  volatile lu32* misc = (volatile lu32*)(smem + L_CTL);

  if (__builtin_expect((w >> 1) == 2, 1)) {
    u32x8 ka;
    asm volatile("s_load_dwordx8 %0, %1, 0x0" : "=s"(ka) : "s"(__builtin_amdgcn_kernarg_segment_ptr()));
    SB();
    const int p = w - 4;
    unsigned va[4], vb[4], vw[4];
#pragma unroll
    for (int j = 0; j < 4; ++j) {
      const int row = 32 * (w & 1) + 8 * j + (lane >> 3);
      va[j] = (unsigned)((m0 + row) * (DM * 2) + (((lane & 7) ^ ((row >> 1) & 7)) * 16) - 1024 * j);
      vb[j] = (unsigned)(row * (N1 * 2) + 128 * nt + (((lane & 7) ^ ((row & 2) | ((j & 1) << 2))) * 16) - 1024 * j);
      PIN(va[j]); PIN(vb[j]);
    }
    unsigned l0 = (unsigned)(uintptr_t)(ldsp)smem + p * 4096;
    int kf = (4 * ml + 4 * (xcd & 3)) & 15, ks = (4 * ml - 2 * (ml >> 2) + 4 * (xcd & 3)) & 15, sres = (3 - nt) & 7;
    asm volatile("" : "+s"(l0), "+s"(kf), "+s"(ks), "+s"(sres));
    SB();
    asm volatile("s_waitcnt lgkmcnt(0)" : "+s"(ka));
    const u16* x = (const u16*)(((u64)ka[1] << 32) | ka[0]);
    const u16* wqkv = (const u16*)(((u64)ka[3] << 32) | ka[2]);
    const u16* wo = (const u16*)(((u64)ka[5] << 32) | ka[4]);
    char* ws = (char*)(((u64)ka[7] << 32) | ka[6]);
    int ko_ = (int)((ka[2] >> 7) & 7u) == sres ? ks : kf;
    const __amdgpu_buffer_rsrc_t ra = MKRSRC(x, SEQ * DM * 2), rb = MKRSRC(wqkv, DM * N1 * 2), rw = MKRSRC(wo, DM * DM * 2);
#define UNIT(r, v, off, so) do { bufl16<0>(r, l0 + (off), v[0], so); bufl16<1024>(r, l0 + (off), v[1], so); bufl16<2048>(r, l0 + (off), v[2], so); bufl16<3072>(r, l0 + (off), v[3], so); } while (0)
#define ISSUE(t)                                                                                           \
  do {                                                                                                     \
    const int k_ = (ko_ + (t)) & 15;                                                                       \
    UNIT(ra, va, ((t) % 3) * SLOT, k_ * 128); UNIT(ra, va, ((t) % 3) * SLOT + 8192, k_ * 128 + 64 * DM * 2); \
    _Pragma("unroll") for (int s_ = 0; s_ < 3; ++s_) UNIT(rb, vb, ((t) % 3) * SLOT + 16384 + s_ * 8192, k_ * (64 * N1 * 2) + s_ * (DM * 2)); \
  } while (0)
#define PACED_BAR()                                                                                        \
  do {                                                                                                     \
    if (p == 0) {                                                                                          \
      unsigned now;                                                                                        \
      do { now = (unsigned)__builtin_amdgcn_s_memrealtime(); } while ((int)(now - tp) < 0);                \
      tp = ((int)(now - tp) > BURST ? now - BURST : tp) + PACE;                                            \
    }                                                                                                      \
    BAR();                                                                                                 \
  } while (0)
    ISSUE(0); ISSUE(1);
    WAIT_VM(20);
    BAR();
    ISSUE(2);
    unsigned tp = (unsigned)__builtin_amdgcn_s_memrealtime() - PACE;
#pragma unroll
    for (int j = 0; j < 4; ++j) vw[j] = (unsigned)((32 * p + 8 * j + (lane >> 3)) * (DM * 2) + 128 * nt3 + (((lane & 7) ^ ((lane >> 3) & 6)) * 16) - 1024 * j);
#define WO_CHUNK(c) UNIT(rw, vw, 16384 + WOPOS(c) * 8192, ((rot + (c)) & 15) * (64 * DM * 2))
#pragma unroll
    for (int t = 0; t < 13; ++t) {
      WAIT_VM(20);
      PACED_BAR();
      asm volatile("" : "+s"(l0), "+s"(ko_));
      ISSUE(t + 3);
    }
    WO_CHUNK(0); WO_CHUNK(1); WO_CHUNK(2);
    SB();
    const bool wslow = ((((uintptr_t)wo >> 7) + nt3) & 7) == 3;
    const int woe = wslow ? WO_ES : WO_E;
    WAIT_VM(32);
    PACED_BAR();
    WAIT_VM(12);
    if (lane == 0) misc[C_F15 + p] = 1u;
#pragma unroll 1
    for (int c = 3; c < woe; ++c) WO_CHUNK(c);
    {
      const __amdgpu_buffer_rsrc_t zr = MKRSRC(ws + OFF_ZP, NH * 2 * 16 * 128);
      const unsigned r0 = (unsigned)((2 * nt + p) * 32);
      const unsigned zo0 = (r0 + (lane & 31)) * 128 + 64, zo1 = (r0 + (lane >> 2)) * 128 + (lane & 3) * 16;
      u32x4 zw[2];
      unsigned sc;
      (void)wait_ge<1>(misc + C_IDX + p, 1u);
#pragma unroll 1
      for (unsigned spins = 0; spins < (1u << 22) && !all_true(WFLAGS(W_KS) != 0u); ++spins) __builtin_amdgcn_s_sleep(1);
      CFENCE();
      const unsigned tk = (unsigned)__builtin_amdgcn_s_memrealtime();
#pragma unroll 1
      for (int c = woe; c < WO_M; ++c) WO_CHUNK(c);
      UNIT(ra, va, 0, nt3 * 128); UNIT(ra, va, 8192, nt3 * 128 + 64 * DM * 2);
      while ((unsigned)__builtin_amdgcn_s_memrealtime() - tk < (unsigned)G_T) __builtin_amdgcn_s_sleep(1);
#define MX4(v) max(max((v)[0], (v)[1]), max((v)[2], (v)[3]))
#pragma unroll 1
      for (unsigned spins = 0; spins <= SPIN_CAP; ++spins) {
        CFENCE();
        zw[0] = __builtin_amdgcn_raw_buffer_load_b128(zr, zo1, 0, 16); zw[1] = __builtin_amdgcn_raw_buffer_load_b128(zr, zo1, 2048, 16);
        sc = __builtin_amdgcn_raw_buffer_load_b32(zr, zo0, 0, 16);
        SB(); misc[C_GO + p] = 1u; SB();
        if (all_true(max(max(MX4(zw[0]), MX4(zw[1])), sc) != ~0u)) break;
        __builtin_amdgcn_s_sleep(2);
      }
      float z0 = merge16(merge32(u2f(zw[0][0]), u2f(zw[0][2])), merge32(u2f(zw[1][0]), u2f(zw[1][2])));
      float z1 = merge16(merge32(u2f(zw[0][1]), u2f(zw[0][3])), merge32(u2f(zw[1][1]), u2f(zw[1][3])));
      z0 += dppf<0x128>(z0); z1 += dppf<0x128>(z1); z0 += dppf<0x124>(z0); z1 += dppf<0x124>(z1);
      *(unsigned*)(smem + L_ZF + p * 64 + (lane & 3) * 16 + ((lane >> 4) & 1) * 8 + (lane >> 5) * 4) = cvt_pk(z0, z1);
      CFENCE();
      misc[C_ZF + p] = all_true(sc == 0x3F800000u) ? 1u : 2u;
    }
#pragma unroll 1
    for (unsigned spins = 0; spins < (1u << 22) && !all_true(WFLAGS(W_AG) != 0u); ++spins) __builtin_amdgcn_s_sleep(2);
    __builtin_amdgcn_s_sleep(14);
    CFENCE();
#pragma unroll 1
    for (int c = WO_M; c < 16; ++c) WO_CHUNK(c);
    WAIT_VM(0);
    if (lane == 0) misc[C_WO + p] = 1u;
#undef ISSUE
#undef PACED_BAR
    if (p == 0) return;
    const unsigned tag = rfl(misc[C_TAG]);
    gu32* of = (gu32*)(ws + OFF_OFLAG + (mt3 * 16 + ((rot + lane) & 15)) * 128);
    const unsigned t6 = wait_ge<1>(misc + C_T6, 1u);
    while ((unsigned)__builtin_amdgcn_s_memrealtime() - t6 < (unsigned)P5_T) __builtin_amdgcn_s_sleep(1);
#pragma unroll 1
    for (unsigned spins = 0;; ++spins) {
      const unsigned f = __hip_atomic_load(of, RLX_AGENT);
      const unsigned miss = (unsigned)__builtin_amdgcn_ballot_w64(f != tag) & 0xFFFFu;
      const unsigned m = (miss == 0u || spins > SPIN_CAP) ? 16u : (unsigned)__builtin_ctz(miss);
      if (lane == 0) misc[C_ORDY] = m;
      if (m == 16u) break;
      __builtin_amdgcn_s_sleep(2);
    }
    return;
  }

  if (__builtin_expect(w >= 6, 0)) {
    const int pa = w - 6;
#define CODE_PF(on) __builtin_amdgcn_raw_buffer_load_b32(MKRSRC((pc0 & ~255ull) + (blockIdx.x >> 3) * (CPF_N * 64u), (on) ? CPF_N * 64 : 0), lane * 64, 0, 0)
    unsigned cpf = CODE_PF(w == 7), cpf2;
    SB();
    float* red = (float*)(smem + L_RED);
    const bool help = bid >= 224, gemv = bid < 252;
    const int gmat = bid & 1;
    const u16* wgu = gmat ? w_up : w_gate;
    const int sres = (int)(3u - (unsigned)((uintptr_t)wgu >> 7)) & 7;
    const int gfs = help ? sres + 8 * (((bid - 224) >> 1) % 7) : (bid >> 1) % 56;
    const int gdh = (help ? ((bid - 224) >> 1) / 7 : (bid >> 1) / 56) & 1;
    const bool act0 = !help, act1 = gemv && (help || (gfs & 7) != sres);
    const int gd0 = gdh * 512 + 256 * pa + 32 * (lane >> 3);
    const __amdgpu_buffer_rsrc_t rgu0 = MKRSRC(wgu, act0 ? DM * FF * 2 : 0), rgu1 = MKRSRC(wgu, act1 ? DM * FF * 2 : 0);
    const unsigned wpo = (unsigned)((gd0 * FF + gfs * 64 + 8 * (lane & 7)) * 2);
    u32x4 wv[32], wd[14];
    const int fg = bid >> 4, cg = bid & 15;
    const int f0 = fg * 224 + pa * 112;
    const __amdgpu_buffer_rsrc_t rdn = MKRSRC(w_down, DM * FF * 2);
    const unsigned wqo = (unsigned)(((f0 + 14 * (lane >> 3)) * DM + cg * 64 + 8 * (lane & 7)) * 2);
    asm volatile("" ::"s"(w_r), "s"(gate_w), "s"(ws), "s"(w_gate), "s"(w_up), "s"(w_down));
    unsigned pf0, pf1;
#define WO_PREFETCH()                                                                                      \
  do {                                                                                                     \
    const u16* pp_ = wo + (size_t)(128 * ml + 64 * pa + (lane >> 1)) * DM + 64 * nt3 + (lane & 1) * 32;    \
    pf0 = *(const unsigned*)pp_; pf1 = *(const unsigned*)(pp_ + 32 * DM);                                  \
  } while (0)
    {
#pragma unroll
      for (int t = 0; t <= R_AT; ++t) BAR();
      const int r0 = m0 + nt * 8 + 4 * pa;
      u32x4 xr[4][2], rv[2], gv[2];
#pragma unroll
      for (int i = 0; i < 2; ++i) {
        rv[i] = *(const u32x4*)(w_r + i * 512 + lane * 8);
        gv[i] = *(const u32x4*)(gate_w + i * 512 + lane * 8);
      }
#pragma unroll
      for (int r = 0; r < 4; ++r)
#pragma unroll
        for (int i = 0; i < 2; ++i) xr[r][i] = *(const u32x4*)(x + (size_t)(r0 + r) * DM + i * 512 + lane * 8);
      const unsigned ep = __hip_atomic_load((gu32*)(ws + OFF_EPOCH), RLX_AGENT);
      {
        const u32x4 nw = {~0u, ~0u, ~0u, ~0u};
        const __amdgpu_buffer_rsrc_t kvr = MKRSRC(ws + OFF_KVP, NH * 2 * 16 * 1024), zr = MKRSRC(ws + OFF_ZP, NH * 2 * 16 * 128);
        const unsigned rec = (unsigned)((2 * nt + pa) * 32 + mt);
        __builtin_amdgcn_raw_buffer_store_b128(nw, kvr, rec * 1024 + lane * 16, 0, 16);
        __builtin_amdgcn_raw_buffer_store_b128(nw, kvr, rec * 1024 + lane * 16 + 16384, 0, 16);
        if (lane < 16) __builtin_amdgcn_raw_buffer_store_b128(nw, zr, (rec + (lane >> 3) * 16) * 128 + (lane & 7) * 16, 0, 16);
      }
#pragma unroll
      for (int t = R_AT + 1; t < R_AT + R_GAP; ++t) BAR();
      float racc[4], gacc[4];
#pragma unroll
      for (int r = 0; r < 4; ++r) {
        BAR();
        if (r == 1) WAIT_VM(0);
        racc[r] = 0.f; gacc[r] = 0.f;
#pragma unroll
        for (int i = 0; i < 2; ++i)
#pragma unroll
          for (int e = 0; e < 4; ++e) {
            const float x0 = blo(xr[r][i][e]), x1 = bhi(xr[r][i][e]);
            racc[r] += x0 * blo(rv[i][e]); racc[r] += x1 * bhi(rv[i][e]);
            const unsigned pr = cvt_pk(x0 * blo(gv[i][e]), x1 * bhi(gv[i][e]));
            gacc[r] += blo(pr); gacc[r] += bhi(pr);
          }
        PIN(racc[r]); PIN(gacc[r]);
        if (r == 3) { SB(); WO_PREFETCH(); cpf2 = CODE_PF(w == 7); }
      }
      BAR();
      float rs = merge16(merge32(racc[0], racc[1]), merge32(racc[2], racc[3]));
      float gs = merge16(merge32(gacc[0], gacc[1]), merge32(gacc[2], gacc[3]));
      rs += dppf<0x128>(rs); gs += dppf<0x128>(gs); rs += dppf<0x124>(rs); gs += dppf<0x124>(gs);
      rs += dppf<0x122>(rs); gs += dppf<0x122>(gs); rs += dppf<0x121>(rs); gs += dppf<0x121>(gs);
      const int rr = r0 + ((lane >> 5) | ((lane >> 3) & 2));
      const unsigned rb = cvt_pk(rs, rs) & 0xFFFFu;
      unsigned key = (((rb & 0x8000u) ? (~rb & 0xFFFFu) : (rb | 0x8000u)) << 16) | (0xFFFFu - (unsigned)rr);
      { const auto t_ = __builtin_amdgcn_permlane16_swap(key, key, false, false); key = max(t_[0], t_[1]); }
      { const auto t_ = __builtin_amdgcn_permlane32_swap(key, key, false, false); key = max(t_[0], t_[1]); }
      const unsigned tg = rfl(ep) + 1u;
      if (lane == 0) misc[C_TAG + pa] = pa ? key : tg;
      WAIT_LGKM0();
      BAR();
      if (pa == 0 && lane == 0) __hip_atomic_store((gu64*)(ws + OFF_RG) + bid, ((u64)tg << 32) | max(key, (unsigned)misc[C_TAG + 1]), RLX_AGENT);
      if ((lane & 15) == 0) __hip_atomic_store((gu64*)(ws + OFF_GG) + rr, ((u64)tg << 32) | f2u(bfr(sigmoid_f(bfr(gs)))), RLX_AGENT);
#pragma unroll
      for (int t = R_AT + R_GAP + 6; t < 13; ++t) BAR();
    }
    u64 rg[4];
#pragma unroll
    for (int i = 0; i < 4; ++i) rg[i] = __hip_atomic_load((gu64*)(ws + OFF_RG) + i * 64 + lane, RLX_AGENT);
    const gu64* ggp = (const gu64*)(ws + OFF_GG) + m0 + 64 * pa + lane;
    u64 gg = __hip_atomic_load(ggp, RLX_AGENT);
    BAR();
    const unsigned tag = rfl(misc[C_TAG]);
    unsigned idx;
    unsigned bestk;
    bool late;
    {
      unsigned best = 0;
      bool ok = (unsigned)(gg >> 32) == tag;
#pragma unroll
      for (int i = 0; i < 4; ++i) { ok = ok && (unsigned)(rg[i] >> 32) == tag; best = max(best, (unsigned)rg[i]); }
      late = !all_true(ok);
      if (__builtin_expect(late, 0)) {
        BAR();
#pragma unroll 1
        for (unsigned spins = 0;; ++spins) {
          u64 rh[4];
#pragma unroll
          for (int i = 0; i < 4; ++i) rh[i] = __hip_atomic_load((gu64*)(ws + OFF_RG) + i * 64 + lane, RLX_AGENT);
          gg = __hip_atomic_load(ggp, RLX_AGENT);
          ok = (unsigned)(gg >> 32) == tag; best = 0;
#pragma unroll
          for (int i = 0; i < 4; ++i) { ok = ok && (unsigned)(rh[i] >> 32) == tag; best = max(best, (unsigned)rh[i]); }
          if (all_true(ok) || spins > SPIN_CAP) break;
          __builtin_amdgcn_s_sleep(8);
        }
      }
      best = wave_max(best);
      idx = (0xFFFFu - (best & 0xFFFFu)) & (SEQ - 1);
      bestk = best;
    }
    asm volatile("" ::"v"(pf0), "v"(pf1), "v"(cpf), "v"(cpf2));
    u32x4 xs[4];
#pragma unroll
    for (int i = 0; i < 4; ++i) xs[i] = *(const u32x4*)(x + (size_t)idx * DM + gd0 + 8 * i);
    if (!late) BAR();
#pragma unroll
    for (int i = 0; i < 32; ++i) wv[i] = __builtin_amdgcn_raw_buffer_load_b128(i < 16 ? rgu0 : rgu1, wpo, i * (FF * 2), 2);
    SB();
    ((unsigned*)(smem + L_GATE))[64 * pa + lane] = (unsigned)gg;
    CFENCE();
    if (lane == 0) misc[C_IDX + pa] = idx + 1u;
    if (bid == 0 && pa == 0 && lane == 0) __hip_atomic_store((gu32*)(ws + OFF_EPOCH), tag, RLX_AGENT);
    unsigned sel[2][4];
#pragma unroll
    for (int i = 0; i < 8; ++i) {
      const unsigned pick = (i >> 2) ? 0x0302u : 0x0100u;
      sel[i >> 2][i & 3] = ((lane & 7) >> 1) == (i & 3) ? ((lane & 1) ? (pick << 16) | 0x0C0Cu : 0x0C0C0000u | pick) : 0x0C0C0C0Cu;
      PIN(sel[i >> 2][i & 3]);
    }
#define AFR(pr, i) FRAG(((u32x4){__builtin_amdgcn_perm(0u, pr, sel[(i) & 1][0]), __builtin_amdgcn_perm(0u, pr, sel[(i) & 1][1]), \
                                 __builtin_amdgcn_perm(0u, pr, sel[(i) & 1][2]), __builtin_amdgcn_perm(0u, pr, sel[(i) & 1][3])}))
    f32x4* rdw = (f32x4*)(red + pa * 128 + (lane >> 5) * 64 + (lane & 7) * 8 + ((lane >> 4) & 1) * 4);
    const bool rvalid = (lane >> 5) == ((lane >> 3) & 1);
    const auto rv = LP(volatile float, L_RED, lane * 4);
    {
      bf16x8 af[32];
      f32x4 a = {0.f, 0.f, 0.f, 0.f};
#pragma unroll
      for (int i = 0; i < GU_AH; ++i) { af[i] = AFR(xs[i >> 3][(i >> 1) & 3], i); PIN(af[i]); }
      SB();
#pragma unroll
      for (int i = 0; i < 32; ++i) {
        a = MFMA(af[i], FRAG(wv[i]), a);
        PIN(a);
        if (i + GU_AH < 32) { af[(i + GU_AH) & 31] = AFR(xs[((i + GU_AH) >> 3) & 3][((i + GU_AH) >> 1) & 3], i + GU_AH); PIN(af[(i + GU_AH) & 31]); }
        SB();
      }
      if (rvalid) *rdw = a;
      CFENCE();
      if (pa == 1) misc[C_GU] = 1u;
      else {
        float v;
#pragma unroll 1
        for (unsigned spins = 0; spins < (1u << 22); ++spins) {
          const unsigned f_ = misc[C_GU];
          CFENCE();
          v = (rv[0] + rv[64]) + (rv[128] + rv[192]);
          if (rfl(f_) != 0u) break;
          __builtin_amdgcn_s_sleep(1);
        }
        if (gemv) __hip_atomic_store((gu64*)(ws + OFF_GU) + ((help ? 4 : 0) + gdh * 2 + gmat) * FF + gfs * 64 + lane, ((u64)tag << 32) | f2u(v), RLX_AGENT);
      }
    }
    {
#pragma unroll 1
      for (unsigned spins = 0; spins < (1u << 22) && !all_true(WFLAGS(W_AG) != 0u); ++spins) __builtin_amdgcn_s_sleep(1);
#pragma unroll
      for (int i = 0; i < 14; ++i) wd[i] = __builtin_amdgcn_raw_buffer_load_b128(rdn, wqo, i * (DM * 2), 2);
      SB();
      float hv[2];
      const int fl = f0 + (lane < 56 ? lane : 55);
      const gu64* gp = (const gu64*)(ws + OFF_GU) + fl;
      bool ex[2][2];
#pragma unroll
      for (int j = 0; j < 2; ++j) {
        ex[j][0] = ((((unsigned)((uintptr_t)w_gate >> 7) + ((fl + 56 * j) >> 6)) & 7u) == 3u);
        ex[j][1] = ((((unsigned)((uintptr_t)w_up >> 7) + ((fl + 56 * j) >> 6)) & 7u) == 3u);
      }
#pragma unroll 1
      for (unsigned spins = 0;; ++spins) {
        u64 g[2][8];
#pragma unroll
        for (int j = 0; j < 2; ++j)
#pragma unroll
          for (int i = 0; i < 8; ++i) {
            g[j][i] = (u64)tag << 32;
            if (i < 4 || ex[j][i & 1]) g[j][i] = __hip_atomic_load(gp + i * FF + j * 56, RLX_AGENT);
          }
        bool ok = true;
#pragma unroll
        for (int j = 0; j < 2; ++j) {
#pragma unroll
          for (int i = 0; i < 8; ++i) ok = ok && (unsigned)(g[j][i] >> 32) == tag;
          const float gt = bfr((u2f((unsigned)g[j][0]) + u2f((unsigned)g[j][4])) + (u2f((unsigned)g[j][2]) + u2f((unsigned)g[j][6])));
          const float up = bfr((u2f((unsigned)g[j][1]) + u2f((unsigned)g[j][5])) + (u2f((unsigned)g[j][3]) + u2f((unsigned)g[j][7])));
          hv[j] = bfr(gt * __builtin_amdgcn_rcpf(1.0f + __expf(-gt))) * up;
        }
        if (all_true(ok) || spins > SPIN_CAP) break;
        __builtin_amdgcn_s_sleep(4);
      }
      u32x4 hp[2];
      {
        char* hb = smem + L_H + pa * 256;
        const unsigned pr = cvt_pk(hv[0], hv[1]);
        if (lane < 56) { *(u16*)(hb + (lane / 14) * 32 + (lane % 14) * 2) = (u16)pr; *(u16*)(hb + 128 + (lane / 14) * 32 + (lane % 14) * 2) = (u16)(pr >> 16); }
        CFENCE();
        hp[0] = *(volatile lu32x4*)(hb + (lane >> 3) * 32); hp[1] = *(volatile lu32x4*)(hb + (lane >> 3) * 32 + 16);
      }
      bf16x8 af[14];
#pragma unroll
      for (int i = 0; i < 14; ++i) { af[i] = AFR(hp[i >> 3][(i >> 1) & 3], i); PIN(af[i]); }
      SB();
      f32x4 a = {0.f, 0.f, 0.f, 0.f};
#pragma unroll
      for (int i = 0; i < 14; ++i) a = MFMA(af[i], FRAG(wd[i]), a);
      if (rvalid) rdw[64] = a;
      CFENCE();
      if (pa == 1) misc[C_DN] = 1u;
      else {
        float y;
#pragma unroll 1
        for (unsigned spins = 0; spins < (1u << 22); ++spins) {
          const unsigned f_ = misc[C_DN];
          CFENCE();
          y = (rv[256] + rv[320]) + (rv[384] + rv[448]);
          if (rfl(f_) != 0u) break;
          __builtin_amdgcn_s_sleep(1);
        }
        __hip_atomic_store((gu64*)(ws + OFF_YG) + fg * 1024 + cg * 64 + lane, ((u64)tag << 32) | f2u(y), RLX_AGENT);
      }
    }
    if (pa == 1 && (int)(idx >> 7) == mt3) {
      const float xe = u2f((unsigned)x[(size_t)idx * DM + 64 * nt3 + lane] << 16);
      const unsigned ob = bestk >> 16;
      const float rsel = bfr(sigmoid_f(u2f(((ob & 0x8000u) ? (ob & 0x7FFFu) : (~ob & 0xFFFFu)) << 16)));
      float y = 0.f, gt = 0.f;
      __builtin_amdgcn_s_sleep(30);
#pragma unroll 1
      for (unsigned spins = 0;; ++spins) {
        u64 g[17];
#pragma unroll
        for (int i = 0; i < 16; ++i) g[i] = __hip_atomic_load((gu64*)(ws + OFF_YG) + i * 1024 + 64 * nt3 + lane, RLX_AGENT);
        g[16] = __hip_atomic_load((gu64*)(ws + OFF_GG) + idx, RLX_AGENT);
        bool ok = (unsigned)(g[16] >> 32) == tag;
        gt = u2f((unsigned)g[16]); y = 0.f;
#pragma unroll
        for (int i = 0; i < 16; ++i) { ok = ok && (unsigned)(g[i] >> 32) == tag; y += u2f((unsigned)g[i]); }
        if (all_true(ok) || spins > SPIN_CAP) break;
        __builtin_amdgcn_s_sleep(8);
      }
      red[512 + lane] = bfr(bfr(1.0f - gt) * bfr(xe + bfr(bfr(y) * rsel))) - bfr(bfr(1.0f - gt) * xe);
      CFENCE();
      if (lane == 0) misc[C_Y] = 1u;
    }
    return;
  }

  if (w == 0) misc[lane & 31] = 0u;
  if (w == 1) *(u32x2*)(smem + L_CW + lane * 8) = (u32x2){0u, 0u};
  const int hh = w & 1, wr = w >> 1;
  const int c16 = lane & 15, g = lane >> 4;
  const int fsw = (c16 >> 3) | ((g & 1) << 1);
  const int head = 2 * nt + hh;
  int aoff[2], boff[2];
#pragma unroll
  for (int i = 0; i < 2; ++i) {
    aoff[i] = (wr * 64 + c16) * 128 + (((4 * i + g) ^ (c16 >> 1)) * 16);
    boff[i] = 16384 + (8 * g + (c16 >> 2)) * 128 + (((2 * hh + (i ^ wr)) ^ fsw) * 32) + (c16 & 3) * 8;
  }
  f32x4 acc[3][4][2];
#pragma unroll
  for (int i = 0; i < 24; ++i) { const float s0 = i < 16 ? 1.f : 0.f; acc[i / 8][(i >> 1) & 3][i & 1] = (f32x4){s0, s0, s0, s0}; PIN(acc[i / 8][(i >> 1) & 3][i & 1]); }
  bf16x8 a[2][4], b[2][6];
  s16x4 tlo;
#define A_AT(sl, h, mi) (*(const bf16x8*)(smem + (sl) * SLOT + aoff[h] + (mi) * 2048))
#define B_AT(sl, h, s, n) (smem + (sl) * SLOT + boff[n] + (s) * 8192 + (h) * 4096)
#define MM(s, mi, n, A, B) do { if ((s) == 0) AMFMA(acc[0][mi][n], B, A); else AMFMA(acc[s][mi][n], A, B); } while (0)
#define RD(sl, h, u)                                                                                       \
  do {                                                                                                     \
    if ((u) == 0) a[h][0] = A_AT(sl, h, 0);                                                                \
    else if ((u) >= 13) a[h][(u) - 12] = A_AT(sl, h, (u) - 12);                                            \
    else {                                                                                                 \
      const int j_ = (((u) - 1) / 2 + 2) % 6;                                                              \
      if ((u) & 1) tlo = TRD(B_AT(sl, h, j_ >> 1, j_ & 1));                                                \
      else b[h][j_] = CAT(tlo, TRD(B_AT(sl, h, j_ >> 1, j_ & 1) + 512));                                   \
    }                                                                                                      \
  } while (0)
#define O_M(n) ((((n) / 4) & 1) ? 3 - (n) % 4 : (n) % 4)
#define O_J(n) ((n) / 4)
#define STEP(h, rs)                                                                                        \
  _Pragma("unroll") for (int n_ = 0; n_ < 24; ++n_) {                                                      \
    const int jj_ = (O_J(n_) + 2) % 6, mi_ = O_M(n_);                                                      \
    MM(jj_ >> 1, mi_, jj_ & 1, a[h][mi_], b[h][jj_]); SB();                                                \
    if (n_ < 16) { RD(rs, 1 - (h), n_); }                                                                 \
    SB();                                                                                                  \
  }
#define LGKM0() __builtin_amdgcn_s_waitcnt(0xC07F)
#define STAGE(sl, nx) do { STEP(0, sl); LGKM0(); BAR(); STEP(1, nx); LGKM0(); SB(); } while (0)

  __amdgpu_buffer_rsrc_t wsr = MKRSRC(ws, OFF_OF + SEQ * DM * 2);
  asm volatile("" : "+s"(wsr));
  const unsigned rec = (unsigned)((head * 2 + wr) * 16 + mt);
  unsigned x1w = L_X1 + w * 2048 + lane * 16, cw = L_CW + w * 128 + g * 16, one = 1u;
  unsigned kvo = (unsigned)OFF_KVP + rec * 1024 + lane * 16;
  unsigned zo = (c16 == 0 || lane == 1) ? (unsigned)OFF_ZP + rec * 128 + g * 16 + c16 * 64 : 0x80000000u;
  PIN(x1w); PIN(cw); PIN(one); PIN(kvo); PIN(zo);
  WAIT_LGKM0();
  BAR();
#pragma unroll
  for (int u = 0; u < 16; ++u) RD(0, 0, u);
  LGKM0();
  SB();
#pragma unroll 1
  for (int it = 0; it < 4; ++it) { STAGE(0, 1); STAGE(1, 2); STAGE(2, 0); }
  STAGE(0, 1);
  STEP(0, 1); LGKM0(); BAR();
  bf16x8 ta0_0, ta0_1, ta0_2, ta0_3, ta1_0, ta1_1, ta1_2, ta1_3, ta2_0, ta2_1, ta2_2, ta2_3;
  bf16x8 ta3_0, ta3_1, ta3_2, ta3_3, tb0_0_0, tb0_0_1, tb0_1_0, tb0_1_1, tb0_2_0, tb0_2_1, tb0_3_0, tb0_3_1;
  bf16x8 tb1_0_0, tb1_0_1, tb1_1_0, tb1_1_1, tb1_2_0, tb1_2_1, tb1_3_0, tb1_3_1, tb2_0_0, tb2_0_1, tb2_1_0, tb2_1_1;
  bf16x8 tb2_2_0, tb2_2_1, tb2_3_0, tb2_3_1;
  s16x4 tb0_0_0_l, tb0_0_1_l, tb0_1_0_l, tb0_1_1_l, tb0_2_0_l, tb0_2_1_l, tb0_3_0_l, tb0_3_1_l, tb1_0_0_l, tb1_0_1_l, tb1_1_0_l, tb1_1_1_l;
  s16x4 tb1_2_0_l, tb1_2_1_l, tb1_3_0_l, tb1_3_1_l, tb2_0_0_l, tb2_0_1_l, tb2_1_0_l, tb2_1_1_l, tb2_2_0_l, tb2_2_1_l, tb2_3_0_l, tb2_3_1_l;
  unsigned kp[16], vp[16], x1r, cr;
  u32x2 lf15;
  float kmin = 0.f, qmin = 0.f;
  u32x4 ones;
  float ek[4];
#define PHI(src, dst, mn, k, st)                                                                           \
  do {                                                                                                     \
    float& e_ = ek[2 * (k) + ((st) & 1)];                                                                  \
    if ((st) < 2) e_ = __builtin_fmaf((src)[2 * (k) + ((st) & 1)], 1.44269504088896341f, -1.44269504088896341f); \
    else if ((st) < 4) e_ = __builtin_amdgcn_fmed3f(__builtin_amdgcn_exp2f(e_), 0.f, 1.f);                 \
    else if ((st) < 6) e_ = fmax_raw((src)[2 * (k) + ((st) & 1)], e_);                                     \
    else if ((st) == 6) dst = cvt_pk(ek[2 * (k)], ek[2 * (k) + 1]);                                        \
    else mn = __builtin_fminf(__builtin_fminf(mn, (src)[2 * (k)]), (src)[2 * (k) + 1]);                     \
    SB();                                                                                                  \
  } while (0)
#define PK(mi, n, k, st) PHI(acc[1][mi][n], kp[8 * (n) + 2 * (mi) + (k)], kmin, k, st)
#define CV(mi, n, k) do { vp[8 * (n) + 2 * (mi) + (k)] = cvt_pk(acc[2][mi][n][2 * (k)], acc[2][mi][n][2 * (k) + 1]); PIN(vp[8 * (n) + 2 * (mi) + (k)]); SB(); } while (0)
#define F15_RD lf15 = *(volatile lu32x2*)(smem + L_CTL + 4 * C_F15)
#define F15_CHK do { if (__builtin_expect(!all_true((lf15[0] & lf15[1]) != 0u), 0)) { (void)wait_ge<1>(misc + C_F15, 1u); (void)wait_ge<1>(misc + C_F15 + 1, 1u); } CFENCE(); } while (0)
#define PRE(i) do { if ((i) == 30) { x1r = x1w ^ 4096u; PIN(x1r); } if ((i) == 31) { cr = cw ^ 256u; PIN(cr); } \
                    if ((i) == 32) { ones = (u32x4){0x3F803F80u, 0x3F803F80u, 0x3F803F80u, 0x3F803F80u}; PIN(ones); } } while (0)
  MM(1, 0, 0, a[1][0], b[1][2]); SB(); tb1_0_1_l = TRD(B_AT(2, 0, 1, 1)); PRE(0); SB();
  MM(1, 0, 1, a[1][0], b[1][3]); SB(); tb1_0_1 = CAT(tb1_0_1_l, TRD(B_AT(2, 0, 1, 1) + 512)); PRE(1); SB();
  MM(2, 0, 0, a[1][0], b[1][4]); SB(); ta0_0 = A_AT(2, 0, 0); PRE(2); SB();
  MM(2, 0, 1, a[1][0], b[1][5]); SB(); ta0_1 = A_AT(2, 0, 1); PRE(3); SB();
  MM(0, 0, 0, a[1][0], b[1][0]); SB(); ta0_2 = A_AT(2, 0, 2); PRE(4); SB();
  MM(0, 0, 1, a[1][0], b[1][1]); SB(); ta0_3 = A_AT(2, 0, 3); PRE(5); SB();
  MM(1, 1, 0, a[1][1], b[1][2]); SB(); tb1_0_0_l = TRD(B_AT(2, 0, 1, 0)); PRE(6); SB();
  MM(1, 1, 1, a[1][1], b[1][3]); SB(); tb1_0_0 = CAT(tb1_0_0_l, TRD(B_AT(2, 0, 1, 0) + 512)); PRE(7); SB();
  MM(2, 1, 0, a[1][1], b[1][4]); SB(); tb1_1_1_l = TRD(B_AT(2, 1, 1, 1)); PRE(8); SB();
  MM(2, 1, 1, a[1][1], b[1][5]); SB(); tb1_1_1 = CAT(tb1_1_1_l, TRD(B_AT(2, 1, 1, 1) + 512)); PRE(9); SB();
  MM(0, 1, 0, a[1][1], b[1][0]); SB(); ta1_0 = A_AT(2, 1, 0); PRE(10); SB();
  MM(0, 1, 1, a[1][1], b[1][1]); SB(); ta1_1 = A_AT(2, 1, 1); PRE(11); SB();
  MM(1, 2, 0, a[1][2], b[1][2]); SB(); ta1_2 = A_AT(2, 1, 2); PRE(12); SB();
  MM(1, 2, 1, a[1][2], b[1][3]); SB(); ta1_3 = A_AT(2, 1, 3); PRE(13); SB();
  MM(2, 2, 0, a[1][2], b[1][4]); SB(); tb1_1_0_l = TRD(B_AT(2, 1, 1, 0)); PRE(14); SB();
  MM(2, 2, 1, a[1][2], b[1][5]); SB(); tb1_1_0 = CAT(tb1_1_0_l, TRD(B_AT(2, 1, 1, 0) + 512)); PRE(15); SB();
  MM(0, 2, 0, a[1][2], b[1][0]); SB(); PRE(16); SB();
  MM(0, 2, 1, a[1][2], b[1][1]); SB(); PRE(17); SB();
  MM(1, 3, 0, a[1][3], b[1][2]); SB(); PRE(18); SB();
  MM(1, 3, 1, a[1][3], b[1][3]); SB(); PRE(19); SB();
  MM(2, 3, 0, a[1][3], b[1][4]); SB(); F15_RD; PRE(20); SB();
  MM(2, 3, 1, a[1][3], b[1][5]); SB(); PRE(21); SB();
  MM(0, 3, 0, a[1][3], b[1][0]); SB(); tb2_0_1_l = TRD(B_AT(2, 0, 2, 1)); PRE(22); SB();
  MM(0, 3, 1, a[1][3], b[1][1]); SB(); tb2_0_1 = CAT(tb2_0_1_l, TRD(B_AT(2, 0, 2, 1) + 512)); PRE(23); SB();
  MM(1, 0, 1, ta0_0, tb1_0_1); SB(); PRE(24); SB();
  MM(1, 1, 1, ta0_1, tb1_0_1); SB(); PRE(25); SB();
  MM(1, 2, 1, ta0_2, tb1_0_1); SB(); F15_CHK; tb2_1_1_l = TRD(B_AT(2, 1, 2, 1)); PRE(26); SB();
  MM(1, 3, 1, ta0_3, tb1_0_1); SB(); tb2_1_1 = CAT(tb2_1_1_l, TRD(B_AT(2, 1, 2, 1) + 512)); PRE(27); SB();
  MM(1, 0, 0, ta0_0, tb1_0_0); SB(); tb1_2_1_l = TRD(B_AT(0, 0, 1, 1)); PRE(28); SB();
  MM(1, 1, 0, ta0_1, tb1_0_0); SB(); tb1_2_1 = CAT(tb1_2_1_l, TRD(B_AT(0, 0, 1, 1) + 512)); PRE(29); SB();
  MM(1, 2, 0, ta0_2, tb1_0_0); SB(); ta2_0 = A_AT(0, 0, 0); PRE(30); SB();
  MM(1, 3, 0, ta0_3, tb1_0_0); SB(); ta2_1 = A_AT(0, 0, 1); PRE(31); SB();
  MM(1, 0, 1, ta1_0, tb1_1_1); SB(); ta2_2 = A_AT(0, 0, 2); PRE(32); SB();
  MM(1, 1, 1, ta1_1, tb1_1_1); SB(); ta2_3 = A_AT(0, 0, 3); PRE(33); SB();
  MM(1, 2, 1, ta1_2, tb1_1_1); SB(); tb1_3_1_l = TRD(B_AT(0, 1, 1, 1)); PRE(34); SB();
  MM(1, 3, 1, ta1_3, tb1_1_1); SB(); tb1_3_1 = CAT(tb1_3_1_l, TRD(B_AT(0, 1, 1, 1) + 512)); PRE(35); SB();
  MM(1, 0, 0, ta1_0, tb1_1_0); SB(); ta3_0 = A_AT(0, 1, 0); PRE(36); SB();
  MM(1, 1, 0, ta1_1, tb1_1_0); SB(); ta3_1 = A_AT(0, 1, 1); PRE(37); SB();
  MM(1, 2, 0, ta1_2, tb1_1_0); SB(); ta3_2 = A_AT(0, 1, 2); PRE(38); SB();
  MM(1, 3, 0, ta1_3, tb1_1_0); SB(); ta3_3 = A_AT(0, 1, 3); PRE(39); SB();
  MM(1, 0, 1, ta2_0, tb1_2_1); SB(); tb1_2_0_l = TRD(B_AT(0, 0, 1, 0)); PRE(40); SB();
  MM(1, 1, 1, ta2_1, tb1_2_1); SB(); tb1_2_0 = CAT(tb1_2_0_l, TRD(B_AT(0, 0, 1, 0) + 512)); PRE(41); SB();
  MM(1, 2, 1, ta2_2, tb1_2_1); SB(); tb1_3_0_l = TRD(B_AT(0, 1, 1, 0)); PRE(42); SB();
  MM(1, 3, 1, ta2_3, tb1_2_1); SB(); tb1_3_0 = CAT(tb1_3_0_l, TRD(B_AT(0, 1, 1, 0) + 512)); PRE(43); SB();
  MM(1, 0, 1, ta3_0, tb1_3_1); SB(); tb2_2_1_l = TRD(B_AT(0, 0, 2, 1)); PRE(44); SB();
  MM(1, 1, 1, ta3_1, tb1_3_1); SB(); tb2_2_1 = CAT(tb2_2_1_l, TRD(B_AT(0, 0, 2, 1) + 512)); PRE(45); SB();
  MM(1, 2, 1, ta3_2, tb1_3_1); SB(); tb2_3_1_l = TRD(B_AT(0, 1, 2, 1)); PRE(46); SB();
  MM(1, 3, 1, ta3_3, tb1_3_1); SB(); tb2_3_1 = CAT(tb2_3_1_l, TRD(B_AT(0, 1, 2, 1) + 512)); PRE(47); SB();
  MM(1, 0, 0, ta2_0, tb1_2_0); SB(); tb2_0_0_l = TRD(B_AT(2, 0, 2, 0)); PK(0, 1, 0, 0); PK(0, 1, 0, 1); PK(0, 1, 1, 0); PK(0, 1, 1, 1); PK(0, 1, 0, 2); SB();
  MM(1, 1, 0, ta2_1, tb1_2_0); SB(); tb2_0_0 = CAT(tb2_0_0_l, TRD(B_AT(2, 0, 2, 0) + 512)); PK(0, 1, 0, 3); PK(0, 1, 0, 7); PK(0, 1, 1, 2); SB();
  MM(1, 2, 0, ta2_2, tb1_2_0); SB(); tb2_1_0_l = TRD(B_AT(2, 1, 2, 0)); PK(0, 1, 1, 3); PK(0, 1, 1, 7); PK(0, 1, 0, 4); PK(0, 1, 0, 5); SB();
  MM(1, 3, 0, ta2_3, tb1_2_0); SB(); tb2_1_0 = CAT(tb2_1_0_l, TRD(B_AT(2, 1, 2, 0) + 512)); PK(0, 1, 1, 4); PK(0, 1, 1, 5); PK(0, 1, 0, 6); PK(0, 1, 1, 6); PK(1, 1, 0, 0); SB();
  MM(1, 0, 0, ta3_0, tb1_3_0); SB(); tb2_2_0_l = TRD(B_AT(0, 0, 2, 0)); PK(1, 1, 0, 1); PK(1, 1, 1, 0); PK(1, 1, 1, 1); PK(1, 1, 0, 2); SB();
  MM(1, 1, 0, ta3_1, tb1_3_0); SB(); tb2_2_0 = CAT(tb2_2_0_l, TRD(B_AT(0, 0, 2, 0) + 512)); PK(1, 1, 0, 3); PK(1, 1, 0, 7); PK(1, 1, 1, 2); SB();
  MM(1, 2, 0, ta3_2, tb1_3_0); SB(); tb2_3_0_l = TRD(B_AT(0, 1, 2, 0)); PK(1, 1, 1, 3); PK(1, 1, 1, 7); PK(1, 1, 0, 4); PK(1, 1, 0, 5); SB();
  MM(1, 3, 0, ta3_3, tb1_3_0); SB(); tb2_3_0 = CAT(tb2_3_0_l, TRD(B_AT(0, 1, 2, 0) + 512)); PK(1, 1, 1, 4); PK(1, 1, 1, 5); PK(1, 1, 0, 6); PK(1, 1, 1, 6); SB();
  MM(2, 0, 1, ta0_0, tb2_0_1); SB(); PK(2, 1, 0, 0); PK(2, 1, 0, 1); PK(2, 1, 1, 0); PK(2, 1, 1, 1); SB();
  MM(2, 1, 1, ta0_1, tb2_0_1); SB(); PK(2, 1, 0, 2); PK(2, 1, 0, 3); SB();
  MM(2, 2, 1, ta0_2, tb2_0_1); SB(); PK(2, 1, 0, 7); PK(2, 1, 1, 2); PK(2, 1, 1, 3); SB();
  MM(2, 3, 1, ta0_3, tb2_0_1); SB(); PK(2, 1, 1, 7); PK(2, 1, 0, 4); PK(2, 1, 0, 5); PK(2, 1, 1, 4); SB();
  MM(2, 0, 1, ta1_0, tb2_1_1); SB(); PK(2, 1, 1, 5); PK(2, 1, 0, 6); PK(2, 1, 1, 6); PK(3, 1, 0, 0); SB();
  MM(2, 1, 1, ta1_1, tb2_1_1); SB(); PK(3, 1, 0, 1); PK(3, 1, 1, 0); PK(3, 1, 1, 1); PK(3, 1, 0, 2); SB();
  MM(2, 2, 1, ta1_2, tb2_1_1); SB(); PK(3, 1, 0, 3); PK(3, 1, 0, 7); PK(3, 1, 1, 2); SB();
  MM(2, 3, 1, ta1_3, tb2_1_1); SB(); PK(3, 1, 1, 3); PK(3, 1, 1, 7); PK(3, 1, 0, 4); SB();
  MM(2, 0, 1, ta2_0, tb2_2_1); SB(); PK(3, 1, 0, 5); PK(3, 1, 1, 4); PK(3, 1, 1, 5); PK(3, 1, 0, 6); SB();
  MM(2, 1, 1, ta2_1, tb2_2_1); SB(); PK(3, 1, 1, 6); PK(0, 0, 0, 0); PK(0, 0, 0, 1); PK(0, 0, 1, 0); SB();
  MM(2, 2, 1, ta2_2, tb2_2_1); SB(); PK(0, 0, 1, 1); PK(0, 0, 0, 2); PK(0, 0, 0, 3); SB();
  MM(2, 3, 1, ta2_3, tb2_2_1); SB(); PK(0, 0, 0, 7); PK(0, 0, 1, 2); PK(0, 0, 1, 3); SB();
  MM(2, 0, 1, ta3_0, tb2_3_1); SB(); PK(0, 0, 1, 7); PK(0, 0, 0, 4); PK(0, 0, 0, 5); PK(0, 0, 1, 4); SB();
  MM(2, 1, 1, ta3_1, tb2_3_1); SB(); PK(0, 0, 1, 5); PK(0, 0, 0, 6); PK(0, 0, 1, 6); PK(1, 0, 0, 0); SB();
  MM(2, 2, 1, ta3_2, tb2_3_1); SB(); PK(1, 0, 0, 1); PK(1, 0, 1, 0); PK(1, 0, 1, 1); PK(1, 0, 0, 2); SB();
  MM(2, 3, 1, ta3_3, tb2_3_1); SB(); PK(1, 0, 0, 3); PK(1, 0, 0, 7); PK(1, 0, 1, 2); SB();
  MM(2, 0, 0, ta0_0, tb2_0_0); SB(); PK(1, 0, 1, 3); PK(1, 0, 1, 7); PK(1, 0, 0, 4); SB();
  MM(2, 1, 0, ta0_1, tb2_0_0); SB(); PK(1, 0, 0, 5); PK(1, 0, 1, 4); PK(1, 0, 1, 5); PK(1, 0, 0, 6); SB();
  MM(2, 2, 0, ta0_2, tb2_0_0); SB(); PK(1, 0, 1, 6); PK(2, 0, 0, 0); PK(2, 0, 0, 1); PK(2, 0, 1, 0); SB();
  MM(2, 3, 0, ta0_3, tb2_0_0); SB(); PK(2, 0, 1, 1); PK(2, 0, 0, 2); PK(2, 0, 0, 3); SB();
  MM(2, 0, 0, ta1_0, tb2_1_0); SB(); PK(2, 0, 0, 7); PK(2, 0, 1, 2); PK(2, 0, 1, 3); SB();
  MM(2, 1, 0, ta1_1, tb2_1_0); SB(); PK(2, 0, 1, 7); PK(2, 0, 0, 4); PK(2, 0, 0, 5); PK(2, 0, 1, 4); SB();
  MM(2, 2, 0, ta1_2, tb2_1_0); SB(); PK(2, 0, 1, 5); PK(2, 0, 0, 6); PK(2, 0, 1, 6); PK(3, 0, 0, 0); SB();
  MM(2, 3, 0, ta1_3, tb2_1_0); SB(); PK(3, 0, 0, 1); PK(3, 0, 1, 0); PK(3, 0, 1, 1); SB();
  MM(2, 0, 0, ta2_0, tb2_2_0); SB(); PK(3, 0, 0, 2); PK(3, 0, 0, 3); SB();
  MM(2, 1, 0, ta2_1, tb2_2_0); SB(); PK(3, 0, 0, 7); PK(3, 0, 1, 2); SB();
  MM(2, 2, 0, ta2_2, tb2_2_0); SB(); PK(3, 0, 1, 3); PK(3, 0, 1, 7); SB();
  MM(2, 3, 0, ta2_3, tb2_2_0); SB(); PK(3, 0, 0, 4); PK(3, 0, 0, 5); PK(3, 0, 1, 4); SB();
  MM(2, 0, 0, ta3_0, tb2_3_0); SB(); PK(3, 0, 1, 5); PK(3, 0, 0, 6); PK(3, 0, 1, 6); SB();
  MM(2, 1, 0, ta3_1, tb2_3_0); SB(); CV(0, 1, 0); CV(0, 1, 1); CV(1, 1, 0); SB();
  MM(2, 2, 0, ta3_2, tb2_3_0); SB(); CV(1, 1, 1); CV(2, 1, 0); CV(2, 1, 1); CV(3, 1, 0); SB();
  MM(2, 3, 0, ta3_3, tb2_3_0); SB(); CV(3, 1, 1); SB();
  const f32x4 zero = {0.f, 0.f, 0.f, 0.f};
#define TAKE(flag, READS)                                                                                  \
  _Pragma("unroll 1") for (unsigned spins = 0; spins < (1u << 22); ++spins) {                              \
    const unsigned f_ = (flag);                                                                            \
    CFENCE();                                                                                              \
    READS;                                                                                                 \
    if (__builtin_expect(rfl(f_) != 0u, 1)) break;                                                         \
  }
  unsigned lidx, ltag;
  {
    phi_fix(kp, kmin);
#define KF(j, n) FRAG(((u32x4){kp[8 * (n) + 4 * (j)], kp[8 * (n) + 4 * (j) + 1], kp[8 * (n) + 4 * (j) + 2], kp[8 * (n) + 4 * (j) + 3]}))
#define VF(j, n) FRAG(((u32x4){vp[8 * (n) + 4 * (j)], vp[8 * (n) + 4 * (j) + 1], vp[8 * (n) + 4 * (j) + 2], vp[8 * (n) + 4 * (j) + 3]}))
    f32x4 s1 = MFMA(KF(0, 1), VF(0, 1), zero), s0 = MFMA(KF(0, 0), VF(0, 1), zero), zs = MFMA(KF(0, 1), FRAG(ones), zero);
    s1 = MFMA(KF(1, 1), VF(1, 1), s1); s0 = MFMA(KF(1, 0), VF(1, 1), s0); zs = MFMA(KF(1, 1), FRAG(ones), zs);
    f32x4 z = MFMA(KF(1, 0), FRAG(ones), MFMA(KF(0, 0), FRAG(ones), zero));
    SB();
    CV(0, 0, 0); CV(0, 0, 1); CV(1, 0, 0); CV(1, 0, 1);
    *LP(f32x4, x1w, 0) = s1; *LP(f32x4, x1w, 1024) = s0; *LP(f32x4, cw, 0) = zs;
    CFENCE();
    *LP(volatile unsigned, cw, W_X1) = one;
    SB();
    CV(2, 0, 0); CV(2, 0, 1); CV(3, 0, 0); CV(3, 0, 1);
    f32x4 kv0 = MFMA(KF(0, 0), VF(0, 0), zero), kv1 = MFMA(KF(0, 1), VF(0, 0), zero);
    kv0 = MFMA(KF(1, 0), VF(1, 0), kv0); kv1 = MFMA(KF(1, 1), VF(1, 0), kv1);
    SB();
    f32x4 p0, p1, pz;
#define X1_RD { p0 = *LP(volatile f32x4, x1r, 0); p1 = *LP(volatile f32x4, x1r, 1024); pz = *LP(volatile f32x4, cr, 0); }
    const unsigned f1 = *LP(volatile unsigned, cr, W_X1);
    CFENCE();
    X1_RD
    SB();
    tb0_0_0_l = TRD(B_AT(2, 0, 0, 0)); tb0_0_0 = CAT(tb0_0_0_l, TRD(B_AT(2, 0, 0, 0) + 512));
    tb0_0_1_l = TRD(B_AT(2, 0, 0, 1)); tb0_0_1 = CAT(tb0_0_1_l, TRD(B_AT(2, 0, 0, 1) + 512));
    tb0_1_0_l = TRD(B_AT(2, 1, 0, 0)); tb0_1_0 = CAT(tb0_1_0_l, TRD(B_AT(2, 1, 0, 0) + 512));
    tb0_1_1_l = TRD(B_AT(2, 1, 0, 1)); tb0_1_1 = CAT(tb0_1_1_l, TRD(B_AT(2, 1, 0, 1) + 512));
    tb0_2_0_l = TRD(B_AT(0, 0, 0, 0)); tb0_2_0 = CAT(tb0_2_0_l, TRD(B_AT(0, 0, 0, 0) + 512));
    tb0_2_1_l = TRD(B_AT(0, 0, 0, 1)); tb0_2_1 = CAT(tb0_2_1_l, TRD(B_AT(0, 0, 0, 1) + 512));
    tb0_3_0_l = TRD(B_AT(0, 1, 0, 0)); tb0_3_0 = CAT(tb0_3_0_l, TRD(B_AT(0, 1, 0, 0) + 512));
    tb0_3_1_l = TRD(B_AT(0, 1, 0, 1)); tb0_3_1 = CAT(tb0_3_1_l, TRD(B_AT(0, 1, 0, 1) + 512));
    SB();
    if (__builtin_expect(!all_true(f1 != 0u), 0)) { TAKE(*LP(volatile unsigned, cr, W_X1), X1_RD); LGKM0(); }
    const f32x4 m0_ = kv0 + p0, m1_ = kv1 + p1;
    float am = fmax_raw(fabsf(m0_[0]), fabsf(m1_[0]));
#pragma unroll
    for (int e = 1; e < 4; ++e) am = fmax_raw(fmax_raw(am, fabsf(m0_[e])), fabsf(m1_[e]));
#define PKH(a_, b_) __builtin_bit_cast(unsigned, __builtin_convertvector((f32x2){(a_), (b_)}, f16x2_t))
    u32x4 hv = {PKH(m0_[0], m0_[1]), PKH(m0_[2], m0_[3]), PKH(m1_[0], m1_[1]), PKH(m1_[2], m1_[3])};
#define Z_STORE(scl) do { unsigned z0_ = zv[0]; asm("v_writelane_b32 %0, %1, 1" : "+v"(z0_) : "s"(scl)); zv[0] = z0_; __builtin_amdgcn_raw_buffer_store_b128(zv, wsr, zo, 0, 16); } while (0)
    PIN(hv);
    if (__builtin_expect(all_true(f2u(am) - 0x3C800000u <= 0x47000000u - 0x3C800000u), 1)) {
      __builtin_amdgcn_raw_buffer_store_b128(hv, wsr, kvo, 0, 16);
      SB();
      u32x4 zv = __builtin_bit_cast(u32x4, z + pz);
      Z_STORE(0x3F800000u);
    } else {
      unsigned ex = max(wave_max(f2u(am) & 0x7FFFFFFFu) >> 23, 15u);
      if (ex >= 121u && ex < 142u) ex = 141u;
      const float inv = u2f((268u - ex) << 23);
      u32x4 hs = {PKH(m0_[0] * inv, m0_[1] * inv), PKH(m0_[2] * inv, m0_[3] * inv), PKH(m1_[0] * inv, m1_[1] * inv), PKH(m1_[2] * inv, m1_[3] * inv)};
      u32x4 zv = __builtin_bit_cast(u32x4, z + pz);
#pragma unroll
      for (int e = 0; e < 4; ++e) { hs[e] = min(hs[e], ~1u); zv[e] = min(zv[e], ~1u); }
      CFENCE();
      __builtin_amdgcn_raw_buffer_store_b128(hs, wsr, kvo, 0, 16);
      Z_STORE((ex - 14u) << 23);
      CFENCE();
    }
  }
  *LP(volatile unsigned, cw, W_KS) = one;
  unsigned ko, oo, x2w, zfa, zfb, zfp, gta;
  u32x2 eye;
  u32x4 t[16];
  float gq[4];
  unsigned pq[16];
#define PQ(mi, n, k, st) PHI(acc[0][mi][n], pq[4 * (mi) + 2 * (n) + (k)], qmin, k, st)
#define NOPQ() asm volatile("s_nop 4" : "+v"(acc[0][3][0]), "+v"(acc[0][3][1]))
#define PRQ(i)                                                                                             \
  do {                                                                                                     \
    if ((i) == 0) { lidx = misc[C_IDX + wr]; ltag = misc[C_TAG]; }                                         \
    if ((i) == 2) { zfp = L_CTL + 4 * (C_ZF + hh); PIN(zfp); }                                             \
    if ((i) == 3) { ko = kvo - mt * 1024; PIN(ko); }                                                       \
    if ((i) == 4) { gta = L_GATE + wr * 256 + c16 * 4; PIN(gta); }                                         \
    if ((i) == 5) { zfa = L_ZF + hh * 64 + wr * 8 + g * 16; PIN(zfa); }                                    \
    if ((i) == 6) { zfb = zfa ^ 8u; PIN(zfb); }                                                            \
    if ((i) == 7) { const unsigned o_ = 0x3C00u << (16 * (lane & 1)); eye = (u32x2){(lane & 2) ? 0u : o_, (lane & 2) ? o_ : 0u}; PIN(eye); } \
    if ((i) == 8) { x2w = L_X2 + w * 1024 + lane * 16; PIN(x2w); }                                         \
    if ((i) == 9) { oo = (unsigned)OFF_OF + (unsigned)(((mt * 8 + wr * 4) * NH + head) * 1024) + lane * 16; PIN(oo); } \
  } while (0)
#define SHOT_GATE                                                                                          \
  do {                                                                                                     \
    if (__builtin_expect(!all_true(lidx != 0u), 0)) (void)wait_ge<1>(misc + C_IDX + wr, 1u);        \
    (void)wait_ge<1>(misc + C_GO + hh, 1u);                                                                \
    CFENCE();                                                                                              \
    _Pragma("unroll") for (int i = 0; i < 4; ++i) gq[i] = *LP(volatile float, gta, i * 64);                \
  } while (0)
#define T_LD(i) t[i] = __builtin_amdgcn_raw_buffer_load_b128(wsr, ko + ((i) & 3) * 1024, ((i) >> 2) * 4096, 16)
  SB();
  MM(0, 0, 0, ta0_0, tb0_0_0); SB(); PRQ(0); SB();
  MM(0, 0, 1, ta0_0, tb0_0_1); SB(); PRQ(1); SB();
  MM(0, 0, 0, ta1_0, tb0_1_0); SB(); PRQ(2); SB();
  MM(0, 0, 1, ta1_0, tb0_1_1); SB(); PRQ(3); SB();
  MM(0, 0, 0, ta2_0, tb0_2_0); SB(); PRQ(4); SB();
  MM(0, 0, 1, ta2_0, tb0_2_1); SB(); PRQ(5); SB();
  MM(0, 0, 0, ta3_0, tb0_3_0); SB(); PRQ(6); SB();
  MM(0, 0, 1, ta3_0, tb0_3_1); SB(); PRQ(7); SB();
  MM(0, 1, 0, ta0_1, tb0_0_0); SB(); PRQ(8); SB();
  MM(0, 1, 1, ta0_1, tb0_0_1); SB(); PRQ(9); SB();
  MM(0, 1, 0, ta1_1, tb0_1_0); SB(); PQ(0, 0, 0, 0); PQ(0, 0, 0, 1); PQ(0, 0, 1, 0); SB();
  MM(0, 1, 1, ta1_1, tb0_1_1); SB(); PQ(0, 0, 1, 1); PQ(0, 0, 0, 2); SB();
  MM(0, 1, 0, ta2_1, tb0_2_0); SB(); PQ(0, 0, 0, 3); PQ(0, 0, 0, 7); SB();
  MM(0, 1, 1, ta2_1, tb0_2_1); SB(); PQ(0, 0, 1, 2); PQ(0, 0, 1, 3); SB();
  MM(0, 1, 0, ta3_1, tb0_3_0); SB(); PQ(0, 0, 1, 7); PQ(0, 0, 0, 4); PQ(0, 0, 0, 5); SB();
  MM(0, 1, 1, ta3_1, tb0_3_1); SB(); PQ(0, 0, 1, 4); PQ(0, 0, 1, 5); PQ(0, 0, 0, 6); SB();
  MM(0, 2, 0, ta0_2, tb0_0_0); SB(); PQ(0, 0, 1, 6); PQ(0, 1, 0, 0); PQ(0, 1, 0, 1); SB();
  MM(0, 2, 1, ta0_2, tb0_0_1); SB(); PQ(0, 1, 1, 0); PQ(0, 1, 1, 1); PQ(0, 1, 0, 2); SB();
  MM(0, 2, 0, ta1_2, tb0_1_0); SB(); PQ(0, 1, 0, 3); PQ(0, 1, 0, 7); SB();
  MM(0, 2, 1, ta1_2, tb0_1_1); SB(); PQ(0, 1, 1, 2); PQ(0, 1, 1, 3); SB();
  MM(0, 2, 0, ta2_2, tb0_2_0); SB(); PQ(0, 1, 1, 7); PQ(0, 1, 0, 4); PQ(0, 1, 0, 5); SB();
  MM(0, 2, 1, ta2_2, tb0_2_1); SB(); PQ(0, 1, 1, 4); PQ(0, 1, 1, 5); PQ(0, 1, 0, 6); SB();
  MM(0, 2, 0, ta3_2, tb0_3_0); SB(); PQ(0, 1, 1, 6); PQ(1, 0, 0, 0); PQ(1, 0, 0, 1); SB();
  MM(0, 2, 1, ta3_2, tb0_3_1); SB(); PQ(1, 0, 1, 0); PQ(1, 0, 1, 1); PQ(1, 0, 0, 2); SB();
  MM(0, 3, 0, ta0_3, tb0_0_0); SB(); PQ(1, 0, 0, 3); PQ(1, 0, 0, 7); SB();
  MM(0, 3, 1, ta0_3, tb0_0_1); SB(); PQ(1, 0, 1, 2); PQ(1, 0, 1, 3); SB();
  MM(0, 3, 0, ta1_3, tb0_1_0); SB(); PQ(1, 0, 1, 7); PQ(1, 0, 0, 4); PQ(1, 0, 0, 5); SB();
  MM(0, 3, 1, ta1_3, tb0_1_1); SB(); PQ(1, 0, 1, 4); PQ(1, 0, 1, 5); PQ(1, 0, 0, 6); SB();
  MM(0, 3, 0, ta2_3, tb0_2_0); SB(); PQ(1, 0, 1, 6); PQ(1, 1, 0, 0); PQ(1, 1, 0, 1); SB();
  MM(0, 3, 1, ta2_3, tb0_2_1); SB(); PQ(1, 1, 1, 0); PQ(1, 1, 1, 1); PQ(1, 1, 0, 2); SB();
  MM(0, 3, 0, ta3_3, tb0_3_0); SB(); PQ(1, 1, 0, 3); PQ(1, 1, 0, 7); SB();
  MM(0, 3, 1, ta3_3, tb0_3_1); SB(); PQ(1, 1, 1, 2); PQ(1, 1, 1, 3); SB();
  PQ(1, 1, 1, 7); SB();
  PQ(1, 1, 0, 4); SB();
  PQ(1, 1, 0, 5); SB();
  PQ(1, 1, 1, 4); SB();
  PQ(1, 1, 1, 5); SB();
  PQ(1, 1, 0, 6); SB();
  PQ(1, 1, 1, 6); SB();
  PQ(2, 0, 0, 0); SB();
  PQ(2, 0, 0, 1); SB();
  PQ(2, 0, 1, 0); SB();
  PQ(2, 0, 1, 1); SB();
  PQ(2, 0, 0, 2); SB();
  PQ(2, 0, 0, 3); SB();
  PQ(2, 0, 0, 7); SB();
  PQ(2, 0, 1, 2); SB();
  PQ(2, 0, 1, 3); SB();
  PQ(2, 0, 1, 7); SB();
  PQ(2, 0, 0, 4); SB();
  PQ(2, 0, 0, 5); SB();
  PQ(2, 0, 1, 4); SB();
  PQ(2, 0, 1, 5); SB();
  PQ(2, 0, 0, 6); SB();
  PQ(2, 0, 1, 6); SB();
  PQ(2, 1, 0, 0); SB();
  PQ(2, 1, 0, 1); SB();
  PQ(2, 1, 1, 0); SB();
  PQ(2, 1, 1, 1); SB();
  PQ(2, 1, 0, 2); SB();
  PQ(2, 1, 0, 3); SB();
  PQ(2, 1, 0, 7); SB();
  PQ(2, 1, 1, 2); SB();
  PQ(2, 1, 1, 3); SB();
  PQ(2, 1, 1, 7); SB();
  PQ(2, 1, 0, 4); SB();
  PQ(2, 1, 0, 5); SB();
  PQ(2, 1, 1, 4); SB();
  PQ(2, 1, 1, 5); SB();
  PQ(2, 1, 0, 6); SB();
  PQ(2, 1, 1, 6); SB();
  NOPQ();
  PQ(3, 0, 0, 0); SB();
  PQ(3, 0, 0, 1); SB();
  PQ(3, 0, 1, 0); SB();
  PQ(3, 0, 1, 1); SB();
  PQ(3, 0, 0, 2); SB();
  PQ(3, 0, 0, 3); SB();
  PQ(3, 0, 0, 7); SB();
  PQ(3, 0, 1, 2); SB();
  PQ(3, 0, 1, 3); SB();
  PQ(3, 0, 1, 7); SB();
  PQ(3, 0, 0, 4); SB();
  PQ(3, 0, 0, 5); SB();
  PQ(3, 0, 1, 4); SB();
  PQ(3, 0, 1, 5); SB();
  PQ(3, 0, 0, 6); SB();
  PQ(3, 0, 1, 6); SB();
  PQ(3, 1, 0, 0); SB();
  PQ(3, 1, 0, 1); SB();
  PQ(3, 1, 1, 0); SB();
  PQ(3, 1, 1, 1); SB();
  PQ(3, 1, 0, 2); SB();
  PQ(3, 1, 0, 3); SB();
  PQ(3, 1, 0, 7); SB();
  PQ(3, 1, 1, 2); SB();
  PQ(3, 1, 1, 3); SB();
  PQ(3, 1, 1, 7); SB();
  PQ(3, 1, 0, 4); SB();
  PQ(3, 1, 0, 5); SB();
  PQ(3, 1, 1, 4); SB();
  PQ(3, 1, 1, 5); SB();
  PQ(3, 1, 0, 6); SB();
  PQ(3, 1, 1, 6); SB();
  SHOT_GATE;
  T_LD(0);
  T_LD(1);
  T_LD(2);
  T_LD(3);
  T_LD(4);
  T_LD(5);
  T_LD(6);
  T_LD(7);
  T_LD(8);
  T_LD(9);
  T_LD(10);
  T_LD(11);
  T_LD(12);
  T_LD(13);
  T_LD(14);
  T_LD(15);
  *LP(volatile unsigned, cw, W_AG) = one;
  phi_fix(pq, qmin);
  const unsigned tag = rfl(ltag);
  {
#define PF(mi) FRAG(((u32x4){pq[4 * (mi)], pq[4 * (mi) + 1], pq[4 * (mi) + 2], pq[4 * (mi) + 3]}))
#define H4(v, k) __builtin_bit_cast(f16x4_t, (u32x2){(v)[2 * (k)], (v)[2 * (k) + 1]})
    f32x4 s0 = zero, s1 = zero;
#define KV_MM(i0, i1)                                                                                      \
  _Pragma("unroll") for (int i = (i0); i < (i1); ++i) {                                                    \
    s0 = __builtin_amdgcn_mfma_f32_4x4x4f16(__builtin_bit_cast(f16x4_t, eye), H4(t[i], 0), s0, 0, 0, 0);   \
    s1 = __builtin_amdgcn_mfma_f32_4x4x4f16(__builtin_bit_cast(f16x4_t, eye), H4(t[i], 1), s1, 0, 0, 0);   \
  }
    float rc[4];
    u32x2 z0, z1;
#define Z_RD { z0 = *LP(volatile u32x2, zfa, 0); z1 = *LP(volatile u32x2, zfb, 0); }
    f32x4 d[4];
#define DEN1() do { const u32x4 zf = {z0[0], z0[1], z1[0], z1[1]}; _Pragma("unroll") for (int mi = 0; mi < 4; ++mi) d[mi] = MFMA(FRAG(zf), PF(mi), zero); } while (0)
#define DEN2()                                                                                             \
  do {                                                                                                     \
    _Pragma("unroll") for (int mi = 0; mi < 4; ++mi) rc[mi] = __builtin_amdgcn_rcpf(d[mi][0] + 1e-6f);     \
    _Pragma("unroll") for (int mi = 0; mi < 4; ++mi) { rc[mi] *= gq[mi]; PIN(rc[mi]); }                    \
  } while (0)
    KV_MM(0, DENAT - 4);
    SB();
    unsigned zfl = *LP(volatile unsigned, zfp, 0);
    CFENCE();
    Z_RD
    SB();
    KV_MM(DENAT - 4, DENAT);
    SB();
    DEN1();
    SB();
    KV_MM(DENAT, DENAT + 2);
    SB();
    DEN2();
    const u64 zbad = __builtin_amdgcn_ballot_w64(zfl != 1u);
    SB();
    KV_MM(DENAT + 2, 16);
    if (__builtin_expect((__builtin_amdgcn_ballot_w64(__builtin_isunordered(s0[0], s1[0])) | zbad) != 0ull, 0)) {
      unsigned todo = 15u;
#pragma unroll 1
      for (unsigned spins = 0; spins <= SPIN_CAP; ++spins) {
        unsigned miss = 0;
#pragma unroll
        for (int q = 0; q < 4; ++q)
          if (todo & (1u << q)) { if (!all_true(max(max(MX4(t[4 * q]), MX4(t[4 * q + 1])), max(MX4(t[4 * q + 2]), MX4(t[4 * q + 3]))) != ~0u)) miss |= 1u << q; }
        todo = miss;
        if (todo == 0u) break;
        __builtin_amdgcn_s_sleep(2);
        CFENCE();
#pragma unroll
        for (int q = 0; q < 4; ++q)
          if (todo & (1u << q)) { T_LD(4 * q); T_LD(4 * q + 1); T_LD(4 * q + 2); T_LD(4 * q + 3); }
      }
      s0 = zero; s1 = zero;
      KV_MM(0, 16);
      zfl = rfl(zfl);
      if (zfl == 0u) { TAKE(zfl = misc[C_ZF + hh], Z_RD); zfl = rfl(zfl); DEN1(); DEN2(); }
      if (zfl != 1u) {
        const unsigned sc = __builtin_amdgcn_raw_buffer_load_b32(wsr, (unsigned)OFF_ZP + (rec - mt + c16) * 128 + 64, 0, 16);
        s0 = zero; s1 = zero;
#pragma unroll
        for (int i = 0; i < 16; ++i) {
          const float s_ = u2f((unsigned)__builtin_amdgcn_readlane((int)sc, i));
#pragma unroll
          for (int e = 0; e < 4; ++e) {
            const unsigned tw = t[i][e];
            const f16x2_t h = __builtin_bit_cast(f16x2_t, tw);
            f32x4& d = e < 2 ? s0 : s1;
            d[2 * (e & 1)] = __builtin_fmaf((float)h[0], s_, d[2 * (e & 1)]); d[2 * (e & 1) + 1] = __builtin_fmaf((float)h[1], s_, d[2 * (e & 1) + 1]);
          }
        }
      }
    }
    const u32x4 kown = {cvt_pk(s0[0], s0[1]), cvt_pk(s0[2], s0[3]), cvt_pk(s1[0], s1[1]), cvt_pk(s1[2], s1[3])};
    *LP(u32x2, x2w, 8) = (u32x2){kown[0], kown[1]}; *LP(u32x2, x2w, 0) = (u32x2){kown[2], kown[3]};
    CFENCE();
    *LP(volatile unsigned, cw, W_X2) = one;
    SB();
    f32x4 o[4];
    u32x2 ov[4][2];
    u32x4 koth;
#pragma unroll
    for (int mi = 0; mi < 4; ++mi) o[mi] = MFMA(FRAG(kown), PF(mi), zero);
    const unsigned x2r = x2w ^ 2048u;
    SB();
#define O_CVT(mi, h) do { ov[mi][h] = (u32x2){cvt_pk(o[mi][0] * rc[mi], o[mi][1] * rc[mi]), cvt_pk(o[mi][2] * rc[mi], o[mi][3] * rc[mi])}; if ((h) == 0) PIN(ov[mi][0]); } while (0)
    O_CVT(0, 0); SB();
    const unsigned f2 = *LP(volatile unsigned, cr, W_X2);
    CFENCE();
    koth = *LP(volatile u32x4, x2r, 0);
    SB();
    O_CVT(1, 0); O_CVT(2, 0); O_CVT(3, 0);
    SB();
    if (__builtin_expect(!all_true(f2 != 0u), 0)) TAKE(*LP(volatile unsigned, cr, W_X2), koth = *LP(volatile u32x4, x2r, 0));
#pragma unroll
    for (int mi = 0; mi < 4; ++mi) o[mi] = MFMA(FRAG(koth), PF(mi), zero);
    SB();
    unsigned oom[4];
#pragma unroll
    for (int mi = 0; mi < 4; ++mi) { oom[mi] = oo + mi * (NH * 1024); PIN(oom[mi]); }
    SB();
#pragma unroll
    for (int mi = 0; mi < 4; ++mi) { O_CVT(mi, 1); __builtin_amdgcn_raw_buffer_store_b128((u32x4){ov[mi][0][0], ov[mi][0][1], ov[mi][1][0], ov[mi][1][1]}, wsr, oom[mi], 0, 16); SB(); }
  }
  SB();
  f32x4 oc[4][2];
  unsigned xa[4], xb[2], so[4];
  {
    u32x2 xl[8];
#pragma unroll
    for (int n = 0; n < 4; ++n) { xa[n] = (unsigned)(((32 * w + c16) * 128 + (((g >> 1) ^ (c16 >> 1)) * 16) + (g & 1) * 8) ^ (n * 32)); PIN(xa[n]); }
#pragma unroll
    for (int i = 0; i < 8; ++i) xl[i] = *LP(volatile u32x2, xa[i & 3], (i >> 2) * 2048);
#pragma unroll
    for (int i = 0; i < 8; ++i) {
      const int tt = i >> 2;
      const float omg = bfr(1.0f - (tt ? (hh ? gq[3] : gq[1]) : (hh ? gq[2] : gq[0])));
#pragma unroll
      for (int k = 0; k < 2; ++k) {
        const unsigned pr = cvt_pk(omg * blo(xl[i][k]), omg * bhi(xl[i][k]));
        oc[i & 3][tt][2 * k] = blo(pr); oc[i & 3][tt][2 * k + 1] = bhi(pr);
      }
      PIN(oc[i & 3][tt]);
    }
#pragma unroll
    for (int i = 0; i < 2; ++i) { xb[i] = (unsigned)((32 * w + 8 * i + (lane >> 3)) * 128 + (((lane & 7) ^ (4 * i + g)) * 16)); PIN(xb[i]); }
#pragma unroll
    for (int i = 0; i < 4; ++i) { so[i] = (unsigned)(((m0 + 32 * w + 8 * i + (lane >> 3)) * DM + 64 * nt3) * 2 + (lane & 7) * 16); PIN(so[i]); }
  }
  int wof[2][2][4];
#pragma unroll
  for (int n = 0; n < 16; ++n) {
    wof[n >> 3][(n >> 2) & 1][n & 3] = 16384 + ((n >> 2) & 1) * 65536 + (((n >> 3) ^ wr) * 16 + 4 * g + (c16 >> 2)) * 128 + (((n & 3) ^ fsw) * 32) + (c16 & 3) * 8;
    PIN(wof[n >> 3][(n >> 2) & 1][n & 3]);
  }
  const unsigned obase = (unsigned)OFF_OF + (unsigned)(((mt3 * 8 + 2 * w) * NH) * 1024 + lane * 16);
  bf16x8 wf[2][4];
#define W_AT(hd, n, r) (smem + wof[r][WOPOS((hd) >> 1) >> 3][n] + (WOPOS((hd) >> 1) & 7) * 8192 + ((hd) & 1) * 4096)
  const unsigned lw = min(misc[C_WO], misc[C_WO + 1]);
  WAIT_VM(0);
  if (lane == 0) {
    const unsigned old = __hip_atomic_fetch_add((lu32*)(smem + L_CTL) + C_OARR, 1u, RLX_WG);
    if (old == 3u) {
      __hip_atomic_store((gu32*)(ws + OFF_OFLAG + (mt * 16 + nt) * 128), tag, RLX_AGENT);
      misc[C_T6] = (unsigned)__builtin_amdgcn_s_memrealtime() | 1u;
    }
  }
  CFENCE();

  if (__builtin_expect(rfl(lw) == 0u, 0)) { (void)wait_ge<1>(misc + C_WO, 1u); (void)wait_ge<1>(misc + C_WO + 1, 1u); }
  CFENCE();
#pragma unroll
  for (int n = 0; n < 4; ++n) wf[0][n] = CAT(TRD(W_AT(0, n, 0)), TRD(W_AT(0, n, 1)));
  u32x4 of[OPF + 1][4];
  unsigned rdy = 0;
#define O_READY(c) do { if (__builtin_expect(rdy <= (unsigned)(c), 0)) rdy = wait_ge<1>(misc + C_ORDY, (unsigned)(c) + 1u); CFENCE(); } while (0)
#define O_LD1(c, k4) of[(c) % (OPF + 1)][k4] = __builtin_amdgcn_raw_buffer_load_b128(wsr, obase + ((k4) >> 1) * 1024 + ((k4) & 1) * (NH * 1024), ((rot + (c)) & 15) * 2048, 16)
#pragma unroll
  for (int c = 0; c < OPF; ++c) { O_READY(c); O_LD1(c, 0); O_LD1(c, 1); O_LD1(c, 2); O_LD1(c, 3); }
  unsigned li = 0;
#define P3_MM(c, j)                                                                                        \
  do {                                                                                                     \
    constexpr int hd = 2 * (c) + ((j) >> 3), n = ((j) >> 1) & 3;                                           \
    AMFMA(oc[n][(j) & 1], wf[(j) >> 3][n], of[(c) % (OPF + 1)][2 * ((j) >> 3) + ((j) & 1)]); SB();         \
    if (hd + 1 < 32) {                                                                                     \
      if (((j) & 1) == 0) tlo = TRD(W_AT((hd + 1) & 31, n, 0)); else wf[1 - ((j) >> 3)][n] = CAT(tlo, TRD(W_AT((hd + 1) & 31, n, 1))); \
    }                                                                                                      \
    if ((c) + OPF < 16) {                                                                                  \
      if ((j) == 0) O_READY((c) + OPF);                                                                    \
      if (((j) & 3) == 1) O_LD1((c) + OPF, (j) >> 2);                                                      \
    }                                                                                                      \
    if ((c) == 15 && (j) == 8) li = misc[C_IDX];                                                           \
    SB();                                                                                                  \
  } while (0)
#define P3_4(c, j) P3_MM(c, j); P3_MM(c, (j) + 1); P3_MM(c, (j) + 2); P3_MM(c, (j) + 3)
#define P3_C(c) P3_4(c, 0); P3_4(c, 4); P3_4(c, 8); P3_4(c, 12)
  P3_C(0); P3_C(1); P3_C(2); P3_C(3); P3_C(4); P3_C(5); P3_C(6); P3_C(7);
  P3_C(8); P3_C(9); P3_C(10); P3_C(11); P3_C(12); P3_C(13); P3_C(14); P3_C(15);
  NOP8(oc);
  li = rfl(li);
  if (__builtin_expect(li == 0u, 0)) li = wait_ge<1>(misc + C_IDX, 1u);
  const int idx = (int)li - 1;
  if ((idx >> 5) == mt3 * 4 + w) {
    f32x4 dl[4];
    TAKE(misc[C_Y], { _Pragma("unroll") for (int n = 0; n < 4; ++n) dl[n] = *(volatile lf32x4*)(smem + L_RED + 2048 + n * 64 + g * 16); });
    const bool mine = c16 == (idx & 15);
    if (idx & 16) {
#pragma unroll
      for (int n = 0; n < 4; ++n) oc[n][1] += mine ? dl[n] : zero;
    } else {
#pragma unroll
      for (int n = 0; n < 4; ++n) oc[n][0] += mine ? dl[n] : zero;
    }
  }
  u32x4 ot[4];
#pragma unroll
  for (int tt = 0; tt < 2; ++tt) {
#pragma unroll
    for (int n = 0; n < 4; ++n) *LP(volatile u32x2, xa[n], tt * 2048) = (u32x2){cvt_pk(oc[n][tt][0], oc[n][tt][1]), cvt_pk(oc[n][tt][2], oc[n][tt][3])};
    ot[2 * tt] = *LP(volatile u32x4, xb[0], tt * 2048); ot[2 * tt + 1] = *LP(volatile u32x4, xb[1], tt * 2048);
  }
#pragma unroll
  for (int i = 0; i < 4; ++i) __builtin_amdgcn_raw_buffer_store_b128(ot[i], MKRSRC(out, SEQ * DM * 2), so[i], 0, 17);
}

extern "C" void kernel_launch(void* const* d_in, const int* in_sizes, int n_in,
                              void* d_out, int out_size, void* d_ws, size_t ws_size,
                              hipStream_t stream) {
  char* ws = (char*)d_ws;
  (void)hipFuncSetAttribute((const void*)fused_kernel, hipFuncAttributeMaxDynamicSharedMemorySize, L_TOTAL);
  fused_kernel<<<256, 512, L_TOTAL, stream>>>((const u16*)d_in[0], (const u16*)d_in[1], (const u16*)d_in[2], ws, (u16*)d_out,
                                              (const u16*)d_in[4], (const u16*)d_in[8], (const u16*)d_in[5], (const u16*)d_in[6],
                                              (const u16*)d_in[7]);
}
```

```cpp
#include <hip/hip_runtime.h>
#include <hip/hip_bf16.h>
#include <stdint.h>

typedef unsigned short u16;
typedef unsigned long long u64;
typedef __attribute__((ext_vector_type(8))) short bf16x8;
typedef __attribute__((ext_vector_type(4))) short s16x4;
typedef __attribute__((ext_vector_type(4))) float f32x4;
typedef __attribute__((ext_vector_type(2))) float f32x2;
typedef __attribute__((ext_vector_type(2))) __bf16 bf16x2_t;
typedef __attribute__((ext_vector_type(2))) _Float16 f16x2_t;
typedef __attribute__((ext_vector_type(4))) _Float16 f16x4_t;
typedef __attribute__((ext_vector_type(4))) unsigned int u32x4;
typedef __attribute__((ext_vector_type(2))) unsigned int u32x2;
typedef __attribute__((ext_vector_type(8))) unsigned int u32x8;
typedef __attribute__((address_space(3))) s16x4* lds_s16x4_ptr;
typedef __attribute__((address_space(1))) unsigned gu32;
typedef __attribute__((address_space(1))) u64 gu64;
typedef __attribute__((address_space(3))) unsigned lu32;
typedef __attribute__((address_space(3))) u32x2 lu32x2;
typedef __attribute__((address_space(3))) u32x4 lu32x4;
typedef __attribute__((address_space(3))) f32x4 lf32x4;
#define RLX_AGENT __ATOMIC_RELAXED, __HIP_MEMORY_SCOPE_AGENT
#define RLX_WG __ATOMIC_RELAXED, __HIP_MEMORY_SCOPE_WORKGROUP

#if defined(__HIP_DEVICE_COMPILE__)
#define NOPK __attribute__((target("no-packed-fp32-ops")))
#else
#define NOPK
#endif

#define SEQ 2048
#define DM 1024
#define N1 3072
#define FF 3584
#define NH 32

#define OFF_EPOCH  ((size_t)0)
#define OFF_RG     ((size_t)4096)
#define OFF_GG     ((size_t)16384)
#define OFF_GU     ((size_t)262144)
#define OFF_YG     ((size_t)524288)
#define OFF_KVP    ((size_t)1 << 20)
#define OFF_ZP     ((size_t)2 << 20)
#define OFF_OFLAG  (((size_t)3 << 20) + 65536)
#define OFF_OF     ((size_t)4 << 20)

#define SPIN_CAP (1u << 18)

__device__ __forceinline__ float u2f(unsigned u) { return __builtin_bit_cast(float, u); }
__device__ __forceinline__ unsigned f2u(float f) { return __builtin_bit_cast(unsigned, f); }
__device__ __forceinline__ bool all_true(bool ok) { return __builtin_amdgcn_ballot_w64(!ok) == 0ull; }
__device__ __forceinline__ unsigned cvt_pk(float a, float b) {
  f32x2 v = {a, b};
  return __builtin_bit_cast(unsigned, __builtin_convertvector(v, bf16x2_t));
}
__device__ __forceinline__ float blo(unsigned u) { return u2f(u << 16); }
__device__ __forceinline__ float bhi(unsigned u) { return u2f(u & 0xFFFF0000u); }
__device__ __forceinline__ float bfr(float f) { return blo(cvt_pk(f, f)); }
__device__ __forceinline__ float fmax_raw(float a, float b) { return __builtin_elementwise_maximum(a, b); }
__device__ __forceinline__ unsigned eps2(unsigned p) { return cvt_pk(blo(p) + 1e-6f, bhi(p) + 1e-6f); }
template <int N>
__device__ __forceinline__ void phi_fix(unsigned (&p)[N], float umin) {
  if (__builtin_expect(__builtin_amdgcn_ballot_w64(umin < -5.9f) != 0ull, 0)) {
#pragma unroll
    for (int i = 0; i < N; ++i) p[i] = eps2(p[i]);
  }
}
__device__ __forceinline__ float sigmoid_f(float x) { return 1.0f / (1.0f + __expf(-x)); }

template <int CTRL, int RM = 0xf> __device__ __forceinline__ int dppi(int old, int v) {
  return __builtin_amdgcn_update_dpp(old, v, CTRL, RM, 0xf, false);
}
template <int CTRL, int RM = 0xf> __device__ __forceinline__ float dppf(float v) {
  return __builtin_bit_cast(float, dppi<CTRL, RM>(0, __builtin_bit_cast(int, v)));
}
__device__ __forceinline__ unsigned wave_max(unsigned v) {
  v = max(v, (unsigned)dppi<0x111>(0, (int)v)); v = max(v, (unsigned)dppi<0x112>(0, (int)v));
  v = max(v, (unsigned)dppi<0x114>(0, (int)v)); v = max(v, (unsigned)dppi<0x118>(0, (int)v));
  v = max(v, (unsigned)dppi<0x142, 0xa>(0, (int)v)); v = max(v, (unsigned)dppi<0x143, 0xc>(0, (int)v));
  return (unsigned)__builtin_amdgcn_readlane((int)v, 63);
}
__device__ __forceinline__ float merge32(float a, float b) {
  const auto r = __builtin_amdgcn_permlane32_swap(f2u(a), f2u(b), false, false);
  return u2f(r[0]) + u2f(r[1]);
}
__device__ __forceinline__ float merge16(float a, float b) {
  const auto r = __builtin_amdgcn_permlane16_swap(f2u(a), f2u(b), false, false);
  return u2f(r[0]) + u2f(r[1]);
}
typedef __attribute__((address_space(3))) void* ldsp;
template <int IMM>
__device__ __forceinline__ void bufl16(__amdgpu_buffer_rsrc_t r, unsigned l, unsigned voff, unsigned soff) {
#if defined(__HIP_DEVICE_COMPILE__)
  __builtin_amdgcn_raw_ptr_buffer_load_lds(r, (ldsp)(uintptr_t)l, 16, voff, soff, IMM, 0);
#endif
}
#define LP(T, base, imm) ((__attribute__((address_space(3))) T*)(uintptr_t)(unsigned)((base) + (imm)))
#define MKRSRC(p, n) __builtin_amdgcn_make_buffer_rsrc((void*)(p), 0, (n), 0x00020000)
#define WAIT_VM(n) asm volatile("s_waitcnt vmcnt(%0)" ::"n"(n) : "memory")
#define WAIT_LGKM0() asm volatile("s_waitcnt lgkmcnt(0)" ::: "memory")
#define BAR() do { __builtin_amdgcn_sched_barrier(0); asm volatile("s_barrier" ::: "memory"); __builtin_amdgcn_sched_barrier(0); } while (0)
#define CFENCE() asm volatile("" ::: "memory")
#define MFMA(a, b, c) __builtin_amdgcn_mfma_f32_16x16x32_bf16((a), (b), (c), 0, 0, 0)
#define PIN(v) asm volatile("" : "+v"(v))
#define AMFMA(acc, a, b) asm volatile("v_mfma_f32_16x16x32_bf16 %0, %1, %2, %0" : "+v"(acc) : "v"(a), "v"(b))
#define NOP8(t) asm volatile("s_nop 7" : "+v"(t[0][0]), "+v"(t[0][1]), "+v"(t[1][0]), "+v"(t[1][1]), "+v"(t[2][0]), "+v"(t[2][1]), "+v"(t[3][0]), "+v"(t[3][1]))
#define SB() __builtin_amdgcn_sched_barrier(0)
#define TRD(p) ((s16x4)__builtin_amdgcn_ds_read_tr16_b64_v4i16((lds_s16x4_ptr)(p)))
#define CAT(lo, hi) __builtin_shufflevector(lo, hi, 0, 1, 2, 3, 4, 5, 6, 7)
#define FRAG(x) __builtin_bit_cast(bf16x8, x)

#define PACE   57
#define BURST  30
#define R_AT   2
#define R_GAP  2
#define CPF_N  20
#define WO_E   6
#define WO_ES  4
#define WO_M   11
#define G_T    80
#define P5_T   70
#define GU_AH  16
#define DENAT  8
#define OPF    3

__device__ __forceinline__ unsigned rfl(unsigned v) { return (unsigned)__builtin_amdgcn_readfirstlane((int)v); }
template <int SLEEP>
__device__ __forceinline__ unsigned wait_ge(volatile lu32* p, unsigned need) {
  unsigned v = 0;
#pragma unroll 1
  for (unsigned spins = 0; spins < (1u << 22); ++spins) {
    v = rfl(*p);
    if (v >= need) break;
    __builtin_amdgcn_s_sleep(SLEEP);
  }
  CFENCE();
  return v;
}

#define SLOT     40960
#define L_X1     147456
#define L_X2     155648
#define L_CW     159744
#define L_ZF     160256
#define L_GATE   160384
#define L_RED    160896
#define L_H      163200
#define L_CTL    163712
#define L_TOTAL  163840
#define W_X1   64
#define W_X2   68
#define W_AG   72
#define W_KS   76
#define WFLAGS(f) (*(volatile lu32*)(smem + L_CW + (lane & 3) * 128 + (f)))
#define WOPOS(c) ((c) < 3 ? 13 + (c) : (c) < 8 ? (c) : (c) < 11 ? (c) - 8 : (c) - 3)
#define C_F15  0
#define C_WO   2
#define C_ZF   4
#define C_TAG  8
#define C_IDX  10
#define C_GO   12
#define C_OARR 17
#define C_ORDY 18
#define C_GU   19
#define C_DN   20
#define C_T6   21
#define C_Y    23

NOPK __global__ __launch_bounds__(512, 2) void fused_kernel(const u16* __restrict__ x, const u16* __restrict__ wqkv,
                                                            const u16* __restrict__ wo, char* ws, u16* __restrict__ out,
                                                            const u16* __restrict__ w_r, const u16* __restrict__ gate_w,
                                                            const u16* __restrict__ w_gate, const u16* __restrict__ w_up,
                                                            const u16* __restrict__ w_down) {
  extern __shared__ __attribute__((aligned(16))) char smem[];
  asm volatile("s_load_dwordx2 s[100:101], %0, 60" :: "s"(__builtin_amdgcn_kernarg_segment_ptr()) : "s100", "s101");
  SB();
  const int tid = threadIdx.x, lane = tid & 63;
  const int w = __builtin_amdgcn_readfirstlane(tid >> 6);
  u64 pc0;
  asm volatile("s_getpc_b64 %0" : "=s"(pc0));
  const int bid = blockIdx.x, xcd = bid & 7, ix = bid >> 3;
  const int ml = ix >> 2, nl = ix & 3;
  const int mt = (xcd >> 2) * 8 + ml, nt = (xcd & 3) * 4 + nl;
  const int m0 = mt * 128;
  const int mt3 = mt, nt3 = nt ^ 1;
#define CH(c) (((4 * (xcd & 3) + (c)) & 15) ^ ((c) < 4 ? nl : 0))
  volatile lu32* misc = (volatile lu32*)(smem + L_CTL);

  if (__builtin_expect((w >> 1) == 2, 1)) {
    u32x8 ka;
    asm volatile("s_load_dwordx8 %0, %1, 0x0" : "=s"(ka) : "s"(__builtin_amdgcn_kernarg_segment_ptr()));
    SB();
    const int p = w - 4;
    unsigned va[4], vb[4], vw[4];
#pragma unroll
    for (int j = 0; j < 4; ++j) {
      const int row = 32 * (w & 1) + 8 * j + (lane >> 3);
      va[j] = (unsigned)((m0 + row) * (DM * 2) + (((lane & 7) ^ ((row >> 1) & 7)) * 16) - 1024 * j);
      vb[j] = (unsigned)(row * (N1 * 2) + 128 * nt + (((lane & 7) ^ ((row & 2) | ((j & 1) << 2))) * 16) - 1024 * j);
      PIN(va[j]); PIN(vb[j]);
    }
    unsigned l0 = (unsigned)(uintptr_t)(ldsp)smem + p * 4096;
    int kf = (4 * ml + 4 * (xcd & 3)) & 15, ks = (4 * ml - 2 * (ml >> 2) + 4 * (xcd & 3)) & 15, sres = (3 - nt) & 7;
    asm volatile("" : "+s"(l0), "+s"(kf), "+s"(ks), "+s"(sres));
    SB();
    asm volatile("s_waitcnt lgkmcnt(0)" : "+s"(ka));
    const u16* x = (const u16*)(((u64)ka[1] << 32) | ka[0]);
    const u16* wqkv = (const u16*)(((u64)ka[3] << 32) | ka[2]);
    const u16* wo = (const u16*)(((u64)ka[5] << 32) | ka[4]);
    char* ws = (char*)(((u64)ka[7] << 32) | ka[6]);
    int ko_ = (int)((ka[2] >> 7) & 7u) == sres ? ks : kf;
    const __amdgpu_buffer_rsrc_t ra = MKRSRC(x, SEQ * DM * 2), rb = MKRSRC(wqkv, DM * N1 * 2), rw = MKRSRC(wo, DM * DM * 2);
#define UNIT(r, v, off, so) do { bufl16<0>(r, l0 + (off), v[0], so); bufl16<1024>(r, l0 + (off), v[1], so); bufl16<2048>(r, l0 + (off), v[2], so); bufl16<3072>(r, l0 + (off), v[3], so); } while (0)
#define ISSUE(t)                                                                                           \
  do {                                                                                                     \
    const int k_ = (ko_ + (t)) & 15;                                                                       \
    UNIT(ra, va, ((t) % 3) * SLOT, k_ * 128); UNIT(ra, va, ((t) % 3) * SLOT + 8192, k_ * 128 + 64 * DM * 2); \
    _Pragma("unroll") for (int s_ = 0; s_ < 3; ++s_) UNIT(rb, vb, ((t) % 3) * SLOT + 16384 + s_ * 8192, k_ * (64 * N1 * 2) + s_ * (DM * 2)); \
  } while (0)
#define PACED_BAR()                                                                                        \
  do {                                                                                                     \
    if (p == 0) {                                                                                          \
      unsigned now;                                                                                        \
      do { now = (unsigned)__builtin_amdgcn_s_memrealtime(); } while ((int)(now - tp) < 0);                \
      tp = ((int)(now - tp) > BURST ? now - BURST : tp) + PACE;                                            \
    }                                                                                                      \
    BAR();                                                                                                 \
  } while (0)
    ISSUE(0); ISSUE(1);
    WAIT_VM(20);
    BAR();
    ISSUE(2);
    unsigned tp = (unsigned)__builtin_amdgcn_s_memrealtime() - PACE;
#pragma unroll
    for (int j = 0; j < 4; ++j) vw[j] = (unsigned)((32 * p + 8 * j + (lane >> 3)) * (DM * 2) + 128 * nt3 + (((lane & 7) ^ ((lane >> 3) & 6)) * 16) - 1024 * j);
#define WO_CHUNK(c) UNIT(rw, vw, 16384 + WOPOS(c) * 8192, CH(c) * (64 * DM * 2))
#pragma unroll
    for (int t = 0; t < 13; ++t) {
      WAIT_VM(20);
      PACED_BAR();
      asm volatile("" : "+s"(l0), "+s"(ko_));
      ISSUE(t + 3);
    }
    WO_CHUNK(0); WO_CHUNK(1); WO_CHUNK(2);
    SB();
    const bool wslow = ((((uintptr_t)wo >> 7) + nt3) & 7) == 3;
    const int woe = wslow ? WO_ES : WO_E;
    WAIT_VM(32);
    PACED_BAR();
    WAIT_VM(12);
    if (lane == 0) misc[C_F15 + p] = 1u;
#pragma unroll 1
    for (int c = 3; c < woe; ++c) WO_CHUNK(c);
    {
      const __amdgpu_buffer_rsrc_t zr = MKRSRC(ws + OFF_ZP, NH * 2 * 16 * 128);
      const unsigned r0 = (unsigned)((2 * nt + p) * 32);
      const unsigned zo0 = (r0 + (lane & 31)) * 128 + 64, zo1 = (r0 + (lane >> 2)) * 128 + (lane & 3) * 16;
      u32x4 zw[2];
      unsigned sc;
      (void)wait_ge<1>(misc + C_IDX + p, 1u);
#pragma unroll 1
      for (unsigned spins = 0; spins < (1u << 22) && !all_true(WFLAGS(W_KS) != 0u); ++spins) __builtin_amdgcn_s_sleep(1);
      CFENCE();
      const unsigned tk = (unsigned)__builtin_amdgcn_s_memrealtime();
#pragma unroll 1
      for (int c = woe; c < WO_M; ++c) WO_CHUNK(c);
      UNIT(ra, va, 0, nt3 * 128); UNIT(ra, va, 8192, nt3 * 128 + 64 * DM * 2);
      while ((unsigned)__builtin_amdgcn_s_memrealtime() - tk < (unsigned)G_T) __builtin_amdgcn_s_sleep(1);
#define MX4(v) max(max((v)[0], (v)[1]), max((v)[2], (v)[3]))
#pragma unroll 1
      for (unsigned spins = 0; spins <= SPIN_CAP; ++spins) {
        CFENCE();
        zw[0] = __builtin_amdgcn_raw_buffer_load_b128(zr, zo1, 0, 16); zw[1] = __builtin_amdgcn_raw_buffer_load_b128(zr, zo1, 2048, 16);
        sc = __builtin_amdgcn_raw_buffer_load_b32(zr, zo0, 0, 16);
        SB(); misc[C_GO + p] = 1u; SB();
        if (all_true(max(max(MX4(zw[0]), MX4(zw[1])), sc) != ~0u)) break;
        __builtin_amdgcn_s_sleep(2);
      }
      float z0 = merge16(merge32(u2f(zw[0][0]), u2f(zw[0][2])), merge32(u2f(zw[1][0]), u2f(zw[1][2])));
      float z1 = merge16(merge32(u2f(zw[0][1]), u2f(zw[0][3])), merge32(u2f(zw[1][1]), u2f(zw[1][3])));
      z0 += dppf<0x128>(z0); z1 += dppf<0x128>(z1); z0 += dppf<0x124>(z0); z1 += dppf<0x124>(z1);
      *(unsigned*)(smem + L_ZF + p * 64 + (lane & 3) * 16 + ((lane >> 4) & 1) * 8 + (lane >> 5) * 4) = cvt_pk(z0, z1);
      CFENCE();
      misc[C_ZF + p] = all_true(sc == 0x3F800000u) ? 1u : 2u;
    }
#pragma unroll 1
    for (unsigned spins = 0; spins < (1u << 22) && !all_true(WFLAGS(W_AG) != 0u); ++spins) __builtin_amdgcn_s_sleep(2);
    CFENCE();
#pragma unroll 1
    for (int c = WO_M; c < 16; ++c) WO_CHUNK(c);
    WAIT_VM(0);
    if (lane == 0) misc[C_WO + p] = 1u;
#undef ISSUE
#undef PACED_BAR
    if (p == 0) return;
    const unsigned tag = rfl(misc[C_TAG]);
    gu32* of = (gu32*)(ws + OFF_OFLAG + (mt3 * 16 + CH(lane & 15)) * 128);
    const unsigned t6 = wait_ge<1>(misc + C_T6, 1u);
    while ((unsigned)__builtin_amdgcn_s_memrealtime() - t6 < (unsigned)P5_T) __builtin_amdgcn_s_sleep(1);
#pragma unroll 1
    for (unsigned spins = 0;; ++spins) {
      const unsigned f = __hip_atomic_load(of, RLX_AGENT);
      const unsigned miss = (unsigned)__builtin_amdgcn_ballot_w64(f != tag) & 0xFFFEu;
      const unsigned m = (miss == 0u || spins > SPIN_CAP) ? 16u : (unsigned)__builtin_ctz(miss);
      if (lane == 0) misc[C_ORDY] = m;
      if (m == 16u) break;
      __builtin_amdgcn_s_sleep(2);
    }
    return;
  }

  if (__builtin_expect(w >= 6, 0)) {
    const int pa = w - 6;
#define CODE_PF(on) __builtin_amdgcn_raw_buffer_load_b32(MKRSRC((pc0 & ~255ull) + (blockIdx.x >> 3) * (CPF_N * 64u), (on) ? CPF_N * 64 : 0), lane * 64, 0, 0)
    unsigned cpf = CODE_PF(w == 7), cpf2;
    SB();
    float* red = (float*)(smem + L_RED);
    const bool help = bid >= 224, gemv = bid < 252;
    const int gmat = bid & 1;
    const u16* wgu = gmat ? w_up : w_gate;
    const int sres = (int)(3u - (unsigned)((uintptr_t)wgu >> 7)) & 7;
    const int gfs = help ? sres + 8 * (((bid - 224) >> 1) % 7) : (bid >> 1) % 56;
    const int gdh = (help ? ((bid - 224) >> 1) / 7 : (bid >> 1) / 56) & 1;
    const bool act0 = !help, act1 = gemv && (help || (gfs & 7) != sres);
    const int gd0 = gdh * 512 + 256 * pa + 32 * (lane >> 3);
    const __amdgpu_buffer_rsrc_t rgu0 = MKRSRC(wgu, act0 ? DM * FF * 2 : 0), rgu1 = MKRSRC(wgu, act1 ? DM * FF * 2 : 0);
    const unsigned wpo = (unsigned)((gd0 * FF + gfs * 64 + 8 * (lane & 7)) * 2);
    u32x4 wv[32], wd[14];
    const int fg = bid >> 4, cg = bid & 15;
    const int f0 = fg * 224 + pa * 112;
    const __amdgpu_buffer_rsrc_t rdn = MKRSRC(w_down, DM * FF * 2);
    const unsigned wqo = (unsigned)(((f0 + 14 * (lane >> 3)) * DM + cg * 64 + 8 * (lane & 7)) * 2);
    asm volatile("" ::"s"(w_r), "s"(gate_w), "s"(ws), "s"(w_gate), "s"(w_up), "s"(w_down));
    unsigned pf0, pf1;
#define WO_PREFETCH()                                                                                      \
  do {                                                                                                     \
    const u16* pp_ = wo + (size_t)(128 * ml + 64 * pa + (lane >> 1)) * DM + 64 * nt3 + (lane & 1) * 32;    \
    pf0 = *(const unsigned*)pp_; pf1 = *(const unsigned*)(pp_ + 32 * DM);                                  \
  } while (0)
    {
#pragma unroll
      for (int t = 0; t <= R_AT; ++t) BAR();
      const int r0 = m0 + nt * 8 + 4 * pa;
      u32x4 xr[4][2], rv[2], gv[2];
#pragma unroll
      for (int i = 0; i < 2; ++i) {
        rv[i] = *(const u32x4*)(w_r + i * 512 + lane * 8);
        gv[i] = *(const u32x4*)(gate_w + i * 512 + lane * 8);
      }
#pragma unroll
      for (int r = 0; r < 4; ++r)
#pragma unroll
        for (int i = 0; i < 2; ++i) xr[r][i] = *(const u32x4*)(x + (size_t)(r0 + r) * DM + i * 512 + lane * 8);
      const unsigned ep = __hip_atomic_load((gu32*)(ws + OFF_EPOCH), RLX_AGENT);
      {
        const u32x4 nw = {~0u, ~0u, ~0u, ~0u};
        const __amdgpu_buffer_rsrc_t kvr = MKRSRC(ws + OFF_KVP, NH * 2 * 16 * 1024), zr = MKRSRC(ws + OFF_ZP, NH * 2 * 16 * 128);
        const unsigned rec = (unsigned)((2 * nt + pa) * 32 + mt);
        __builtin_amdgcn_raw_buffer_store_b128(nw, kvr, rec * 1024 + lane * 16, 0, 16);
        __builtin_amdgcn_raw_buffer_store_b128(nw, kvr, rec * 1024 + lane * 16 + 16384, 0, 16);
        if (lane < 16) __builtin_amdgcn_raw_buffer_store_b128(nw, zr, (rec + (lane >> 3) * 16) * 128 + (lane & 7) * 16, 0, 16);
      }
#pragma unroll
      for (int t = R_AT + 1; t < R_AT + R_GAP; ++t) BAR();
      float racc[4], gacc[4];
#pragma unroll
      for (int r = 0; r < 4; ++r) {
        BAR();
        if (r == 1) WAIT_VM(0);
        racc[r] = 0.f; gacc[r] = 0.f;
#pragma unroll
        for (int i = 0; i < 2; ++i)
#pragma unroll
          for (int e = 0; e < 4; ++e) {
            const float x0 = blo(xr[r][i][e]), x1 = bhi(xr[r][i][e]);
            racc[r] += x0 * blo(rv[i][e]); racc[r] += x1 * bhi(rv[i][e]);
            const unsigned pr = cvt_pk(x0 * blo(gv[i][e]), x1 * bhi(gv[i][e]));
            gacc[r] += blo(pr); gacc[r] += bhi(pr);
          }
        PIN(racc[r]); PIN(gacc[r]);
        if (r == 3) { SB(); WO_PREFETCH(); cpf2 = CODE_PF(w == 7); }
      }
      BAR();
      float rs = merge16(merge32(racc[0], racc[1]), merge32(racc[2], racc[3]));
      float gs = merge16(merge32(gacc[0], gacc[1]), merge32(gacc[2], gacc[3]));
      rs += dppf<0x128>(rs); gs += dppf<0x128>(gs); rs += dppf<0x124>(rs); gs += dppf<0x124>(gs);
      rs += dppf<0x122>(rs); gs += dppf<0x122>(gs); rs += dppf<0x121>(rs); gs += dppf<0x121>(gs);
      const int rr = r0 + ((lane >> 5) | ((lane >> 3) & 2));
      const unsigned rb = cvt_pk(rs, rs) & 0xFFFFu;
      unsigned key = (((rb & 0x8000u) ? (~rb & 0xFFFFu) : (rb | 0x8000u)) << 16) | (0xFFFFu - (unsigned)rr);
      { const auto t_ = __builtin_amdgcn_permlane16_swap(key, key, false, false); key = max(t_[0], t_[1]); }
      { const auto t_ = __builtin_amdgcn_permlane32_swap(key, key, false, false); key = max(t_[0], t_[1]); }
      const unsigned tg = rfl(ep) + 1u;
      if (lane == 0) misc[C_TAG + pa] = pa ? key : tg;
      WAIT_LGKM0();
      BAR();
      if (pa == 0 && lane == 0) __hip_atomic_store((gu64*)(ws + OFF_RG) + bid, ((u64)tg << 32) | max(key, (unsigned)misc[C_TAG + 1]), RLX_AGENT);
      if ((lane & 15) == 0) __hip_atomic_store((gu64*)(ws + OFF_GG) + rr, ((u64)tg << 32) | f2u(bfr(sigmoid_f(bfr(gs)))), RLX_AGENT);
#pragma unroll
      for (int t = R_AT + R_GAP + 6; t < 13; ++t) BAR();
    }
    u64 rg[4];
#pragma unroll
    for (int i = 0; i < 4; ++i) rg[i] = __hip_atomic_load((gu64*)(ws + OFF_RG) + i * 64 + lane, RLX_AGENT);
    const gu64* ggp = (const gu64*)(ws + OFF_GG) + m0 + 64 * pa + lane;
    u64 gg = __hip_atomic_load(ggp, RLX_AGENT);
    BAR();
    const unsigned tag = rfl(misc[C_TAG]);
    unsigned idx;
    unsigned bestk;
    bool late;
    {
      unsigned best = 0;
      bool ok = (unsigned)(gg >> 32) == tag;
#pragma unroll
      for (int i = 0; i < 4; ++i) { ok = ok && (unsigned)(rg[i] >> 32) == tag; best = max(best, (unsigned)rg[i]); }
      late = !all_true(ok);
      if (__builtin_expect(late, 0)) {
        BAR();
#pragma unroll 1
        for (unsigned spins = 0;; ++spins) {
          u64 rh[4];
#pragma unroll
          for (int i = 0; i < 4; ++i) rh[i] = __hip_atomic_load((gu64*)(ws + OFF_RG) + i * 64 + lane, RLX_AGENT);
          gg = __hip_atomic_load(ggp, RLX_AGENT);
          ok = (unsigned)(gg >> 32) == tag; best = 0;
#pragma unroll
          for (int i = 0; i < 4; ++i) { ok = ok && (unsigned)(rh[i] >> 32) == tag; best = max(best, (unsigned)rh[i]); }
          if (all_true(ok) || spins > SPIN_CAP) break;
          __builtin_amdgcn_s_sleep(8);
        }
      }
      best = wave_max(best);
      idx = (0xFFFFu - (best & 0xFFFFu)) & (SEQ - 1);
      bestk = best;
    }
    asm volatile("" ::"v"(pf0), "v"(pf1), "v"(cpf), "v"(cpf2));
    u32x4 xs[4];
#pragma unroll
    for (int i = 0; i < 4; ++i) xs[i] = *(const u32x4*)(x + (size_t)idx * DM + gd0 + 8 * i);
    if (!late) BAR();
#pragma unroll
    for (int i = 0; i < 32; ++i) wv[i] = __builtin_amdgcn_raw_buffer_load_b128(i < 16 ? rgu0 : rgu1, wpo, i * (FF * 2), 2);
    SB();
    ((unsigned*)(smem + L_GATE))[64 * pa + lane] = (unsigned)gg;
    CFENCE();
    if (lane == 0) misc[C_IDX + pa] = idx + 1u;
    if (bid == 0 && pa == 0 && lane == 0) __hip_atomic_store((gu32*)(ws + OFF_EPOCH), tag, RLX_AGENT);
    unsigned sel[2][4];
#pragma unroll
    for (int i = 0; i < 8; ++i) {
      const unsigned pick = (i >> 2) ? 0x0302u : 0x0100u;
      sel[i >> 2][i & 3] = ((lane & 7) >> 1) == (i & 3) ? ((lane & 1) ? (pick << 16) | 0x0C0Cu : 0x0C0C0000u | pick) : 0x0C0C0C0Cu;
      PIN(sel[i >> 2][i & 3]);
    }
#define AFR(pr, i) FRAG(((u32x4){__builtin_amdgcn_perm(0u, pr, sel[(i) & 1][0]), __builtin_amdgcn_perm(0u, pr, sel[(i) & 1][1]), \
                                 __builtin_amdgcn_perm(0u, pr, sel[(i) & 1][2]), __builtin_amdgcn_perm(0u, pr, sel[(i) & 1][3])}))
    f32x4* rdw = (f32x4*)(red + pa * 128 + (lane >> 5) * 64 + (lane & 7) * 8 + ((lane >> 4) & 1) * 4);
    const bool rvalid = (lane >> 5) == ((lane >> 3) & 1);
    const auto rv = LP(volatile float, L_RED, lane * 4);
    {
      bf16x8 af[32];
      f32x4 a = {0.f, 0.f, 0.f, 0.f};
#pragma unroll
      for (int i = 0; i < GU_AH; ++i) { af[i] = AFR(xs[i >> 3][(i >> 1) & 3], i); PIN(af[i]); }
      SB();
#pragma unroll
      for (int i = 0; i < 32; ++i) {
        a = MFMA(af[i], FRAG(wv[i]), a);
        PIN(a);
        if (i + GU_AH < 32) { af[(i + GU_AH) & 31] = AFR(xs[((i + GU_AH) >> 3) & 3][((i + GU_AH) >> 1) & 3], i + GU_AH); PIN(af[(i + GU_AH) & 31]); }
        SB();
      }
      if (rvalid) *rdw = a;
      CFENCE();
      if (pa == 1) misc[C_GU] = 1u;
      else {
        float v;
#pragma unroll 1
        for (unsigned spins = 0; spins < (1u << 22); ++spins) {
          const unsigned f_ = misc[C_GU];
          CFENCE();
          v = (rv[0] + rv[64]) + (rv[128] + rv[192]);
          if (rfl(f_) != 0u) break;
          __builtin_amdgcn_s_sleep(1);
        }
        if (gemv) __hip_atomic_store((gu64*)(ws + OFF_GU) + ((help ? 4 : 0) + gdh * 2 + gmat) * FF + gfs * 64 + lane, ((u64)tag << 32) | f2u(v), RLX_AGENT);
      }
    }
    {
#pragma unroll 1
      for (unsigned spins = 0; spins < (1u << 22) && !all_true(WFLAGS(W_X2) != 0u); ++spins) __builtin_amdgcn_s_sleep(1);
      __builtin_amdgcn_s_sleep(10);
#pragma unroll
      for (int i = 0; i < 14; ++i) wd[i] = __builtin_amdgcn_raw_buffer_load_b128(rdn, wqo, i * (DM * 2), 2);
      SB();
      float hv[2];
      const int fl = f0 + (lane < 56 ? lane : 55);
      const gu64* gp = (const gu64*)(ws + OFF_GU) + fl;
      bool ex[2][2];
#pragma unroll
      for (int j = 0; j < 2; ++j) {
        ex[j][0] = ((((unsigned)((uintptr_t)w_gate >> 7) + ((fl + 56 * j) >> 6)) & 7u) == 3u);
        ex[j][1] = ((((unsigned)((uintptr_t)w_up >> 7) + ((fl + 56 * j) >> 6)) & 7u) == 3u);
      }
#pragma unroll 1
      for (unsigned spins = 0;; ++spins) {
        u64 g[2][8];
#pragma unroll
        for (int j = 0; j < 2; ++j)
#pragma unroll
          for (int i = 0; i < 8; ++i) {
            g[j][i] = (u64)tag << 32;
            if (i < 4 || ex[j][i & 1]) g[j][i] = __hip_atomic_load(gp + i * FF + j * 56, RLX_AGENT);
          }
        bool ok = true;
#pragma unroll
        for (int j = 0; j < 2; ++j) {
#pragma unroll
          for (int i = 0; i < 8; ++i) ok = ok && (unsigned)(g[j][i] >> 32) == tag;
          const float gt = bfr((u2f((unsigned)g[j][0]) + u2f((unsigned)g[j][4])) + (u2f((unsigned)g[j][2]) + u2f((unsigned)g[j][6])));
          const float up = bfr((u2f((unsigned)g[j][1]) + u2f((unsigned)g[j][5])) + (u2f((unsigned)g[j][3]) + u2f((unsigned)g[j][7])));
          hv[j] = bfr(gt * __builtin_amdgcn_rcpf(1.0f + __expf(-gt))) * up;
        }
        if (all_true(ok) || spins > SPIN_CAP) break;
        __builtin_amdgcn_s_sleep(4);
      }
      u32x4 hp[2];
      {
        char* hb = smem + L_H + pa * 256;
        const unsigned pr = cvt_pk(hv[0], hv[1]);
        if (lane < 56) { *(u16*)(hb + (lane / 14) * 32 + (lane % 14) * 2) = (u16)pr; *(u16*)(hb + 128 + (lane / 14) * 32 + (lane % 14) * 2) = (u16)(pr >> 16); }
        CFENCE();
        hp[0] = *(volatile lu32x4*)(hb + (lane >> 3) * 32); hp[1] = *(volatile lu32x4*)(hb + (lane >> 3) * 32 + 16);
      }
      bf16x8 af[14];
#pragma unroll
      for (int i = 0; i < 14; ++i) { af[i] = AFR(hp[i >> 3][(i >> 1) & 3], i); PIN(af[i]); }
      SB();
      f32x4 a = {0.f, 0.f, 0.f, 0.f};
#pragma unroll
      for (int i = 0; i < 14; ++i) a = MFMA(af[i], FRAG(wd[i]), a);
      if (rvalid) rdw[64] = a;
      CFENCE();
      if (pa == 1) misc[C_DN] = 1u;
      else {
        float y;
#pragma unroll 1
        for (unsigned spins = 0; spins < (1u << 22); ++spins) {
          const unsigned f_ = misc[C_DN];
          CFENCE();
          y = (rv[256] + rv[320]) + (rv[384] + rv[448]);
          if (rfl(f_) != 0u) break;
          __builtin_amdgcn_s_sleep(1);
        }
        __hip_atomic_store((gu64*)(ws + OFF_YG) + fg * 1024 + cg * 64 + lane, ((u64)tag << 32) | f2u(y), RLX_AGENT);
      }
    }
    if (pa == 1 && (int)(idx >> 7) == mt3) {
      const float xe = u2f((unsigned)x[(size_t)idx * DM + 64 * nt3 + lane] << 16);
      const unsigned ob = bestk >> 16;
      const float rsel = bfr(sigmoid_f(u2f(((ob & 0x8000u) ? (ob & 0x7FFFu) : (~ob & 0xFFFFu)) << 16)));
      float y = 0.f, gt = 0.f;
      __builtin_amdgcn_s_sleep(30);
#pragma unroll 1
      for (unsigned spins = 0;; ++spins) {
        u64 g[17];
#pragma unroll
        for (int i = 0; i < 16; ++i) g[i] = __hip_atomic_load((gu64*)(ws + OFF_YG) + i * 1024 + 64 * nt3 + lane, RLX_AGENT);
        g[16] = __hip_atomic_load((gu64*)(ws + OFF_GG) + idx, RLX_AGENT);
        bool ok = (unsigned)(g[16] >> 32) == tag;
        gt = u2f((unsigned)g[16]); y = 0.f;
#pragma unroll
        for (int i = 0; i < 16; ++i) { ok = ok && (unsigned)(g[i] >> 32) == tag; y += u2f((unsigned)g[i]); }
        if (all_true(ok) || spins > SPIN_CAP) break;
        __builtin_amdgcn_s_sleep(8);
      }
      red[512 + lane] = bfr(bfr(1.0f - gt) * bfr(xe + bfr(bfr(y) * rsel))) - bfr(bfr(1.0f - gt) * xe);
      CFENCE();
      if (lane == 0) misc[C_Y] = 1u;
    }
    return;
  }

  if (w == 0) misc[lane & 31] = 0u;
  if (w == 1) *(u32x2*)(smem + L_CW + lane * 8) = (u32x2){0u, 0u};
  const int hh = w & 1, wr = w >> 1;
  const int c16 = lane & 15, g = lane >> 4;
  const int fsw = (c16 >> 3) | ((g & 1) << 1);
  const int head = 2 * nt + hh;
  int aoff[2], boff[2];
#pragma unroll
  for (int i = 0; i < 2; ++i) {
    aoff[i] = (wr * 64 + c16) * 128 + (((4 * i + g) ^ (c16 >> 1)) * 16);
    boff[i] = 16384 + (8 * g + (c16 >> 2)) * 128 + (((2 * hh + (i ^ wr)) ^ fsw) * 32) + (c16 & 3) * 8;
  }
  f32x4 acc[3][4][2];
#pragma unroll
  for (int i = 0; i < 24; ++i) { const float s0 = i < 16 ? 1.f : 0.f; acc[i / 8][(i >> 1) & 3][i & 1] = (f32x4){s0, s0, s0, s0}; PIN(acc[i / 8][(i >> 1) & 3][i & 1]); }
  bf16x8 a[2][4], b[2][6];
  s16x4 tlo;
#define A_AT(sl, h, mi) (*(const bf16x8*)(smem + (sl) * SLOT + aoff[h] + (mi) * 2048))
#define B_AT(sl, h, s, n) (smem + (sl) * SLOT + boff[n] + (s) * 8192 + (h) * 4096)
#define MM(s, mi, n, A, B) do { if ((s) == 0) AMFMA(acc[0][mi][n], B, A); else AMFMA(acc[s][mi][n], A, B); } while (0)
#define RD(sl, h, u)                                                                                       \
  do {                                                                                                     \
    if ((u) == 0) a[h][0] = A_AT(sl, h, 0);                                                                \
    else if ((u) >= 13) a[h][(u) - 12] = A_AT(sl, h, (u) - 12);                                            \
    else {                                                                                                 \
      const int j_ = (((u) - 1) / 2 + 2) % 6;                                                              \
      if ((u) & 1) tlo = TRD(B_AT(sl, h, j_ >> 1, j_ & 1));                                                \
      else b[h][j_] = CAT(tlo, TRD(B_AT(sl, h, j_ >> 1, j_ & 1) + 512));                                   \
    }                                                                                                      \
  } while (0)
#define O_M(n) ((((n) / 4) & 1) ? 3 - (n) % 4 : (n) % 4)
#define O_J(n) ((n) / 4)
#define STEP(h, rs)                                                                                        \
  _Pragma("unroll") for (int n_ = 0; n_ < 24; ++n_) {                                                      \
    const int jj_ = (O_J(n_) + 2) % 6, mi_ = O_M(n_);                                                      \
    MM(jj_ >> 1, mi_, jj_ & 1, a[h][mi_], b[h][jj_]); SB();                                                \
    if (n_ < 16) { RD(rs, 1 - (h), n_); }                                                                 \
    SB();                                                                                                  \
  }
#define LGKM0() __builtin_amdgcn_s_waitcnt(0xC07F)
#define STAGE(sl, nx) do { STEP(0, sl); LGKM0(); BAR(); STEP(1, nx); LGKM0(); SB(); } while (0)

  __amdgpu_buffer_rsrc_t wsr = MKRSRC(ws, OFF_OF + SEQ * DM * 2);
  asm volatile("" : "+s"(wsr));
  const unsigned rec = (unsigned)((head * 2 + wr) * 16 + mt);
  unsigned x1w = L_X1 + w * 2048 + lane * 16, cw = L_CW + w * 128 + g * 16, one = 1u;
  unsigned kvo = (unsigned)OFF_KVP + rec * 1024 + lane * 16;
  unsigned zo = (c16 == 0 || lane == 1) ? (unsigned)OFF_ZP + rec * 128 + g * 16 + c16 * 64 : 0x80000000u;
  PIN(x1w); PIN(cw); PIN(one); PIN(kvo); PIN(zo);
  WAIT_LGKM0();
  BAR();
#pragma unroll
  for (int u = 0; u < 16; ++u) RD(0, 0, u);
  LGKM0();
  SB();
#pragma unroll 1
  for (int it = 0; it < 4; ++it) { STAGE(0, 1); STAGE(1, 2); STAGE(2, 0); }
  STAGE(0, 1);
  STEP(0, 1); LGKM0(); BAR();
  bf16x8 ta0_0, ta0_1, ta0_2, ta0_3, ta1_0, ta1_1, ta1_2, ta1_3, ta2_0, ta2_1, ta2_2, ta2_3;
  bf16x8 ta3_0, ta3_1, ta3_2, ta3_3, tb0_0_0, tb0_0_1, tb0_1_0, tb0_1_1, tb0_2_0, tb0_2_1, tb0_3_0, tb0_3_1;
  bf16x8 tb1_0_0, tb1_0_1, tb1_1_0, tb1_1_1, tb1_2_0, tb1_2_1, tb1_3_0, tb1_3_1, tb2_0_0, tb2_0_1, tb2_1_0, tb2_1_1;
  bf16x8 tb2_2_0, tb2_2_1, tb2_3_0, tb2_3_1;
  s16x4 tb0_0_0_l, tb0_0_1_l, tb0_1_0_l, tb0_1_1_l, tb0_2_0_l, tb0_2_1_l, tb0_3_0_l, tb0_3_1_l, tb1_0_0_l, tb1_0_1_l, tb1_1_0_l, tb1_1_1_l;
  s16x4 tb1_2_0_l, tb1_2_1_l, tb1_3_0_l, tb1_3_1_l, tb2_0_0_l, tb2_0_1_l, tb2_1_0_l, tb2_1_1_l, tb2_2_0_l, tb2_2_1_l, tb2_3_0_l, tb2_3_1_l;
  unsigned kp[16], vp[16], x1r, cr;
  u32x2 lf15;
  float kmin = 0.f, qmin = 0.f;
  u32x4 ones;
  float ek[4];
#define PHI(src, dst, mn, k, st)                                                                           \
  do {                                                                                                     \
    float& e_ = ek[2 * (k) + ((st) & 1)];                                                                  \
    if ((st) < 2) e_ = __builtin_fmaf((src)[2 * (k) + ((st) & 1)], 1.44269504088896341f, -1.44269504088896341f); \
    else if ((st) < 4) e_ = __builtin_amdgcn_fmed3f(__builtin_amdgcn_exp2f(e_), 0.f, 1.f);                 \
    else if ((st) < 6) e_ = fmax_raw((src)[2 * (k) + ((st) & 1)], e_);                                     \
    else if ((st) == 6) dst = cvt_pk(ek[2 * (k)], ek[2 * (k) + 1]);                                        \
    else mn = __builtin_fminf(__builtin_fminf(mn, (src)[2 * (k)]), (src)[2 * (k) + 1]);                     \
    SB();                                                                                                  \
  } while (0)
#define PK(mi, n, k, st) PHI(acc[1][mi][n], kp[8 * (n) + 2 * (mi) + (k)], kmin, k, st)
#define CV(mi, n, k) do { vp[8 * (n) + 2 * (mi) + (k)] = cvt_pk(acc[2][mi][n][2 * (k)], acc[2][mi][n][2 * (k) + 1]); PIN(vp[8 * (n) + 2 * (mi) + (k)]); SB(); } while (0)
#define F15_RD lf15 = *(volatile lu32x2*)(smem + L_CTL + 4 * C_F15)
#define F15_CHK do { if (__builtin_expect(!all_true((lf15[0] & lf15[1]) != 0u), 0)) { (void)wait_ge<1>(misc + C_F15, 1u); (void)wait_ge<1>(misc + C_F15 + 1, 1u); } CFENCE(); } while (0)
#define PRE(i) do { if ((i) == 30) { x1r = x1w ^ 4096u; PIN(x1r); } if ((i) == 31) { cr = cw ^ 256u; PIN(cr); } \
                    if ((i) == 32) { ones = (u32x4){0x3F803F80u, 0x3F803F80u, 0x3F803F80u, 0x3F803F80u}; PIN(ones); } } while (0)
  MM(1, 0, 0, a[1][0], b[1][2]); SB(); tb1_0_1_l = TRD(B_AT(2, 0, 1, 1)); PRE(0); SB();
  MM(1, 0, 1, a[1][0], b[1][3]); SB(); tb1_0_1 = CAT(tb1_0_1_l, TRD(B_AT(2, 0, 1, 1) + 512)); PRE(1); SB();
  MM(2, 0, 0, a[1][0], b[1][4]); SB(); ta0_0 = A_AT(2, 0, 0); PRE(2); SB();
  MM(2, 0, 1, a[1][0], b[1][5]); SB(); ta0_1 = A_AT(2, 0, 1); PRE(3); SB();
  MM(0, 0, 0, a[1][0], b[1][0]); SB(); ta0_2 = A_AT(2, 0, 2); PRE(4); SB();
  MM(0, 0, 1, a[1][0], b[1][1]); SB(); ta0_3 = A_AT(2, 0, 3); PRE(5); SB();
  MM(1, 1, 0, a[1][1], b[1][2]); SB(); tb1_0_0_l = TRD(B_AT(2, 0, 1, 0)); PRE(6); SB();
  MM(1, 1, 1, a[1][1], b[1][3]); SB(); tb1_0_0 = CAT(tb1_0_0_l, TRD(B_AT(2, 0, 1, 0) + 512)); PRE(7); SB();
  MM(2, 1, 0, a[1][1], b[1][4]); SB(); tb1_1_1_l = TRD(B_AT(2, 1, 1, 1)); PRE(8); SB();
  MM(2, 1, 1, a[1][1], b[1][5]); SB(); tb1_1_1 = CAT(tb1_1_1_l, TRD(B_AT(2, 1, 1, 1) + 512)); PRE(9); SB();
  MM(0, 1, 0, a[1][1], b[1][0]); SB(); ta1_0 = A_AT(2, 1, 0); PRE(10); SB();
  MM(0, 1, 1, a[1][1], b[1][1]); SB(); ta1_1 = A_AT(2, 1, 1); PRE(11); SB();
  MM(1, 2, 0, a[1][2], b[1][2]); SB(); ta1_2 = A_AT(2, 1, 2); PRE(12); SB();
  MM(1, 2, 1, a[1][2], b[1][3]); SB(); ta1_3 = A_AT(2, 1, 3); PRE(13); SB();
  MM(2, 2, 0, a[1][2], b[1][4]); SB(); tb1_1_0_l = TRD(B_AT(2, 1, 1, 0)); PRE(14); SB();
  MM(2, 2, 1, a[1][2], b[1][5]); SB(); tb1_1_0 = CAT(tb1_1_0_l, TRD(B_AT(2, 1, 1, 0) + 512)); PRE(15); SB();
  MM(0, 2, 0, a[1][2], b[1][0]); SB(); PRE(16); SB();
  MM(0, 2, 1, a[1][2], b[1][1]); SB(); PRE(17); SB();
  MM(1, 3, 0, a[1][3], b[1][2]); SB(); PRE(18); SB();
  MM(1, 3, 1, a[1][3], b[1][3]); SB(); PRE(19); SB();
  MM(2, 3, 0, a[1][3], b[1][4]); SB(); F15_RD; PRE(20); SB();
  MM(2, 3, 1, a[1][3], b[1][5]); SB(); PRE(21); SB();
  MM(0, 3, 0, a[1][3], b[1][0]); SB(); tb2_0_1_l = TRD(B_AT(2, 0, 2, 1)); PRE(22); SB();
  MM(0, 3, 1, a[1][3], b[1][1]); SB(); tb2_0_1 = CAT(tb2_0_1_l, TRD(B_AT(2, 0, 2, 1) + 512)); PRE(23); SB();
  MM(1, 0, 1, ta0_0, tb1_0_1); SB(); PRE(24); SB();
  MM(1, 1, 1, ta0_1, tb1_0_1); SB(); PRE(25); SB();
  MM(1, 2, 1, ta0_2, tb1_0_1); SB(); F15_CHK; tb2_1_1_l = TRD(B_AT(2, 1, 2, 1)); PRE(26); SB();
  MM(1, 3, 1, ta0_3, tb1_0_1); SB(); tb2_1_1 = CAT(tb2_1_1_l, TRD(B_AT(2, 1, 2, 1) + 512)); PRE(27); SB();
  MM(1, 0, 0, ta0_0, tb1_0_0); SB(); tb1_2_1_l = TRD(B_AT(0, 0, 1, 1)); PRE(28); SB();
  MM(1, 1, 0, ta0_1, tb1_0_0); SB(); tb1_2_1 = CAT(tb1_2_1_l, TRD(B_AT(0, 0, 1, 1) + 512)); PRE(29); SB();
  MM(1, 2, 0, ta0_2, tb1_0_0); SB(); ta2_0 = A_AT(0, 0, 0); PRE(30); SB();
  MM(1, 3, 0, ta0_3, tb1_0_0); SB(); ta2_1 = A_AT(0, 0, 1); PRE(31); SB();
  MM(1, 0, 1, ta1_0, tb1_1_1); SB(); ta2_2 = A_AT(0, 0, 2); PRE(32); SB();
  MM(1, 1, 1, ta1_1, tb1_1_1); SB(); ta2_3 = A_AT(0, 0, 3); PRE(33); SB();
  MM(1, 2, 1, ta1_2, tb1_1_1); SB(); tb1_3_1_l = TRD(B_AT(0, 1, 1, 1)); PRE(34); SB();
  MM(1, 3, 1, ta1_3, tb1_1_1); SB(); tb1_3_1 = CAT(tb1_3_1_l, TRD(B_AT(0, 1, 1, 1) + 512)); PRE(35); SB();
  MM(1, 0, 0, ta1_0, tb1_1_0); SB(); ta3_0 = A_AT(0, 1, 0); PRE(36); SB();
  MM(1, 1, 0, ta1_1, tb1_1_0); SB(); ta3_1 = A_AT(0, 1, 1); PRE(37); SB();
  MM(1, 2, 0, ta1_2, tb1_1_0); SB(); ta3_2 = A_AT(0, 1, 2); PRE(38); SB();
  MM(1, 3, 0, ta1_3, tb1_1_0); SB(); ta3_3 = A_AT(0, 1, 3); PRE(39); SB();
  MM(1, 0, 1, ta2_0, tb1_2_1); SB(); tb1_2_0_l = TRD(B_AT(0, 0, 1, 0)); PRE(40); SB();
  MM(1, 1, 1, ta2_1, tb1_2_1); SB(); tb1_2_0 = CAT(tb1_2_0_l, TRD(B_AT(0, 0, 1, 0) + 512)); PRE(41); SB();
  MM(1, 2, 1, ta2_2, tb1_2_1); SB(); tb1_3_0_l = TRD(B_AT(0, 1, 1, 0)); PRE(42); SB();
  MM(1, 3, 1, ta2_3, tb1_2_1); SB(); tb1_3_0 = CAT(tb1_3_0_l, TRD(B_AT(0, 1, 1, 0) + 512)); PRE(43); SB();
  MM(1, 0, 1, ta3_0, tb1_3_1); SB(); tb2_2_1_l = TRD(B_AT(0, 0, 2, 1)); PRE(44); SB();
  MM(1, 1, 1, ta3_1, tb1_3_1); SB(); tb2_2_1 = CAT(tb2_2_1_l, TRD(B_AT(0, 0, 2, 1) + 512)); PRE(45); SB();
  MM(1, 2, 1, ta3_2, tb1_3_1); SB(); tb2_3_1_l = TRD(B_AT(0, 1, 2, 1)); PRE(46); SB();
  MM(1, 3, 1, ta3_3, tb1_3_1); SB(); tb2_3_1 = CAT(tb2_3_1_l, TRD(B_AT(0, 1, 2, 1) + 512)); PRE(47); SB();
  MM(1, 0, 0, ta2_0, tb1_2_0); SB(); tb2_0_0_l = TRD(B_AT(2, 0, 2, 0)); PK(0, 1, 0, 0); PK(0, 1, 0, 1); PK(0, 1, 1, 0); PK(0, 1, 1, 1); PK(0, 1, 0, 2); SB();
  MM(1, 1, 0, ta2_1, tb1_2_0); SB(); tb2_0_0 = CAT(tb2_0_0_l, TRD(B_AT(2, 0, 2, 0) + 512)); PK(0, 1, 0, 3); PK(0, 1, 0, 7); PK(0, 1, 1, 2); SB();
  MM(1, 2, 0, ta2_2, tb1_2_0); SB(); tb2_1_0_l = TRD(B_AT(2, 1, 2, 0)); PK(0, 1, 1, 3); PK(0, 1, 1, 7); PK(0, 1, 0, 4); PK(0, 1, 0, 5); SB();
  MM(1, 3, 0, ta2_3, tb1_2_0); SB(); tb2_1_0 = CAT(tb2_1_0_l, TRD(B_AT(2, 1, 2, 0) + 512)); PK(0, 1, 1, 4); PK(0, 1, 1, 5); PK(0, 1, 0, 6); PK(0, 1, 1, 6); PK(1, 1, 0, 0); SB();
  MM(1, 0, 0, ta3_0, tb1_3_0); SB(); tb2_2_0_l = TRD(B_AT(0, 0, 2, 0)); PK(1, 1, 0, 1); PK(1, 1, 1, 0); PK(1, 1, 1, 1); PK(1, 1, 0, 2); SB();
  MM(1, 1, 0, ta3_1, tb1_3_0); SB(); tb2_2_0 = CAT(tb2_2_0_l, TRD(B_AT(0, 0, 2, 0) + 512)); PK(1, 1, 0, 3); PK(1, 1, 0, 7); PK(1, 1, 1, 2); SB();
  MM(1, 2, 0, ta3_2, tb1_3_0); SB(); tb2_3_0_l = TRD(B_AT(0, 1, 2, 0)); PK(1, 1, 1, 3); PK(1, 1, 1, 7); PK(1, 1, 0, 4); PK(1, 1, 0, 5); SB();
  MM(1, 3, 0, ta3_3, tb1_3_0); SB(); tb2_3_0 = CAT(tb2_3_0_l, TRD(B_AT(0, 1, 2, 0) + 512)); PK(1, 1, 1, 4); PK(1, 1, 1, 5); PK(1, 1, 0, 6); PK(1, 1, 1, 6); SB();
  MM(2, 0, 1, ta0_0, tb2_0_1); SB(); PK(2, 1, 0, 0); PK(2, 1, 0, 1); PK(2, 1, 1, 0); PK(2, 1, 1, 1); SB();
  MM(2, 1, 1, ta0_1, tb2_0_1); SB(); PK(2, 1, 0, 2); PK(2, 1, 0, 3); SB();
  MM(2, 2, 1, ta0_2, tb2_0_1); SB(); PK(2, 1, 0, 7); PK(2, 1, 1, 2); PK(2, 1, 1, 3); SB();
  MM(2, 3, 1, ta0_3, tb2_0_1); SB(); PK(2, 1, 1, 7); PK(2, 1, 0, 4); PK(2, 1, 0, 5); PK(2, 1, 1, 4); SB();
  MM(2, 0, 1, ta1_0, tb2_1_1); SB(); PK(2, 1, 1, 5); PK(2, 1, 0, 6); PK(2, 1, 1, 6); PK(3, 1, 0, 0); SB();
  MM(2, 1, 1, ta1_1, tb2_1_1); SB(); PK(3, 1, 0, 1); PK(3, 1, 1, 0); PK(3, 1, 1, 1); PK(3, 1, 0, 2); SB();
  MM(2, 2, 1, ta1_2, tb2_1_1); SB(); PK(3, 1, 0, 3); PK(3, 1, 0, 7); PK(3, 1, 1, 2); SB();
  MM(2, 3, 1, ta1_3, tb2_1_1); SB(); PK(3, 1, 1, 3); PK(3, 1, 1, 7); PK(3, 1, 0, 4); SB();
  MM(2, 0, 1, ta2_0, tb2_2_1); SB(); PK(3, 1, 0, 5); PK(3, 1, 1, 4); PK(3, 1, 1, 5); PK(3, 1, 0, 6); SB();
  MM(2, 1, 1, ta2_1, tb2_2_1); SB(); PK(3, 1, 1, 6); PK(0, 0, 0, 0); PK(0, 0, 0, 1); PK(0, 0, 1, 0); SB();
  MM(2, 2, 1, ta2_2, tb2_2_1); SB(); PK(0, 0, 1, 1); PK(0, 0, 0, 2); PK(0, 0, 0, 3); SB();
  MM(2, 3, 1, ta2_3, tb2_2_1); SB(); PK(0, 0, 0, 7); PK(0, 0, 1, 2); PK(0, 0, 1, 3); SB();
  MM(2, 0, 1, ta3_0, tb2_3_1); SB(); PK(0, 0, 1, 7); PK(0, 0, 0, 4); PK(0, 0, 0, 5); PK(0, 0, 1, 4); SB();
  MM(2, 1, 1, ta3_1, tb2_3_1); SB(); PK(0, 0, 1, 5); PK(0, 0, 0, 6); PK(0, 0, 1, 6); PK(1, 0, 0, 0); SB();
  MM(2, 2, 1, ta3_2, tb2_3_1); SB(); PK(1, 0, 0, 1); PK(1, 0, 1, 0); PK(1, 0, 1, 1); PK(1, 0, 0, 2); SB();
  MM(2, 3, 1, ta3_3, tb2_3_1); SB(); PK(1, 0, 0, 3); PK(1, 0, 0, 7); PK(1, 0, 1, 2); SB();
  MM(2, 0, 0, ta0_0, tb2_0_0); SB(); PK(1, 0, 1, 3); PK(1, 0, 1, 7); PK(1, 0, 0, 4); SB();
  MM(2, 1, 0, ta0_1, tb2_0_0); SB(); PK(1, 0, 0, 5); PK(1, 0, 1, 4); PK(1, 0, 1, 5); PK(1, 0, 0, 6); SB();
  MM(2, 2, 0, ta0_2, tb2_0_0); SB(); PK(1, 0, 1, 6); PK(2, 0, 0, 0); PK(2, 0, 0, 1); PK(2, 0, 1, 0); SB();
  MM(2, 3, 0, ta0_3, tb2_0_0); SB(); PK(2, 0, 1, 1); PK(2, 0, 0, 2); PK(2, 0, 0, 3); SB();
  MM(2, 0, 0, ta1_0, tb2_1_0); SB(); PK(2, 0, 0, 7); PK(2, 0, 1, 2); PK(2, 0, 1, 3); SB();
  MM(2, 1, 0, ta1_1, tb2_1_0); SB(); PK(2, 0, 1, 7); PK(2, 0, 0, 4); PK(2, 0, 0, 5); PK(2, 0, 1, 4); SB();
  MM(2, 2, 0, ta1_2, tb2_1_0); SB(); PK(2, 0, 1, 5); PK(2, 0, 0, 6); PK(2, 0, 1, 6); PK(3, 0, 0, 0); SB();
  MM(2, 3, 0, ta1_3, tb2_1_0); SB(); PK(3, 0, 0, 1); PK(3, 0, 1, 0); PK(3, 0, 1, 1); SB();
  MM(2, 0, 0, ta2_0, tb2_2_0); SB(); PK(3, 0, 0, 2); PK(3, 0, 0, 3); SB();
  MM(2, 1, 0, ta2_1, tb2_2_0); SB(); PK(3, 0, 0, 7); PK(3, 0, 1, 2); SB();
  MM(2, 2, 0, ta2_2, tb2_2_0); SB(); PK(3, 0, 1, 3); PK(3, 0, 1, 7); SB();
  MM(2, 3, 0, ta2_3, tb2_2_0); SB(); PK(3, 0, 0, 4); PK(3, 0, 0, 5); PK(3, 0, 1, 4); SB();
  MM(2, 0, 0, ta3_0, tb2_3_0); SB(); PK(3, 0, 1, 5); PK(3, 0, 0, 6); PK(3, 0, 1, 6); SB();
  MM(2, 1, 0, ta3_1, tb2_3_0); SB(); CV(0, 1, 0); CV(0, 1, 1); CV(1, 1, 0); SB();
  MM(2, 2, 0, ta3_2, tb2_3_0); SB(); CV(1, 1, 1); CV(2, 1, 0); CV(2, 1, 1); CV(3, 1, 0); SB();
  MM(2, 3, 0, ta3_3, tb2_3_0); SB(); CV(3, 1, 1); SB();
  const f32x4 zero = {0.f, 0.f, 0.f, 0.f};
#define TAKE(flag, READS)                                                                                  \
  _Pragma("unroll 1") for (unsigned spins = 0; spins < (1u << 22); ++spins) {                              \
    const unsigned f_ = (flag);                                                                            \
    CFENCE();                                                                                              \
    READS;                                                                                                 \
    if (__builtin_expect(rfl(f_) != 0u, 1)) break;                                                         \
  }
  unsigned lidx, ltag;
  {
    phi_fix(kp, kmin);
#define KF(j, n) FRAG(((u32x4){kp[8 * (n) + 4 * (j)], kp[8 * (n) + 4 * (j) + 1], kp[8 * (n) + 4 * (j) + 2], kp[8 * (n) + 4 * (j) + 3]}))
#define VF(j, n) FRAG(((u32x4){vp[8 * (n) + 4 * (j)], vp[8 * (n) + 4 * (j) + 1], vp[8 * (n) + 4 * (j) + 2], vp[8 * (n) + 4 * (j) + 3]}))
    f32x4 s1 = MFMA(KF(0, 1), VF(0, 1), zero), s0 = MFMA(KF(0, 0), VF(0, 1), zero), zs = MFMA(KF(0, 1), FRAG(ones), zero);
    s1 = MFMA(KF(1, 1), VF(1, 1), s1); s0 = MFMA(KF(1, 0), VF(1, 1), s0); zs = MFMA(KF(1, 1), FRAG(ones), zs);
    f32x4 z = MFMA(KF(1, 0), FRAG(ones), MFMA(KF(0, 0), FRAG(ones), zero));
    SB();
    CV(0, 0, 0); CV(0, 0, 1); CV(1, 0, 0); CV(1, 0, 1);
    *LP(f32x4, x1w, 0) = s1; *LP(f32x4, x1w, 1024) = s0; *LP(f32x4, cw, 0) = zs;
    CFENCE();
    *LP(volatile unsigned, cw, W_X1) = one;
    SB();
    CV(2, 0, 0); CV(2, 0, 1); CV(3, 0, 0); CV(3, 0, 1);
    f32x4 kv0 = MFMA(KF(0, 0), VF(0, 0), zero), kv1 = MFMA(KF(0, 1), VF(0, 0), zero);
    kv0 = MFMA(KF(1, 0), VF(1, 0), kv0); kv1 = MFMA(KF(1, 1), VF(1, 0), kv1);
    SB();
    f32x4 p0, p1, pz;
#define X1_RD { p0 = *LP(volatile f32x4, x1r, 0); p1 = *LP(volatile f32x4, x1r, 1024); pz = *LP(volatile f32x4, cr, 0); }
    const unsigned f1 = *LP(volatile unsigned, cr, W_X1);
    CFENCE();
    X1_RD
    SB();
    tb0_0_0_l = TRD(B_AT(2, 0, 0, 0)); tb0_0_0 = CAT(tb0_0_0_l, TRD(B_AT(2, 0, 0, 0) + 512));
    tb0_0_1_l = TRD(B_AT(2, 0, 0, 1)); tb0_0_1 = CAT(tb0_0_1_l, TRD(B_AT(2, 0, 0, 1) + 512));
    tb0_1_0_l = TRD(B_AT(2, 1, 0, 0)); tb0_1_0 = CAT(tb0_1_0_l, TRD(B_AT(2, 1, 0, 0) + 512));
    tb0_1_1_l = TRD(B_AT(2, 1, 0, 1)); tb0_1_1 = CAT(tb0_1_1_l, TRD(B_AT(2, 1, 0, 1) + 512));
    tb0_2_0_l = TRD(B_AT(0, 0, 0, 0)); tb0_2_0 = CAT(tb0_2_0_l, TRD(B_AT(0, 0, 0, 0) + 512));
    tb0_2_1_l = TRD(B_AT(0, 0, 0, 1)); tb0_2_1 = CAT(tb0_2_1_l, TRD(B_AT(0, 0, 0, 1) + 512));
    tb0_3_0_l = TRD(B_AT(0, 1, 0, 0)); tb0_3_0 = CAT(tb0_3_0_l, TRD(B_AT(0, 1, 0, 0) + 512));
    tb0_3_1_l = TRD(B_AT(0, 1, 0, 1)); tb0_3_1 = CAT(tb0_3_1_l, TRD(B_AT(0, 1, 0, 1) + 512));
    SB();
    if (__builtin_expect(!all_true(f1 != 0u), 0)) { TAKE(*LP(volatile unsigned, cr, W_X1), X1_RD); LGKM0(); }
    const f32x4 m0_ = kv0 + p0, m1_ = kv1 + p1;
    float am = fmax_raw(fabsf(m0_[0]), fabsf(m1_[0]));
#pragma unroll
    for (int e = 1; e < 4; ++e) am = fmax_raw(fmax_raw(am, fabsf(m0_[e])), fabsf(m1_[e]));
#define PKH(a_, b_) __builtin_bit_cast(unsigned, __builtin_convertvector((f32x2){(a_), (b_)}, f16x2_t))
    u32x4 hv = {PKH(m0_[0], m0_[1]), PKH(m0_[2], m0_[3]), PKH(m1_[0], m1_[1]), PKH(m1_[2], m1_[3])};
#define Z_STORE(scl) do { unsigned z0_ = zv[0]; asm("v_writelane_b32 %0, %1, 1" : "+v"(z0_) : "s"(scl)); zv[0] = z0_; __builtin_amdgcn_raw_buffer_store_b128(zv, wsr, zo, 0, 16); } while (0)
    PIN(hv);
    if (__builtin_expect(all_true(f2u(am) - 0x3C800000u <= 0x47000000u - 0x3C800000u), 1)) {
      __builtin_amdgcn_raw_buffer_store_b128(hv, wsr, kvo, 0, 16);
      SB();
      u32x4 zv = __builtin_bit_cast(u32x4, z + pz);
      Z_STORE(0x3F800000u);
    } else {
      unsigned ex = max(wave_max(f2u(am) & 0x7FFFFFFFu) >> 23, 15u);
      if (ex >= 121u && ex < 142u) ex = 141u;
      const float inv = u2f((268u - ex) << 23);
      u32x4 hs = {PKH(m0_[0] * inv, m0_[1] * inv), PKH(m0_[2] * inv, m0_[3] * inv), PKH(m1_[0] * inv, m1_[1] * inv), PKH(m1_[2] * inv, m1_[3] * inv)};
      u32x4 zv = __builtin_bit_cast(u32x4, z + pz);
#pragma unroll
      for (int e = 0; e < 4; ++e) { hs[e] = min(hs[e], ~1u); zv[e] = min(zv[e], ~1u); }
      CFENCE();
      __builtin_amdgcn_raw_buffer_store_b128(hs, wsr, kvo, 0, 16);
      Z_STORE((ex - 14u) << 23);
      CFENCE();
    }
  }
  *LP(volatile unsigned, cw, W_KS) = one;
  unsigned ko, oo, x2w, zfa, zfb, zfp, gta;
  u32x2 eye, ov[4][2];
  u32x4 t[16];
  float gq[4];
  unsigned pq[16];
#define PQ(mi, n, k, st) PHI(acc[0][mi][n], pq[4 * (mi) + 2 * (n) + (k)], qmin, k, st)
#define NOPQ() asm volatile("s_nop 4" : "+v"(acc[0][3][0]), "+v"(acc[0][3][1]))
#define PRQ(i)                                                                                             \
  do {                                                                                                     \
    if ((i) == 0) { lidx = misc[C_IDX + wr]; ltag = misc[C_TAG]; }                                         \
    if ((i) == 2) { zfp = L_CTL + 4 * (C_ZF + hh); PIN(zfp); }                                             \
    if ((i) == 3) { ko = kvo - mt * 1024; PIN(ko); }                                                       \
    if ((i) == 4) { gta = L_GATE + wr * 256 + c16 * 4; PIN(gta); }                                         \
    if ((i) == 5) { zfa = L_ZF + hh * 64 + wr * 8 + g * 16; PIN(zfa); }                                    \
    if ((i) == 6) { zfb = zfa ^ 8u; PIN(zfb); }                                                            \
    if ((i) == 7) { const unsigned o_ = 0x3C00u << (16 * (lane & 1)); eye = (u32x2){(lane & 2) ? 0u : o_, (lane & 2) ? o_ : 0u}; PIN(eye); } \
    if ((i) == 8) { x2w = L_X2 + w * 1024 + lane * 16; PIN(x2w); }                                         \
    if ((i) == 9) { oo = (unsigned)OFF_OF + (unsigned)(((mt * 8 + wr * 4) * NH + head) * 1024) + lane * 16; PIN(oo); } \
  } while (0)
#define SHOT_GATE                                                                                          \
  do {                                                                                                     \
    if (__builtin_expect(!all_true(lidx != 0u), 0)) (void)wait_ge<1>(misc + C_IDX + wr, 1u);        \
    (void)wait_ge<1>(misc + C_GO + hh, 1u);                                                                \
    CFENCE();                                                                                              \
    _Pragma("unroll") for (int i = 0; i < 4; ++i) gq[i] = *LP(volatile float, gta, i * 64);                \
  } while (0)
#define T_LD(i) t[i] = __builtin_amdgcn_raw_buffer_load_b128(wsr, ko + ((i) & 3) * 1024, ((i) >> 2) * 4096, 16)
  SB();
  MM(0, 0, 0, ta0_0, tb0_0_0); SB(); PRQ(0); SB();
  MM(0, 0, 1, ta0_0, tb0_0_1); SB(); PRQ(1); SB();
  MM(0, 0, 0, ta1_0, tb0_1_0); SB(); PRQ(2); SB();
  MM(0, 0, 1, ta1_0, tb0_1_1); SB(); PRQ(3); SB();
  MM(0, 0, 0, ta2_0, tb0_2_0); SB(); PRQ(4); SB();
  MM(0, 0, 1, ta2_0, tb0_2_1); SB(); PRQ(5); SB();
  MM(0, 0, 0, ta3_0, tb0_3_0); SB(); PRQ(6); SB();
  MM(0, 0, 1, ta3_0, tb0_3_1); SB(); PRQ(7); SB();
  MM(0, 1, 0, ta0_1, tb0_0_0); SB(); PRQ(8); SB();
  MM(0, 1, 1, ta0_1, tb0_0_1); SB(); PRQ(9); SB();
  MM(0, 1, 0, ta1_1, tb0_1_0); SB(); PQ(0, 0, 0, 0); PQ(0, 0, 0, 1); PQ(0, 0, 1, 0); SB();
  MM(0, 1, 1, ta1_1, tb0_1_1); SB(); PQ(0, 0, 1, 1); PQ(0, 0, 0, 2); SB();
  MM(0, 1, 0, ta2_1, tb0_2_0); SB(); PQ(0, 0, 0, 3); PQ(0, 0, 0, 7); SB();
  MM(0, 1, 1, ta2_1, tb0_2_1); SB(); PQ(0, 0, 1, 2); PQ(0, 0, 1, 3); SB();
  MM(0, 1, 0, ta3_1, tb0_3_0); SB(); PQ(0, 0, 1, 7); PQ(0, 0, 0, 4); PQ(0, 0, 0, 5); SB();
  MM(0, 1, 1, ta3_1, tb0_3_1); SB(); PQ(0, 0, 1, 4); PQ(0, 0, 1, 5); PQ(0, 0, 0, 6); SB();
  MM(0, 2, 0, ta0_2, tb0_0_0); SB(); PQ(0, 0, 1, 6); PQ(0, 1, 0, 0); PQ(0, 1, 0, 1); SB();
  MM(0, 2, 1, ta0_2, tb0_0_1); SB(); PQ(0, 1, 1, 0); PQ(0, 1, 1, 1); PQ(0, 1, 0, 2); SB();
  MM(0, 2, 0, ta1_2, tb0_1_0); SB(); PQ(0, 1, 0, 3); PQ(0, 1, 0, 7); SB();
  MM(0, 2, 1, ta1_2, tb0_1_1); SB(); PQ(0, 1, 1, 2); PQ(0, 1, 1, 3); SB();
  MM(0, 2, 0, ta2_2, tb0_2_0); SB(); PQ(0, 1, 1, 7); PQ(0, 1, 0, 4); PQ(0, 1, 0, 5); SB();
  MM(0, 2, 1, ta2_2, tb0_2_1); SB(); PQ(0, 1, 1, 4); PQ(0, 1, 1, 5); PQ(0, 1, 0, 6); SB();
  MM(0, 2, 0, ta3_2, tb0_3_0); SB(); PQ(0, 1, 1, 6); PQ(1, 0, 0, 0); PQ(1, 0, 0, 1); SB();
  MM(0, 2, 1, ta3_2, tb0_3_1); SB(); PQ(1, 0, 1, 0); PQ(1, 0, 1, 1); PQ(1, 0, 0, 2); SB();
  MM(0, 3, 0, ta0_3, tb0_0_0); SB(); PQ(1, 0, 0, 3); PQ(1, 0, 0, 7); SB();
  MM(0, 3, 1, ta0_3, tb0_0_1); SB(); PQ(1, 0, 1, 2); PQ(1, 0, 1, 3); SB();
  MM(0, 3, 0, ta1_3, tb0_1_0); SB(); PQ(1, 0, 1, 7); PQ(1, 0, 0, 4); PQ(1, 0, 0, 5); SB();
  MM(0, 3, 1, ta1_3, tb0_1_1); SB(); PQ(1, 0, 1, 4); PQ(1, 0, 1, 5); PQ(1, 0, 0, 6); SB();
  MM(0, 3, 0, ta2_3, tb0_2_0); SB(); PQ(1, 0, 1, 6); PQ(1, 1, 0, 0); PQ(1, 1, 0, 1); SB();
  MM(0, 3, 1, ta2_3, tb0_2_1); SB(); PQ(1, 1, 1, 0); PQ(1, 1, 1, 1); PQ(1, 1, 0, 2); SB();
  MM(0, 3, 0, ta3_3, tb0_3_0); SB(); PQ(1, 1, 0, 3); PQ(1, 1, 0, 7); SB();
  MM(0, 3, 1, ta3_3, tb0_3_1); SB(); PQ(1, 1, 1, 2); PQ(1, 1, 1, 3); SB();
  PQ(1, 1, 1, 7); SB();
  PQ(1, 1, 0, 4); SB();
  PQ(1, 1, 0, 5); SB();
  PQ(1, 1, 1, 4); SB();
  PQ(1, 1, 1, 5); SB();
  PQ(1, 1, 0, 6); SB();
  PQ(1, 1, 1, 6); SB();
  PQ(2, 0, 0, 0); SB();
  PQ(2, 0, 0, 1); SB();
  PQ(2, 0, 1, 0); SB();
  PQ(2, 0, 1, 1); SB();
  PQ(2, 0, 0, 2); SB();
  PQ(2, 0, 0, 3); SB();
  PQ(2, 0, 0, 7); SB();
  PQ(2, 0, 1, 2); SB();
  PQ(2, 0, 1, 3); SB();
  PQ(2, 0, 1, 7); SB();
  PQ(2, 0, 0, 4); SB();
  PQ(2, 0, 0, 5); SB();
  PQ(2, 0, 1, 4); SB();
  PQ(2, 0, 1, 5); SB();
  PQ(2, 0, 0, 6); SB();
  PQ(2, 0, 1, 6); SB();
  PQ(2, 1, 0, 0); SB();
  PQ(2, 1, 0, 1); SB();
  PQ(2, 1, 1, 0); SB();
  PQ(2, 1, 1, 1); SB();
  PQ(2, 1, 0, 2); SB();
  PQ(2, 1, 0, 3); SB();
  PQ(2, 1, 0, 7); SB();
  PQ(2, 1, 1, 2); SB();
  PQ(2, 1, 1, 3); SB();
  PQ(2, 1, 1, 7); SB();
  PQ(2, 1, 0, 4); SB();
  PQ(2, 1, 0, 5); SB();
  PQ(2, 1, 1, 4); SB();
  PQ(2, 1, 1, 5); SB();
  PQ(2, 1, 0, 6); SB();
  PQ(2, 1, 1, 6); SB();
  NOPQ();
  PQ(3, 0, 0, 0); SB();
  PQ(3, 0, 0, 1); SB();
  PQ(3, 0, 1, 0); SB();
  PQ(3, 0, 1, 1); SB();
  PQ(3, 0, 0, 2); SB();
  PQ(3, 0, 0, 3); SB();
  PQ(3, 0, 0, 7); SB();
  PQ(3, 0, 1, 2); SB();
  PQ(3, 0, 1, 3); SB();
  PQ(3, 0, 1, 7); SB();
  PQ(3, 0, 0, 4); SB();
  PQ(3, 0, 0, 5); SB();
  PQ(3, 0, 1, 4); SB();
  PQ(3, 0, 1, 5); SB();
  PQ(3, 0, 0, 6); SB();
  PQ(3, 0, 1, 6); SB();
  PQ(3, 1, 0, 0); SB();
  PQ(3, 1, 0, 1); SB();
  PQ(3, 1, 1, 0); SB();
  PQ(3, 1, 1, 1); SB();
  PQ(3, 1, 0, 2); SB();
  PQ(3, 1, 0, 3); SB();
  PQ(3, 1, 0, 7); SB();
  PQ(3, 1, 1, 2); SB();
  PQ(3, 1, 1, 3); SB();
  PQ(3, 1, 1, 7); SB();
  PQ(3, 1, 0, 4); SB();
  PQ(3, 1, 0, 5); SB();
  PQ(3, 1, 1, 4); SB();
  PQ(3, 1, 1, 5); SB();
  PQ(3, 1, 0, 6); SB();
  PQ(3, 1, 1, 6); SB();
  SHOT_GATE;
  T_LD(0);
  T_LD(1);
  T_LD(2);
  T_LD(3);
  T_LD(4);
  T_LD(5);
  T_LD(6);
  T_LD(7);
  T_LD(8);
  T_LD(9);
  T_LD(10);
  T_LD(11);
  T_LD(12);
  T_LD(13);
  T_LD(14);
  T_LD(15);
  *LP(volatile unsigned, cw, W_AG) = one;
  phi_fix(pq, qmin);
  const unsigned tag = rfl(ltag);
  {
#define PF(mi) FRAG(((u32x4){pq[4 * (mi)], pq[4 * (mi) + 1], pq[4 * (mi) + 2], pq[4 * (mi) + 3]}))
#define H4(v, k) __builtin_bit_cast(f16x4_t, (u32x2){(v)[2 * (k)], (v)[2 * (k) + 1]})
    f32x4 s0 = zero, s1 = zero;
#define KV_MM(i0, i1)                                                                                      \
  _Pragma("unroll") for (int i = (i0); i < (i1); ++i) {                                                    \
    s0 = __builtin_amdgcn_mfma_f32_4x4x4f16(__builtin_bit_cast(f16x4_t, eye), H4(t[i], 0), s0, 0, 0, 0);   \
    s1 = __builtin_amdgcn_mfma_f32_4x4x4f16(__builtin_bit_cast(f16x4_t, eye), H4(t[i], 1), s1, 0, 0, 0);   \
  }
    float rc[4];
    u32x2 z0, z1;
#define Z_RD { z0 = *LP(volatile u32x2, zfa, 0); z1 = *LP(volatile u32x2, zfb, 0); }
    f32x4 d[4];
#define DEN1() do { const u32x4 zf = {z0[0], z0[1], z1[0], z1[1]}; _Pragma("unroll") for (int mi = 0; mi < 4; ++mi) d[mi] = MFMA(FRAG(zf), PF(mi), zero); } while (0)
#define DEN2()                                                                                             \
  do {                                                                                                     \
    _Pragma("unroll") for (int mi = 0; mi < 4; ++mi) rc[mi] = __builtin_amdgcn_rcpf(d[mi][0] + 1e-6f);     \
    _Pragma("unroll") for (int mi = 0; mi < 4; ++mi) { rc[mi] *= gq[mi]; PIN(rc[mi]); }                    \
  } while (0)
    KV_MM(0, DENAT - 4);
    SB();
    unsigned zfl = *LP(volatile unsigned, zfp, 0);
    CFENCE();
    Z_RD
    SB();
    KV_MM(DENAT - 4, DENAT);
    SB();
    DEN1();
    SB();
    KV_MM(DENAT, DENAT + 2);
    SB();
    DEN2();
    const u64 zbad = __builtin_amdgcn_ballot_w64(zfl != 1u);
    SB();
    KV_MM(DENAT + 2, 16);
    if (__builtin_expect((__builtin_amdgcn_ballot_w64(__builtin_isunordered(s0[0], s1[0])) | zbad) != 0ull, 0)) {
      unsigned todo = 15u;
#pragma unroll 1
      for (unsigned spins = 0; spins <= SPIN_CAP; ++spins) {
        unsigned miss = 0;
#pragma unroll
        for (int q = 0; q < 4; ++q)
          if (todo & (1u << q)) { if (!all_true(max(max(MX4(t[4 * q]), MX4(t[4 * q + 1])), max(MX4(t[4 * q + 2]), MX4(t[4 * q + 3]))) != ~0u)) miss |= 1u << q; }
        todo = miss;
        if (todo == 0u) break;
        __builtin_amdgcn_s_sleep(2);
        CFENCE();
#pragma unroll
        for (int q = 0; q < 4; ++q)
          if (todo & (1u << q)) { T_LD(4 * q); T_LD(4 * q + 1); T_LD(4 * q + 2); T_LD(4 * q + 3); }
      }
      s0 = zero; s1 = zero;
      KV_MM(0, 16);
      zfl = rfl(zfl);
      if (zfl == 0u) { TAKE(zfl = misc[C_ZF + hh], Z_RD); zfl = rfl(zfl); DEN1(); DEN2(); }
      if (zfl != 1u) {
        const unsigned sc = __builtin_amdgcn_raw_buffer_load_b32(wsr, (unsigned)OFF_ZP + (rec - mt + c16) * 128 + 64, 0, 16);
        s0 = zero; s1 = zero;
#pragma unroll
        for (int i = 0; i < 16; ++i) {
          const float s_ = u2f((unsigned)__builtin_amdgcn_readlane((int)sc, i));
#pragma unroll
          for (int e = 0; e < 4; ++e) {
            const unsigned tw = t[i][e];
            const f16x2_t h = __builtin_bit_cast(f16x2_t, tw);
            f32x4& d = e < 2 ? s0 : s1;
            d[2 * (e & 1)] = __builtin_fmaf((float)h[0], s_, d[2 * (e & 1)]); d[2 * (e & 1) + 1] = __builtin_fmaf((float)h[1], s_, d[2 * (e & 1) + 1]);
          }
        }
      }
    }
    const u32x4 kown = {cvt_pk(s0[0], s0[1]), cvt_pk(s0[2], s0[3]), cvt_pk(s1[0], s1[1]), cvt_pk(s1[2], s1[3])};
    *LP(u32x2, x2w, 8) = (u32x2){kown[0], kown[1]}; *LP(u32x2, x2w, 0) = (u32x2){kown[2], kown[3]};
    CFENCE();
    *LP(volatile unsigned, cw, W_X2) = one;
    SB();
    f32x4 o[4];
    u32x4 koth;
#pragma unroll
    for (int mi = 0; mi < 4; ++mi) o[mi] = MFMA(FRAG(kown), PF(mi), zero);
    const unsigned x2r = x2w ^ 2048u;
    SB();
#define O_CVT(mi, h) do { ov[mi][h] = (u32x2){cvt_pk(o[mi][0] * rc[mi], o[mi][1] * rc[mi]), cvt_pk(o[mi][2] * rc[mi], o[mi][3] * rc[mi])}; if ((h) == 0) PIN(ov[mi][0]); } while (0)
    O_CVT(0, 0); SB();
    const unsigned f2 = *LP(volatile unsigned, cr, W_X2);
    CFENCE();
    koth = *LP(volatile u32x4, x2r, 0);
    SB();
    O_CVT(1, 0); O_CVT(2, 0); O_CVT(3, 0);
    SB();
    if (__builtin_expect(!all_true(f2 != 0u), 0)) TAKE(*LP(volatile unsigned, cr, W_X2), koth = *LP(volatile u32x4, x2r, 0));
#pragma unroll
    for (int mi = 0; mi < 4; ++mi) o[mi] = MFMA(FRAG(koth), PF(mi), zero);
    SB();
    unsigned oom[4];
#pragma unroll
    for (int mi = 0; mi < 4; ++mi) { oom[mi] = oo + mi * (NH * 1024); PIN(oom[mi]); }
    SB();
#pragma unroll
    for (int mi = 0; mi < 4; ++mi) { O_CVT(mi, 1); __builtin_amdgcn_raw_buffer_store_b128((u32x4){ov[mi][0][0], ov[mi][0][1], ov[mi][1][0], ov[mi][1][1]}, wsr, oom[mi], 0, 16); SB(); }
  }
  SB();
  f32x4 oc[4][2];
  unsigned xa[4], xb[2], so[4];
  unsigned rlo, rhi;
  {
    u32x2 xl[8];
#pragma unroll
    for (int n = 0; n < 4; ++n) { xa[n] = (unsigned)(((32 * w + c16) * 128 + (((g >> 1) ^ (c16 >> 1)) * 16) + (g & 1) * 8) ^ (n * 32)); PIN(xa[n]); }
#pragma unroll
    for (int i = 0; i < 8; ++i) xl[i] = *LP(volatile u32x2, xa[i & 3], (i >> 2) * 2048);
    const unsigned kw = w * 4096 + lane * 16, wlo = hh ? x1w : kw, whi = hh ? kw : x1w;
    rlo = hh ? x1w ^ 2048u : kw; rhi = hh ? kw : x1w ^ 2048u;
#pragma unroll
    for (int mi = 0; mi < 4; ++mi) *LP(volatile u32x4, mi < 2 ? wlo : whi, (mi & 1) * 1024) = (u32x4){ov[mi][0][0], ov[mi][0][1], ov[mi][1][0], ov[mi][1][1]};
    *LP(volatile unsigned, cw, W_X1) = 2u;
#pragma unroll
    for (int i = 0; i < 8; ++i) {
      const int tt = i >> 2;
      const float omg = bfr(1.0f - (tt ? (hh ? gq[3] : gq[1]) : (hh ? gq[2] : gq[0])));
#pragma unroll
      for (int k = 0; k < 2; ++k) {
        const unsigned pr = cvt_pk(omg * blo(xl[i][k]), omg * bhi(xl[i][k]));
        oc[i & 3][tt][2 * k] = blo(pr); oc[i & 3][tt][2 * k + 1] = bhi(pr);
      }
      PIN(oc[i & 3][tt]);
    }
#pragma unroll
    for (int i = 0; i < 2; ++i) { xb[i] = (unsigned)((32 * w + 8 * i + (lane >> 3)) * 128 + (((lane & 7) ^ (4 * i + g)) * 16)); PIN(xb[i]); }
#pragma unroll
    for (int i = 0; i < 4; ++i) { so[i] = (unsigned)(((m0 + 32 * w + 8 * i + (lane >> 3)) * DM + 64 * nt3) * 2 + (lane & 7) * 16); PIN(so[i]); }
  }
  int wof[2][2][4];
#pragma unroll
  for (int n = 0; n < 16; ++n) {
    wof[n >> 3][(n >> 2) & 1][n & 3] = 16384 + ((n >> 2) & 1) * 65536 + (((n >> 3) ^ wr) * 16 + 4 * g + (c16 >> 2)) * 128 + (((n & 3) ^ fsw) * 32) + (c16 & 3) * 8;
    PIN(wof[n >> 3][(n >> 2) & 1][n & 3]);
  }
  const unsigned obase = (unsigned)OFF_OF + (unsigned)(((mt3 * 8 + 2 * w) * NH) * 1024 + lane * 16);
  bf16x8 wf[2][4];
#define W_AT(hd, n, r) (smem + wof[r][WOPOS((hd) >> 1) >> 3][n] + (WOPOS((hd) >> 1) & 7) * 8192 + ((hd) & 1) * 4096)
  const unsigned lw = min(misc[C_WO], misc[C_WO + 1]);
  WAIT_VM(0);
  if (lane == 0) {
    const unsigned old = __hip_atomic_fetch_add((lu32*)(smem + L_CTL) + C_OARR, 1u, RLX_WG);
    if (old == 3u) {
      __hip_atomic_store((gu32*)(ws + OFF_OFLAG + (mt * 16 + nt) * 128), tag, RLX_AGENT);
      misc[C_T6] = (unsigned)__builtin_amdgcn_s_memrealtime() | 1u;
    }
  }
  CFENCE();

  if (__builtin_expect(rfl(lw) == 0u, 0)) { (void)wait_ge<1>(misc + C_WO, 1u); (void)wait_ge<1>(misc + C_WO + 1, 1u); }
  CFENCE();
#pragma unroll
  for (int n = 0; n < 4; ++n) wf[0][n] = CAT(TRD(W_AT(0, n, 0)), TRD(W_AT(0, n, 1)));
  u32x4 of[OPF + 1][4];
  unsigned rdy = 0;
#define O_READY(c) do { if (__builtin_expect(rdy <= (unsigned)(c), 0)) rdy = wait_ge<1>(misc + C_ORDY, (unsigned)(c) + 1u); CFENCE(); } while (0)
#define O_LD1(c, k4) of[(c) % (OPF + 1)][k4] = __builtin_amdgcn_raw_buffer_load_b128(wsr, obase + ((k4) >> 1) * 1024 + ((k4) & 1) * (NH * 1024), CH(c) * 2048, 16)
  TAKE(*LP(volatile unsigned, cw ^ 128u, W_X1) >> 1, { _Pragma("unroll") for (int k = 0; k < 4; ++k) of[0][k] = *LP(volatile u32x4, k < 2 ? rlo : rhi, (k & 1) * 1024); });
  unsigned li = 0;
#define P3_MM(c, j)                                                                                        \
  do {                                                                                                     \
    constexpr int hd = 2 * (c) + ((j) >> 3), n = ((j) >> 1) & 3;                                           \
    AMFMA(oc[n][(j) & 1], wf[(j) >> 3][n], of[(c) % (OPF + 1)][2 * ((j) >> 3) + ((j) & 1)]); SB();         \
    if (hd + 1 < 32) {                                                                                     \
      if (((j) & 1) == 0) tlo = TRD(W_AT((hd + 1) & 31, n, 0)); else wf[1 - ((j) >> 3)][n] = CAT(tlo, TRD(W_AT((hd + 1) & 31, n, 1))); \
    }                                                                                                      \
    if ((c) > 0 && (c) + OPF < 16) {                                                                       \
      if ((j) == 0) O_READY((c) + OPF);                                                                    \
      if (((j) & 3) == 1) O_LD1((c) + OPF, (j) >> 2);                                                      \
    }                                                                                                      \
    if ((c) == 15 && (j) == 8) li = misc[C_IDX];                                                           \
    SB();                                                                                                  \
  } while (0)
#define P3_4(c, j) P3_MM(c, j); P3_MM(c, (j) + 1); P3_MM(c, (j) + 2); P3_MM(c, (j) + 3)
#define P3_C(c) P3_4(c, 0); P3_4(c, 4); P3_4(c, 8); P3_4(c, 12)
  P3_C(0);
#pragma unroll
  for (int c = 1; c <= OPF; ++c) { O_READY(c); O_LD1(c, 0); O_LD1(c, 1); O_LD1(c, 2); O_LD1(c, 3); }
  SB();
  P3_C(1); P3_C(2); P3_C(3); P3_C(4); P3_C(5); P3_C(6); P3_C(7);
  P3_C(8); P3_C(9); P3_C(10); P3_C(11); P3_C(12); P3_C(13); P3_C(14); P3_C(15);
  NOP8(oc);
  li = rfl(li);
  if (__builtin_expect(li == 0u, 0)) li = wait_ge<1>(misc + C_IDX, 1u);
  const int idx = (int)li - 1;
  if ((idx >> 5) == mt3 * 4 + w) {
    f32x4 dl[4];
    TAKE(misc[C_Y], { _Pragma("unroll") for (int n = 0; n < 4; ++n) dl[n] = *(volatile lf32x4*)(smem + L_RED + 2048 + n * 64 + g * 16); });
    const bool mine = c16 == (idx & 15);
    if (idx & 16) {
#pragma unroll
      for (int n = 0; n < 4; ++n) oc[n][1] += mine ? dl[n] : zero;
    } else {
#pragma unroll
      for (int n = 0; n < 4; ++n) oc[n][0] += mine ? dl[n] : zero;
    }
  }
  u32x4 ot[4];
#pragma unroll
  for (int tt = 0; tt < 2; ++tt) {
#pragma unroll
    for (int n = 0; n < 4; ++n) *LP(volatile u32x2, xa[n], tt * 2048) = (u32x2){cvt_pk(oc[n][tt][0], oc[n][tt][1]), cvt_pk(oc[n][tt][2], oc[n][tt][3])};
    ot[2 * tt] = *LP(volatile u32x4, xb[0], tt * 2048); ot[2 * tt + 1] = *LP(volatile u32x4, xb[1], tt * 2048);
  }
#pragma unroll
  for (int i = 0; i < 4; ++i) __builtin_amdgcn_raw_buffer_store_b128(ot[i], MKRSRC(out, SEQ * DM * 2), so[i], 0, 17);
}

extern "C" void kernel_launch(void* const* d_in, const int* in_sizes, int n_in,
                              void* d_out, int out_size, void* d_ws, size_t ws_size,
                              hipStream_t stream) {
  char* ws = (char*)d_ws;
  (void)hipFuncSetAttribute((const void*)fused_kernel, hipFuncAttributeMaxDynamicSharedMemorySize, L_TOTAL);
  fused_kernel<<<256, 512, L_TOTAL, stream>>>((const u16*)d_in[0], (const u16*)d_in[1], (const u16*)d_in[2], ws, (u16*)d_out,
                                              (const u16*)d_in[4], (const u16*)d_in[8], (const u16*)d_in[5], (const u16*)d_in[6],
                                              (const u16*)d_in[7]);
}
```

```cpp
#include <hip/hip_runtime.h>
#include <hip/hip_bf16.h>
#include <stdint.h>

typedef unsigned short u16;
typedef unsigned long long u64;
typedef __attribute__((ext_vector_type(8))) short bf16x8;
typedef __attribute__((ext_vector_type(4))) short s16x4;
typedef __attribute__((ext_vector_type(4))) float f32x4;
typedef __attribute__((ext_vector_type(2))) float f32x2;
typedef __attribute__((ext_vector_type(2))) __bf16 bf16x2_t;
typedef __attribute__((ext_vector_type(2))) _Float16 f16x2_t;
typedef __attribute__((ext_vector_type(4))) _Float16 f16x4_t;
typedef __attribute__((ext_vector_type(4))) unsigned int u32x4;
typedef __attribute__((ext_vector_type(2))) unsigned int u32x2;
typedef __attribute__((ext_vector_type(8))) unsigned int u32x8;
typedef __attribute__((address_space(3))) s16x4* lds_s16x4_ptr;
typedef __attribute__((address_space(1))) unsigned gu32;
typedef __attribute__((address_space(1))) u64 gu64;
typedef __attribute__((address_space(3))) unsigned lu32;
typedef __attribute__((address_space(3))) u32x2 lu32x2;
typedef __attribute__((address_space(3))) u32x4 lu32x4;
typedef __attribute__((address_space(3))) f32x4 lf32x4;
#define RLX_AGENT __ATOMIC_RELAXED, __HIP_MEMORY_SCOPE_AGENT
#define RLX_WG __ATOMIC_RELAXED, __HIP_MEMORY_SCOPE_WORKGROUP

#if defined(__HIP_DEVICE_COMPILE__)
#define NOPK __attribute__((target("no-packed-fp32-ops")))
#else
#define NOPK
#endif

#define SEQ 2048
#define DM 1024
#define N1 3072
#define FF 3584
#define NH 32

#define OFF_EPOCH  ((size_t)0)
#define OFF_RG     ((size_t)4096)
#define OFF_GG     ((size_t)16384)
#define OFF_GU     ((size_t)262144)
#define OFF_YG     ((size_t)524288)
#define OFF_KVP    ((size_t)1 << 20)
#define OFF_ZP     ((size_t)2 << 20)
#define OFF_OFLAG  (((size_t)3 << 20) + 65536)
#define OFF_OF     ((size_t)4 << 20)

#define SPIN_CAP (1u << 18)

__device__ __forceinline__ float u2f(unsigned u) { return __builtin_bit_cast(float, u); }
__device__ __forceinline__ unsigned f2u(float f) { return __builtin_bit_cast(unsigned, f); }
__device__ __forceinline__ bool all_true(bool ok) { return __builtin_amdgcn_ballot_w64(!ok) == 0ull; }
__device__ __forceinline__ unsigned cvt_pk(float a, float b) {
  f32x2 v = {a, b};
  return __builtin_bit_cast(unsigned, __builtin_convertvector(v, bf16x2_t));
}
__device__ __forceinline__ float blo(unsigned u) { return u2f(u << 16); }
__device__ __forceinline__ float bhi(unsigned u) { return u2f(u & 0xFFFF0000u); }
__device__ __forceinline__ float bfr(float f) { return blo(cvt_pk(f, f)); }
__device__ __forceinline__ float fmax_raw(float a, float b) { return __builtin_elementwise_maximum(a, b); }
__device__ __forceinline__ unsigned eps2(unsigned p) { return cvt_pk(blo(p) + 1e-6f, bhi(p) + 1e-6f); }
template <int N>
__device__ __forceinline__ void phi_fix(unsigned (&p)[N], float umin) {
  if (__builtin_expect(__builtin_amdgcn_ballot_w64(umin < -5.9f) != 0ull, 0)) {
#pragma unroll
    for (int i = 0; i < N; ++i) p[i] = eps2(p[i]);
  }
}
__device__ __forceinline__ float sigmoid_f(float x) { return 1.0f / (1.0f + __expf(-x)); }

template <int CTRL, int RM = 0xf> __device__ __forceinline__ int dppi(int old, int v) {
  return __builtin_amdgcn_update_dpp(old, v, CTRL, RM, 0xf, false);
}
template <int CTRL, int RM = 0xf> __device__ __forceinline__ float dppf(float v) {
  return __builtin_bit_cast(float, dppi<CTRL, RM>(0, __builtin_bit_cast(int, v)));
}
__device__ __forceinline__ unsigned wave_max(unsigned v) {
  v = max(v, (unsigned)dppi<0x111>(0, (int)v)); v = max(v, (unsigned)dppi<0x112>(0, (int)v));
  v = max(v, (unsigned)dppi<0x114>(0, (int)v)); v = max(v, (unsigned)dppi<0x118>(0, (int)v));
  v = max(v, (unsigned)dppi<0x142, 0xa>(0, (int)v)); v = max(v, (unsigned)dppi<0x143, 0xc>(0, (int)v));
  return (unsigned)__builtin_amdgcn_readlane((int)v, 63);
}
__device__ __forceinline__ float merge32(float a, float b) {
  const auto r = __builtin_amdgcn_permlane32_swap(f2u(a), f2u(b), false, false);
  return u2f(r[0]) + u2f(r[1]);
}
__device__ __forceinline__ float merge16(float a, float b) {
  const auto r = __builtin_amdgcn_permlane16_swap(f2u(a), f2u(b), false, false);
  return u2f(r[0]) + u2f(r[1]);
}
typedef __attribute__((address_space(3))) void* ldsp;
template <int IMM>
__device__ __forceinline__ void bufl16(__amdgpu_buffer_rsrc_t r, unsigned l, unsigned voff, unsigned soff) {
#if defined(__HIP_DEVICE_COMPILE__)
  __builtin_amdgcn_raw_ptr_buffer_load_lds(r, (ldsp)(uintptr_t)l, 16, voff, soff, IMM, 0);
#endif
}
#define LP(T, base, imm) ((__attribute__((address_space(3))) T*)(uintptr_t)(unsigned)((base) + (imm)))
#define MKRSRC(p, n) __builtin_amdgcn_make_buffer_rsrc((void*)(p), 0, (n), 0x00020000)
#define WAIT_VM(n) asm volatile("s_waitcnt vmcnt(%0)" ::"n"(n) : "memory")
#define WAIT_LGKM0() asm volatile("s_waitcnt lgkmcnt(0)" ::: "memory")
#define BAR() do { __builtin_amdgcn_sched_barrier(0); asm volatile("s_barrier" ::: "memory"); __builtin_amdgcn_sched_barrier(0); } while (0)
#define CFENCE() asm volatile("" ::: "memory")
#define MFMA(a, b, c) __builtin_amdgcn_mfma_f32_16x16x32_bf16((a), (b), (c), 0, 0, 0)
#define PIN(v) asm volatile("" : "+v"(v))
#define AMFMA(acc, a, b) asm volatile("v_mfma_f32_16x16x32_bf16 %0, %1, %2, %0" : "+v"(acc) : "v"(a), "v"(b))
#define NOP8(t) asm volatile("s_nop 7" : "+v"(t[0][0]), "+v"(t[0][1]), "+v"(t[1][0]), "+v"(t[1][1]), "+v"(t[2][0]), "+v"(t[2][1]), "+v"(t[3][0]), "+v"(t[3][1]))
#define SB() __builtin_amdgcn_sched_barrier(0)
#define TRD(p) ((s16x4)__builtin_amdgcn_ds_read_tr16_b64_v4i16((lds_s16x4_ptr)(p)))
#define CAT(lo, hi) __builtin_shufflevector(lo, hi, 0, 1, 2, 3, 4, 5, 6, 7)
#define FRAG(x) __builtin_bit_cast(bf16x8, x)

#define PACE   57
#define BURST  30
#define R_AT   2
#define R_GAP  2
#define CPF_N  20
#define WO_E   6
#define WO_ES  4
#define WO_M   10
#define G_T    65
#define P5_T   40
#define GU_AH  8
#define GU_E   4
#define DN_E   7
#define DENAT  8
#define OPF    3

__device__ __forceinline__ unsigned rfl(unsigned v) { return (unsigned)__builtin_amdgcn_readfirstlane((int)v); }
template <int SLEEP>
__device__ __forceinline__ unsigned wait_ge(volatile lu32* p, unsigned need) {
  unsigned v = 0;
#pragma unroll 1
  for (unsigned spins = 0; spins < (1u << 22); ++spins) {
    v = rfl(*p);
    if (v >= need) break;
    __builtin_amdgcn_s_sleep(SLEEP);
  }
  CFENCE();
  return v;
}

#define SLOT     40960
#define L_X1     147456
#define L_X2     155648
#define L_CW     159744
#define L_ZF     160256
#define L_GATE   160384
#define L_RED    160896
#define L_H      163200
#define L_CTL    163712
#define L_TOTAL  163840
#define W_X1   64
#define W_X2   68
#define W_AG   72
#define W_KS   76
#define WFLAGS(f) (*(volatile lu32*)(smem + L_CW + (lane & 3) * 128 + (f)))
#define WOPOS(c) ((c) < 3 ? 13 + (c) : (c) < 8 ? (c) : (c) < 11 ? (c) - 8 : (c) - 3)
#define C_F15  0
#define C_WO   2
#define C_ZF   4
#define C_TAG  8
#define C_IDX  10
#define C_GO   12
#define C_OARR 17
#define C_ORDY 18
#define C_GU   19
#define C_DN   20
#define C_T6   21
#define C_Y    23

NOPK __global__ __launch_bounds__(512, 2) void fused_kernel(const u16* __restrict__ x, const u16* __restrict__ wqkv,
                                                            const u16* __restrict__ wo, char* ws, u16* __restrict__ out,
                                                            const u16* __restrict__ w_r, const u16* __restrict__ gate_w,
                                                            const u16* __restrict__ w_gate, const u16* __restrict__ w_up,
                                                            const u16* __restrict__ w_down) {
  extern __shared__ __attribute__((aligned(16))) char smem[];
  asm volatile("s_load_dwordx2 s[100:101], %0, 60" :: "s"(__builtin_amdgcn_kernarg_segment_ptr()) : "s100", "s101");
  SB();
  const int tid = threadIdx.x, lane = tid & 63;
  const int w = __builtin_amdgcn_readfirstlane(tid >> 6);
  u64 pc0;
  asm volatile("s_getpc_b64 %0" : "=s"(pc0));
  const int bid = blockIdx.x, xcd = bid & 7, ix = bid >> 3;
  const int ml = ix >> 2, nl = ix & 3;
  const int mt = (xcd >> 2) * 8 + ml, nt = (xcd & 3) * 4 + nl;
  const int m0 = mt * 128;
  const int mt3 = mt, nt3 = nt ^ 1;
#define CH(c) (((4 * (xcd & 3) + (c)) & 15) ^ ((c) < 4 ? nl : 0))
  volatile lu32* misc = (volatile lu32*)(smem + L_CTL);

  if (__builtin_expect((w >> 1) == 2, 1)) {
    u32x8 ka;
    asm volatile("s_load_dwordx8 %0, %1, 0x0" : "=s"(ka) : "s"(__builtin_amdgcn_kernarg_segment_ptr()));
    SB();
    const int p = w - 4;
    unsigned va[4], vb[4], vw[4];
#pragma unroll
    for (int j = 0; j < 4; ++j) {
      const int row = 32 * (w & 1) + 8 * j + (lane >> 3);
      va[j] = (unsigned)((m0 + row) * (DM * 2) + (((lane & 7) ^ ((row >> 1) & 7)) * 16) - 1024 * j);
      vb[j] = (unsigned)(row * (N1 * 2) + 128 * nt + (((lane & 7) ^ ((row & 2) | ((j & 1) << 2))) * 16) - 1024 * j);
      PIN(va[j]); PIN(vb[j]);
    }
    unsigned l0 = (unsigned)(uintptr_t)(ldsp)smem + p * 4096;
    int kf = (4 * ml + 4 * (xcd & 3)) & 15, ks = (4 * ml - 2 * (ml >> 2) + 4 * (xcd & 3)) & 15, sres = (3 - nt) & 7;
    asm volatile("" : "+s"(l0), "+s"(kf), "+s"(ks), "+s"(sres));
    SB();
    asm volatile("s_waitcnt lgkmcnt(0)" : "+s"(ka));
    const u16* x = (const u16*)(((u64)ka[1] << 32) | ka[0]);
    const u16* wqkv = (const u16*)(((u64)ka[3] << 32) | ka[2]);
    const u16* wo = (const u16*)(((u64)ka[5] << 32) | ka[4]);
    char* ws = (char*)(((u64)ka[7] << 32) | ka[6]);
    int ko_ = (int)((ka[2] >> 7) & 7u) == sres ? ks : kf;
    const __amdgpu_buffer_rsrc_t ra = MKRSRC(x, SEQ * DM * 2), rb = MKRSRC(wqkv, DM * N1 * 2), rw = MKRSRC(wo, DM * DM * 2);
#define UNIT(r, v, off, so) do { bufl16<0>(r, l0 + (off), v[0], so); bufl16<1024>(r, l0 + (off), v[1], so); bufl16<2048>(r, l0 + (off), v[2], so); bufl16<3072>(r, l0 + (off), v[3], so); } while (0)
#define ISSUE(t)                                                                                           \
  do {                                                                                                     \
    const int k_ = (ko_ + (t)) & 15;                                                                       \
    UNIT(ra, va, ((t) % 3) * SLOT, k_ * 128); UNIT(ra, va, ((t) % 3) * SLOT + 8192, k_ * 128 + 64 * DM * 2); \
    _Pragma("unroll") for (int s_ = 0; s_ < 3; ++s_) UNIT(rb, vb, ((t) % 3) * SLOT + 16384 + s_ * 8192, k_ * (64 * N1 * 2) + s_ * (DM * 2)); \
  } while (0)
#define PACED_BAR()                                                                                        \
  do {                                                                                                     \
    if (p == 0) {                                                                                          \
      unsigned now;                                                                                        \
      do { now = (unsigned)__builtin_amdgcn_s_memrealtime(); } while ((int)(now - tp) < 0);                \
      tp = ((int)(now - tp) > BURST ? now - BURST : tp) + PACE;                                            \
    }                                                                                                      \
    BAR();                                                                                                 \
  } while (0)
    ISSUE(0); ISSUE(1);
    WAIT_VM(20);
    BAR();
    ISSUE(2);
    unsigned tp = (unsigned)__builtin_amdgcn_s_memrealtime() - PACE;
#pragma unroll
    for (int j = 0; j < 4; ++j) vw[j] = (unsigned)((32 * p + 8 * j + (lane >> 3)) * (DM * 2) + 128 * nt3 + (((lane & 7) ^ ((lane >> 3) & 6)) * 16) - 1024 * j);
#define WO_CHUNK(c) UNIT(rw, vw, 16384 + WOPOS(c) * 8192, CH(c) * (64 * DM * 2))
#pragma unroll
    for (int t = 0; t < 13; ++t) {
      WAIT_VM(20);
      PACED_BAR();
      asm volatile("" : "+s"(l0), "+s"(ko_));
      ISSUE(t + 3);
    }
    WO_CHUNK(0); WO_CHUNK(1); WO_CHUNK(2);
    SB();
    const bool wslow = ((((uintptr_t)wo >> 7) + nt3) & 7) == 3;
    const int woe = wslow ? WO_ES : WO_E;
    WAIT_VM(32);
    PACED_BAR();
    WAIT_VM(12);
    if (lane == 0) misc[C_F15 + p] = 1u;
#pragma unroll 1
    for (int c = 3; c < woe; ++c) WO_CHUNK(c);
    {
      const __amdgpu_buffer_rsrc_t zr = MKRSRC(ws + OFF_ZP, NH * 2 * 16 * 128);
      const unsigned r0 = (unsigned)((2 * nt + p) * 32);
      const unsigned zo0 = (r0 + (lane & 31)) * 128 + 64, zo1 = (r0 + (lane >> 2)) * 128 + (lane & 3) * 16;
      u32x4 zw[2];
      unsigned sc;
      (void)wait_ge<1>(misc + C_IDX + p, 1u);
#pragma unroll 1
      for (unsigned spins = 0; spins < (1u << 22) && !all_true(WFLAGS(W_KS) != 0u); ++spins) __builtin_amdgcn_s_sleep(1);
      CFENCE();
      const unsigned tk = (unsigned)__builtin_amdgcn_s_memrealtime();
#pragma unroll 1
      for (int c = woe; c < WO_M; ++c) WO_CHUNK(c);
      UNIT(ra, va, 0, nt3 * 128); UNIT(ra, va, 8192, nt3 * 128 + 64 * DM * 2);
      while ((unsigned)__builtin_amdgcn_s_memrealtime() - tk < (unsigned)G_T) __builtin_amdgcn_s_sleep(1);
#define MX4(v) max(max((v)[0], (v)[1]), max((v)[2], (v)[3]))
#pragma unroll 1
      for (unsigned spins = 0; spins <= SPIN_CAP; ++spins) {
        CFENCE();
        zw[0] = __builtin_amdgcn_raw_buffer_load_b128(zr, zo1, 0, 16); zw[1] = __builtin_amdgcn_raw_buffer_load_b128(zr, zo1, 2048, 16);
        sc = __builtin_amdgcn_raw_buffer_load_b32(zr, zo0, 0, 16);
        SB(); misc[C_GO + p] = 1u; SB();
        if (all_true(max(max(MX4(zw[0]), MX4(zw[1])), sc) != ~0u)) break;
        __builtin_amdgcn_s_sleep(2);
      }
      float z0 = merge16(merge32(u2f(zw[0][0]), u2f(zw[0][2])), merge32(u2f(zw[1][0]), u2f(zw[1][2])));
      float z1 = merge16(merge32(u2f(zw[0][1]), u2f(zw[0][3])), merge32(u2f(zw[1][1]), u2f(zw[1][3])));
      z0 += dppf<0x128>(z0); z1 += dppf<0x128>(z1); z0 += dppf<0x124>(z0); z1 += dppf<0x124>(z1);
      *(unsigned*)(smem + L_ZF + p * 64 + (lane & 3) * 16 + ((lane >> 4) & 1) * 8 + (lane >> 5) * 4) = cvt_pk(z0, z1);
      CFENCE();
      misc[C_ZF + p] = all_true(sc == 0x3F800000u) ? 1u : 2u;
    }
#pragma unroll 1
    for (unsigned spins = 0; spins < (1u << 22) && !all_true(WFLAGS(W_AG) != 0u); ++spins) __builtin_amdgcn_s_sleep(2);
    CFENCE();
#pragma unroll 1
    for (int c = WO_M; c < 16; ++c) WO_CHUNK(c);
    WAIT_VM(0);
    if (lane == 0) misc[C_WO + p] = 1u;
#undef ISSUE
#undef PACED_BAR
    if (p == 0) return;
    const unsigned tag = rfl(misc[C_TAG]);
    gu32* of = (gu32*)(ws + OFF_OFLAG + (mt3 * 16 + CH(lane & 15)) * 128);
    const unsigned t6 = wait_ge<1>(misc + C_T6, 1u);
    while ((unsigned)__builtin_amdgcn_s_memrealtime() - t6 < (unsigned)P5_T) __builtin_amdgcn_s_sleep(1);
#pragma unroll 1
    for (unsigned spins = 0;; ++spins) {
      const unsigned f = __hip_atomic_load(of, RLX_AGENT);
      const unsigned miss = (unsigned)__builtin_amdgcn_ballot_w64(f != tag) & 0xFFFEu;
      const unsigned m = (miss == 0u || spins > SPIN_CAP) ? 16u : (unsigned)__builtin_ctz(miss);
      if (lane == 0) misc[C_ORDY] = m;
      if (m == 16u) break;
      __builtin_amdgcn_s_sleep(2);
    }
    return;
  }

  if (__builtin_expect(w >= 6, 0)) {
    const int pa = w - 6;
#define CODE_PF(on) __builtin_amdgcn_raw_buffer_load_b32(MKRSRC((pc0 & ~255ull) + (blockIdx.x >> 3) * (CPF_N * 64u), (on) ? CPF_N * 64 : 0), lane * 64, 0, 0)
    unsigned cpf = CODE_PF(w == 7), cpf2;
    SB();
    float* red = (float*)(smem + L_RED);
    const bool help = bid >= 224, gemv = bid < 252;
    const int gmat = bid & 1;
    const u16* wgu = gmat ? w_up : w_gate;
    const int sres = (int)(3u - (unsigned)((uintptr_t)wgu >> 7)) & 7;
    const int gfs = help ? sres + 8 * (((bid - 224) >> 1) % 7) : (bid >> 1) % 56;
    const int gdh = (help ? ((bid - 224) >> 1) / 7 : (bid >> 1) / 56) & 1;
    const bool act0 = !help, act1 = gemv && (help || (gfs & 7) != sres);
    const int gd0 = gdh * 512 + 256 * pa + 32 * (lane >> 3);
    const __amdgpu_buffer_rsrc_t rgu0 = MKRSRC(wgu, act0 ? DM * FF * 2 : 0), rgu1 = MKRSRC(wgu, act1 ? DM * FF * 2 : 0);
    const unsigned wpo = (unsigned)((gd0 * FF + gfs * 64 + 8 * (lane & 7)) * 2);
    u32x4 wv[32], wd[14];
    const int fg = bid >> 4, cg = bid & 15;
    const int f0 = fg * 224 + pa * 112;
    const __amdgpu_buffer_rsrc_t rdn = MKRSRC(w_down, DM * FF * 2);
    const unsigned wqo = (unsigned)(((f0 + 14 * (lane >> 3)) * DM + cg * 64 + 8 * (lane & 7)) * 2);
    asm volatile("" ::"s"(w_r), "s"(gate_w), "s"(ws), "s"(w_gate), "s"(w_up), "s"(w_down));
    unsigned pf0, pf1;
#define WO_PREFETCH()                                                                                      \
  do {                                                                                                     \
    const u16* pp_ = wo + (size_t)(128 * ml + 64 * pa + (lane >> 1)) * DM + 64 * nt3 + (lane & 1) * 32;    \
    pf0 = *(const unsigned*)pp_; pf1 = *(const unsigned*)(pp_ + 32 * DM);                                  \
  } while (0)
#define GU_LD(i0, i1) do { SB(); _Pragma("unroll") for (int i = (i0); i < (i1); ++i) wv[i] = __builtin_amdgcn_raw_buffer_load_b128(i < 16 ? rgu0 : rgu1, wpo, i * (FF * 2), 2); SB(); } while (0)
#define DN_LD(i0, i1) do { SB(); _Pragma("unroll") for (int i = (i0); i < (i1); ++i) wd[i] = __builtin_amdgcn_raw_buffer_load_b128(rdn, wqo, i * (DM * 2), 2); SB(); } while (0)
    GU_LD(0, GU_E);
    {
#pragma unroll
      for (int t = 0; t <= R_AT; ++t) BAR();
      const int r0 = m0 + nt * 8 + 4 * pa;
      u32x4 xr[4][2], rv[2], gv[2];
#pragma unroll
      for (int i = 0; i < 2; ++i) {
        rv[i] = *(const u32x4*)(w_r + i * 512 + lane * 8);
        gv[i] = *(const u32x4*)(gate_w + i * 512 + lane * 8);
      }
#pragma unroll
      for (int r = 0; r < 4; ++r)
#pragma unroll
        for (int i = 0; i < 2; ++i) xr[r][i] = *(const u32x4*)(x + (size_t)(r0 + r) * DM + i * 512 + lane * 8);
      const unsigned ep = __hip_atomic_load((gu32*)(ws + OFF_EPOCH), RLX_AGENT);
      {
        const u32x4 nw = {~0u, ~0u, ~0u, ~0u};
        const __amdgpu_buffer_rsrc_t kvr = MKRSRC(ws + OFF_KVP, NH * 2 * 16 * 1024), zr = MKRSRC(ws + OFF_ZP, NH * 2 * 16 * 128);
        const unsigned rec = (unsigned)((2 * nt + pa) * 32 + mt);
        __builtin_amdgcn_raw_buffer_store_b128(nw, kvr, rec * 1024 + lane * 16, 0, 16);
        __builtin_amdgcn_raw_buffer_store_b128(nw, kvr, rec * 1024 + lane * 16 + 16384, 0, 16);
        if (lane < 16) __builtin_amdgcn_raw_buffer_store_b128(nw, zr, (rec + (lane >> 3) * 16) * 128 + (lane & 7) * 16, 0, 16);
      }
#pragma unroll
      for (int t = R_AT + 1; t < R_AT + R_GAP; ++t) BAR();
      float racc[4], gacc[4];
#pragma unroll
      for (int r = 0; r < 4; ++r) {
        BAR();
        if (r == 1) { WAIT_VM(0); SB(); }
        racc[r] = 0.f; gacc[r] = 0.f;
#pragma unroll
        for (int i = 0; i < 2; ++i)
#pragma unroll
          for (int e = 0; e < 4; ++e) {
            const float x0 = blo(xr[r][i][e]), x1 = bhi(xr[r][i][e]);
            racc[r] += x0 * blo(rv[i][e]); racc[r] += x1 * bhi(rv[i][e]);
            const unsigned pr = cvt_pk(x0 * blo(gv[i][e]), x1 * bhi(gv[i][e]));
            gacc[r] += blo(pr); gacc[r] += bhi(pr);
          }
        PIN(racc[r]); PIN(gacc[r]);
        if (r == 3) { SB(); WO_PREFETCH(); cpf2 = CODE_PF(w == 7); }
      }
      BAR();
      float rs = merge16(merge32(racc[0], racc[1]), merge32(racc[2], racc[3]));
      float gs = merge16(merge32(gacc[0], gacc[1]), merge32(gacc[2], gacc[3]));
      rs += dppf<0x128>(rs); gs += dppf<0x128>(gs); rs += dppf<0x124>(rs); gs += dppf<0x124>(gs);
      rs += dppf<0x122>(rs); gs += dppf<0x122>(gs); rs += dppf<0x121>(rs); gs += dppf<0x121>(gs);
      const int rr = r0 + ((lane >> 5) | ((lane >> 3) & 2));
      const unsigned rb = cvt_pk(rs, rs) & 0xFFFFu;
      unsigned key = (((rb & 0x8000u) ? (~rb & 0xFFFFu) : (rb | 0x8000u)) << 16) | (0xFFFFu - (unsigned)rr);
      { const auto t_ = __builtin_amdgcn_permlane16_swap(key, key, false, false); key = max(t_[0], t_[1]); }
      { const auto t_ = __builtin_amdgcn_permlane32_swap(key, key, false, false); key = max(t_[0], t_[1]); }
      const unsigned tg = rfl(ep) + 1u;
      if (lane == 0) misc[C_TAG + pa] = pa ? key : tg;
      WAIT_LGKM0();
      BAR();
      if (pa == 0 && lane == 0) __hip_atomic_store((gu64*)(ws + OFF_RG) + bid, ((u64)tg << 32) | max(key, (unsigned)misc[C_TAG + 1]), RLX_AGENT);
      if ((lane & 15) == 0) __hip_atomic_store((gu64*)(ws + OFF_GG) + rr, ((u64)tg << 32) | f2u(bfr(sigmoid_f(bfr(gs)))), RLX_AGENT);
      DN_LD(0, DN_E);
#pragma unroll
      for (int t = R_AT + R_GAP + 6; t < 13; ++t) BAR();
    }
    u64 rg[4];
#pragma unroll
    for (int i = 0; i < 4; ++i) rg[i] = __hip_atomic_load((gu64*)(ws + OFF_RG) + i * 64 + lane, RLX_AGENT);
    const gu64* ggp = (const gu64*)(ws + OFF_GG) + m0 + 64 * pa + lane;
    u64 gg = __hip_atomic_load(ggp, RLX_AGENT);
    BAR();
    const unsigned tag = rfl(misc[C_TAG]);
    unsigned idx;
    unsigned bestk;
    bool late;
    {
      unsigned best = 0;
      bool ok = (unsigned)(gg >> 32) == tag;
#pragma unroll
      for (int i = 0; i < 4; ++i) { ok = ok && (unsigned)(rg[i] >> 32) == tag; best = max(best, (unsigned)rg[i]); }
      late = !all_true(ok);
      if (__builtin_expect(late, 0)) {
        BAR();
#pragma unroll 1
        for (unsigned spins = 0;; ++spins) {
          u64 rh[4];
#pragma unroll
          for (int i = 0; i < 4; ++i) rh[i] = __hip_atomic_load((gu64*)(ws + OFF_RG) + i * 64 + lane, RLX_AGENT);
          gg = __hip_atomic_load(ggp, RLX_AGENT);
          ok = (unsigned)(gg >> 32) == tag; best = 0;
#pragma unroll
          for (int i = 0; i < 4; ++i) { ok = ok && (unsigned)(rh[i] >> 32) == tag; best = max(best, (unsigned)rh[i]); }
          if (all_true(ok) || spins > SPIN_CAP) break;
          __builtin_amdgcn_s_sleep(8);
        }
      }
      best = wave_max(best);
      idx = (0xFFFFu - (best & 0xFFFFu)) & (SEQ - 1);
      bestk = best;
    }
    asm volatile("" ::"v"(pf0), "v"(pf1), "v"(cpf), "v"(cpf2));
    u32x4 xs[4];
#pragma unroll
    for (int i = 0; i < 4; ++i) xs[i] = *(const u32x4*)(x + (size_t)idx * DM + gd0 + 8 * i);
    if (!late) BAR();
    GU_LD(GU_E, 32);
    ((unsigned*)(smem + L_GATE))[64 * pa + lane] = (unsigned)gg;
    CFENCE();
    if (lane == 0) misc[C_IDX + pa] = idx + 1u;
    if (bid == 0 && pa == 0 && lane == 0) __hip_atomic_store((gu32*)(ws + OFF_EPOCH), tag, RLX_AGENT);
    unsigned sel[2][4];
#pragma unroll
    for (int i = 0; i < 8; ++i) {
      const unsigned pick = (i >> 2) ? 0x0302u : 0x0100u;
      sel[i >> 2][i & 3] = ((lane & 7) >> 1) == (i & 3) ? ((lane & 1) ? (pick << 16) | 0x0C0Cu : 0x0C0C0000u | pick) : 0x0C0C0C0Cu;
      PIN(sel[i >> 2][i & 3]);
    }
#define AFR(pr, i) FRAG(((u32x4){__builtin_amdgcn_perm(0u, pr, sel[(i) & 1][0]), __builtin_amdgcn_perm(0u, pr, sel[(i) & 1][1]), \
                                 __builtin_amdgcn_perm(0u, pr, sel[(i) & 1][2]), __builtin_amdgcn_perm(0u, pr, sel[(i) & 1][3])}))
    f32x4* rdw = (f32x4*)(red + pa * 128 + (lane >> 5) * 64 + (lane & 7) * 8 + ((lane >> 4) & 1) * 4);
    const bool rvalid = (lane >> 5) == ((lane >> 3) & 1);
    const auto rv = LP(volatile float, L_RED, lane * 4);
    {
      bf16x8 af[32];
      f32x4 a = {0.f, 0.f, 0.f, 0.f};
#pragma unroll
      for (int i = 0; i < GU_AH; ++i) { af[i] = AFR(xs[i >> 3][(i >> 1) & 3], i); PIN(af[i]); }
      SB();
#pragma unroll
      for (int i = 0; i < 32; ++i) {
        a = MFMA(af[i], FRAG(wv[i]), a);
        PIN(a);
        if (i + GU_AH < 32) { af[(i + GU_AH) & 31] = AFR(xs[((i + GU_AH) >> 3) & 3][((i + GU_AH) >> 1) & 3], i + GU_AH); PIN(af[(i + GU_AH) & 31]); }
        SB();
      }
      if (rvalid) *rdw = a;
      CFENCE();
      if (pa == 1) misc[C_GU] = 1u;
      else {
        float v;
#pragma unroll 1
        for (unsigned spins = 0; spins < (1u << 22); ++spins) {
          const unsigned f_ = misc[C_GU];
          CFENCE();
          v = (rv[0] + rv[64]) + (rv[128] + rv[192]);
          if (rfl(f_) != 0u) break;
          __builtin_amdgcn_s_sleep(1);
        }
        if (gemv) __hip_atomic_store((gu64*)(ws + OFF_GU) + ((help ? 4 : 0) + gdh * 2 + gmat) * FF + gfs * 64 + lane, ((u64)tag << 32) | f2u(v), RLX_AGENT);
      }
    }
    {
#pragma unroll 1
      for (unsigned spins = 0; spins < (1u << 22) && !all_true(WFLAGS(W_X2) != 0u); ++spins) __builtin_amdgcn_s_sleep(1);
      __builtin_amdgcn_s_sleep(10);
      DN_LD(DN_E, 14);
      float hv[2];
      const int fl = f0 + (lane < 56 ? lane : 55);
      const gu64* gp = (const gu64*)(ws + OFF_GU) + fl;
      bool ex[2][2];
#pragma unroll
      for (int j = 0; j < 2; ++j) {
        ex[j][0] = ((((unsigned)((uintptr_t)w_gate >> 7) + ((fl + 56 * j) >> 6)) & 7u) == 3u);
        ex[j][1] = ((((unsigned)((uintptr_t)w_up >> 7) + ((fl + 56 * j) >> 6)) & 7u) == 3u);
      }
#pragma unroll 1
      for (unsigned spins = 0;; ++spins) {
        u64 g[2][8];
#pragma unroll
        for (int j = 0; j < 2; ++j)
#pragma unroll
          for (int i = 0; i < 8; ++i) {
            g[j][i] = (u64)tag << 32;
            if (i < 4 || ex[j][i & 1]) g[j][i] = __hip_atomic_load(gp + i * FF + j * 56, RLX_AGENT);
          }
        bool ok = true;
#pragma unroll
        for (int j = 0; j < 2; ++j) {
#pragma unroll
          for (int i = 0; i < 8; ++i) ok = ok && (unsigned)(g[j][i] >> 32) == tag;
          const float gt = bfr((u2f((unsigned)g[j][0]) + u2f((unsigned)g[j][4])) + (u2f((unsigned)g[j][2]) + u2f((unsigned)g[j][6])));
          const float up = bfr((u2f((unsigned)g[j][1]) + u2f((unsigned)g[j][5])) + (u2f((unsigned)g[j][3]) + u2f((unsigned)g[j][7])));
          hv[j] = bfr(gt * __builtin_amdgcn_rcpf(1.0f + __expf(-gt))) * up;
        }
        if (all_true(ok) || spins > SPIN_CAP) break;
        __builtin_amdgcn_s_sleep(4);
      }
      u32x4 hp[2];
      {
        char* hb = smem + L_H + pa * 256;
        const unsigned pr = cvt_pk(hv[0], hv[1]);
        if (lane < 56) { *(u16*)(hb + (lane / 14) * 32 + (lane % 14) * 2) = (u16)pr; *(u16*)(hb + 128 + (lane / 14) * 32 + (lane % 14) * 2) = (u16)(pr >> 16); }
        CFENCE();
        hp[0] = *(volatile lu32x4*)(hb + (lane >> 3) * 32); hp[1] = *(volatile lu32x4*)(hb + (lane >> 3) * 32 + 16);
      }
      bf16x8 af[14];
#pragma unroll
      for (int i = 0; i < 14; ++i) { af[i] = AFR(hp[i >> 3][(i >> 1) & 3], i); PIN(af[i]); }
      SB();
      f32x4 a = {0.f, 0.f, 0.f, 0.f};
#pragma unroll
      for (int i = 0; i < 14; ++i) a = MFMA(af[i], FRAG(wd[i]), a);
      if (rvalid) rdw[64] = a;
      CFENCE();
      if (pa == 1) misc[C_DN] = 1u;
      else {
        float y;
#pragma unroll 1
        for (unsigned spins = 0; spins < (1u << 22); ++spins) {
          const unsigned f_ = misc[C_DN];
          CFENCE();
          y = (rv[256] + rv[320]) + (rv[384] + rv[448]);
          if (rfl(f_) != 0u) break;
          __builtin_amdgcn_s_sleep(1);
        }
        __hip_atomic_store((gu64*)(ws + OFF_YG) + fg * 1024 + cg * 64 + lane, ((u64)tag << 32) | f2u(y), RLX_AGENT);
      }
    }
    if (pa == 1 && (int)(idx >> 7) == mt3) {
      const float xe = u2f((unsigned)x[(size_t)idx * DM + 64 * nt3 + lane] << 16);
      const unsigned ob = bestk >> 16;
      const float rsel = bfr(sigmoid_f(u2f(((ob & 0x8000u) ? (ob & 0x7FFFu) : (~ob & 0xFFFFu)) << 16)));
      float y = 0.f, gt = 0.f;
      __builtin_amdgcn_s_sleep(30);
#pragma unroll 1
      for (unsigned spins = 0;; ++spins) {
        u64 g[17];
#pragma unroll
        for (int i = 0; i < 16; ++i) g[i] = __hip_atomic_load((gu64*)(ws + OFF_YG) + i * 1024 + 64 * nt3 + lane, RLX_AGENT);
        g[16] = __hip_atomic_load((gu64*)(ws + OFF_GG) + idx, RLX_AGENT);
        bool ok = (unsigned)(g[16] >> 32) == tag;
        gt = u2f((unsigned)g[16]); y = 0.f;
#pragma unroll
        for (int i = 0; i < 16; ++i) { ok = ok && (unsigned)(g[i] >> 32) == tag; y += u2f((unsigned)g[i]); }
        if (all_true(ok) || spins > SPIN_CAP) break;
        __builtin_amdgcn_s_sleep(8);
      }
      red[512 + lane] = bfr(bfr(1.0f - gt) * bfr(xe + bfr(bfr(y) * rsel))) - bfr(bfr(1.0f - gt) * xe);
      CFENCE();
      if (lane == 0) misc[C_Y] = 1u;
    }
    return;
  }

  if (w == 0) misc[lane & 31] = 0u;
  if (w == 1) *(u32x2*)(smem + L_CW + lane * 8) = (u32x2){0u, 0u};
  const int hh = w & 1, wr = w >> 1;
  const int c16 = lane & 15, g = lane >> 4;
  const int fsw = (c16 >> 3) | ((g & 1) << 1);
  const int head = 2 * nt + hh;
  int aoff[2], boff[2];
#pragma unroll
  for (int i = 0; i < 2; ++i) {
    aoff[i] = (wr * 64 + c16) * 128 + (((4 * i + g) ^ (c16 >> 1)) * 16);
    boff[i] = 16384 + (8 * g + (c16 >> 2)) * 128 + (((2 * hh + (i ^ wr)) ^ fsw) * 32) + (c16 & 3) * 8;
  }
  f32x4 acc[3][4][2];
#pragma unroll
  for (int i = 0; i < 24; ++i) { const float s0 = i < 16 ? 1.f : 0.f; acc[i / 8][(i >> 1) & 3][i & 1] = (f32x4){s0, s0, s0, s0}; PIN(acc[i / 8][(i >> 1) & 3][i & 1]); }
  bf16x8 a[2][4], b[2][6];
  s16x4 tlo;
#define A_AT(sl, h, mi) (*(const bf16x8*)(smem + (sl) * SLOT + aoff[h] + (mi) * 2048))
#define B_AT(sl, h, s, n) (smem + (sl) * SLOT + boff[n] + (s) * 8192 + (h) * 4096)
#define MM(s, mi, n, A, B) do { if ((s) == 0) AMFMA(acc[0][mi][n], B, A); else AMFMA(acc[s][mi][n], A, B); } while (0)
#define RD(sl, h, u)                                                                                       \
  do {                                                                                                     \
    if ((u) == 0) a[h][0] = A_AT(sl, h, 0);                                                                \
    else if ((u) >= 13) a[h][(u) - 12] = A_AT(sl, h, (u) - 12);                                            \
    else {                                                                                                 \
      const int j_ = (((u) - 1) / 2 + 2) % 6;                                                              \
      if ((u) & 1) tlo = TRD(B_AT(sl, h, j_ >> 1, j_ & 1));                                                \
      else b[h][j_] = CAT(tlo, TRD(B_AT(sl, h, j_ >> 1, j_ & 1) + 512));                                   \
    }                                                                                                      \
  } while (0)
#define O_M(n) ((((n) / 4) & 1) ? 3 - (n) % 4 : (n) % 4)
#define O_J(n) ((n) / 4)
#define STEP(h, rs)                                                                                        \
  _Pragma("unroll") for (int n_ = 0; n_ < 24; ++n_) {                                                      \
    const int jj_ = (O_J(n_) + 2) % 6, mi_ = O_M(n_);                                                      \
    MM(jj_ >> 1, mi_, jj_ & 1, a[h][mi_], b[h][jj_]); SB();                                                \
    if (n_ < 16) { RD(rs, 1 - (h), n_); }                                                                 \
    SB();                                                                                                  \
  }
#define LGKM0() __builtin_amdgcn_s_waitcnt(0xC07F)
#define STAGE(sl, nx) do { STEP(0, sl); LGKM0(); BAR(); STEP(1, nx); LGKM0(); SB(); } while (0)

  __amdgpu_buffer_rsrc_t wsr = MKRSRC(ws, OFF_OF + SEQ * DM * 2);
  asm volatile("" : "+s"(wsr));
  const unsigned rec = (unsigned)((head * 2 + wr) * 16 + mt);
  unsigned x1w = L_X1 + w * 2048 + lane * 16, cw = L_CW + w * 128 + g * 16, one = 1u;
  unsigned kvo = (unsigned)OFF_KVP + rec * 1024 + lane * 16;
  unsigned zo = (c16 == 0 || lane == 1) ? (unsigned)OFF_ZP + rec * 128 + g * 16 + c16 * 64 : 0x80000000u;
  PIN(x1w); PIN(cw); PIN(one); PIN(kvo); PIN(zo);
  WAIT_LGKM0();
  BAR();
#pragma unroll
  for (int u = 0; u < 16; ++u) RD(0, 0, u);
  LGKM0();
  SB();
#pragma unroll 1
  for (int it = 0; it < 4; ++it) { STAGE(0, 1); STAGE(1, 2); STAGE(2, 0); }
  STAGE(0, 1);
  STEP(0, 1); LGKM0(); BAR();
  bf16x8 ta0_0, ta0_1, ta0_2, ta0_3, ta1_0, ta1_1, ta1_2, ta1_3, ta2_0, ta2_1, ta2_2, ta2_3;
  bf16x8 ta3_0, ta3_1, ta3_2, ta3_3, tb0_0_0, tb0_0_1, tb0_1_0, tb0_1_1, tb0_2_0, tb0_2_1, tb0_3_0, tb0_3_1;
  bf16x8 tb1_0_0, tb1_0_1, tb1_1_0, tb1_1_1, tb1_2_0, tb1_2_1, tb1_3_0, tb1_3_1, tb2_0_0, tb2_0_1, tb2_1_0, tb2_1_1;
  bf16x8 tb2_2_0, tb2_2_1, tb2_3_0, tb2_3_1;
  s16x4 tb0_0_0_l, tb0_0_1_l, tb0_1_0_l, tb0_1_1_l, tb0_2_0_l, tb0_2_1_l, tb0_3_0_l, tb0_3_1_l, tb1_0_0_l, tb1_0_1_l, tb1_1_0_l, tb1_1_1_l;
  s16x4 tb1_2_0_l, tb1_2_1_l, tb1_3_0_l, tb1_3_1_l, tb2_0_0_l, tb2_0_1_l, tb2_1_0_l, tb2_1_1_l, tb2_2_0_l, tb2_2_1_l, tb2_3_0_l, tb2_3_1_l;
  unsigned kp[16], vp[16], x1r, cr;
  u32x2 lf15;
  float kmin = 0.f, qmin = 0.f;
  u32x4 ones;
  float ek[4];
#define PHI(src, dst, mn, k, st)                                                                           \
  do {                                                                                                     \
    float& e_ = ek[2 * (k) + ((st) & 1)];                                                                  \
    if ((st) < 2) e_ = __builtin_fmaf((src)[2 * (k) + ((st) & 1)], 1.44269504088896341f, -1.44269504088896341f); \
    else if ((st) < 4) e_ = __builtin_amdgcn_fmed3f(__builtin_amdgcn_exp2f(e_), 0.f, 1.f);                 \
    else if ((st) < 6) e_ = fmax_raw((src)[2 * (k) + ((st) & 1)], e_);                                     \
    else if ((st) == 6) dst = cvt_pk(ek[2 * (k)], ek[2 * (k) + 1]);                                        \
    else mn = __builtin_fminf(__builtin_fminf(mn, (src)[2 * (k)]), (src)[2 * (k) + 1]);                     \
    SB();                                                                                                  \
  } while (0)
#define PK(mi, n, k, st) PHI(acc[1][mi][n], kp[8 * (n) + 2 * (mi) + (k)], kmin, k, st)
#define CV(mi, n, k) do { vp[8 * (n) + 2 * (mi) + (k)] = cvt_pk(acc[2][mi][n][2 * (k)], acc[2][mi][n][2 * (k) + 1]); PIN(vp[8 * (n) + 2 * (mi) + (k)]); SB(); } while (0)
#define F15_RD lf15 = *(volatile lu32x2*)(smem + L_CTL + 4 * C_F15)
#define F15_CHK do { if (__builtin_expect(!all_true((lf15[0] & lf15[1]) != 0u), 0)) { (void)wait_ge<1>(misc + C_F15, 1u); (void)wait_ge<1>(misc + C_F15 + 1, 1u); } CFENCE(); } while (0)
#define PRE(i) do { if ((i) == 30) { x1r = x1w ^ 4096u; PIN(x1r); } if ((i) == 31) { cr = cw ^ 256u; PIN(cr); } \
                    if ((i) == 32) { ones = (u32x4){0x3F803F80u, 0x3F803F80u, 0x3F803F80u, 0x3F803F80u}; PIN(ones); } } while (0)
  MM(1, 0, 0, a[1][0], b[1][2]); SB(); tb1_0_1_l = TRD(B_AT(2, 0, 1, 1)); PRE(0); SB();
  MM(1, 0, 1, a[1][0], b[1][3]); SB(); tb1_0_1 = CAT(tb1_0_1_l, TRD(B_AT(2, 0, 1, 1) + 512)); PRE(1); SB();
  MM(2, 0, 0, a[1][0], b[1][4]); SB(); ta0_0 = A_AT(2, 0, 0); PRE(2); SB();
  MM(2, 0, 1, a[1][0], b[1][5]); SB(); ta0_1 = A_AT(2, 0, 1); PRE(3); SB();
  MM(0, 0, 0, a[1][0], b[1][0]); SB(); ta0_2 = A_AT(2, 0, 2); PRE(4); SB();
  MM(0, 0, 1, a[1][0], b[1][1]); SB(); ta0_3 = A_AT(2, 0, 3); PRE(5); SB();
  MM(1, 1, 0, a[1][1], b[1][2]); SB(); tb1_0_0_l = TRD(B_AT(2, 0, 1, 0)); PRE(6); SB();
  MM(1, 1, 1, a[1][1], b[1][3]); SB(); tb1_0_0 = CAT(tb1_0_0_l, TRD(B_AT(2, 0, 1, 0) + 512)); PRE(7); SB();
  MM(2, 1, 0, a[1][1], b[1][4]); SB(); tb1_1_1_l = TRD(B_AT(2, 1, 1, 1)); PRE(8); SB();
  MM(2, 1, 1, a[1][1], b[1][5]); SB(); tb1_1_1 = CAT(tb1_1_1_l, TRD(B_AT(2, 1, 1, 1) + 512)); PRE(9); SB();
  MM(0, 1, 0, a[1][1], b[1][0]); SB(); ta1_0 = A_AT(2, 1, 0); PRE(10); SB();
  MM(0, 1, 1, a[1][1], b[1][1]); SB(); ta1_1 = A_AT(2, 1, 1); PRE(11); SB();
  MM(1, 2, 0, a[1][2], b[1][2]); SB(); ta1_2 = A_AT(2, 1, 2); PRE(12); SB();
  MM(1, 2, 1, a[1][2], b[1][3]); SB(); ta1_3 = A_AT(2, 1, 3); PRE(13); SB();
  MM(2, 2, 0, a[1][2], b[1][4]); SB(); tb1_1_0_l = TRD(B_AT(2, 1, 1, 0)); PRE(14); SB();
  MM(2, 2, 1, a[1][2], b[1][5]); SB(); tb1_1_0 = CAT(tb1_1_0_l, TRD(B_AT(2, 1, 1, 0) + 512)); PRE(15); SB();
  MM(0, 2, 0, a[1][2], b[1][0]); SB(); PRE(16); SB();
  MM(0, 2, 1, a[1][2], b[1][1]); SB(); PRE(17); SB();
  MM(1, 3, 0, a[1][3], b[1][2]); SB(); PRE(18); SB();
  MM(1, 3, 1, a[1][3], b[1][3]); SB(); PRE(19); SB();
  MM(2, 3, 0, a[1][3], b[1][4]); SB(); F15_RD; PRE(20); SB();
  MM(2, 3, 1, a[1][3], b[1][5]); SB(); PRE(21); SB();
  MM(0, 3, 0, a[1][3], b[1][0]); SB(); tb2_0_1_l = TRD(B_AT(2, 0, 2, 1)); PRE(22); SB();
  MM(0, 3, 1, a[1][3], b[1][1]); SB(); tb2_0_1 = CAT(tb2_0_1_l, TRD(B_AT(2, 0, 2, 1) + 512)); PRE(23); SB();
  MM(1, 0, 1, ta0_0, tb1_0_1); SB(); PRE(24); SB();
  MM(1, 1, 1, ta0_1, tb1_0_1); SB(); PRE(25); SB();
  MM(1, 2, 1, ta0_2, tb1_0_1); SB(); F15_CHK; tb2_1_1_l = TRD(B_AT(2, 1, 2, 1)); PRE(26); SB();
  MM(1, 3, 1, ta0_3, tb1_0_1); SB(); tb2_1_1 = CAT(tb2_1_1_l, TRD(B_AT(2, 1, 2, 1) + 512)); PRE(27); SB();
  MM(1, 0, 0, ta0_0, tb1_0_0); SB(); tb1_2_1_l = TRD(B_AT(0, 0, 1, 1)); PRE(28); SB();
  MM(1, 1, 0, ta0_1, tb1_0_0); SB(); tb1_2_1 = CAT(tb1_2_1_l, TRD(B_AT(0, 0, 1, 1) + 512)); PRE(29); SB();
  MM(1, 2, 0, ta0_2, tb1_0_0); SB(); ta2_0 = A_AT(0, 0, 0); PRE(30); SB();
  MM(1, 3, 0, ta0_3, tb1_0_0); SB(); ta2_1 = A_AT(0, 0, 1); PRE(31); SB();
  MM(1, 0, 1, ta1_0, tb1_1_1); SB(); ta2_2 = A_AT(0, 0, 2); PRE(32); SB();
  MM(1, 1, 1, ta1_1, tb1_1_1); SB(); ta2_3 = A_AT(0, 0, 3); PRE(33); SB();
  MM(1, 2, 1, ta1_2, tb1_1_1); SB(); tb1_3_1_l = TRD(B_AT(0, 1, 1, 1)); PRE(34); SB();
  MM(1, 3, 1, ta1_3, tb1_1_1); SB(); tb1_3_1 = CAT(tb1_3_1_l, TRD(B_AT(0, 1, 1, 1) + 512)); PRE(35); SB();
  MM(1, 0, 0, ta1_0, tb1_1_0); SB(); ta3_0 = A_AT(0, 1, 0); PRE(36); SB();
  MM(1, 1, 0, ta1_1, tb1_1_0); SB(); ta3_1 = A_AT(0, 1, 1); PRE(37); SB();
  MM(1, 2, 0, ta1_2, tb1_1_0); SB(); ta3_2 = A_AT(0, 1, 2); PRE(38); SB();
  MM(1, 3, 0, ta1_3, tb1_1_0); SB(); ta3_3 = A_AT(0, 1, 3); PRE(39); SB();
  MM(1, 0, 1, ta2_0, tb1_2_1); SB(); tb1_2_0_l = TRD(B_AT(0, 0, 1, 0)); PRE(40); SB();
  MM(1, 1, 1, ta2_1, tb1_2_1); SB(); tb1_2_0 = CAT(tb1_2_0_l, TRD(B_AT(0, 0, 1, 0) + 512)); PRE(41); SB();
  MM(1, 2, 1, ta2_2, tb1_2_1); SB(); tb1_3_0_l = TRD(B_AT(0, 1, 1, 0)); PRE(42); SB();
  MM(1, 3, 1, ta2_3, tb1_2_1); SB(); tb1_3_0 = CAT(tb1_3_0_l, TRD(B_AT(0, 1, 1, 0) + 512)); PRE(43); SB();
  MM(1, 0, 1, ta3_0, tb1_3_1); SB(); tb2_2_1_l = TRD(B_AT(0, 0, 2, 1)); PRE(44); SB();
  MM(1, 1, 1, ta3_1, tb1_3_1); SB(); tb2_2_1 = CAT(tb2_2_1_l, TRD(B_AT(0, 0, 2, 1) + 512)); PRE(45); SB();
  MM(1, 2, 1, ta3_2, tb1_3_1); SB(); tb2_3_1_l = TRD(B_AT(0, 1, 2, 1)); PRE(46); SB();
  MM(1, 3, 1, ta3_3, tb1_3_1); SB(); tb2_3_1 = CAT(tb2_3_1_l, TRD(B_AT(0, 1, 2, 1) + 512)); PRE(47); SB();
  MM(1, 0, 0, ta2_0, tb1_2_0); SB(); tb2_0_0_l = TRD(B_AT(2, 0, 2, 0)); PK(0, 1, 0, 0); PK(0, 1, 0, 1); PK(0, 1, 1, 0); PK(0, 1, 1, 1); PK(0, 1, 0, 2); SB();
  MM(1, 1, 0, ta2_1, tb1_2_0); SB(); tb2_0_0 = CAT(tb2_0_0_l, TRD(B_AT(2, 0, 2, 0) + 512)); PK(0, 1, 0, 3); PK(0, 1, 0, 7); PK(0, 1, 1, 2); SB();
  MM(1, 2, 0, ta2_2, tb1_2_0); SB(); tb2_1_0_l = TRD(B_AT(2, 1, 2, 0)); PK(0, 1, 1, 3); PK(0, 1, 1, 7); PK(0, 1, 0, 4); PK(0, 1, 0, 5); SB();
  MM(1, 3, 0, ta2_3, tb1_2_0); SB(); tb2_1_0 = CAT(tb2_1_0_l, TRD(B_AT(2, 1, 2, 0) + 512)); PK(0, 1, 1, 4); PK(0, 1, 1, 5); PK(0, 1, 0, 6); PK(0, 1, 1, 6); PK(1, 1, 0, 0); SB();
  MM(1, 0, 0, ta3_0, tb1_3_0); SB(); tb2_2_0_l = TRD(B_AT(0, 0, 2, 0)); PK(1, 1, 0, 1); PK(1, 1, 1, 0); PK(1, 1, 1, 1); PK(1, 1, 0, 2); SB();
  MM(1, 1, 0, ta3_1, tb1_3_0); SB(); tb2_2_0 = CAT(tb2_2_0_l, TRD(B_AT(0, 0, 2, 0) + 512)); PK(1, 1, 0, 3); PK(1, 1, 0, 7); PK(1, 1, 1, 2); SB();
  MM(1, 2, 0, ta3_2, tb1_3_0); SB(); tb2_3_0_l = TRD(B_AT(0, 1, 2, 0)); PK(1, 1, 1, 3); PK(1, 1, 1, 7); PK(1, 1, 0, 4); PK(1, 1, 0, 5); SB();
  MM(1, 3, 0, ta3_3, tb1_3_0); SB(); tb2_3_0 = CAT(tb2_3_0_l, TRD(B_AT(0, 1, 2, 0) + 512)); PK(1, 1, 1, 4); PK(1, 1, 1, 5); PK(1, 1, 0, 6); PK(1, 1, 1, 6); SB();
  MM(2, 0, 1, ta0_0, tb2_0_1); SB(); PK(2, 1, 0, 0); PK(2, 1, 0, 1); PK(2, 1, 1, 0); PK(2, 1, 1, 1); SB();
  MM(2, 1, 1, ta0_1, tb2_0_1); SB(); PK(2, 1, 0, 2); PK(2, 1, 0, 3); SB();
  MM(2, 2, 1, ta0_2, tb2_0_1); SB(); PK(2, 1, 0, 7); PK(2, 1, 1, 2); PK(2, 1, 1, 3); SB();
  MM(2, 3, 1, ta0_3, tb2_0_1); SB(); PK(2, 1, 1, 7); PK(2, 1, 0, 4); PK(2, 1, 0, 5); PK(2, 1, 1, 4); SB();
  MM(2, 0, 1, ta1_0, tb2_1_1); SB(); PK(2, 1, 1, 5); PK(2, 1, 0, 6); PK(2, 1, 1, 6); PK(3, 1, 0, 0); SB();
  MM(2, 1, 1, ta1_1, tb2_1_1); SB(); PK(3, 1, 0, 1); PK(3, 1, 1, 0); PK(3, 1, 1, 1); PK(3, 1, 0, 2); SB();
  MM(2, 2, 1, ta1_2, tb2_1_1); SB(); PK(3, 1, 0, 3); PK(3, 1, 0, 7); PK(3, 1, 1, 2); SB();
  MM(2, 3, 1, ta1_3, tb2_1_1); SB(); PK(3, 1, 1, 3); PK(3, 1, 1, 7); PK(3, 1, 0, 4); SB();
  MM(2, 0, 1, ta2_0, tb2_2_1); SB(); PK(3, 1, 0, 5); PK(3, 1, 1, 4); PK(3, 1, 1, 5); PK(3, 1, 0, 6); SB();
  MM(2, 1, 1, ta2_1, tb2_2_1); SB(); PK(3, 1, 1, 6); PK(0, 0, 0, 0); PK(0, 0, 0, 1); PK(0, 0, 1, 0); SB();
  MM(2, 2, 1, ta2_2, tb2_2_1); SB(); PK(0, 0, 1, 1); PK(0, 0, 0, 2); PK(0, 0, 0, 3); SB();
  MM(2, 3, 1, ta2_3, tb2_2_1); SB(); PK(0, 0, 0, 7); PK(0, 0, 1, 2); PK(0, 0, 1, 3); SB();
  MM(2, 0, 1, ta3_0, tb2_3_1); SB(); PK(0, 0, 1, 7); PK(0, 0, 0, 4); PK(0, 0, 0, 5); PK(0, 0, 1, 4); SB();
  MM(2, 1, 1, ta3_1, tb2_3_1); SB(); PK(0, 0, 1, 5); PK(0, 0, 0, 6); PK(0, 0, 1, 6); PK(1, 0, 0, 0); SB();
  MM(2, 2, 1, ta3_2, tb2_3_1); SB(); PK(1, 0, 0, 1); PK(1, 0, 1, 0); PK(1, 0, 1, 1); PK(1, 0, 0, 2); SB();
  MM(2, 3, 1, ta3_3, tb2_3_1); SB(); PK(1, 0, 0, 3); PK(1, 0, 0, 7); PK(1, 0, 1, 2); SB();
  MM(2, 0, 0, ta0_0, tb2_0_0); SB(); PK(1, 0, 1, 3); PK(1, 0, 1, 7); PK(1, 0, 0, 4); SB();
  MM(2, 1, 0, ta0_1, tb2_0_0); SB(); PK(1, 0, 0, 5); PK(1, 0, 1, 4); PK(1, 0, 1, 5); PK(1, 0, 0, 6); SB();
  MM(2, 2, 0, ta0_2, tb2_0_0); SB(); PK(1, 0, 1, 6); PK(2, 0, 0, 0); PK(2, 0, 0, 1); PK(2, 0, 1, 0); SB();
  MM(2, 3, 0, ta0_3, tb2_0_0); SB(); PK(2, 0, 1, 1); PK(2, 0, 0, 2); PK(2, 0, 0, 3); SB();
  MM(2, 0, 0, ta1_0, tb2_1_0); SB(); PK(2, 0, 0, 7); PK(2, 0, 1, 2); PK(2, 0, 1, 3); SB();
  MM(2, 1, 0, ta1_1, tb2_1_0); SB(); PK(2, 0, 1, 7); PK(2, 0, 0, 4); PK(2, 0, 0, 5); PK(2, 0, 1, 4); SB();
  MM(2, 2, 0, ta1_2, tb2_1_0); SB(); PK(2, 0, 1, 5); PK(2, 0, 0, 6); PK(2, 0, 1, 6); PK(3, 0, 0, 0); SB();
  MM(2, 3, 0, ta1_3, tb2_1_0); SB(); PK(3, 0, 0, 1); PK(3, 0, 1, 0); PK(3, 0, 1, 1); SB();
  MM(2, 0, 0, ta2_0, tb2_2_0); SB(); PK(3, 0, 0, 2); PK(3, 0, 0, 3); SB();
  MM(2, 1, 0, ta2_1, tb2_2_0); SB(); PK(3, 0, 0, 7); PK(3, 0, 1, 2); SB();
  MM(2, 2, 0, ta2_2, tb2_2_0); SB(); PK(3, 0, 1, 3); PK(3, 0, 1, 7); SB();
  MM(2, 3, 0, ta2_3, tb2_2_0); SB(); PK(3, 0, 0, 4); PK(3, 0, 0, 5); PK(3, 0, 1, 4); SB();
  MM(2, 0, 0, ta3_0, tb2_3_0); SB(); PK(3, 0, 1, 5); PK(3, 0, 0, 6); PK(3, 0, 1, 6); SB();
  MM(2, 1, 0, ta3_1, tb2_3_0); SB(); CV(0, 1, 0); CV(0, 1, 1); CV(1, 1, 0); SB();
  MM(2, 2, 0, ta3_2, tb2_3_0); SB(); CV(1, 1, 1); CV(2, 1, 0); CV(2, 1, 1); CV(3, 1, 0); SB();
  MM(2, 3, 0, ta3_3, tb2_3_0); SB(); CV(3, 1, 1); SB();
  const f32x4 zero = {0.f, 0.f, 0.f, 0.f};
#define TAKE(flag, READS)                                                                                  \
  _Pragma("unroll 1") for (unsigned spins = 0; spins < (1u << 22); ++spins) {                              \
    const unsigned f_ = (flag);                                                                            \
    CFENCE();                                                                                              \
    READS;                                                                                                 \
    if (__builtin_expect(rfl(f_) != 0u, 1)) break;                                                         \
  }
  unsigned lidx, ltag;
  {
    phi_fix(kp, kmin);
#define KF(j, n) FRAG(((u32x4){kp[8 * (n) + 4 * (j)], kp[8 * (n) + 4 * (j) + 1], kp[8 * (n) + 4 * (j) + 2], kp[8 * (n) + 4 * (j) + 3]}))
#define VF(j, n) FRAG(((u32x4){vp[8 * (n) + 4 * (j)], vp[8 * (n) + 4 * (j) + 1], vp[8 * (n) + 4 * (j) + 2], vp[8 * (n) + 4 * (j) + 3]}))
    f32x4 s1 = MFMA(KF(0, 1), VF(0, 1), zero), s0 = MFMA(KF(0, 0), VF(0, 1), zero), zs = MFMA(KF(0, 1), FRAG(ones), zero);
    s1 = MFMA(KF(1, 1), VF(1, 1), s1); s0 = MFMA(KF(1, 0), VF(1, 1), s0); zs = MFMA(KF(1, 1), FRAG(ones), zs);
    f32x4 z = MFMA(KF(1, 0), FRAG(ones), MFMA(KF(0, 0), FRAG(ones), zero));
    SB();
    CV(0, 0, 0); CV(0, 0, 1); CV(1, 0, 0); CV(1, 0, 1);
    *LP(f32x4, x1w, 0) = s1; *LP(f32x4, x1w, 1024) = s0; *LP(f32x4, cw, 0) = zs;
    CFENCE();
    *LP(volatile unsigned, cw, W_X1) = one;
    SB();
    CV(2, 0, 0); CV(2, 0, 1); CV(3, 0, 0); CV(3, 0, 1);
    f32x4 kv0 = MFMA(KF(0, 0), VF(0, 0), zero), kv1 = MFMA(KF(0, 1), VF(0, 0), zero);
    kv0 = MFMA(KF(1, 0), VF(1, 0), kv0); kv1 = MFMA(KF(1, 1), VF(1, 0), kv1);
    SB();
    f32x4 p0, p1, pz;
#define X1_RD { p0 = *LP(volatile f32x4, x1r, 0); p1 = *LP(volatile f32x4, x1r, 1024); pz = *LP(volatile f32x4, cr, 0); }
    const unsigned f1 = *LP(volatile unsigned, cr, W_X1);
    CFENCE();
    X1_RD
    SB();
    tb0_0_0_l = TRD(B_AT(2, 0, 0, 0)); tb0_0_0 = CAT(tb0_0_0_l, TRD(B_AT(2, 0, 0, 0) + 512));
    tb0_0_1_l = TRD(B_AT(2, 0, 0, 1)); tb0_0_1 = CAT(tb0_0_1_l, TRD(B_AT(2, 0, 0, 1) + 512));
    tb0_1_0_l = TRD(B_AT(2, 1, 0, 0)); tb0_1_0 = CAT(tb0_1_0_l, TRD(B_AT(2, 1, 0, 0) + 512));
    tb0_1_1_l = TRD(B_AT(2, 1, 0, 1)); tb0_1_1 = CAT(tb0_1_1_l, TRD(B_AT(2, 1, 0, 1) + 512));
    tb0_2_0_l = TRD(B_AT(0, 0, 0, 0)); tb0_2_0 = CAT(tb0_2_0_l, TRD(B_AT(0, 0, 0, 0) + 512));
    tb0_2_1_l = TRD(B_AT(0, 0, 0, 1)); tb0_2_1 = CAT(tb0_2_1_l, TRD(B_AT(0, 0, 0, 1) + 512));
    tb0_3_0_l = TRD(B_AT(0, 1, 0, 0)); tb0_3_0 = CAT(tb0_3_0_l, TRD(B_AT(0, 1, 0, 0) + 512));
    tb0_3_1_l = TRD(B_AT(0, 1, 0, 1)); tb0_3_1 = CAT(tb0_3_1_l, TRD(B_AT(0, 1, 0, 1) + 512));
    SB();
    if (__builtin_expect(!all_true(f1 != 0u), 0)) { TAKE(*LP(volatile unsigned, cr, W_X1), X1_RD); LGKM0(); }
    const f32x4 m0_ = kv0 + p0, m1_ = kv1 + p1;
    float am = fmax_raw(fabsf(m0_[0]), fabsf(m1_[0]));
#pragma unroll
    for (int e = 1; e < 4; ++e) am = fmax_raw(fmax_raw(am, fabsf(m0_[e])), fabsf(m1_[e]));
#define PKH(a_, b_) __builtin_bit_cast(unsigned, __builtin_convertvector((f32x2){(a_), (b_)}, f16x2_t))
    u32x4 hv = {PKH(m0_[0], m0_[1]), PKH(m0_[2], m0_[3]), PKH(m1_[0], m1_[1]), PKH(m1_[2], m1_[3])};
#define Z_STORE(scl) do { unsigned z0_ = zv[0]; asm("v_writelane_b32 %0, %1, 1" : "+v"(z0_) : "s"(scl)); zv[0] = z0_; __builtin_amdgcn_raw_buffer_store_b128(zv, wsr, zo, 0, 16); } while (0)
    PIN(hv);
    if (__builtin_expect(all_true(f2u(am) - 0x3C800000u <= 0x47000000u - 0x3C800000u), 1)) {
      __builtin_amdgcn_raw_buffer_store_b128(hv, wsr, kvo, 0, 16);
      SB();
      u32x4 zv = __builtin_bit_cast(u32x4, z + pz);
      Z_STORE(0x3F800000u);
    } else {
      unsigned ex = max(wave_max(f2u(am) & 0x7FFFFFFFu) >> 23, 15u);
      if (ex >= 121u && ex < 142u) ex = 141u;
      const float inv = u2f((268u - ex) << 23);
      u32x4 hs = {PKH(m0_[0] * inv, m0_[1] * inv), PKH(m0_[2] * inv, m0_[3] * inv), PKH(m1_[0] * inv, m1_[1] * inv), PKH(m1_[2] * inv, m1_[3] * inv)};
      u32x4 zv = __builtin_bit_cast(u32x4, z + pz);
#pragma unroll
      for (int e = 0; e < 4; ++e) { hs[e] = min(hs[e], ~1u); zv[e] = min(zv[e], ~1u); }
      CFENCE();
      __builtin_amdgcn_raw_buffer_store_b128(hs, wsr, kvo, 0, 16);
      Z_STORE((ex - 14u) << 23);
      CFENCE();
    }
  }
  *LP(volatile unsigned, cw, W_KS) = one;
  unsigned ko, oo, x2w, zfa, zfb, zfp, gta;
  u32x2 eye, ov[4][2];
  u32x4 t[16];
  float gq[4];
  unsigned pq[16];
#define PQ(mi, n, k, st) PHI(acc[0][mi][n], pq[4 * (mi) + 2 * (n) + (k)], qmin, k, st)
#define NOPQ() asm volatile("s_nop 4" : "+v"(acc[0][3][0]), "+v"(acc[0][3][1]))
#define PRQ(i)                                                                                             \
  do {                                                                                                     \
    if ((i) == 0) { lidx = misc[C_IDX + wr]; ltag = misc[C_TAG]; }                                         \
    if ((i) == 2) { zfp = L_CTL + 4 * (C_ZF + hh); PIN(zfp); }                                             \
    if ((i) == 3) { ko = kvo - mt * 1024; PIN(ko); }                                                       \
    if ((i) == 4) { gta = L_GATE + wr * 256 + c16 * 4; PIN(gta); }                                         \
    if ((i) == 5) { zfa = L_ZF + hh * 64 + wr * 8 + g * 16; PIN(zfa); }                                    \
    if ((i) == 6) { zfb = zfa ^ 8u; PIN(zfb); }                                                            \
    if ((i) == 7) { const unsigned o_ = 0x3C00u << (16 * (lane & 1)); eye = (u32x2){(lane & 2) ? 0u : o_, (lane & 2) ? o_ : 0u}; PIN(eye); } \
    if ((i) == 8) { x2w = L_X2 + w * 1024 + lane * 16; PIN(x2w); }                                         \
    if ((i) == 9) { oo = (unsigned)OFF_OF + (unsigned)(((mt * 8 + wr * 4) * NH + head) * 1024) + lane * 16; PIN(oo); } \
  } while (0)
#define SHOT_GATE                                                                                          \
  do {                                                                                                     \
    if (__builtin_expect(!all_true(lidx != 0u), 0)) (void)wait_ge<1>(misc + C_IDX + wr, 1u);        \
    (void)wait_ge<1>(misc + C_GO + hh, 1u);                                                                \
    CFENCE();                                                                                              \
    _Pragma("unroll") for (int i = 0; i < 4; ++i) gq[i] = *LP(volatile float, gta, i * 64);                \
  } while (0)
#define T_LD(i) t[i] = __builtin_amdgcn_raw_buffer_load_b128(wsr, ko + ((i) & 3) * 1024, ((i) >> 2) * 4096, 16)
  SB();
  MM(0, 0, 0, ta0_0, tb0_0_0); SB(); PRQ(0); SB();
  MM(0, 0, 1, ta0_0, tb0_0_1); SB(); PRQ(1); SB();
  MM(0, 0, 0, ta1_0, tb0_1_0); SB(); PRQ(2); SB();
  MM(0, 0, 1, ta1_0, tb0_1_1); SB(); PRQ(3); SB();
  MM(0, 0, 0, ta2_0, tb0_2_0); SB(); PRQ(4); SB();
  MM(0, 0, 1, ta2_0, tb0_2_1); SB(); PRQ(5); SB();
  MM(0, 0, 0, ta3_0, tb0_3_0); SB(); PRQ(6); SB();
  MM(0, 0, 1, ta3_0, tb0_3_1); SB(); PRQ(7); SB();
  MM(0, 1, 0, ta0_1, tb0_0_0); SB(); PRQ(8); SB();
  MM(0, 1, 1, ta0_1, tb0_0_1); SB(); PRQ(9); SB();
  MM(0, 1, 0, ta1_1, tb0_1_0); SB(); PQ(0, 0, 0, 0); PQ(0, 0, 0, 1); PQ(0, 0, 1, 0); SB();
  MM(0, 1, 1, ta1_1, tb0_1_1); SB(); PQ(0, 0, 1, 1); PQ(0, 0, 0, 2); SB();
  MM(0, 1, 0, ta2_1, tb0_2_0); SB(); PQ(0, 0, 0, 3); PQ(0, 0, 0, 7); SB();
  MM(0, 1, 1, ta2_1, tb0_2_1); SB(); PQ(0, 0, 1, 2); PQ(0, 0, 1, 3); SB();
  MM(0, 1, 0, ta3_1, tb0_3_0); SB(); PQ(0, 0, 1, 7); PQ(0, 0, 0, 4); PQ(0, 0, 0, 5); SB();
  MM(0, 1, 1, ta3_1, tb0_3_1); SB(); PQ(0, 0, 1, 4); PQ(0, 0, 1, 5); PQ(0, 0, 0, 6); SB();
  MM(0, 2, 0, ta0_2, tb0_0_0); SB(); PQ(0, 0, 1, 6); PQ(0, 1, 0, 0); PQ(0, 1, 0, 1); SB();
  MM(0, 2, 1, ta0_2, tb0_0_1); SB(); PQ(0, 1, 1, 0); PQ(0, 1, 1, 1); PQ(0, 1, 0, 2); SB();
  MM(0, 2, 0, ta1_2, tb0_1_0); SB(); PQ(0, 1, 0, 3); PQ(0, 1, 0, 7); SB();
  MM(0, 2, 1, ta1_2, tb0_1_1); SB(); PQ(0, 1, 1, 2); PQ(0, 1, 1, 3); SB();
  MM(0, 2, 0, ta2_2, tb0_2_0); SB(); PQ(0, 1, 1, 7); PQ(0, 1, 0, 4); PQ(0, 1, 0, 5); SB();
  MM(0, 2, 1, ta2_2, tb0_2_1); SB(); PQ(0, 1, 1, 4); PQ(0, 1, 1, 5); PQ(0, 1, 0, 6); SB();
  MM(0, 2, 0, ta3_2, tb0_3_0); SB(); PQ(0, 1, 1, 6); PQ(1, 0, 0, 0); PQ(1, 0, 0, 1); SB();
  MM(0, 2, 1, ta3_2, tb0_3_1); SB(); PQ(1, 0, 1, 0); PQ(1, 0, 1, 1); PQ(1, 0, 0, 2); SB();
  MM(0, 3, 0, ta0_3, tb0_0_0); SB(); PQ(1, 0, 0, 3); PQ(1, 0, 0, 7); SB();
  MM(0, 3, 1, ta0_3, tb0_0_1); SB(); PQ(1, 0, 1, 2); PQ(1, 0, 1, 3); SB();
  MM(0, 3, 0, ta1_3, tb0_1_0); SB(); PQ(1, 0, 1, 7); PQ(1, 0, 0, 4); PQ(1, 0, 0, 5); SB();
  MM(0, 3, 1, ta1_3, tb0_1_1); SB(); PQ(1, 0, 1, 4); PQ(1, 0, 1, 5); PQ(1, 0, 0, 6); SB();
  MM(0, 3, 0, ta2_3, tb0_2_0); SB(); PQ(1, 0, 1, 6); PQ(1, 1, 0, 0); PQ(1, 1, 0, 1); SB();
  MM(0, 3, 1, ta2_3, tb0_2_1); SB(); PQ(1, 1, 1, 0); PQ(1, 1, 1, 1); PQ(1, 1, 0, 2); SB();
  MM(0, 3, 0, ta3_3, tb0_3_0); SB(); PQ(1, 1, 0, 3); PQ(1, 1, 0, 7); SB();
  MM(0, 3, 1, ta3_3, tb0_3_1); SB(); PQ(1, 1, 1, 2); PQ(1, 1, 1, 3); SB();
  PQ(1, 1, 1, 7); SB();
  PQ(1, 1, 0, 4); SB();
  PQ(1, 1, 0, 5); SB();
  PQ(1, 1, 1, 4); SB();
  PQ(1, 1, 1, 5); SB();
  PQ(1, 1, 0, 6); SB();
  PQ(1, 1, 1, 6); SB();
  PQ(2, 0, 0, 0); SB();
  PQ(2, 0, 0, 1); SB();
  PQ(2, 0, 1, 0); SB();
  PQ(2, 0, 1, 1); SB();
  PQ(2, 0, 0, 2); SB();
  PQ(2, 0, 0, 3); SB();
  PQ(2, 0, 0, 7); SB();
  PQ(2, 0, 1, 2); SB();
  PQ(2, 0, 1, 3); SB();
  PQ(2, 0, 1, 7); SB();
  PQ(2, 0, 0, 4); SB();
  PQ(2, 0, 0, 5); SB();
  PQ(2, 0, 1, 4); SB();
  PQ(2, 0, 1, 5); SB();
  PQ(2, 0, 0, 6); SB();
  PQ(2, 0, 1, 6); SB();
  PQ(2, 1, 0, 0); SB();
  PQ(2, 1, 0, 1); SB();
  PQ(2, 1, 1, 0); SB();
  PQ(2, 1, 1, 1); SB();
  PQ(2, 1, 0, 2); SB();
  PQ(2, 1, 0, 3); SB();
  PQ(2, 1, 0, 7); SB();
  PQ(2, 1, 1, 2); SB();
  PQ(2, 1, 1, 3); SB();
  PQ(2, 1, 1, 7); SB();
  PQ(2, 1, 0, 4); SB();
  PQ(2, 1, 0, 5); SB();
  PQ(2, 1, 1, 4); SB();
  PQ(2, 1, 1, 5); SB();
  PQ(2, 1, 0, 6); SB();
  PQ(2, 1, 1, 6); SB();
  NOPQ();
  PQ(3, 0, 0, 0); SB();
  PQ(3, 0, 0, 1); SB();
  PQ(3, 0, 1, 0); SB();
  PQ(3, 0, 1, 1); SB();
  PQ(3, 0, 0, 2); SB();
  PQ(3, 0, 0, 3); SB();
  PQ(3, 0, 0, 7); SB();
  PQ(3, 0, 1, 2); SB();
  PQ(3, 0, 1, 3); SB();
  PQ(3, 0, 1, 7); SB();
  PQ(3, 0, 0, 4); SB();
  PQ(3, 0, 0, 5); SB();
  PQ(3, 0, 1, 4); SB();
  PQ(3, 0, 1, 5); SB();
  PQ(3, 0, 0, 6); SB();
  PQ(3, 0, 1, 6); SB();
  PQ(3, 1, 0, 0); SB();
  PQ(3, 1, 0, 1); SB();
  PQ(3, 1, 1, 0); SB();
  PQ(3, 1, 1, 1); SB();
  PQ(3, 1, 0, 2); SB();
  PQ(3, 1, 0, 3); SB();
  PQ(3, 1, 0, 7); SB();
  PQ(3, 1, 1, 2); SB();
  PQ(3, 1, 1, 3); SB();
  PQ(3, 1, 1, 7); SB();
  PQ(3, 1, 0, 4); SB();
  PQ(3, 1, 0, 5); SB();
  PQ(3, 1, 1, 4); SB();
  PQ(3, 1, 1, 5); SB();
  PQ(3, 1, 0, 6); SB();
  PQ(3, 1, 1, 6); SB();
  SHOT_GATE;
  T_LD(0);
  T_LD(1);
  T_LD(2);
  T_LD(3);
  T_LD(4);
  T_LD(5);
  T_LD(6);
  T_LD(7);
  T_LD(8);
  T_LD(9);
  T_LD(10);
  T_LD(11);
  T_LD(12);
  T_LD(13);
  T_LD(14);
  T_LD(15);
  *LP(volatile unsigned, cw, W_AG) = one;
  phi_fix(pq, qmin);
  const unsigned tag = rfl(ltag);
  {
#define PF(mi) FRAG(((u32x4){pq[4 * (mi)], pq[4 * (mi) + 1], pq[4 * (mi) + 2], pq[4 * (mi) + 3]}))
#define H4(v, k) __builtin_bit_cast(f16x4_t, (u32x2){(v)[2 * (k)], (v)[2 * (k) + 1]})
    f32x4 s0 = zero, s1 = zero;
#define KV_MM(i0, i1)                                                                                      \
  _Pragma("unroll") for (int i = (i0); i < (i1); ++i) {                                                    \
    s0 = __builtin_amdgcn_mfma_f32_4x4x4f16(__builtin_bit_cast(f16x4_t, eye), H4(t[i], 0), s0, 0, 0, 0);   \
    s1 = __builtin_amdgcn_mfma_f32_4x4x4f16(__builtin_bit_cast(f16x4_t, eye), H4(t[i], 1), s1, 0, 0, 0);   \
  }
    float rc[4];
    u32x2 z0, z1;
#define Z_RD { z0 = *LP(volatile u32x2, zfa, 0); z1 = *LP(volatile u32x2, zfb, 0); }
    f32x4 d[4];
#define DEN1() do { const u32x4 zf = {z0[0], z0[1], z1[0], z1[1]}; _Pragma("unroll") for (int mi = 0; mi < 4; ++mi) d[mi] = MFMA(FRAG(zf), PF(mi), zero); } while (0)
#define DEN2()                                                                                             \
  do {                                                                                                     \
    _Pragma("unroll") for (int mi = 0; mi < 4; ++mi) rc[mi] = __builtin_amdgcn_rcpf(d[mi][0] + 1e-6f);     \
    _Pragma("unroll") for (int mi = 0; mi < 4; ++mi) { rc[mi] *= gq[mi]; PIN(rc[mi]); }                    \
  } while (0)
    KV_MM(0, DENAT - 4);
    SB();
    unsigned zfl = *LP(volatile unsigned, zfp, 0);
    CFENCE();
    Z_RD
    SB();
    KV_MM(DENAT - 4, DENAT);
    SB();
    DEN1();
    SB();
    KV_MM(DENAT, DENAT + 2);
    SB();
    DEN2();
    const u64 zbad = __builtin_amdgcn_ballot_w64(zfl != 1u);
    SB();
    KV_MM(DENAT + 2, 16);
    if (__builtin_expect((__builtin_amdgcn_ballot_w64(__builtin_isunordered(s0[0], s1[0])) | zbad) != 0ull, 0)) {
      unsigned todo = 15u;
#pragma unroll 1
      for (unsigned spins = 0; spins <= SPIN_CAP; ++spins) {
        unsigned miss = 0;
#pragma unroll
        for (int q = 0; q < 4; ++q)
          if (todo & (1u << q)) { if (!all_true(max(max(MX4(t[4 * q]), MX4(t[4 * q + 1])), max(MX4(t[4 * q + 2]), MX4(t[4 * q + 3]))) != ~0u)) miss |= 1u << q; }
        todo = miss;
        if (todo == 0u) break;
        __builtin_amdgcn_s_sleep(2);
        CFENCE();
#pragma unroll
        for (int q = 0; q < 4; ++q)
          if (todo & (1u << q)) { T_LD(4 * q); T_LD(4 * q + 1); T_LD(4 * q + 2); T_LD(4 * q + 3); }
      }
      s0 = zero; s1 = zero;
      KV_MM(0, 16);
      zfl = rfl(zfl);
      if (zfl == 0u) { TAKE(zfl = misc[C_ZF + hh], Z_RD); zfl = rfl(zfl); DEN1(); DEN2(); }
      if (zfl != 1u) {
        const unsigned sc = __builtin_amdgcn_raw_buffer_load_b32(wsr, (unsigned)OFF_ZP + (rec - mt + c16) * 128 + 64, 0, 16);
        s0 = zero; s1 = zero;
#pragma unroll
        for (int i = 0; i < 16; ++i) {
          const float s_ = u2f((unsigned)__builtin_amdgcn_readlane((int)sc, i));
#pragma unroll
          for (int e = 0; e < 4; ++e) {
            const unsigned tw = t[i][e];
            const f16x2_t h = __builtin_bit_cast(f16x2_t, tw);
            f32x4& d = e < 2 ? s0 : s1;
            d[2 * (e & 1)] = __builtin_fmaf((float)h[0], s_, d[2 * (e & 1)]); d[2 * (e & 1) + 1] = __builtin_fmaf((float)h[1], s_, d[2 * (e & 1) + 1]);
          }
        }
      }
    }
    const u32x4 kown = {cvt_pk(s0[0], s0[1]), cvt_pk(s0[2], s0[3]), cvt_pk(s1[0], s1[1]), cvt_pk(s1[2], s1[3])};
    *LP(u32x2, x2w, 8) = (u32x2){kown[0], kown[1]}; *LP(u32x2, x2w, 0) = (u32x2){kown[2], kown[3]};
    CFENCE();
    *LP(volatile unsigned, cw, W_X2) = one;
    SB();
    f32x4 o[4];
    u32x4 koth;
#pragma unroll
    for (int mi = 0; mi < 4; ++mi) o[mi] = MFMA(FRAG(kown), PF(mi), zero);
    const unsigned x2r = x2w ^ 2048u;
    SB();
#define O_CVT(mi, h) do { ov[mi][h] = (u32x2){cvt_pk(o[mi][0] * rc[mi], o[mi][1] * rc[mi]), cvt_pk(o[mi][2] * rc[mi], o[mi][3] * rc[mi])}; if ((h) == 0) PIN(ov[mi][0]); } while (0)
    O_CVT(0, 0); SB();
    const unsigned f2 = *LP(volatile unsigned, cr, W_X2);
    CFENCE();
    koth = *LP(volatile u32x4, x2r, 0);
    SB();
    O_CVT(1, 0); O_CVT(2, 0); O_CVT(3, 0);
    SB();
    if (__builtin_expect(!all_true(f2 != 0u), 0)) TAKE(*LP(volatile unsigned, cr, W_X2), koth = *LP(volatile u32x4, x2r, 0));
#pragma unroll
    for (int mi = 0; mi < 4; ++mi) o[mi] = MFMA(FRAG(koth), PF(mi), zero);
    SB();
    unsigned oom[4];
#pragma unroll
    for (int mi = 0; mi < 4; ++mi) { oom[mi] = oo + mi * (NH * 1024); PIN(oom[mi]); }
    SB();
#pragma unroll
    for (int mi = 0; mi < 4; ++mi) { O_CVT(mi, 1); __builtin_amdgcn_raw_buffer_store_b128((u32x4){ov[mi][0][0], ov[mi][0][1], ov[mi][1][0], ov[mi][1][1]}, wsr, oom[mi], 0, 16); SB(); }
  }
  SB();
  f32x4 oc[4][2];
  unsigned xa[4], xb[2], so[4];
  unsigned rlo, rhi;
  {
    u32x2 xl[8];
#pragma unroll
    for (int n = 0; n < 4; ++n) { xa[n] = (unsigned)(((32 * w + c16) * 128 + (((g >> 1) ^ (c16 >> 1)) * 16) + (g & 1) * 8) ^ (n * 32)); PIN(xa[n]); }
#pragma unroll
    for (int i = 0; i < 8; ++i) xl[i] = *LP(volatile u32x2, xa[i & 3], (i >> 2) * 2048);
    const unsigned kw = w * 4096 + lane * 16, wlo = hh ? x1w : kw, whi = hh ? kw : x1w;
    rlo = hh ? x1w ^ 2048u : kw; rhi = hh ? kw : x1w ^ 2048u;
#pragma unroll
    for (int mi = 0; mi < 4; ++mi) *LP(volatile u32x4, mi < 2 ? wlo : whi, (mi & 1) * 1024) = (u32x4){ov[mi][0][0], ov[mi][0][1], ov[mi][1][0], ov[mi][1][1]};
    *LP(volatile unsigned, cw, W_X1) = 2u;
#pragma unroll
    for (int i = 0; i < 8; ++i) {
      const int tt = i >> 2;
      const float omg = bfr(1.0f - (tt ? (hh ? gq[3] : gq[1]) : (hh ? gq[2] : gq[0])));
#pragma unroll
      for (int k = 0; k < 2; ++k) {
        const unsigned pr = cvt_pk(omg * blo(xl[i][k]), omg * bhi(xl[i][k]));
        oc[i & 3][tt][2 * k] = blo(pr); oc[i & 3][tt][2 * k + 1] = bhi(pr);
      }
      PIN(oc[i & 3][tt]);
    }
#pragma unroll
    for (int i = 0; i < 2; ++i) { xb[i] = (unsigned)((32 * w + 8 * i + (lane >> 3)) * 128 + (((lane & 7) ^ (4 * i + g)) * 16)); PIN(xb[i]); }
#pragma unroll
    for (int i = 0; i < 4; ++i) { so[i] = (unsigned)(((m0 + 32 * w + 8 * i + (lane >> 3)) * DM + 64 * nt3) * 2 + (lane & 7) * 16); PIN(so[i]); }
  }
  int wof[2][2][4];
#pragma unroll
  for (int n = 0; n < 16; ++n) {
    wof[n >> 3][(n >> 2) & 1][n & 3] = 16384 + ((n >> 2) & 1) * 65536 + (((n >> 3) ^ wr) * 16 + 4 * g + (c16 >> 2)) * 128 + (((n & 3) ^ fsw) * 32) + (c16 & 3) * 8;
    PIN(wof[n >> 3][(n >> 2) & 1][n & 3]);
  }
  const unsigned obase = (unsigned)OFF_OF + (unsigned)(((mt3 * 8 + 2 * w) * NH) * 1024 + lane * 16);
  bf16x8 wf[2][4];
#define W_AT(hd, n, r) (smem + wof[r][WOPOS((hd) >> 1) >> 3][n] + (WOPOS((hd) >> 1) & 7) * 8192 + ((hd) & 1) * 4096)
  const unsigned lw = min(misc[C_WO], misc[C_WO + 1]);
  WAIT_VM(0);
  if (lane == 0) {
    const unsigned old = __hip_atomic_fetch_add((lu32*)(smem + L_CTL) + C_OARR, 1u, RLX_WG);
    if (old == 3u) {
      __hip_atomic_store((gu32*)(ws + OFF_OFLAG + (mt * 16 + nt) * 128), tag, RLX_AGENT);
      misc[C_T6] = (unsigned)__builtin_amdgcn_s_memrealtime() | 1u;
    }
  }
  CFENCE();

  if (__builtin_expect(rfl(lw) == 0u, 0)) { (void)wait_ge<1>(misc + C_WO, 1u); (void)wait_ge<1>(misc + C_WO + 1, 1u); }
  CFENCE();
#pragma unroll
  for (int n = 0; n < 4; ++n) wf[0][n] = CAT(TRD(W_AT(0, n, 0)), TRD(W_AT(0, n, 1)));
  u32x4 of[OPF + 1][4];
  unsigned rdy = 0;
#define O_READY(c) do { if (__builtin_expect(rdy <= (unsigned)(c), 0)) rdy = wait_ge<1>(misc + C_ORDY, (unsigned)(c) + 1u); CFENCE(); } while (0)
#define O_LD1(c, k4) of[(c) % (OPF + 1)][k4] = __builtin_amdgcn_raw_buffer_load_b128(wsr, obase + ((k4) >> 1) * 1024 + ((k4) & 1) * (NH * 1024), CH(c) * 2048, 16)
  TAKE(*LP(volatile unsigned, cw ^ 128u, W_X1) >> 1, { _Pragma("unroll") for (int k = 0; k < 4; ++k) of[0][k] = *LP(volatile u32x4, k < 2 ? rlo : rhi, (k & 1) * 1024); });
  unsigned li = 0;
#define P3_MM(c, j)                                                                                        \
  do {                                                                                                     \
    constexpr int hd = 2 * (c) + ((j) >> 3), n = ((j) >> 1) & 3;                                           \
    AMFMA(oc[n][(j) & 1], wf[(j) >> 3][n], of[(c) % (OPF + 1)][2 * ((j) >> 3) + ((j) & 1)]); SB();         \
    if (hd + 1 < 32) {                                                                                     \
      if (((j) & 1) == 0) tlo = TRD(W_AT((hd + 1) & 31, n, 0)); else wf[1 - ((j) >> 3)][n] = CAT(tlo, TRD(W_AT((hd + 1) & 31, n, 1))); \
    }                                                                                                      \
    if ((c) > 0 && (c) + OPF < 16) {                                                                       \
      if ((j) == 0) O_READY((c) + OPF);                                                                    \
      if (((j) & 3) == 1) O_LD1((c) + OPF, (j) >> 2);                                                      \
    }                                                                                                      \
    if ((c) == 15 && (j) == 8) li = misc[C_IDX];                                                           \
    SB();                                                                                                  \
  } while (0)
#define P3_4(c, j) P3_MM(c, j); P3_MM(c, (j) + 1); P3_MM(c, (j) + 2); P3_MM(c, (j) + 3)
#define P3_C(c) P3_4(c, 0); P3_4(c, 4); P3_4(c, 8); P3_4(c, 12)
  P3_C(0);
#pragma unroll
  for (int c = 1; c <= OPF; ++c) { O_READY(c); O_LD1(c, 0); O_LD1(c, 1); O_LD1(c, 2); O_LD1(c, 3); }
  SB();
  P3_C(1); P3_C(2); P3_C(3); P3_C(4); P3_C(5); P3_C(6); P3_C(7);
  P3_C(8); P3_C(9); P3_C(10); P3_C(11); P3_C(12); P3_C(13); P3_C(14); P3_C(15);
  NOP8(oc);
  li = rfl(li);
  if (__builtin_expect(li == 0u, 0)) li = wait_ge<1>(misc + C_IDX, 1u);
  const int idx = (int)li - 1;
  if ((idx >> 5) == mt3 * 4 + w) {
    f32x4 dl[4];
    TAKE(misc[C_Y], { _Pragma("unroll") for (int n = 0; n < 4; ++n) dl[n] = *(volatile lf32x4*)(smem + L_RED + 2048 + n * 64 + g * 16); });
    const bool mine = c16 == (idx & 15);
    if (idx & 16) {
#pragma unroll
      for (int n = 0; n < 4; ++n) oc[n][1] += mine ? dl[n] : zero;
    } else {
#pragma unroll
      for (int n = 0; n < 4; ++n) oc[n][0] += mine ? dl[n] : zero;
    }
  }
  u32x4 ot[4];
#pragma unroll
  for (int tt = 0; tt < 2; ++tt) {
#pragma unroll
    for (int n = 0; n < 4; ++n) *LP(volatile u32x2, xa[n], tt * 2048) = (u32x2){cvt_pk(oc[n][tt][0], oc[n][tt][1]), cvt_pk(oc[n][tt][2], oc[n][tt][3])};
    ot[2 * tt] = *LP(volatile u32x4, xb[0], tt * 2048); ot[2 * tt + 1] = *LP(volatile u32x4, xb[1], tt * 2048);
  }
#pragma unroll
  for (int i = 0; i < 4; ++i) __builtin_amdgcn_raw_buffer_store_b128(ot[i], MKRSRC(out, SEQ * DM * 2), so[i], 0, 17);
}

extern "C" void kernel_launch(void* const* d_in, const int* in_sizes, int n_in,
                              void* d_out, int out_size, void* d_ws, size_t ws_size,
                              hipStream_t stream) {
  char* ws = (char*)d_ws;
  (void)hipFuncSetAttribute((const void*)fused_kernel, hipFuncAttributeMaxDynamicSharedMemorySize, L_TOTAL);
  fused_kernel<<<256, 512, L_TOTAL, stream>>>((const u16*)d_in[0], (const u16*)d_in[1], (const u16*)d_in[2], ws, (u16*)d_out,
                                              (const u16*)d_in[4], (const u16*)d_in[8], (const u16*)d_in[5], (const u16*)d_in[6],
                                              (const u16*)d_in[7]);
}
```

```cpp
#include <hip/hip_runtime.h>
#include <hip/hip_bf16.h>
#include <stdint.h>

typedef unsigned short u16;
typedef unsigned long long u64;
typedef __attribute__((ext_vector_type(8))) short bf16x8;
typedef __attribute__((ext_vector_type(4))) short s16x4;
typedef __attribute__((ext_vector_type(4))) float f32x4;
typedef __attribute__((ext_vector_type(2))) float f32x2;
typedef __attribute__((ext_vector_type(2))) __bf16 bf16x2_t;
typedef __attribute__((ext_vector_type(2))) _Float16 f16x2_t;
typedef __attribute__((ext_vector_type(4))) _Float16 f16x4_t;
typedef __attribute__((ext_vector_type(4))) unsigned int u32x4;
typedef __attribute__((ext_vector_type(2))) unsigned int u32x2;
typedef __attribute__((ext_vector_type(8))) unsigned int u32x8;
typedef __attribute__((address_space(3))) s16x4* lds_s16x4_ptr;
typedef __attribute__((address_space(1))) unsigned gu32;
typedef __attribute__((address_space(1))) u64 gu64;
typedef __attribute__((address_space(3))) unsigned lu32;
typedef __attribute__((address_space(3))) u32x2 lu32x2;
typedef __attribute__((address_space(3))) u32x4 lu32x4;
typedef __attribute__((address_space(3))) f32x4 lf32x4;
#define RLX_AGENT __ATOMIC_RELAXED, __HIP_MEMORY_SCOPE_AGENT
#define RLX_WG __ATOMIC_RELAXED, __HIP_MEMORY_SCOPE_WORKGROUP

#if defined(__HIP_DEVICE_COMPILE__)
#define NOPK __attribute__((target("no-packed-fp32-ops")))
#else
#define NOPK
#endif

#define SEQ 2048
#define DM 1024
#define N1 3072
#define FF 3584
#define NH 32

#define OFF_EPOCH  ((size_t)0)
#define OFF_RG     ((size_t)4096)
#define OFF_GG     ((size_t)16384)
#define OFF_GU     ((size_t)262144)
#define OFF_YG     ((size_t)524288)
#define OFF_KVP    ((size_t)1 << 20)
#define OFF_ZP     ((size_t)2 << 20)
#define ZSC        65536u
#define OFF_OFLAG  (((size_t)3 << 20) + 65536)
#define OFF_OF     ((size_t)4 << 20)

#define SPIN_CAP (1u << 18)

__device__ __forceinline__ float u2f(unsigned u) { return __builtin_bit_cast(float, u); }
__device__ __forceinline__ unsigned f2u(float f) { return __builtin_bit_cast(unsigned, f); }
__device__ __forceinline__ bool all_true(bool ok) { return __builtin_amdgcn_ballot_w64(!ok) == 0ull; }
__device__ __forceinline__ unsigned cvt_pk(float a, float b) {
  f32x2 v = {a, b};
  return __builtin_bit_cast(unsigned, __builtin_convertvector(v, bf16x2_t));
}
__device__ __forceinline__ float blo(unsigned u) { return u2f(u << 16); }
__device__ __forceinline__ float bhi(unsigned u) { return u2f(u & 0xFFFF0000u); }
__device__ __forceinline__ float bfr(float f) { return blo(cvt_pk(f, f)); }
__device__ __forceinline__ float fmax_raw(float a, float b) { return __builtin_elementwise_maximum(a, b); }
__device__ __forceinline__ unsigned eps2(unsigned p) { return cvt_pk(blo(p) + 1e-6f, bhi(p) + 1e-6f); }
template <int N>
__device__ __forceinline__ void phi_fix(unsigned (&p)[N], float umin) {
  if (__builtin_expect(__builtin_amdgcn_ballot_w64(umin < -5.9f) != 0ull, 0)) {
#pragma unroll
    for (int i = 0; i < N; ++i) p[i] = eps2(p[i]);
  }
}
__device__ __forceinline__ float sigmoid_f(float x) { return 1.0f / (1.0f + __expf(-x)); }

template <int CTRL, int RM = 0xf> __device__ __forceinline__ int dppi(int old, int v) {
  return __builtin_amdgcn_update_dpp(old, v, CTRL, RM, 0xf, false);
}
template <int CTRL, int RM = 0xf> __device__ __forceinline__ float dppf(float v) {
  return __builtin_bit_cast(float, dppi<CTRL, RM>(0, __builtin_bit_cast(int, v)));
}
__device__ __forceinline__ unsigned wave_max(unsigned v) {
  v = max(v, (unsigned)dppi<0x111>(0, (int)v)); v = max(v, (unsigned)dppi<0x112>(0, (int)v));
  v = max(v, (unsigned)dppi<0x114>(0, (int)v)); v = max(v, (unsigned)dppi<0x118>(0, (int)v));
  v = max(v, (unsigned)dppi<0x142, 0xa>(0, (int)v)); v = max(v, (unsigned)dppi<0x143, 0xc>(0, (int)v));
  return (unsigned)__builtin_amdgcn_readlane((int)v, 63);
}
__device__ __forceinline__ float merge32(float a, float b) {
  const auto r = __builtin_amdgcn_permlane32_swap(f2u(a), f2u(b), false, false);
  return u2f(r[0]) + u2f(r[1]);
}
__device__ __forceinline__ float merge16(float a, float b) {
  const auto r = __builtin_amdgcn_permlane16_swap(f2u(a), f2u(b), false, false);
  return u2f(r[0]) + u2f(r[1]);
}
typedef __attribute__((address_space(3))) void* ldsp;
template <int IMM>
__device__ __forceinline__ void bufl16(__amdgpu_buffer_rsrc_t r, unsigned l, unsigned voff, unsigned soff) {
#if defined(__HIP_DEVICE_COMPILE__)
  __builtin_amdgcn_raw_ptr_buffer_load_lds(r, (ldsp)(uintptr_t)l, 16, voff, soff, IMM, 0);
#endif
}
#define LP(T, base, imm) ((__attribute__((address_space(3))) T*)(uintptr_t)(unsigned)((base) + (imm)))
#define MKRSRC(p, n) __builtin_amdgcn_make_buffer_rsrc((void*)(p), 0, (n), 0x00020000)
#define WAIT_VM(n) asm volatile("s_waitcnt vmcnt(%0)" ::"n"(n) : "memory")
#define WAIT_LGKM0() asm volatile("s_waitcnt lgkmcnt(0)" ::: "memory")
#define BAR() do { __builtin_amdgcn_sched_barrier(0); asm volatile("s_barrier" ::: "memory"); __builtin_amdgcn_sched_barrier(0); } while (0)
#define CFENCE() asm volatile("" ::: "memory")
#define MFMA(a, b, c) __builtin_amdgcn_mfma_f32_16x16x32_bf16((a), (b), (c), 0, 0, 0)
#define PIN(v) asm volatile("" : "+v"(v))
#define AMFMA(acc, a, b) asm volatile("v_mfma_f32_16x16x32_bf16 %0, %1, %2, %0" : "+v"(acc) : "v"(a), "v"(b))
#define NOP8(t) asm volatile("s_nop 7" : "+v"(t[0][0]), "+v"(t[0][1]), "+v"(t[1][0]), "+v"(t[1][1]), "+v"(t[2][0]), "+v"(t[2][1]), "+v"(t[3][0]), "+v"(t[3][1]))
#define SB() __builtin_amdgcn_sched_barrier(0)
#define TRD(p) ((s16x4)__builtin_amdgcn_ds_read_tr16_b64_v4i16((lds_s16x4_ptr)(p)))
#define CAT(lo, hi) __builtin_shufflevector(lo, hi, 0, 1, 2, 3, 4, 5, 6, 7)
#define FRAG(x) __builtin_bit_cast(bf16x8, x)

#define PACE   57
#define BURST  30
#define R_AT   2
#define R_GAP  2
#define CPF_N  20
#define WO_E   6
#define WO_ES  4
#define WO_M   10
#define G_T    65
#define P5_T   40
#define GU_AH  8
#define GU_E   4
#define DN_E   7
#define DENAT  8
#define OPF    3

__device__ __forceinline__ unsigned rfl(unsigned v) { return (unsigned)__builtin_amdgcn_readfirstlane((int)v); }
template <int SLEEP>
__device__ __forceinline__ unsigned wait_ge(volatile lu32* p, unsigned need) {
  unsigned v = 0;
#pragma unroll 1
  for (unsigned spins = 0; spins < (1u << 22); ++spins) {
    v = rfl(*p);
    if (v >= need) break;
    if (SLEEP) __builtin_amdgcn_s_sleep(SLEEP);
  }
  CFENCE();
  return v;
}

#define SLOT     40960
#define L_X1     147456
#define L_X2     155648
#define L_CW     159744
#define L_ZF     160256
#define L_GATE   160384
#define L_RED    160896
#define L_H      163200
#define L_CTL    163712
#define L_TOTAL  163840
#define W_X1   64
#define W_X2   68
#define W_AG   72
#define W_KS   76
#define WFLAGS(f) (*(volatile lu32*)(smem + L_CW + (lane & 3) * 128 + (f)))
#define WOPOS(c) ((c) < 3 ? 13 + (c) : (c) < 8 ? (c) : (c) < 11 ? (c) - 8 : (c) - 3)
#define C_F15  0
#define C_WO   2
#define C_ZF   4
#define C_TAG  8
#define C_IDX  10
#define C_GO   12
#define C_OARR 17
#define C_ORDY 18
#define C_GU   19
#define C_DN   20
#define C_T6   21
#define C_Y    23

NOPK __global__ __launch_bounds__(512, 2) void fused_kernel(const u16* __restrict__ x, const u16* __restrict__ wqkv,
                                                            const u16* __restrict__ wo, char* ws, u16* __restrict__ out,
                                                            const u16* __restrict__ w_r, const u16* __restrict__ gate_w,
                                                            const u16* __restrict__ w_gate, const u16* __restrict__ w_up,
                                                            const u16* __restrict__ w_down) {
  extern __shared__ __attribute__((aligned(16))) char smem[];
  asm volatile("s_load_dwordx2 s[100:101], %0, 60" :: "s"(__builtin_amdgcn_kernarg_segment_ptr()) : "s100", "s101");
  SB();
  const int tid = threadIdx.x, lane = tid & 63;
  const int w = __builtin_amdgcn_readfirstlane(tid >> 6);
  u64 pc0;
  asm volatile("s_getpc_b64 %0" : "=s"(pc0));
  const int bid = blockIdx.x, xcd = bid & 7, ix = bid >> 3;
  const int ml = ix >> 2, nl = ix & 3;
  const int mt = (xcd >> 2) * 8 + ml, nt = (xcd & 3) * 4 + nl;
  const int m0 = mt * 128;
  const int mt3 = mt, nt3 = nt ^ 1;
#define CH(c) (((4 * (xcd & 3) + (c)) & 15) ^ ((c) < 4 ? nl : 0))
  volatile lu32* misc = (volatile lu32*)(smem + L_CTL);

  if (__builtin_expect((w >> 1) == 2, 1)) {
    u32x8 ka;
    asm volatile("s_load_dwordx8 %0, %1, 0x0" : "=s"(ka) : "s"(__builtin_amdgcn_kernarg_segment_ptr()));
    SB();
    const int p = w - 4;
    unsigned va[4], vb[4], vw[4];
#pragma unroll
    for (int j = 0; j < 4; ++j) {
      const int row = 32 * (w & 1) + 8 * j + (lane >> 3);
      va[j] = (unsigned)((m0 + row) * (DM * 2) + (((lane & 7) ^ ((row >> 1) & 7)) * 16) - 1024 * j);
      vb[j] = (unsigned)(row * (N1 * 2) + 128 * nt + (((lane & 7) ^ ((row & 2) | ((j & 1) << 2))) * 16) - 1024 * j);
      PIN(va[j]); PIN(vb[j]);
    }
    unsigned l0 = (unsigned)(uintptr_t)(ldsp)smem + p * 4096;
    int kf = (4 * ml + 4 * (xcd & 3)) & 15, ks = (4 * ml - 2 * (ml >> 2) + 4 * (xcd & 3)) & 15, sres = (3 - nt) & 7;
    asm volatile("" : "+s"(l0), "+s"(kf), "+s"(ks), "+s"(sres));
    SB();
    asm volatile("s_waitcnt lgkmcnt(0)" : "+s"(ka));
    const u16* x = (const u16*)(((u64)ka[1] << 32) | ka[0]);
    const u16* wqkv = (const u16*)(((u64)ka[3] << 32) | ka[2]);
    const u16* wo = (const u16*)(((u64)ka[5] << 32) | ka[4]);
    char* ws = (char*)(((u64)ka[7] << 32) | ka[6]);
    int ko_ = (int)((ka[2] >> 7) & 7u) == sres ? ks : kf;
    const __amdgpu_buffer_rsrc_t ra = MKRSRC(x, SEQ * DM * 2), rb = MKRSRC(wqkv, DM * N1 * 2), rw = MKRSRC(wo, DM * DM * 2);
#define UNIT(r, v, off, so) do { bufl16<0>(r, l0 + (off), v[0], so); bufl16<1024>(r, l0 + (off), v[1], so); bufl16<2048>(r, l0 + (off), v[2], so); bufl16<3072>(r, l0 + (off), v[3], so); } while (0)
#define ISSUE(t)                                                                                           \
  do {                                                                                                     \
    const int k_ = (ko_ + (t)) & 15;                                                                       \
    UNIT(ra, va, ((t) % 3) * SLOT, k_ * 128); UNIT(ra, va, ((t) % 3) * SLOT + 8192, k_ * 128 + 64 * DM * 2); \
    _Pragma("unroll") for (int s_ = 0; s_ < 3; ++s_) UNIT(rb, vb, ((t) % 3) * SLOT + 16384 + s_ * 8192, k_ * (64 * N1 * 2) + s_ * (DM * 2)); \
  } while (0)
#define PACED_BAR()                                                                                        \
  do {                                                                                                     \
    if (p == 0) {                                                                                          \
      unsigned now;                                                                                        \
      do { now = (unsigned)__builtin_amdgcn_s_memrealtime(); } while ((int)(now - tp) < 0);                \
      tp = ((int)(now - tp) > BURST ? now - BURST : tp) + PACE;                                            \
    }                                                                                                      \
    BAR();                                                                                                 \
  } while (0)
    ISSUE(0); ISSUE(1);
    WAIT_VM(20);
    BAR();
    ISSUE(2);
    unsigned tp = (unsigned)__builtin_amdgcn_s_memrealtime() - PACE;
#pragma unroll
    for (int j = 0; j < 4; ++j) vw[j] = (unsigned)((32 * p + 8 * j + (lane >> 3)) * (DM * 2) + 128 * nt3 + (((lane & 7) ^ ((lane >> 3) & 6)) * 16) - 1024 * j);
#define WO_CHUNK(c) UNIT(rw, vw, 16384 + WOPOS(c) * 8192, CH(c) * (64 * DM * 2))
#pragma unroll
    for (int t = 0; t < 13; ++t) {
      WAIT_VM(20);
      PACED_BAR();
      asm volatile("" : "+s"(l0), "+s"(ko_));
      ISSUE(t + 3);
    }
    WO_CHUNK(0); WO_CHUNK(1); WO_CHUNK(2);
    SB();
    const bool wslow = ((((uintptr_t)wo >> 7) + nt3) & 7) == 3;
    const int woe = wslow ? WO_ES : WO_E;
    WAIT_VM(32);
    PACED_BAR();
    WAIT_VM(12);
    if (lane == 0) misc[C_F15 + p] = 1u;
#pragma unroll 1
    for (int c = 3; c < woe; ++c) WO_CHUNK(c);
    {
      const __amdgpu_buffer_rsrc_t zr = MKRSRC(ws + OFF_ZP, NH * 2 * 16 * 128);
      const unsigned zo = (unsigned)((2 * nt + p) * 2048 + lane * 16);
      u32x4 zw[2];
      unsigned zm;
      (void)wait_ge<1>(misc + C_IDX + p, 1u);
#pragma unroll 1
      for (unsigned spins = 0; spins < (1u << 22) && !all_true(WFLAGS(W_KS) != 0u); ++spins) __builtin_amdgcn_s_sleep(1);
      CFENCE();
      const unsigned tk = (unsigned)__builtin_amdgcn_s_memrealtime();
#pragma unroll 1
      for (int c = woe; c < WO_M; ++c) WO_CHUNK(c);
      UNIT(ra, va, 0, nt3 * 128); UNIT(ra, va, 8192, nt3 * 128 + 64 * DM * 2);
      while ((unsigned)__builtin_amdgcn_s_memrealtime() - tk < (unsigned)G_T) __builtin_amdgcn_s_sleep(1);
#define MX4(v) max(max((v)[0], (v)[1]), max((v)[2], (v)[3]))
#pragma unroll 1
      for (unsigned spins = 0; spins <= SPIN_CAP; ++spins) {
        CFENCE();
        zw[0] = __builtin_amdgcn_raw_buffer_load_b128(zr, zo, 0, 16); zw[1] = __builtin_amdgcn_raw_buffer_load_b128(zr, zo + 1024, 0, 16);
        SB(); misc[C_GO + p] = 1u; SB();
        zm = max(MX4(zw[0]), MX4(zw[1]));
        if (all_true(zm != ~0u)) break;
        __builtin_amdgcn_s_sleep(2);
      }
      const bool plain = all_true((int)zm >= 0);
      if (__builtin_expect(!plain, 0)) { zw[0] &= 0x7FFFFFFFu; zw[1] &= 0x7FFFFFFFu; }
      float z0 = merge16(merge32(u2f(zw[0][0]), u2f(zw[0][2])), merge32(u2f(zw[1][0]), u2f(zw[1][2])));
      float z1 = merge16(merge32(u2f(zw[0][1]), u2f(zw[0][3])), merge32(u2f(zw[1][1]), u2f(zw[1][3])));
      z0 += dppf<0x128>(z0); z1 += dppf<0x128>(z1); z0 += dppf<0x124>(z0); z1 += dppf<0x124>(z1);
      *(unsigned*)(smem + L_ZF + p * 64 + (lane & 3) * 16 + ((lane >> 4) & 1) * 8 + (lane >> 5) * 4) = cvt_pk(z0, z1);
      CFENCE();
      misc[C_ZF + p] = plain ? 1u : 2u;
    }
#pragma unroll 1
    for (unsigned spins = 0; spins < (1u << 22) && !all_true(WFLAGS(W_AG) != 0u); ++spins) __builtin_amdgcn_s_sleep(2);
    CFENCE();
#pragma unroll 1
    for (int c = WO_M; c < 16; ++c) WO_CHUNK(c);
    WAIT_VM(0);
    if (lane == 0) misc[C_WO + p] = 1u;
#undef ISSUE
#undef PACED_BAR
    if (p == 0) return;
    const unsigned tag = rfl(misc[C_TAG]);
    gu32* of = (gu32*)(ws + OFF_OFLAG + (mt3 * 16 + CH(lane & 15)) * 128);
    const unsigned t6 = wait_ge<1>(misc + C_T6, 1u);
    while ((unsigned)__builtin_amdgcn_s_memrealtime() - t6 < (unsigned)P5_T) __builtin_amdgcn_s_sleep(1);
#pragma unroll 1
    for (unsigned spins = 0;; ++spins) {
      const unsigned f = __hip_atomic_load(of, RLX_AGENT);
      const unsigned miss = (unsigned)__builtin_amdgcn_ballot_w64(f != tag) & 0xFFFEu;
      const unsigned m = (unsigned)__builtin_ctz((spins > SPIN_CAP ? 0u : miss) | 0x10000u);
      misc[C_ORDY] = m;
      if (m == 16u) break;
      __builtin_amdgcn_s_sleep(2);
    }
    return;
  }

  if (__builtin_expect(w >= 6, 0)) {
    const int pa = w - 6;
#define CODE_PF(on) __builtin_amdgcn_raw_buffer_load_b32(MKRSRC((pc0 & ~255ull) + (blockIdx.x >> 3) * (CPF_N * 64u), (on) ? CPF_N * 64 : 0), lane * 64, 0, 0)
    unsigned cpf = CODE_PF(w == 7), cpf2;
    SB();
    float* red = (float*)(smem + L_RED);
    const bool help = bid >= 224, gemv = bid < 252;
    const int gmat = bid & 1;
    const u16* wgu = gmat ? w_up : w_gate;
    const int sres = (int)(3u - (unsigned)((uintptr_t)wgu >> 7)) & 7;
    const int gfs = help ? sres + 8 * (((bid - 224) >> 1) % 7) : (bid >> 1) % 56;
    const int gdh = (help ? ((bid - 224) >> 1) / 7 : (bid >> 1) / 56) & 1;
    const bool act0 = !help, act1 = gemv && (help || (gfs & 7) != sres);
    const int gd0 = gdh * 512 + 256 * pa + 32 * (lane >> 3);
    const __amdgpu_buffer_rsrc_t rgu0 = MKRSRC(wgu, act0 ? DM * FF * 2 : 0), rgu1 = MKRSRC(wgu, act1 ? DM * FF * 2 : 0);
    const unsigned wpo = (unsigned)((gd0 * FF + gfs * 64 + 8 * (lane & 7)) * 2);
    u32x4 wv[32], wd[14];
    const int fg = bid >> 4, cg = bid & 15;
    const int f0 = fg * 224 + pa * 112;
    const __amdgpu_buffer_rsrc_t rdn = MKRSRC(w_down, DM * FF * 2);
    const unsigned wqo = (unsigned)(((f0 + 14 * (lane >> 3)) * DM + cg * 64 + 8 * (lane & 7)) * 2);
    asm volatile("" ::"s"(w_r), "s"(gate_w), "s"(ws), "s"(w_gate), "s"(w_up), "s"(w_down));
    unsigned pf;
#define GU_LD(i0, i1) do { SB(); _Pragma("unroll") for (int i = (i0); i < (i1); ++i) wv[i] = __builtin_amdgcn_raw_buffer_load_b128(i < 16 ? rgu0 : rgu1, wpo, i * (FF * 2), 2); SB(); } while (0)
#define DN_LD(i0, i1) do { SB(); _Pragma("unroll") for (int i = (i0); i < (i1); ++i) wd[i] = __builtin_amdgcn_raw_buffer_load_b128(rdn, wqo, i * (DM * 2), 2); SB(); } while (0)
    GU_LD(0, GU_E);
    {
#pragma unroll
      for (int t = 0; t <= R_AT; ++t) BAR();
      const int r0 = m0 + nt * 8 + 4 * pa;
      u32x4 xr[4][2], rv[2], gv[2];
#pragma unroll
      for (int i = 0; i < 2; ++i) {
        rv[i] = *(const u32x4*)(w_r + i * 512 + lane * 8);
        gv[i] = *(const u32x4*)(gate_w + i * 512 + lane * 8);
      }
#pragma unroll
      for (int r = 0; r < 4; ++r)
#pragma unroll
        for (int i = 0; i < 2; ++i) xr[r][i] = *(const u32x4*)(x + (size_t)(r0 + r) * DM + i * 512 + lane * 8);
      const unsigned ep = __hip_atomic_load((gu32*)(ws + OFF_EPOCH), RLX_AGENT);
      {
        const u32x4 nw = {~0u, ~0u, ~0u, ~0u};
        const __amdgpu_buffer_rsrc_t kvr = MKRSRC(ws + OFF_KVP, NH * 2 * 16 * 1024), zr = MKRSRC(ws + OFF_ZP, NH * 2 * 16 * 128);
        const unsigned rec = (unsigned)((2 * nt + pa) * 32 + mt);
        __builtin_amdgcn_raw_buffer_store_b128(nw, kvr, rec * 1024 + lane * 16, 0, 16);
        __builtin_amdgcn_raw_buffer_store_b128(nw, kvr, rec * 1024 + lane * 16 + 16384, 0, 16);
        if (lane < 10) __builtin_amdgcn_raw_buffer_store_b128(nw, zr, lane < 8 ? (rec + (lane >> 2) * 16) * 64 + (lane & 3) * 16 : ZSC + (rec + (lane & 1) * 16) * 16, 0, 16);
      }
#pragma unroll
      for (int t = R_AT + 1; t < R_AT + R_GAP; ++t) BAR();
      float racc[4], gacc[4];
#pragma unroll
      for (int r = 0; r < 4; ++r) {
        BAR();
        if (r == 1) { WAIT_VM(0); SB(); }
        racc[r] = 0.f; gacc[r] = 0.f;
#pragma unroll
        for (int i = 0; i < 2; ++i)
#pragma unroll
          for (int e = 0; e < 4; ++e) {
            const float x0 = blo(xr[r][i][e]), x1 = bhi(xr[r][i][e]);
            racc[r] += x0 * blo(rv[i][e]); racc[r] += x1 * bhi(rv[i][e]);
            const unsigned pr = cvt_pk(x0 * blo(gv[i][e]), x1 * bhi(gv[i][e]));
            gacc[r] += blo(pr); gacc[r] += bhi(pr);
          }
        PIN(racc[r]); PIN(gacc[r]);
        if (r == 3) { SB(); pf = *(const unsigned*)(wo + (size_t)(128 * ml + 64 * pa + lane) * DM + 64 * nt3); cpf2 = CODE_PF(w == 7); }
      }
      BAR();
      float rs = merge16(merge32(racc[0], racc[1]), merge32(racc[2], racc[3]));
      float gs = merge16(merge32(gacc[0], gacc[1]), merge32(gacc[2], gacc[3]));
      rs += dppf<0x128>(rs); gs += dppf<0x128>(gs); rs += dppf<0x124>(rs); gs += dppf<0x124>(gs);
      rs += dppf<0x122>(rs); gs += dppf<0x122>(gs); rs += dppf<0x121>(rs); gs += dppf<0x121>(gs);
      const int rr = r0 + ((lane >> 5) | ((lane >> 3) & 2));
      const unsigned rb = cvt_pk(rs, rs) & 0xFFFFu;
      unsigned key = (((rb & 0x8000u) ? (~rb & 0xFFFFu) : (rb | 0x8000u)) << 16) | (0xFFFFu - (unsigned)rr);
      { const auto t_ = __builtin_amdgcn_permlane16_swap(key, key, false, false); key = max(t_[0], t_[1]); }
      { const auto t_ = __builtin_amdgcn_permlane32_swap(key, key, false, false); key = max(t_[0], t_[1]); }
      const unsigned tg = rfl(ep) + 1u;
      if (lane == 0) misc[C_TAG + pa] = pa ? key : tg;
      WAIT_LGKM0();
      BAR();
      if (pa == 0 && lane == 0) __hip_atomic_store((gu64*)(ws + OFF_RG) + bid, ((u64)tg << 32) | max(key, (unsigned)misc[C_TAG + 1]), RLX_AGENT);
      if ((lane & 15) == 0) __hip_atomic_store((gu64*)(ws + OFF_GG) + rr, ((u64)tg << 32) | f2u(bfr(sigmoid_f(bfr(gs)))), RLX_AGENT);
      DN_LD(0, DN_E);
#pragma unroll
      for (int t = R_AT + R_GAP + 6; t < 13; ++t) BAR();
    }
    u32x4 rg[2];
#pragma unroll
    for (int i = 0; i < 2; ++i) rg[i] = __builtin_amdgcn_raw_buffer_load_b128(MKRSRC(ws + OFF_RG, 2048), lane * 16 + i * 1024, 0, 16);
    const gu64* ggp = (const gu64*)(ws + OFF_GG) + m0 + 64 * pa + lane;
    u64 gg = __hip_atomic_load(ggp, RLX_AGENT);
    BAR();
    const unsigned tag = rfl(misc[C_TAG]);
    unsigned idx;
    unsigned bestk;
    bool late;
    {
      unsigned best = 0;
      bool ok = (unsigned)(gg >> 32) == tag;
#pragma unroll
      for (int i = 0; i < 4; ++i) { ok = ok && rg[i >> 1][2 * (i & 1) + 1] == tag; best = max(best, rg[i >> 1][2 * (i & 1)]); }
      late = !all_true(ok);
      if (__builtin_expect(late, 0)) {
        BAR();
#pragma unroll 1
        for (unsigned spins = 0;; ++spins) {
          u64 rh[4];
#pragma unroll
          for (int i = 0; i < 4; ++i) rh[i] = __hip_atomic_load((gu64*)(ws + OFF_RG) + i * 64 + lane, RLX_AGENT);
          gg = __hip_atomic_load(ggp, RLX_AGENT);
          ok = (unsigned)(gg >> 32) == tag; best = 0;
#pragma unroll
          for (int i = 0; i < 4; ++i) { ok = ok && (unsigned)(rh[i] >> 32) == tag; best = max(best, (unsigned)rh[i]); }
          if (all_true(ok) || spins > SPIN_CAP) break;
          __builtin_amdgcn_s_sleep(8);
        }
      }
      best = wave_max(best);
      idx = (0xFFFFu - (best & 0xFFFFu)) & (SEQ - 1);
      bestk = best;
    }
    asm volatile("" ::"v"(pf), "v"(cpf), "v"(cpf2));
    u32x4 xs[4];
#pragma unroll
    for (int i = 0; i < 4; ++i) xs[i] = *(const u32x4*)(x + (size_t)idx * DM + gd0 + 8 * i);
    if (!late) BAR();
    __builtin_amdgcn_s_sleep(2);
    GU_LD(GU_E, 32);
    ((unsigned*)(smem + L_GATE))[64 * pa + lane] = (unsigned)gg;
    CFENCE();
    if (lane == 0) misc[C_IDX + pa] = idx + 1u;
    if (bid == 0 && pa == 0 && lane == 0) __hip_atomic_store((gu32*)(ws + OFF_EPOCH), tag, RLX_AGENT);
    unsigned sel[2][4];
#pragma unroll
    for (int i = 0; i < 8; ++i) {
      const unsigned pick = (i >> 2) ? 0x0302u : 0x0100u;
      sel[i >> 2][i & 3] = ((lane & 7) >> 1) == (i & 3) ? ((lane & 1) ? (pick << 16) | 0x0C0Cu : 0x0C0C0000u | pick) : 0x0C0C0C0Cu;
      PIN(sel[i >> 2][i & 3]);
    }
#define AFR(pr, i) FRAG(((u32x4){__builtin_amdgcn_perm(0u, pr, sel[(i) & 1][0]), __builtin_amdgcn_perm(0u, pr, sel[(i) & 1][1]), \
                                 __builtin_amdgcn_perm(0u, pr, sel[(i) & 1][2]), __builtin_amdgcn_perm(0u, pr, sel[(i) & 1][3])}))
    f32x4* rdw = (f32x4*)(red + pa * 128 + (lane >> 5) * 64 + (lane & 7) * 8 + ((lane >> 4) & 1) * 4);
    const bool rvalid = (lane >> 5) == ((lane >> 3) & 1);
    const auto rv = LP(volatile float, L_RED, lane * 4);
    {
      bf16x8 af[32];
      f32x4 a = {0.f, 0.f, 0.f, 0.f};
#pragma unroll
      for (int i = 0; i < GU_AH; ++i) { af[i] = AFR(xs[i >> 3][(i >> 1) & 3], i); PIN(af[i]); }
      SB();
#pragma unroll
      for (int i = 0; i < 32; ++i) {
        a = MFMA(af[i], FRAG(wv[i]), a);
        PIN(a);
        if (i + GU_AH < 32) { af[(i + GU_AH) & 31] = AFR(xs[((i + GU_AH) >> 3) & 3][((i + GU_AH) >> 1) & 3], i + GU_AH); PIN(af[(i + GU_AH) & 31]); }
        SB();
      }
      if (rvalid) *rdw = a;
      CFENCE();
      if (pa == 1) misc[C_GU] = 1u;
      else {
        float v;
#pragma unroll 1
        for (unsigned spins = 0; spins < (1u << 22); ++spins) {
          const unsigned f_ = misc[C_GU];
          CFENCE();
          v = (rv[0] + rv[64]) + (rv[128] + rv[192]);
          if (rfl(f_) != 0u) break;
          __builtin_amdgcn_s_sleep(1);
        }
        if (gemv) __hip_atomic_store((gu64*)(ws + OFF_GU) + ((help ? 4 : 0) + gdh * 2 + gmat) * FF + gfs * 64 + lane, ((u64)tag << 32) | f2u(v), RLX_AGENT);
      }
    }
    {
#pragma unroll 1
      for (unsigned spins = 0; spins < (1u << 22) && !all_true(WFLAGS(W_X2) != 0u); ++spins) __builtin_amdgcn_s_sleep(1);
      __builtin_amdgcn_s_sleep(10);
      DN_LD(DN_E, 14);
      __builtin_amdgcn_s_sleep(8);
      float hv[2];
      const int fl = f0 + 2 * (lane < 56 ? lane : 55);
      const __amdgpu_buffer_rsrc_t gr = MKRSRC(ws + OFF_GU, 8 * FF * 8);
      const bool ex[2] = {(((unsigned)((uintptr_t)w_gate >> 7) + (fl >> 6)) & 7u) == 3u, (((unsigned)((uintptr_t)w_up >> 7) + (fl >> 6)) & 7u) == 3u};
#pragma unroll 1
      for (unsigned spins = 0;; ++spins) {
        u32x4 g[8];
        CFENCE();
#pragma unroll
        for (int i = 0; i < 8; ++i) {
          g[i] = (u32x4){0u, tag, 0u, tag};
          if (i < 4 || ex[i & 1]) g[i] = __builtin_amdgcn_raw_buffer_load_b128(gr, fl * 8, i * (FF * 8), 16);
        }
        bool ok = true;
#pragma unroll
        for (int j = 0; j < 2; ++j) {
#pragma unroll
          for (int i = 0; i < 8; ++i) ok = ok && g[i][2 * j + 1] == tag;
          const float gt = bfr((u2f(g[0][2 * j]) + u2f(g[4][2 * j])) + (u2f(g[2][2 * j]) + u2f(g[6][2 * j])));
          const float up = bfr((u2f(g[1][2 * j]) + u2f(g[5][2 * j])) + (u2f(g[3][2 * j]) + u2f(g[7][2 * j])));
          hv[j] = bfr(gt * __builtin_amdgcn_rcpf(1.0f + __expf(-gt))) * up;
        }
        if (all_true(ok) || spins > SPIN_CAP) break;
        __builtin_amdgcn_s_sleep(4);
      }
      u32x4 hp[2];
      {
        char* hb = smem + L_H + pa * 256;
        if (lane < 56) *(unsigned*)(hb + (lane / 7) * 32 + (lane % 7) * 4) = cvt_pk(hv[0], hv[1]);
        CFENCE();
        hp[0] = *(volatile lu32x4*)(hb + (lane >> 3) * 32); hp[1] = *(volatile lu32x4*)(hb + (lane >> 3) * 32 + 16);
      }
      bf16x8 af[14];
#pragma unroll
      for (int i = 0; i < 14; ++i) { af[i] = AFR(hp[i >> 3][(i >> 1) & 3], i); PIN(af[i]); }
      SB();
      f32x4 a = {0.f, 0.f, 0.f, 0.f};
#pragma unroll
      for (int i = 0; i < 14; ++i) a = MFMA(af[i], FRAG(wd[i]), a);
      if (rvalid) rdw[64] = a;
      CFENCE();
      if (pa == 1) misc[C_DN] = 1u;
      else {
        float y;
#pragma unroll 1
        for (unsigned spins = 0; spins < (1u << 22); ++spins) {
          const unsigned f_ = misc[C_DN];
          CFENCE();
          y = (rv[256] + rv[320]) + (rv[384] + rv[448]);
          if (rfl(f_) != 0u) break;
          __builtin_amdgcn_s_sleep(1);
        }
        __hip_atomic_store((gu64*)(ws + OFF_YG) + fg * 1024 + cg * 64 + lane, ((u64)tag << 32) | f2u(y), RLX_AGENT);
      }
    }
    if (pa == 1 && (int)(idx >> 7) == mt3) {
      const float xe = u2f((unsigned)x[(size_t)idx * DM + 64 * nt3 + lane] << 16);
      const unsigned ob = bestk >> 16;
      const float rsel = bfr(sigmoid_f(u2f(((ob & 0x8000u) ? (ob & 0x7FFFu) : (~ob & 0xFFFFu)) << 16)));
      float y = 0.f, gt = 0.f;
      __builtin_amdgcn_s_sleep(30);
#pragma unroll 1
      for (unsigned spins = 0;; ++spins) {
        u64 g[17];
#pragma unroll
        for (int i = 0; i < 16; ++i) g[i] = __hip_atomic_load((gu64*)(ws + OFF_YG) + i * 1024 + 64 * nt3 + lane, RLX_AGENT);
        g[16] = __hip_atomic_load((gu64*)(ws + OFF_GG) + idx, RLX_AGENT);
        bool ok = (unsigned)(g[16] >> 32) == tag;
        gt = u2f((unsigned)g[16]); y = 0.f;
#pragma unroll
        for (int i = 0; i < 16; ++i) { ok = ok && (unsigned)(g[i] >> 32) == tag; y += u2f((unsigned)g[i]); }
        if (all_true(ok) || spins > SPIN_CAP) break;
        __builtin_amdgcn_s_sleep(8);
      }
      red[512 + lane] = bfr(bfr(1.0f - gt) * bfr(xe + bfr(bfr(y) * rsel))) - bfr(bfr(1.0f - gt) * xe);
      CFENCE();
      if (lane == 0) misc[C_Y] = 1u;
    }
    return;
  }

  if (w == 0) misc[lane & 31] = 0u;
  if (w == 1) *(u32x2*)(smem + L_CW + lane * 8) = (u32x2){0u, 0u};
  const int hh = w & 1, wr = w >> 1;
  const int c16 = lane & 15, g = lane >> 4;
  const int fsw = (c16 >> 3) | ((g & 1) << 1);
  const int head = 2 * nt + hh;
  int aoff[2], boff[2];
#pragma unroll
  for (int i = 0; i < 2; ++i) {
    aoff[i] = (wr * 64 + c16) * 128 + (((4 * i + g) ^ (c16 >> 1)) * 16);
    boff[i] = 16384 + (8 * g + (c16 >> 2)) * 128 + (((2 * hh + (i ^ wr)) ^ fsw) * 32) + (c16 & 3) * 8;
  }
  f32x4 acc[3][4][2];
#pragma unroll
  for (int i = 0; i < 24; ++i) { const float s0 = i < 16 ? 1.f : 0.f; acc[i / 8][(i >> 1) & 3][i & 1] = (f32x4){s0, s0, s0, s0}; PIN(acc[i / 8][(i >> 1) & 3][i & 1]); }
  bf16x8 a[2][4], b[2][6];
  s16x4 tlo;
#define A_AT(sl, h, mi) (*(const bf16x8*)(smem + (sl) * SLOT + aoff[h] + (mi) * 2048))
#define B_AT(sl, h, s, n) (smem + (sl) * SLOT + boff[n] + (s) * 8192 + (h) * 4096)
#define MM(s, mi, n, A, B) do { if ((s) == 0) AMFMA(acc[0][mi][n], B, A); else AMFMA(acc[s][mi][n], A, B); } while (0)
#define RD(sl, h, u)                                                                                       \
  do {                                                                                                     \
    if ((u) == 0) a[h][0] = A_AT(sl, h, 0);                                                                \
    else if ((u) >= 13) a[h][(u) - 12] = A_AT(sl, h, (u) - 12);                                            \
    else {                                                                                                 \
      const int j_ = (((u) - 1) / 2 + 2) % 6;                                                              \
      if ((u) & 1) tlo = TRD(B_AT(sl, h, j_ >> 1, j_ & 1));                                                \
      else b[h][j_] = CAT(tlo, TRD(B_AT(sl, h, j_ >> 1, j_ & 1) + 512));                                   \
    }                                                                                                      \
  } while (0)
#define O_M(n) ((((n) / 4) & 1) ? 3 - (n) % 4 : (n) % 4)
#define O_J(n) ((n) / 4)
#define STEP(h, rs)                                                                                        \
  _Pragma("unroll") for (int n_ = 0; n_ < 24; ++n_) {                                                      \
    const int jj_ = (O_J(n_) + 2) % 6, mi_ = O_M(n_);                                                      \
    MM(jj_ >> 1, mi_, jj_ & 1, a[h][mi_], b[h][jj_]); SB();                                                \
    if (n_ < 16) { RD(rs, 1 - (h), n_); }                                                                 \
    SB();                                                                                                  \
  }
#define LGKM0() __builtin_amdgcn_s_waitcnt(0xC07F)
#define STAGE(sl, nx) do { STEP(0, sl); LGKM0(); BAR(); STEP(1, nx); LGKM0(); SB(); } while (0)

  __amdgpu_buffer_rsrc_t wsr = MKRSRC(ws, OFF_OF + SEQ * DM * 2);
  asm volatile("" : "+s"(wsr));
  const unsigned rec = (unsigned)((head * 2 + wr) * 16 + mt);
  unsigned x1w = L_X1 + w * 2048 + lane * 16, cw = L_CW + w * 128 + g * 16, one = 1u;
  unsigned kvo = (unsigned)OFF_KVP + rec * 1024 + lane * 16;
  unsigned zo = c16 == 0 ? (unsigned)OFF_ZP + rec * 64 + g * 16 : lane == 1 ? (unsigned)OFF_ZP + ZSC + rec * 16 : 0x80000000u;
  PIN(x1w); PIN(cw); PIN(one); PIN(kvo); PIN(zo);
  WAIT_LGKM0();
  BAR();
#pragma unroll
  for (int u = 0; u < 16; ++u) RD(0, 0, u);
  LGKM0();
  SB();
#pragma unroll 1
  for (int it = 0; it < 4; ++it) { STAGE(0, 1); STAGE(1, 2); STAGE(2, 0); }
  STAGE(0, 1);
  STEP(0, 1); LGKM0(); BAR();
  bf16x8 ta0_0, ta0_1, ta0_2, ta0_3, ta1_0, ta1_1, ta1_2, ta1_3, ta2_0, ta2_1, ta2_2, ta2_3;
  bf16x8 ta3_0, ta3_1, ta3_2, ta3_3, tb0_0_0, tb0_0_1, tb0_1_0, tb0_1_1, tb0_2_0, tb0_2_1, tb0_3_0, tb0_3_1;
  bf16x8 tb1_0_0, tb1_0_1, tb1_1_0, tb1_1_1, tb1_2_0, tb1_2_1, tb1_3_0, tb1_3_1, tb2_0_0, tb2_0_1, tb2_1_0, tb2_1_1;
  bf16x8 tb2_2_0, tb2_2_1, tb2_3_0, tb2_3_1;
  s16x4 tb0_0_0_l, tb0_0_1_l, tb0_1_0_l, tb0_1_1_l, tb0_2_0_l, tb0_2_1_l, tb0_3_0_l, tb0_3_1_l, tb1_0_0_l, tb1_0_1_l, tb1_1_0_l, tb1_1_1_l;
  s16x4 tb1_2_0_l, tb1_2_1_l, tb1_3_0_l, tb1_3_1_l, tb2_0_0_l, tb2_0_1_l, tb2_1_0_l, tb2_1_1_l, tb2_2_0_l, tb2_2_1_l, tb2_3_0_l, tb2_3_1_l;
  unsigned kp[16], vp[16], x1r, cr;
  u32x2 lf15;
  float kmin = 0.f, qmin = 0.f;
  u32x4 ones;
  float ek[4];
#define PHI(src, dst, mn, k, st)                                                                           \
  do {                                                                                                     \
    float& e_ = ek[2 * (k) + ((st) & 1)];                                                                  \
    if ((st) < 2) e_ = __builtin_fmaf((src)[2 * (k) + ((st) & 1)], 1.44269504088896341f, -1.44269504088896341f); \
    else if ((st) < 4) e_ = __builtin_amdgcn_fmed3f(__builtin_amdgcn_exp2f(e_), 0.f, 1.f);                 \
    else if ((st) < 6) e_ = fmax_raw((src)[2 * (k) + ((st) & 1)], e_);                                     \
    else if ((st) == 6) dst = cvt_pk(ek[2 * (k)], ek[2 * (k) + 1]);                                        \
    else mn = __builtin_fminf(__builtin_fminf(mn, (src)[2 * (k)]), (src)[2 * (k) + 1]);                     \
    SB();                                                                                                  \
  } while (0)
#define PK(mi, n, k, st) PHI(acc[1][mi][n], kp[8 * (n) + 2 * (mi) + (k)], kmin, k, st)
#define CV(mi, n, k) do { vp[8 * (n) + 2 * (mi) + (k)] = cvt_pk(acc[2][mi][n][2 * (k)], acc[2][mi][n][2 * (k) + 1]); PIN(vp[8 * (n) + 2 * (mi) + (k)]); SB(); } while (0)
#define F15_RD lf15 = *(volatile lu32x2*)(smem + L_CTL + 4 * C_F15)
#define F15_CHK do { if (__builtin_expect(!all_true((lf15[0] & lf15[1]) != 0u), 0)) { (void)wait_ge<1>(misc + C_F15, 1u); (void)wait_ge<1>(misc + C_F15 + 1, 1u); } CFENCE(); } while (0)
#define PRE(i) do { if ((i) == 30) { x1r = x1w ^ 4096u; PIN(x1r); } if ((i) == 31) { cr = cw ^ 256u; PIN(cr); } \
                    if ((i) == 32) { ones = (u32x4){0x3F803F80u, 0x3F803F80u, 0x3F803F80u, 0x3F803F80u}; PIN(ones); } } while (0)
  MM(1, 0, 0, a[1][0], b[1][2]); SB(); tb1_0_1_l = TRD(B_AT(2, 0, 1, 1)); PRE(0); SB();
  MM(1, 0, 1, a[1][0], b[1][3]); SB(); tb1_0_1 = CAT(tb1_0_1_l, TRD(B_AT(2, 0, 1, 1) + 512)); PRE(1); SB();
  MM(2, 0, 0, a[1][0], b[1][4]); SB(); ta0_0 = A_AT(2, 0, 0); PRE(2); SB();
  MM(2, 0, 1, a[1][0], b[1][5]); SB(); ta0_1 = A_AT(2, 0, 1); PRE(3); SB();
  MM(0, 0, 0, a[1][0], b[1][0]); SB(); ta0_2 = A_AT(2, 0, 2); PRE(4); SB();
  MM(0, 0, 1, a[1][0], b[1][1]); SB(); ta0_3 = A_AT(2, 0, 3); PRE(5); SB();
  MM(1, 1, 0, a[1][1], b[1][2]); SB(); tb1_0_0_l = TRD(B_AT(2, 0, 1, 0)); PRE(6); SB();
  MM(1, 1, 1, a[1][1], b[1][3]); SB(); tb1_0_0 = CAT(tb1_0_0_l, TRD(B_AT(2, 0, 1, 0) + 512)); PRE(7); SB();
  MM(2, 1, 0, a[1][1], b[1][4]); SB(); tb1_1_1_l = TRD(B_AT(2, 1, 1, 1)); PRE(8); SB();
  MM(2, 1, 1, a[1][1], b[1][5]); SB(); tb1_1_1 = CAT(tb1_1_1_l, TRD(B_AT(2, 1, 1, 1) + 512)); PRE(9); SB();
  MM(0, 1, 0, a[1][1], b[1][0]); SB(); ta1_0 = A_AT(2, 1, 0); PRE(10); SB();
  MM(0, 1, 1, a[1][1], b[1][1]); SB(); ta1_1 = A_AT(2, 1, 1); PRE(11); SB();
  MM(1, 2, 0, a[1][2], b[1][2]); SB(); ta1_2 = A_AT(2, 1, 2); PRE(12); SB();
  MM(1, 2, 1, a[1][2], b[1][3]); SB(); ta1_3 = A_AT(2, 1, 3); PRE(13); SB();
  MM(2, 2, 0, a[1][2], b[1][4]); SB(); tb1_1_0_l = TRD(B_AT(2, 1, 1, 0)); PRE(14); SB();
  MM(2, 2, 1, a[1][2], b[1][5]); SB(); tb1_1_0 = CAT(tb1_1_0_l, TRD(B_AT(2, 1, 1, 0) + 512)); PRE(15); SB();
  MM(0, 2, 0, a[1][2], b[1][0]); SB(); PRE(16); SB();
  MM(0, 2, 1, a[1][2], b[1][1]); SB(); PRE(17); SB();
  MM(1, 3, 0, a[1][3], b[1][2]); SB(); PRE(18); SB();
  MM(1, 3, 1, a[1][3], b[1][3]); SB(); PRE(19); SB();
  MM(2, 3, 0, a[1][3], b[1][4]); SB(); F15_RD; PRE(20); SB();
  MM(2, 3, 1, a[1][3], b[1][5]); SB(); PRE(21); SB();
  MM(0, 3, 0, a[1][3], b[1][0]); SB(); tb2_0_1_l = TRD(B_AT(2, 0, 2, 1)); PRE(22); SB();
  MM(0, 3, 1, a[1][3], b[1][1]); SB(); tb2_0_1 = CAT(tb2_0_1_l, TRD(B_AT(2, 0, 2, 1) + 512)); PRE(23); SB();
  MM(1, 0, 1, ta0_0, tb1_0_1); SB(); PRE(24); SB();
  MM(1, 1, 1, ta0_1, tb1_0_1); SB(); PRE(25); SB();
  MM(1, 2, 1, ta0_2, tb1_0_1); SB(); F15_CHK; tb2_1_1_l = TRD(B_AT(2, 1, 2, 1)); PRE(26); SB();
  MM(1, 3, 1, ta0_3, tb1_0_1); SB(); tb2_1_1 = CAT(tb2_1_1_l, TRD(B_AT(2, 1, 2, 1) + 512)); PRE(27); SB();
  MM(1, 0, 0, ta0_0, tb1_0_0); SB(); tb1_2_1_l = TRD(B_AT(0, 0, 1, 1)); PRE(28); SB();
  MM(1, 1, 0, ta0_1, tb1_0_0); SB(); tb1_2_1 = CAT(tb1_2_1_l, TRD(B_AT(0, 0, 1, 1) + 512)); PRE(29); SB();
  MM(1, 2, 0, ta0_2, tb1_0_0); SB(); ta2_0 = A_AT(0, 0, 0); PRE(30); SB();
  MM(1, 3, 0, ta0_3, tb1_0_0); SB(); ta2_1 = A_AT(0, 0, 1); PRE(31); SB();
  MM(1, 0, 1, ta1_0, tb1_1_1); SB(); ta2_2 = A_AT(0, 0, 2); PRE(32); SB();
  MM(1, 1, 1, ta1_1, tb1_1_1); SB(); ta2_3 = A_AT(0, 0, 3); PRE(33); SB();
  MM(1, 2, 1, ta1_2, tb1_1_1); SB(); tb1_3_1_l = TRD(B_AT(0, 1, 1, 1)); PRE(34); SB();
  MM(1, 3, 1, ta1_3, tb1_1_1); SB(); tb1_3_1 = CAT(tb1_3_1_l, TRD(B_AT(0, 1, 1, 1) + 512)); PRE(35); SB();
  MM(1, 0, 0, ta1_0, tb1_1_0); SB(); ta3_0 = A_AT(0, 1, 0); PRE(36); SB();
  MM(1, 1, 0, ta1_1, tb1_1_0); SB(); ta3_1 = A_AT(0, 1, 1); PRE(37); SB();
  MM(1, 2, 0, ta1_2, tb1_1_0); SB(); ta3_2 = A_AT(0, 1, 2); PRE(38); SB();
  MM(1, 3, 0, ta1_3, tb1_1_0); SB(); ta3_3 = A_AT(0, 1, 3); PRE(39); SB();
  MM(1, 0, 1, ta2_0, tb1_2_1); SB(); tb1_2_0_l = TRD(B_AT(0, 0, 1, 0)); PRE(40); SB();
  MM(1, 1, 1, ta2_1, tb1_2_1); SB(); tb1_2_0 = CAT(tb1_2_0_l, TRD(B_AT(0, 0, 1, 0) + 512)); PRE(41); SB();
  MM(1, 2, 1, ta2_2, tb1_2_1); SB(); tb1_3_0_l = TRD(B_AT(0, 1, 1, 0)); PRE(42); SB();
  MM(1, 3, 1, ta2_3, tb1_2_1); SB(); tb1_3_0 = CAT(tb1_3_0_l, TRD(B_AT(0, 1, 1, 0) + 512)); PRE(43); SB();
  MM(1, 0, 1, ta3_0, tb1_3_1); SB(); tb2_2_1_l = TRD(B_AT(0, 0, 2, 1)); PRE(44); SB();
  MM(1, 1, 1, ta3_1, tb1_3_1); SB(); tb2_2_1 = CAT(tb2_2_1_l, TRD(B_AT(0, 0, 2, 1) + 512)); PRE(45); SB();
  MM(1, 2, 1, ta3_2, tb1_3_1); SB(); tb2_3_1_l = TRD(B_AT(0, 1, 2, 1)); PRE(46); SB();
  MM(1, 3, 1, ta3_3, tb1_3_1); SB(); tb2_3_1 = CAT(tb2_3_1_l, TRD(B_AT(0, 1, 2, 1) + 512)); PRE(47); SB();
  MM(1, 0, 0, ta2_0, tb1_2_0); SB(); tb2_0_0_l = TRD(B_AT(2, 0, 2, 0)); PK(0, 1, 0, 0); PK(0, 1, 0, 1); PK(0, 1, 1, 0); PK(0, 1, 1, 1); PK(0, 1, 0, 2); SB();
  MM(1, 1, 0, ta2_1, tb1_2_0); SB(); tb2_0_0 = CAT(tb2_0_0_l, TRD(B_AT(2, 0, 2, 0) + 512)); PK(0, 1, 0, 3); PK(0, 1, 0, 7); PK(0, 1, 1, 2); SB();
  MM(1, 2, 0, ta2_2, tb1_2_0); SB(); tb2_1_0_l = TRD(B_AT(2, 1, 2, 0)); PK(0, 1, 1, 3); PK(0, 1, 1, 7); PK(0, 1, 0, 4); PK(0, 1, 0, 5); SB();
  MM(1, 3, 0, ta2_3, tb1_2_0); SB(); tb2_1_0 = CAT(tb2_1_0_l, TRD(B_AT(2, 1, 2, 0) + 512)); PK(0, 1, 1, 4); PK(0, 1, 1, 5); PK(0, 1, 0, 6); PK(0, 1, 1, 6); PK(1, 1, 0, 0); SB();
  MM(1, 0, 0, ta3_0, tb1_3_0); SB(); tb2_2_0_l = TRD(B_AT(0, 0, 2, 0)); PK(1, 1, 0, 1); PK(1, 1, 1, 0); PK(1, 1, 1, 1); PK(1, 1, 0, 2); SB();
  MM(1, 1, 0, ta3_1, tb1_3_0); SB(); tb2_2_0 = CAT(tb2_2_0_l, TRD(B_AT(0, 0, 2, 0) + 512)); PK(1, 1, 0, 3); PK(1, 1, 0, 7); PK(1, 1, 1, 2); SB();
  MM(1, 2, 0, ta3_2, tb1_3_0); SB(); tb2_3_0_l = TRD(B_AT(0, 1, 2, 0)); PK(1, 1, 1, 3); PK(1, 1, 1, 7); PK(1, 1, 0, 4); PK(1, 1, 0, 5); SB();
  MM(1, 3, 0, ta3_3, tb1_3_0); SB(); tb2_3_0 = CAT(tb2_3_0_l, TRD(B_AT(0, 1, 2, 0) + 512)); PK(1, 1, 1, 4); PK(1, 1, 1, 5); PK(1, 1, 0, 6); PK(1, 1, 1, 6); SB();
  MM(2, 0, 1, ta0_0, tb2_0_1); SB(); PK(2, 1, 0, 0); PK(2, 1, 0, 1); PK(2, 1, 1, 0); PK(2, 1, 1, 1); SB();
  MM(2, 1, 1, ta0_1, tb2_0_1); SB(); PK(2, 1, 0, 2); PK(2, 1, 0, 3); SB();
  MM(2, 2, 1, ta0_2, tb2_0_1); SB(); PK(2, 1, 0, 7); PK(2, 1, 1, 2); PK(2, 1, 1, 3); SB();
  MM(2, 3, 1, ta0_3, tb2_0_1); SB(); PK(2, 1, 1, 7); PK(2, 1, 0, 4); PK(2, 1, 0, 5); PK(2, 1, 1, 4); SB();
  MM(2, 0, 1, ta1_0, tb2_1_1); SB(); PK(2, 1, 1, 5); PK(2, 1, 0, 6); PK(2, 1, 1, 6); PK(3, 1, 0, 0); SB();
  MM(2, 1, 1, ta1_1, tb2_1_1); SB(); PK(3, 1, 0, 1); PK(3, 1, 1, 0); PK(3, 1, 1, 1); PK(3, 1, 0, 2); SB();
  MM(2, 2, 1, ta1_2, tb2_1_1); SB(); PK(3, 1, 0, 3); PK(3, 1, 0, 7); PK(3, 1, 1, 2); SB();
  MM(2, 3, 1, ta1_3, tb2_1_1); SB(); PK(3, 1, 1, 3); PK(3, 1, 1, 7); PK(3, 1, 0, 4); SB();
  MM(2, 0, 1, ta2_0, tb2_2_1); SB(); PK(3, 1, 0, 5); PK(3, 1, 1, 4); PK(3, 1, 1, 5); PK(3, 1, 0, 6); SB();
  MM(2, 1, 1, ta2_1, tb2_2_1); SB(); PK(3, 1, 1, 6); PK(0, 0, 0, 0); PK(0, 0, 0, 1); PK(0, 0, 1, 0); SB();
  MM(2, 2, 1, ta2_2, tb2_2_1); SB(); PK(0, 0, 1, 1); PK(0, 0, 0, 2); PK(0, 0, 0, 3); SB();
  MM(2, 3, 1, ta2_3, tb2_2_1); SB(); PK(0, 0, 0, 7); PK(0, 0, 1, 2); PK(0, 0, 1, 3); SB();
  MM(2, 0, 1, ta3_0, tb2_3_1); SB(); PK(0, 0, 1, 7); PK(0, 0, 0, 4); PK(0, 0, 0, 5); PK(0, 0, 1, 4); SB();
  MM(2, 1, 1, ta3_1, tb2_3_1); SB(); PK(0, 0, 1, 5); PK(0, 0, 0, 6); PK(0, 0, 1, 6); PK(1, 0, 0, 0); SB();
  MM(2, 2, 1, ta3_2, tb2_3_1); SB(); PK(1, 0, 0, 1); PK(1, 0, 1, 0); PK(1, 0, 1, 1); PK(1, 0, 0, 2); SB();
  MM(2, 3, 1, ta3_3, tb2_3_1); SB(); PK(1, 0, 0, 3); PK(1, 0, 0, 7); PK(1, 0, 1, 2); SB();
  MM(2, 0, 0, ta0_0, tb2_0_0); SB(); PK(1, 0, 1, 3); PK(1, 0, 1, 7); PK(1, 0, 0, 4); SB();
  MM(2, 1, 0, ta0_1, tb2_0_0); SB(); PK(1, 0, 0, 5); PK(1, 0, 1, 4); PK(1, 0, 1, 5); PK(1, 0, 0, 6); SB();
  MM(2, 2, 0, ta0_2, tb2_0_0); SB(); PK(1, 0, 1, 6); PK(2, 0, 0, 0); PK(2, 0, 0, 1); PK(2, 0, 1, 0); SB();
  MM(2, 3, 0, ta0_3, tb2_0_0); SB(); PK(2, 0, 1, 1); PK(2, 0, 0, 2); PK(2, 0, 0, 3); SB();
  MM(2, 0, 0, ta1_0, tb2_1_0); SB(); PK(2, 0, 0, 7); PK(2, 0, 1, 2); PK(2, 0, 1, 3); SB();
  MM(2, 1, 0, ta1_1, tb2_1_0); SB(); PK(2, 0, 1, 7); PK(2, 0, 0, 4); PK(2, 0, 0, 5); PK(2, 0, 1, 4); SB();
  MM(2, 2, 0, ta1_2, tb2_1_0); SB(); PK(2, 0, 1, 5); PK(2, 0, 0, 6); PK(2, 0, 1, 6); PK(3, 0, 0, 0); SB();
  MM(2, 3, 0, ta1_3, tb2_1_0); SB(); PK(3, 0, 0, 1); PK(3, 0, 1, 0); PK(3, 0, 1, 1); SB();
  MM(2, 0, 0, ta2_0, tb2_2_0); SB(); PK(3, 0, 0, 2); PK(3, 0, 0, 3); SB();
  MM(2, 1, 0, ta2_1, tb2_2_0); SB(); PK(3, 0, 0, 7); PK(3, 0, 1, 2); SB();
  MM(2, 2, 0, ta2_2, tb2_2_0); SB(); PK(3, 0, 1, 3); PK(3, 0, 1, 7); SB();
  MM(2, 3, 0, ta2_3, tb2_2_0); SB(); PK(3, 0, 0, 4); PK(3, 0, 0, 5); PK(3, 0, 1, 4); SB();
  MM(2, 0, 0, ta3_0, tb2_3_0); SB(); PK(3, 0, 1, 5); PK(3, 0, 0, 6); PK(3, 0, 1, 6); SB();
  MM(2, 1, 0, ta3_1, tb2_3_0); SB(); CV(0, 1, 0); CV(0, 1, 1); CV(1, 1, 0); SB();
  MM(2, 2, 0, ta3_2, tb2_3_0); SB(); CV(1, 1, 1); CV(2, 1, 0); CV(2, 1, 1); CV(3, 1, 0); SB();
  MM(2, 3, 0, ta3_3, tb2_3_0); SB(); CV(3, 1, 1); SB();
  const f32x4 zero = {0.f, 0.f, 0.f, 0.f};
#define TAKE(flag, READS)                                                                                  \
  _Pragma("unroll 1") for (unsigned spins = 0; spins < (1u << 22); ++spins) {                              \
    const unsigned f_ = (flag);                                                                            \
    CFENCE();                                                                                              \
    READS;                                                                                                 \
    if (__builtin_expect(rfl(f_) != 0u, 1)) break;                                                         \
  }
  unsigned lidx, ltag;
  {
    phi_fix(kp, kmin);
#define KF(j, n) FRAG(((u32x4){kp[8 * (n) + 4 * (j)], kp[8 * (n) + 4 * (j) + 1], kp[8 * (n) + 4 * (j) + 2], kp[8 * (n) + 4 * (j) + 3]}))
#define VF(j, n) FRAG(((u32x4){vp[8 * (n) + 4 * (j)], vp[8 * (n) + 4 * (j) + 1], vp[8 * (n) + 4 * (j) + 2], vp[8 * (n) + 4 * (j) + 3]}))
    f32x4 s1 = MFMA(KF(0, 1), VF(0, 1), zero), s0 = MFMA(KF(0, 0), VF(0, 1), zero), zs = MFMA(KF(0, 1), FRAG(ones), zero);
    s1 = MFMA(KF(1, 1), VF(1, 1), s1); s0 = MFMA(KF(1, 0), VF(1, 1), s0); zs = MFMA(KF(1, 1), FRAG(ones), zs);
    f32x4 z = MFMA(KF(1, 0), FRAG(ones), MFMA(KF(0, 0), FRAG(ones), zero));
    SB();
    CV(0, 0, 0); CV(0, 0, 1); CV(1, 0, 0); CV(1, 0, 1);
    *LP(f32x4, x1w, 0) = s1; *LP(f32x4, x1w, 1024) = s0; *LP(f32x4, cw, 0) = zs;
    CFENCE();
    *LP(volatile unsigned, cw, W_X1) = one;
    SB();
    CV(2, 0, 0); CV(2, 0, 1); CV(3, 0, 0); CV(3, 0, 1);
    f32x4 kv0 = MFMA(KF(0, 0), VF(0, 0), zero), kv1 = MFMA(KF(0, 1), VF(0, 0), zero);
    kv0 = MFMA(KF(1, 0), VF(1, 0), kv0); kv1 = MFMA(KF(1, 1), VF(1, 0), kv1);
    SB();
    f32x4 p0, p1, pz;
#define X1_RD { p0 = *LP(volatile f32x4, x1r, 0); p1 = *LP(volatile f32x4, x1r, 1024); pz = *LP(volatile f32x4, cr, 0); }
    const unsigned f1 = *LP(volatile unsigned, cr, W_X1);
    CFENCE();
    X1_RD
    SB();
    tb0_0_0_l = TRD(B_AT(2, 0, 0, 0)); tb0_0_0 = CAT(tb0_0_0_l, TRD(B_AT(2, 0, 0, 0) + 512));
    tb0_0_1_l = TRD(B_AT(2, 0, 0, 1)); tb0_0_1 = CAT(tb0_0_1_l, TRD(B_AT(2, 0, 0, 1) + 512));
    tb0_1_0_l = TRD(B_AT(2, 1, 0, 0)); tb0_1_0 = CAT(tb0_1_0_l, TRD(B_AT(2, 1, 0, 0) + 512));
    tb0_1_1_l = TRD(B_AT(2, 1, 0, 1)); tb0_1_1 = CAT(tb0_1_1_l, TRD(B_AT(2, 1, 0, 1) + 512));
    tb0_2_0_l = TRD(B_AT(0, 0, 0, 0)); tb0_2_0 = CAT(tb0_2_0_l, TRD(B_AT(0, 0, 0, 0) + 512));
    tb0_2_1_l = TRD(B_AT(0, 0, 0, 1)); tb0_2_1 = CAT(tb0_2_1_l, TRD(B_AT(0, 0, 0, 1) + 512));
    tb0_3_0_l = TRD(B_AT(0, 1, 0, 0)); tb0_3_0 = CAT(tb0_3_0_l, TRD(B_AT(0, 1, 0, 0) + 512));
    tb0_3_1_l = TRD(B_AT(0, 1, 0, 1)); tb0_3_1 = CAT(tb0_3_1_l, TRD(B_AT(0, 1, 0, 1) + 512));
    SB();
    if (__builtin_expect(!all_true(f1 != 0u), 0)) { TAKE(*LP(volatile unsigned, cr, W_X1), X1_RD); LGKM0(); }
    const f32x4 m0_ = kv0 + p0, m1_ = kv1 + p1;
    float am = fmax_raw(fabsf(m0_[0]), fabsf(m1_[0]));
#pragma unroll
    for (int e = 1; e < 4; ++e) am = fmax_raw(fmax_raw(am, fabsf(m0_[e])), fabsf(m1_[e]));
#define PKH(a_, b_) __builtin_bit_cast(unsigned, __builtin_convertvector((f32x2){(a_), (b_)}, f16x2_t))
    u32x4 hv = {PKH(m0_[0], m0_[1]), PKH(m0_[2], m0_[3]), PKH(m1_[0], m1_[1]), PKH(m1_[2], m1_[3])};
#define Z_STORE(scl) do { unsigned z0_ = zv[0]; asm("v_writelane_b32 %0, %1, 1" : "+v"(z0_) : "s"(scl)); zv[0] = z0_; __builtin_amdgcn_raw_buffer_store_b128(zv, wsr, zo, 0, 16); } while (0)
    PIN(hv);
    if (__builtin_expect(all_true(f2u(am) - 0x3C800000u <= 0x47000000u - 0x3C800000u), 1)) {
      __builtin_amdgcn_raw_buffer_store_b128(hv, wsr, kvo, 0, 16);
      SB();
      u32x4 zv = __builtin_bit_cast(u32x4, z + pz);
      Z_STORE(0x3F800000u);
    } else {
      unsigned ex = max(wave_max(f2u(am) & 0x7FFFFFFFu) >> 23, 15u);
      if (ex >= 121u && ex < 142u) ex = 141u;
      const float inv = u2f((268u - ex) << 23);
      u32x4 hs = {PKH(m0_[0] * inv, m0_[1] * inv), PKH(m0_[2] * inv, m0_[3] * inv), PKH(m1_[0] * inv, m1_[1] * inv), PKH(m1_[2] * inv, m1_[3] * inv)};
      u32x4 zv = __builtin_bit_cast(u32x4, z + pz);
#pragma unroll
      for (int e = 0; e < 4; ++e) { hs[e] = min(hs[e], ~1u); zv[e] = min(zv[e] | (ex != 141u ? 0x80000000u : 0u), ~1u); }
      CFENCE();
      __builtin_amdgcn_raw_buffer_store_b128(hs, wsr, kvo, 0, 16);
      Z_STORE((ex - 14u) << 23);
      CFENCE();
    }
  }
  *LP(volatile unsigned, cw, W_KS) = one;
  unsigned ko, oo, x2w, zfa, zfb, zfp, gta;
  u32x2 eye, ov[4][2];
  u32x4 t[16];
  float gq[4];
  unsigned pq[16];
#define PQ(mi, n, k, st) PHI(acc[0][mi][n], pq[4 * (mi) + 2 * (n) + (k)], qmin, k, st)
#define NOPQ() asm volatile("s_nop 4" : "+v"(acc[0][3][0]), "+v"(acc[0][3][1]))
#define PRQ(i)                                                                                             \
  do {                                                                                                     \
    if ((i) == 0) { lidx = misc[C_IDX + wr]; ltag = misc[C_TAG]; }                                         \
    if ((i) == 2) { zfp = L_CTL + 4 * (C_ZF + hh); PIN(zfp); }                                             \
    if ((i) == 3) { ko = kvo - mt * 1024; PIN(ko); }                                                       \
    if ((i) == 4) { gta = L_GATE + wr * 256 + c16 * 4; PIN(gta); }                                         \
    if ((i) == 5) { zfa = L_ZF + hh * 64 + wr * 8 + g * 16; PIN(zfa); }                                    \
    if ((i) == 6) { zfb = zfa ^ 8u; PIN(zfb); }                                                            \
    if ((i) == 7) { const unsigned o_ = 0x3C00u << (16 * (lane & 1)); eye = (u32x2){(lane & 2) ? 0u : o_, (lane & 2) ? o_ : 0u}; PIN(eye); } \
    if ((i) == 8) { x2w = L_X2 + w * 1024 + lane * 16; PIN(x2w); }                                         \
    if ((i) == 9) { oo = (unsigned)OFF_OF + (unsigned)(((mt * 8 + wr * 4) * NH + head) * 1024) + lane * 16; PIN(oo); } \
  } while (0)
#define SHOT_GATE                                                                                          \
  do {                                                                                                     \
    if (__builtin_expect(!all_true(lidx != 0u), 0)) (void)wait_ge<1>(misc + C_IDX + wr, 1u);        \
    (void)wait_ge<1>(misc + C_GO + hh, 1u);                                                                \
    CFENCE();                                                                                              \
    _Pragma("unroll") for (int i = 0; i < 4; ++i) gq[i] = *LP(volatile float, gta, i * 64);                \
  } while (0)
#define T_LD(i) t[i] = __builtin_amdgcn_raw_buffer_load_b128(wsr, ko + ((i) & 3) * 1024, ((i) >> 2) * 4096, 16)
  SB();
  MM(0, 0, 0, ta0_0, tb0_0_0); SB(); PRQ(0); SB();
  MM(0, 0, 1, ta0_0, tb0_0_1); SB(); PRQ(1); SB();
  MM(0, 0, 0, ta1_0, tb0_1_0); SB(); PRQ(2); SB();
  MM(0, 0, 1, ta1_0, tb0_1_1); SB(); PRQ(3); SB();
  MM(0, 0, 0, ta2_0, tb0_2_0); SB(); PRQ(4); SB();
  MM(0, 0, 1, ta2_0, tb0_2_1); SB(); PRQ(5); SB();
  MM(0, 0, 0, ta3_0, tb0_3_0); SB(); PRQ(6); SB();
  MM(0, 0, 1, ta3_0, tb0_3_1); SB(); PRQ(7); SB();
  MM(0, 1, 0, ta0_1, tb0_0_0); SB(); PRQ(8); SB();
  MM(0, 1, 1, ta0_1, tb0_0_1); SB(); PRQ(9); SB();
  MM(0, 1, 0, ta1_1, tb0_1_0); SB(); PQ(0, 0, 0, 0); PQ(0, 0, 0, 1); PQ(0, 0, 1, 0); SB();
  MM(0, 1, 1, ta1_1, tb0_1_1); SB(); PQ(0, 0, 1, 1); PQ(0, 0, 0, 2); SB();
  MM(0, 1, 0, ta2_1, tb0_2_0); SB(); PQ(0, 0, 0, 3); PQ(0, 0, 0, 7); SB();
  MM(0, 1, 1, ta2_1, tb0_2_1); SB(); PQ(0, 0, 1, 2); PQ(0, 0, 1, 3); SB();
  MM(0, 1, 0, ta3_1, tb0_3_0); SB(); PQ(0, 0, 1, 7); PQ(0, 0, 0, 4); PQ(0, 0, 0, 5); SB();
  MM(0, 1, 1, ta3_1, tb0_3_1); SB(); PQ(0, 0, 1, 4); PQ(0, 0, 1, 5); PQ(0, 0, 0, 6); SB();
  MM(0, 2, 0, ta0_2, tb0_0_0); SB(); PQ(0, 0, 1, 6); PQ(0, 1, 0, 0); PQ(0, 1, 0, 1); SB();
  MM(0, 2, 1, ta0_2, tb0_0_1); SB(); PQ(0, 1, 1, 0); PQ(0, 1, 1, 1); PQ(0, 1, 0, 2); SB();
  MM(0, 2, 0, ta1_2, tb0_1_0); SB(); PQ(0, 1, 0, 3); PQ(0, 1, 0, 7); SB();
  MM(0, 2, 1, ta1_2, tb0_1_1); SB(); PQ(0, 1, 1, 2); PQ(0, 1, 1, 3); SB();
  MM(0, 2, 0, ta2_2, tb0_2_0); SB(); PQ(0, 1, 1, 7); PQ(0, 1, 0, 4); PQ(0, 1, 0, 5); SB();
  MM(0, 2, 1, ta2_2, tb0_2_1); SB(); PQ(0, 1, 1, 4); PQ(0, 1, 1, 5); PQ(0, 1, 0, 6); SB();
  MM(0, 2, 0, ta3_2, tb0_3_0); SB(); PQ(0, 1, 1, 6); PQ(1, 0, 0, 0); PQ(1, 0, 0, 1); SB();
  MM(0, 2, 1, ta3_2, tb0_3_1); SB(); PQ(1, 0, 1, 0); PQ(1, 0, 1, 1); PQ(1, 0, 0, 2); SB();
  MM(0, 3, 0, ta0_3, tb0_0_0); SB(); PQ(1, 0, 0, 3); PQ(1, 0, 0, 7); SB();
  MM(0, 3, 1, ta0_3, tb0_0_1); SB(); PQ(1, 0, 1, 2); PQ(1, 0, 1, 3); SB();
  MM(0, 3, 0, ta1_3, tb0_1_0); SB(); PQ(1, 0, 1, 7); PQ(1, 0, 0, 4); PQ(1, 0, 0, 5); SB();
  MM(0, 3, 1, ta1_3, tb0_1_1); SB(); PQ(1, 0, 1, 4); PQ(1, 0, 1, 5); PQ(1, 0, 0, 6); SB();
  MM(0, 3, 0, ta2_3, tb0_2_0); SB(); PQ(1, 0, 1, 6); PQ(1, 1, 0, 0); PQ(1, 1, 0, 1); SB();
  MM(0, 3, 1, ta2_3, tb0_2_1); SB(); PQ(1, 1, 1, 0); PQ(1, 1, 1, 1); PQ(1, 1, 0, 2); SB();
  MM(0, 3, 0, ta3_3, tb0_3_0); SB(); PQ(1, 1, 0, 3); PQ(1, 1, 0, 7); SB();
  MM(0, 3, 1, ta3_3, tb0_3_1); SB(); PQ(1, 1, 1, 2); PQ(1, 1, 1, 3); SB();
  PQ(1, 1, 1, 7); SB();
  PQ(1, 1, 0, 4); SB();
  PQ(1, 1, 0, 5); SB();
  PQ(1, 1, 1, 4); SB();
  PQ(1, 1, 1, 5); SB();
  PQ(1, 1, 0, 6); SB();
  PQ(1, 1, 1, 6); SB();
  PQ(2, 0, 0, 0); SB();
  PQ(2, 0, 0, 1); SB();
  PQ(2, 0, 1, 0); SB();
  PQ(2, 0, 1, 1); SB();
  PQ(2, 0, 0, 2); SB();
  PQ(2, 0, 0, 3); SB();
  PQ(2, 0, 0, 7); SB();
  PQ(2, 0, 1, 2); SB();
  PQ(2, 0, 1, 3); SB();
  PQ(2, 0, 1, 7); SB();
  PQ(2, 0, 0, 4); SB();
  PQ(2, 0, 0, 5); SB();
  PQ(2, 0, 1, 4); SB();
  PQ(2, 0, 1, 5); SB();
  PQ(2, 0, 0, 6); SB();
  PQ(2, 0, 1, 6); SB();
  PQ(2, 1, 0, 0); SB();
  PQ(2, 1, 0, 1); SB();
  PQ(2, 1, 1, 0); SB();
  PQ(2, 1, 1, 1); SB();
  PQ(2, 1, 0, 2); SB();
  PQ(2, 1, 0, 3); SB();
  PQ(2, 1, 0, 7); SB();
  PQ(2, 1, 1, 2); SB();
  PQ(2, 1, 1, 3); SB();
  PQ(2, 1, 1, 7); SB();
  PQ(2, 1, 0, 4); SB();
  PQ(2, 1, 0, 5); SB();
  PQ(2, 1, 1, 4); SB();
  PQ(2, 1, 1, 5); SB();
  PQ(2, 1, 0, 6); SB();
  PQ(2, 1, 1, 6); SB();
  NOPQ();
  PQ(3, 0, 0, 0); SB();
  PQ(3, 0, 0, 1); SB();
  PQ(3, 0, 1, 0); SB();
  PQ(3, 0, 1, 1); SB();
  PQ(3, 0, 0, 2); SB();
  PQ(3, 0, 0, 3); SB();
  PQ(3, 0, 0, 7); SB();
  PQ(3, 0, 1, 2); SB();
  PQ(3, 0, 1, 3); SB();
  PQ(3, 0, 1, 7); SB();
  PQ(3, 0, 0, 4); SB();
  PQ(3, 0, 0, 5); SB();
  PQ(3, 0, 1, 4); SB();
  PQ(3, 0, 1, 5); SB();
  PQ(3, 0, 0, 6); SB();
  PQ(3, 0, 1, 6); SB();
  PQ(3, 1, 0, 0); SB();
  PQ(3, 1, 0, 1); SB();
  PQ(3, 1, 1, 0); SB();
  PQ(3, 1, 1, 1); SB();
  PQ(3, 1, 0, 2); SB();
  PQ(3, 1, 0, 3); SB();
  PQ(3, 1, 0, 7); SB();
  PQ(3, 1, 1, 2); SB();
  PQ(3, 1, 1, 3); SB();
  PQ(3, 1, 1, 7); SB();
  PQ(3, 1, 0, 4); SB();
  PQ(3, 1, 0, 5); SB();
  PQ(3, 1, 1, 4); SB();
  PQ(3, 1, 1, 5); SB();
  PQ(3, 1, 0, 6); SB();
  PQ(3, 1, 1, 6); SB();
  SHOT_GATE;
  T_LD(0);
  T_LD(1);
  T_LD(2);
  T_LD(3);
  T_LD(4);
  T_LD(5);
  T_LD(6);
  T_LD(7);
  T_LD(8);
  T_LD(9);
  T_LD(10);
  T_LD(11);
  T_LD(12);
  T_LD(13);
  T_LD(14);
  T_LD(15);
  *LP(volatile unsigned, cw, W_AG) = one;
  phi_fix(pq, qmin);
  const unsigned tag = rfl(ltag);
  {
#define PF(mi) FRAG(((u32x4){pq[4 * (mi)], pq[4 * (mi) + 1], pq[4 * (mi) + 2], pq[4 * (mi) + 3]}))
#define H4(v, k) __builtin_bit_cast(f16x4_t, (u32x2){(v)[2 * (k)], (v)[2 * (k) + 1]})
    f32x4 s0 = zero, s1 = zero;
#define KV_MM(i0, i1)                                                                                      \
  _Pragma("unroll") for (int i = (i0); i < (i1); ++i) {                                                    \
    s0 = __builtin_amdgcn_mfma_f32_4x4x4f16(__builtin_bit_cast(f16x4_t, eye), H4(t[i], 0), s0, 0, 0, 0);   \
    s1 = __builtin_amdgcn_mfma_f32_4x4x4f16(__builtin_bit_cast(f16x4_t, eye), H4(t[i], 1), s1, 0, 0, 0);   \
  }
    float rc[4];
    u32x2 z0, z1;
#define Z_RD { z0 = *LP(volatile u32x2, zfa, 0); z1 = *LP(volatile u32x2, zfb, 0); }
    f32x4 d[4];
#define DEN1() do { const u32x4 zf = {z0[0], z0[1], z1[0], z1[1]}; _Pragma("unroll") for (int mi = 0; mi < 4; ++mi) d[mi] = MFMA(FRAG(zf), PF(mi), zero); } while (0)
#define DEN2()                                                                                             \
  do {                                                                                                     \
    _Pragma("unroll") for (int mi = 0; mi < 4; ++mi) rc[mi] = __builtin_amdgcn_rcpf(d[mi][0] + 1e-6f);     \
    _Pragma("unroll") for (int mi = 0; mi < 4; ++mi) { rc[mi] *= gq[mi]; PIN(rc[mi]); }                    \
  } while (0)
    KV_MM(0, DENAT - 4);
    SB();
    unsigned zfl = *LP(volatile unsigned, zfp, 0);
    CFENCE();
    Z_RD
    SB();
    KV_MM(DENAT - 4, DENAT);
    SB();
    DEN1();
    SB();
    KV_MM(DENAT, DENAT + 2);
    SB();
    DEN2();
    const u64 zbad = __builtin_amdgcn_ballot_w64(zfl != 1u);
    SB();
    KV_MM(DENAT + 2, 16);
    if (__builtin_expect((__builtin_amdgcn_ballot_w64(__builtin_isunordered(s0[0], s1[0])) | zbad) != 0ull, 0)) {
      unsigned todo = 15u;
#pragma unroll 1
      for (unsigned spins = 0; spins <= SPIN_CAP; ++spins) {
        unsigned miss = 0;
#pragma unroll
        for (int q = 0; q < 4; ++q)
          if (todo & (1u << q)) { if (!all_true(max(max(MX4(t[4 * q]), MX4(t[4 * q + 1])), max(MX4(t[4 * q + 2]), MX4(t[4 * q + 3]))) != ~0u)) miss |= 1u << q; }
        todo = miss;
        if (todo == 0u) break;
        __builtin_amdgcn_s_sleep(2);
        CFENCE();
#pragma unroll
        for (int q = 0; q < 4; ++q)
          if (todo & (1u << q)) { T_LD(4 * q); T_LD(4 * q + 1); T_LD(4 * q + 2); T_LD(4 * q + 3); }
      }
      s0 = zero; s1 = zero;
      KV_MM(0, 16);
      zfl = rfl(zfl);
      if (zfl == 0u) { TAKE(zfl = misc[C_ZF + hh], Z_RD); zfl = rfl(zfl); DEN1(); DEN2(); }
      if (zfl != 1u) {
        unsigned sc;
#pragma unroll 1
        for (unsigned spins = 0; spins <= SPIN_CAP; ++spins) {
          CFENCE();
          sc = __builtin_amdgcn_raw_buffer_load_b32(wsr, (unsigned)OFF_ZP + ZSC + (rec - mt + c16) * 16, 0, 16);
          if (all_true(sc != ~0u)) break;
        }
        s0 = zero; s1 = zero;
#pragma unroll
        for (int i = 0; i < 16; ++i) {
          const float s_ = u2f((unsigned)__builtin_amdgcn_readlane((int)sc, i));
#pragma unroll
          for (int e = 0; e < 4; ++e) {
            const unsigned tw = t[i][e];
            const f16x2_t h = __builtin_bit_cast(f16x2_t, tw);
            f32x4& d = e < 2 ? s0 : s1;
            d[2 * (e & 1)] = __builtin_fmaf((float)h[0], s_, d[2 * (e & 1)]); d[2 * (e & 1) + 1] = __builtin_fmaf((float)h[1], s_, d[2 * (e & 1) + 1]);
          }
        }
      }
    }
    const u32x4 kown = {cvt_pk(s0[0], s0[1]), cvt_pk(s0[2], s0[3]), cvt_pk(s1[0], s1[1]), cvt_pk(s1[2], s1[3])};
    *LP(u32x2, x2w, 8) = (u32x2){kown[0], kown[1]}; *LP(u32x2, x2w, 0) = (u32x2){kown[2], kown[3]};
    CFENCE();
    *LP(volatile unsigned, cw, W_X2) = one;
    SB();
    f32x4 o[4];
    u32x4 koth;
#pragma unroll
    for (int mi = 0; mi < 4; ++mi) o[mi] = MFMA(FRAG(kown), PF(mi), zero);
    const unsigned x2r = x2w ^ 2048u;
    SB();
#define O_CVT(mi, h) do { ov[mi][h] = (u32x2){cvt_pk(o[mi][0] * rc[mi], o[mi][1] * rc[mi]), cvt_pk(o[mi][2] * rc[mi], o[mi][3] * rc[mi])}; if ((h) == 0) PIN(ov[mi][0]); } while (0)
    O_CVT(0, 0); SB();
    const unsigned f2 = *LP(volatile unsigned, cr, W_X2);
    CFENCE();
    koth = *LP(volatile u32x4, x2r, 0);
    SB();
    O_CVT(1, 0); O_CVT(2, 0); O_CVT(3, 0);
    SB();
    if (__builtin_expect(!all_true(f2 != 0u), 0)) TAKE(*LP(volatile unsigned, cr, W_X2), koth = *LP(volatile u32x4, x2r, 0));
#pragma unroll
    for (int mi = 0; mi < 4; ++mi) o[mi] = MFMA(FRAG(koth), PF(mi), zero);
    SB();
    unsigned oom[4];
#pragma unroll
    for (int mi = 0; mi < 4; ++mi) { oom[mi] = oo + mi * (NH * 1024); PIN(oom[mi]); }
    SB();
#pragma unroll
    for (int mi = 0; mi < 4; ++mi) { O_CVT(mi, 1); __builtin_amdgcn_raw_buffer_store_b128((u32x4){ov[mi][0][0], ov[mi][0][1], ov[mi][1][0], ov[mi][1][1]}, wsr, oom[mi], 0, 16); SB(); }
  }
  SB();
  f32x4 oc[4][2];
  unsigned xa[4], xb[2], so[4];
  unsigned rlo, rhi;
  {
    u32x2 xl[8];
#pragma unroll
    for (int n = 0; n < 4; ++n) { xa[n] = (unsigned)(((32 * w + c16) * 128 + (((g >> 1) ^ (c16 >> 1)) * 16) + (g & 1) * 8) ^ (n * 32)); PIN(xa[n]); }
#pragma unroll
    for (int i = 0; i < 8; ++i) xl[i] = *LP(volatile u32x2, xa[i & 3], (i >> 2) * 2048);
    const unsigned kw = w * 4096 + lane * 16, wlo = hh ? x1w : kw, whi = hh ? kw : x1w;
    rlo = hh ? x1w ^ 2048u : kw; rhi = hh ? kw : x1w ^ 2048u;
#pragma unroll
    for (int mi = 0; mi < 4; ++mi) *LP(volatile u32x4, mi < 2 ? wlo : whi, (mi & 1) * 1024) = (u32x4){ov[mi][0][0], ov[mi][0][1], ov[mi][1][0], ov[mi][1][1]};
    *LP(volatile unsigned, cw, W_X1) = 2u;
#pragma unroll
    for (int i = 0; i < 8; ++i) {
      const int tt = i >> 2;
      const float omg = bfr(1.0f - (tt ? (hh ? gq[3] : gq[1]) : (hh ? gq[2] : gq[0])));
#pragma unroll
      for (int k = 0; k < 2; ++k) {
        const unsigned pr = cvt_pk(omg * blo(xl[i][k]), omg * bhi(xl[i][k]));
        oc[i & 3][tt][2 * k] = blo(pr); oc[i & 3][tt][2 * k + 1] = bhi(pr);
      }
      PIN(oc[i & 3][tt]);
    }
#pragma unroll
    for (int i = 0; i < 2; ++i) { xb[i] = (unsigned)((32 * w + 8 * i + (lane >> 3)) * 128 + (((lane & 7) ^ (4 * i + g)) * 16)); PIN(xb[i]); }
#pragma unroll
    for (int i = 0; i < 4; ++i) { so[i] = (unsigned)(((m0 + 32 * w + 8 * i + (lane >> 3)) * DM + 64 * nt3) * 2 + (lane & 7) * 16); PIN(so[i]); }
  }
  int wof[2][2][4];
#pragma unroll
  for (int n = 0; n < 16; ++n) {
    wof[n >> 3][(n >> 2) & 1][n & 3] = 16384 + ((n >> 2) & 1) * 65536 + (((n >> 3) ^ wr) * 16 + 4 * g + (c16 >> 2)) * 128 + (((n & 3) ^ fsw) * 32) + (c16 & 3) * 8;
    PIN(wof[n >> 3][(n >> 2) & 1][n & 3]);
  }
  unsigned ob0 = (unsigned)OFF_OF + (unsigned)(((mt3 * 8 + 2 * w) * NH) * 1024 + lane * 16), ob1 = ob0 + NH * 1024;
  int cs1 = CH(1) * 2048, cs2 = CH(2) * 2048, cs3 = CH(3) * 2048;
  bf16x8 wf[2][4], wl[4];
  s16x4 tl2;
#define W_AT(hd, n, r) (smem + wof[r][WOPOS((hd) >> 1) >> 3][n] + (WOPOS((hd) >> 1) & 7) * 8192 + ((hd) & 1) * 4096)
  const unsigned lw = min(misc[C_WO], misc[C_WO + 1]);
  PIN(ob0); PIN(ob1); asm volatile("" : "+s"(cs1), "+s"(cs2), "+s"(cs3));
  SB();
  WAIT_VM(0);
  if (lane == 0) {
    const unsigned old = __hip_atomic_fetch_add((lu32*)(smem + L_CTL) + C_OARR, 1u, RLX_WG);
    if (old == 3u) {
      __hip_atomic_store((gu32*)(ws + OFF_OFLAG + (mt * 16 + nt) * 128), tag, RLX_AGENT);
      misc[C_T6] = (unsigned)__builtin_amdgcn_s_memrealtime() | 1u;
    }
  }
  CFENCE();

  if (__builtin_expect(rfl(lw) == 0u, 0)) { (void)wait_ge<1>(misc + C_WO, 1u); (void)wait_ge<1>(misc + C_WO + 1, 1u); }
  CFENCE();
#pragma unroll
  for (int n = 0; n < 4; ++n) wf[0][n] = CAT(TRD(W_AT(0, n, 0)), TRD(W_AT(0, n, 1)));
  u32x4 of[OPF + 1][4];
  unsigned rdy = 0;
#define O_READY(c) do { if (__builtin_expect(rdy <= (unsigned)(c), 0)) rdy = wait_ge<0>(misc + C_ORDY, (unsigned)(c) + 1u); CFENCE(); } while (0)
#define O_LD1(c, k4) of[(c) % (OPF + 1)][k4] = __builtin_amdgcn_raw_buffer_load_b128(wsr, (((k4) & 1) ? ob1 : ob0) + ((k4) >> 1) * 1024, (c) == 1 ? cs1 : (c) == 2 ? cs2 : (c) == 3 ? cs3 : CH(c) * 2048, 16)
  TAKE(*LP(volatile unsigned, cw ^ 128u, W_X1) >> 1, { _Pragma("unroll") for (int k = 0; k < 4; ++k) of[0][k] = *LP(volatile u32x4, k < 2 ? rlo : rhi, (k & 1) * 1024); });
  unsigned li = 0;
#define P3_MM(c, j)                                                                                        \
  do {                                                                                                     \
    constexpr int hd = 2 * (c) + ((j) >> 3), n = ((j) >> 1) & 3;                                           \
    AMFMA(oc[n][(j) & 1], wf[(j) >> 3][n], of[(c) % (OPF + 1)][2 * ((j) >> 3) + ((j) & 1)]); SB();         \
    if (((j) & 1) == 0) tlo = TRD(W_AT(hd + 1, n, 0)); else wf[1 - ((j) >> 3)][n] = CAT(tlo, TRD(W_AT(hd + 1, n, 1))); \
    if ((c) > 0 && (c) + OPF < 16) {                                                                       \
      if ((j) == 0) O_READY((c) + OPF);                                                                    \
      if (((j) & 3) == 1) O_LD1((c) + OPF, (c) + OPF == 15 ? (((j) >> 3) | (((j) >> 1) & 2)) : (j) >> 2);  \
    }                                                                                                      \
    if ((c) == 14 && (j) < 8) { if (((j) & 1) == 0) tl2 = TRD(W_AT(31, n, 0)); else wl[n] = CAT(tl2, TRD(W_AT(31, n, 1))); } \
    if ((c) == 14 && (j) == 8) li = misc[C_IDX];                                                           \
    SB();                                                                                                  \
  } while (0)
#define P3_4(c, j) P3_MM(c, j); P3_MM(c, (j) + 1); P3_MM(c, (j) + 2); P3_MM(c, (j) + 3)
#define P3_C(c) P3_4(c, 0); P3_4(c, 4); P3_4(c, 8); P3_4(c, 12)
  P3_C(0);
#pragma unroll
  for (int c = 1; c <= OPF; ++c) { O_READY(c); O_LD1(c, 0); SB(); O_LD1(c, 1); SB(); O_LD1(c, 2); SB(); O_LD1(c, 3); SB(); }
  P3_C(1); P3_C(2); P3_C(3); P3_C(4); P3_C(5); P3_C(6); P3_C(7);
  P3_C(8); P3_C(9); P3_C(10); P3_C(11); P3_C(12); P3_C(13); P3_C(14);
  li = rfl(li);
  if (__builtin_expect(li == 0u, 0)) li = wait_ge<1>(misc + C_IDX, 1u);
  const int idx = (int)li - 1;
  if ((idx >> 5) == mt3 * 4 + w) {
    f32x4 dl[4];
    NOP8(oc);
    TAKE(misc[C_Y], { _Pragma("unroll") for (int n = 0; n < 4; ++n) dl[n] = *(volatile lf32x4*)(smem + L_RED + 2048 + n * 64 + g * 16); });
    const bool mine = c16 == (idx & 15);
    if (idx & 16) {
#pragma unroll
      for (int n = 0; n < 4; ++n) oc[n][1] += mine ? dl[n] : zero;
    } else {
#pragma unroll
      for (int n = 0; n < 4; ++n) oc[n][0] += mine ? dl[n] : zero;
    }
    NOP8(oc);
  }
  u32x4 ot[4];
  const __amdgpu_buffer_rsrc_t outr = MKRSRC(out, SEQ * DM * 2);
#define E_MM(tt, h, n) do { AMFMA(oc[n][tt], (h) ? wl[n] : wf[0][n], of[15 % (OPF + 1)][2 * (h) + (tt)]); SB(); } while (0)
#define E_WR(tt, n) do { *LP(volatile u32x2, xa[n], (tt) * 2048) = (u32x2){cvt_pk(oc[n][tt][0], oc[n][tt][1]), cvt_pk(oc[n][tt][2], oc[n][tt][3])}; SB(); } while (0)
#define E_RD(tt) do { ot[2 * (tt)] = *LP(volatile u32x4, xb[0], (tt) * 2048); ot[2 * (tt) + 1] = *LP(volatile u32x4, xb[1], (tt) * 2048); SB(); } while (0)
#define E_ST(i) do { __builtin_amdgcn_raw_buffer_store_b128(ot[i], outr, so[i], 0, 17); SB(); } while (0)
#define E_NOP(tt) asm volatile("s_nop 4" : "+v"(oc[0][tt]))
  SB();
  E_MM(0, 0, 0); E_MM(0, 0, 1); E_MM(0, 0, 2); E_MM(0, 0, 3); E_MM(0, 1, 0); E_MM(0, 1, 1); E_MM(0, 1, 2); E_MM(0, 1, 3);
  E_NOP(0); E_WR(0, 0); E_WR(0, 1);
  E_MM(1, 0, 0); E_WR(0, 2); E_MM(1, 0, 1); E_WR(0, 3); E_MM(1, 0, 2); E_RD(0); E_MM(1, 0, 3);
  E_MM(1, 1, 0); E_MM(1, 1, 1); E_MM(1, 1, 2); E_MM(1, 1, 3);
  E_ST(0); E_ST(1);
  E_NOP(1); E_WR(1, 0); E_WR(1, 1); E_WR(1, 2); E_WR(1, 3); E_RD(1);
  E_ST(2); E_ST(3);
}

extern "C" void kernel_launch(void* const* d_in, const int* in_sizes, int n_in,
                              void* d_out, int out_size, void* d_ws, size_t ws_size,
                              hipStream_t stream) {
  char* ws = (char*)d_ws;
  (void)hipFuncSetAttribute((const void*)fused_kernel, hipFuncAttributeMaxDynamicSharedMemorySize, L_TOTAL);
  fused_kernel<<<256, 512, L_TOTAL, stream>>>((const u16*)d_in[0], (const u16*)d_in[1], (const u16*)d_in[2], ws, (u16*)d_out,
                                              (const u16*)d_in[4], (const u16*)d_in[8], (const u16*)d_in[5], (const u16*)d_in[6],
                                              (const u16*)d_in[7]);
}
```

```cpp
#include <hip/hip_runtime.h>
#include <hip/hip_bf16.h>
#include <stdint.h>

typedef unsigned short u16;
typedef unsigned long long u64;
typedef __attribute__((ext_vector_type(8))) short bf16x8;
typedef __attribute__((ext_vector_type(4))) short s16x4;
typedef __attribute__((ext_vector_type(4))) float f32x4;
typedef __attribute__((ext_vector_type(2))) float f32x2;
typedef __attribute__((ext_vector_type(2))) __bf16 bf16x2_t;
typedef __attribute__((ext_vector_type(2))) _Float16 f16x2_t;
typedef __attribute__((ext_vector_type(4))) _Float16 f16x4_t;
typedef __attribute__((ext_vector_type(4))) unsigned int u32x4;
typedef __attribute__((ext_vector_type(2))) unsigned int u32x2;
typedef __attribute__((ext_vector_type(8))) unsigned int u32x8;
typedef __attribute__((address_space(3))) s16x4* lds_s16x4_ptr;
typedef __attribute__((address_space(1))) unsigned gu32;
typedef __attribute__((address_space(1))) u64 gu64;
typedef __attribute__((address_space(3))) unsigned lu32;
typedef __attribute__((address_space(3))) u32x2 lu32x2;
typedef __attribute__((address_space(3))) u32x4 lu32x4;
typedef __attribute__((address_space(3))) f32x4 lf32x4;
#define RLX_AGENT __ATOMIC_RELAXED, __HIP_MEMORY_SCOPE_AGENT

#if defined(__HIP_DEVICE_COMPILE__)
#define NOPK __attribute__((target("no-packed-fp32-ops")))
#else
#define NOPK
#endif

#define SEQ 2048
#define DM 1024
#define N1 3072
#define FF 3584
#define NH 32

#define OFF_EPOCH  ((size_t)0)
#define OFF_RG     ((size_t)4096)
#define OFF_GG     ((size_t)16384)
#define OFF_GU     ((size_t)262144)
#define OFF_YG     ((size_t)524288)
#define OFF_KVP    ((size_t)1 << 20)
#define OFF_ZP     ((size_t)2 << 20)
#define ZSC        65536u
#define OFF_OFLAG  (((size_t)3 << 20) + 65536)
#define OFF_OF     ((size_t)4 << 20)

#define SPIN_CAP (1u << 18)

__device__ __forceinline__ float u2f(unsigned u) { return __builtin_bit_cast(float, u); }
__device__ __forceinline__ unsigned f2u(float f) { return __builtin_bit_cast(unsigned, f); }
__device__ __forceinline__ bool all_true(bool ok) { return __builtin_amdgcn_ballot_w64(!ok) == 0ull; }
__device__ __forceinline__ unsigned cvt_pk(float a, float b) {
  f32x2 v = {a, b};
  return __builtin_bit_cast(unsigned, __builtin_convertvector(v, bf16x2_t));
}
__device__ __forceinline__ float blo(unsigned u) { return u2f(u << 16); }
__device__ __forceinline__ float bhi(unsigned u) { return u2f(u & 0xFFFF0000u); }
__device__ __forceinline__ float bfr(float f) { return blo(cvt_pk(f, f)); }
__device__ __forceinline__ float fmax_raw(float a, float b) { return __builtin_elementwise_maximum(a, b); }
__device__ __forceinline__ unsigned eps2(unsigned p) { return cvt_pk(blo(p) + 1e-6f, bhi(p) + 1e-6f); }
template <int N>
__device__ __forceinline__ void phi_fix(unsigned (&p)[N], float umin) {
  if (__builtin_expect(__builtin_amdgcn_ballot_w64(umin < -5.9f) != 0ull, 0)) {
#pragma unroll
    for (int i = 0; i < N; ++i) p[i] = eps2(p[i]);
  }
}
__device__ __forceinline__ float sigmoid_f(float x) { return 1.0f / (1.0f + __expf(-x)); }

template <int CTRL, int RM = 0xf> __device__ __forceinline__ int dppi(int old, int v) {
  return __builtin_amdgcn_update_dpp(old, v, CTRL, RM, 0xf, false);
}
template <int CTRL, int RM = 0xf> __device__ __forceinline__ float dppf(float v) {
  return __builtin_bit_cast(float, dppi<CTRL, RM>(0, __builtin_bit_cast(int, v)));
}
__device__ __forceinline__ unsigned wave_max(unsigned v) {
  v = max(v, (unsigned)dppi<0x111>(0, (int)v)); v = max(v, (unsigned)dppi<0x112>(0, (int)v));
  v = max(v, (unsigned)dppi<0x114>(0, (int)v)); v = max(v, (unsigned)dppi<0x118>(0, (int)v));
  v = max(v, (unsigned)dppi<0x142, 0xa>(0, (int)v)); v = max(v, (unsigned)dppi<0x143, 0xc>(0, (int)v));
  return (unsigned)__builtin_amdgcn_readlane((int)v, 63);
}
__device__ __forceinline__ float merge32(float a, float b) {
  const auto r = __builtin_amdgcn_permlane32_swap(f2u(a), f2u(b), false, false);
  return u2f(r[0]) + u2f(r[1]);
}
__device__ __forceinline__ float merge16(float a, float b) {
  const auto r = __builtin_amdgcn_permlane16_swap(f2u(a), f2u(b), false, false);
  return u2f(r[0]) + u2f(r[1]);
}
typedef __attribute__((address_space(3))) void* ldsp;
template <int IMM>
__device__ __forceinline__ void bufl16(__amdgpu_buffer_rsrc_t r, unsigned l, unsigned voff, unsigned soff) {
#if defined(__HIP_DEVICE_COMPILE__)
  __builtin_amdgcn_raw_ptr_buffer_load_lds(r, (ldsp)(uintptr_t)l, 16, voff, soff, IMM, 0);
#endif
}
#define LP(T, base, imm) ((__attribute__((address_space(3))) T*)(uintptr_t)(unsigned)((base) + (imm)))
#define MKRSRC(p, n) __builtin_amdgcn_make_buffer_rsrc((void*)(p), 0, (n), 0x00020000)
#define WAIT_VM(n) asm volatile("s_waitcnt vmcnt(%0)" ::"n"(n) : "memory")
#define WAIT_LGKM0() asm volatile("s_waitcnt lgkmcnt(0)" ::: "memory")
#define BAR() do { __builtin_amdgcn_sched_barrier(0); asm volatile("s_barrier" ::: "memory"); __builtin_amdgcn_sched_barrier(0); } while (0)
#define CFENCE() asm volatile("" ::: "memory")
#define MFMA(a, b, c) __builtin_amdgcn_mfma_f32_16x16x32_bf16((a), (b), (c), 0, 0, 0)
#define PIN(v) asm volatile("" : "+v"(v))
#define AMFMA(acc, a, b) asm volatile("v_mfma_f32_16x16x32_bf16 %0, %1, %2, %0" : "+v"(acc) : "v"(a), "v"(b))
#define NOP8(t) asm volatile("s_nop 7" : "+v"(t[0][0]), "+v"(t[0][1]), "+v"(t[1][0]), "+v"(t[1][1]), "+v"(t[2][0]), "+v"(t[2][1]), "+v"(t[3][0]), "+v"(t[3][1]))
#define SB() __builtin_amdgcn_sched_barrier(0)
#define TRD(p) ((s16x4)__builtin_amdgcn_ds_read_tr16_b64_v4i16((lds_s16x4_ptr)(p)))
#define CAT(lo, hi) __builtin_shufflevector(lo, hi, 0, 1, 2, 3, 4, 5, 6, 7)
#define FRAG(x) __builtin_bit_cast(bf16x8, x)

#define PACE   57
#define BURST  30
#define R_AT   2
#define R_GAP  2
#define CPF_N  20
#define WO_E   6
#define WO_ES  4
#define WO_M   9
#define G_T    35
#define P5_T   30
#define GU_AH  8
#define GU_E   4
#define DN_E   7
#define DENAT  12
#define OPF    3

__device__ __forceinline__ unsigned rfl(unsigned v) { return (unsigned)__builtin_amdgcn_readfirstlane((int)v); }
template <int SLEEP>
__device__ __forceinline__ unsigned wait_ge(volatile lu32* p, unsigned need) {
  unsigned v = 0;
#pragma unroll 1
  for (unsigned spins = 0; spins < (1u << 22); ++spins) {
    v = rfl(*p);
    if (v >= need) break;
    if (SLEEP) __builtin_amdgcn_s_sleep(SLEEP);
  }
  CFENCE();
  return v;
}

#define SLOT     40960
#define L_X1     147456
#define L_X2     155648
#define L_CW     159744
#define L_ZF     160256
#define L_GATE   160384
#define L_RED    160896
#define L_H      163200
#define L_CTL    163712
#define L_TOTAL  163840
#define W_X1   64
#define W_X2   68
#define W_AG   72
#define W_KS   76
#define WFLAGS(f) (*(volatile lu32*)(smem + L_CW + (lane & 3) * 128 + (f)))
#define WOPOS(c) ((c) < 3 ? 13 + (c) : (c) < 8 ? (c) : (c) < 11 ? (c) - 8 : (c) - 3)
#define C_F15  0
#define C_WO   2
#define C_ZF   4
#define C_TAG  8
#define C_IDX  10
#define C_GO   12
#define C_ORDY 18
#define C_GU   19
#define C_DN   20
#define C_Y    23

NOPK __global__ __launch_bounds__(512, 2) void fused_kernel(const u16* __restrict__ x, const u16* __restrict__ wqkv,
                                                            const u16* __restrict__ wo, char* ws, u16* __restrict__ out,
                                                            const u16* __restrict__ w_r, const u16* __restrict__ gate_w,
                                                            const u16* __restrict__ w_gate, const u16* __restrict__ w_up,
                                                            const u16* __restrict__ w_down) {
  extern __shared__ __attribute__((aligned(16))) char smem[];
  asm volatile("s_load_dwordx2 s[100:101], %0, 60" :: "s"(__builtin_amdgcn_kernarg_segment_ptr()) : "s100", "s101");
  SB();
  const int tid = threadIdx.x, lane = tid & 63;
  const int w = __builtin_amdgcn_readfirstlane(tid >> 6);
  u64 pc0;
  asm volatile("s_getpc_b64 %0" : "=s"(pc0));
  const int bid = blockIdx.x, xcd = bid & 7, ix = bid >> 3;
  const int ml = ix >> 2, nl = ix & 3;
  const int mt = (xcd >> 2) * 8 + ml, nt = (xcd & 3) * 4 + nl;
  const int m0 = mt * 128;
  const int mt3 = mt, nt3 = nt ^ 1;
#define CH(c) (((4 * (xcd & 3) + (c)) & 15) ^ ((c) < 4 ? nl : 0))
  volatile lu32* misc = (volatile lu32*)(smem + L_CTL);

  if (__builtin_expect((w >> 1) == 2, 1)) {
    u32x8 ka;
    asm volatile("s_load_dwordx8 %0, %1, 0x0" : "=s"(ka) : "s"(__builtin_amdgcn_kernarg_segment_ptr()));
    SB();
    const int p = w - 4;
    unsigned va[4], vb[4], vw[4];
#pragma unroll
    for (int j = 0; j < 4; ++j) {
      const int row = 32 * (w & 1) + 8 * j + (lane >> 3);
      va[j] = (unsigned)((m0 + row) * (DM * 2) + (((lane & 7) ^ ((row >> 1) & 7)) * 16) - 1024 * j);
      vb[j] = (unsigned)(row * (N1 * 2) + 128 * nt + (((lane & 7) ^ ((row & 2) | ((j & 1) << 2))) * 16) - 1024 * j);
      PIN(va[j]); PIN(vb[j]);
    }
    unsigned l0 = (unsigned)(uintptr_t)(ldsp)smem + p * 4096;
    int kf = (4 * ml + 4 * (xcd & 3)) & 15, ks = (4 * ml - 2 * (ml >> 2) + 4 * (xcd & 3)) & 15, sres = (3 - nt) & 7;
    asm volatile("" : "+s"(l0), "+s"(kf), "+s"(ks), "+s"(sres));
    SB();
    asm volatile("s_waitcnt lgkmcnt(0)" : "+s"(ka));
    const u16* x = (const u16*)(((u64)ka[1] << 32) | ka[0]);
    const u16* wqkv = (const u16*)(((u64)ka[3] << 32) | ka[2]);
    const u16* wo = (const u16*)(((u64)ka[5] << 32) | ka[4]);
    char* ws = (char*)(((u64)ka[7] << 32) | ka[6]);
    int ko_ = (int)((ka[2] >> 7) & 7u) == sres ? ks : kf;
    const __amdgpu_buffer_rsrc_t ra = MKRSRC(x, SEQ * DM * 2), rb = MKRSRC(wqkv, DM * N1 * 2), rw = MKRSRC(wo, DM * DM * 2);
#define UNIT(r, v, off, so) do { bufl16<0>(r, l0 + (off), v[0], so); bufl16<1024>(r, l0 + (off), v[1], so); bufl16<2048>(r, l0 + (off), v[2], so); bufl16<3072>(r, l0 + (off), v[3], so); } while (0)
#define ISSUE(t)                                                                                           \
  do {                                                                                                     \
    const int k_ = (ko_ + (t)) & 15;                                                                       \
    UNIT(ra, va, ((t) % 3) * SLOT, k_ * 128); UNIT(ra, va, ((t) % 3) * SLOT + 8192, k_ * 128 + 64 * DM * 2); \
    _Pragma("unroll") for (int s_ = 0; s_ < 3; ++s_) UNIT(rb, vb, ((t) % 3) * SLOT + 16384 + s_ * 8192, k_ * (64 * N1 * 2) + s_ * (DM * 2)); \
  } while (0)
#define PACED_BAR()                                                                                        \
  do {                                                                                                     \
    if (p == 0) {                                                                                          \
      unsigned now;                                                                                        \
      do { now = (unsigned)__builtin_amdgcn_s_memrealtime(); } while ((int)(now - tp) < 0);                \
      tp = ((int)(now - tp) > BURST ? now - BURST : tp) + PACE;                                            \
    }                                                                                                      \
    BAR();                                                                                                 \
  } while (0)
    ISSUE(0); ISSUE(1);
    WAIT_VM(20);
    BAR();
    ISSUE(2);
    unsigned tp = (unsigned)__builtin_amdgcn_s_memrealtime() - PACE;
#pragma unroll
    for (int j = 0; j < 4; ++j) vw[j] = (unsigned)((32 * p + 8 * j + (lane >> 3)) * (DM * 2) + 128 * nt3 + (((lane & 7) ^ ((lane >> 3) & 6)) * 16) - 1024 * j);
#define WO_CHUNK(c) UNIT(rw, vw, 16384 + WOPOS(c) * 8192, CH(c) * (64 * DM * 2))
#pragma unroll
    for (int t = 0; t < 13; ++t) {
      WAIT_VM(20);
      PACED_BAR();
      asm volatile("" : "+s"(l0), "+s"(ko_));
      ISSUE(t + 3);
    }
    WO_CHUNK(0); WO_CHUNK(1); WO_CHUNK(2);
    SB();
    const bool wslow = ((((uintptr_t)wo >> 7) + nt3) & 7) == 3;
    const int woe = wslow ? WO_ES : WO_E;
    WAIT_VM(32);
    PACED_BAR();
    WAIT_VM(12);
    if (lane == 0) misc[C_F15 + p] = 1u;
#pragma unroll 1
    for (int c = 3; c < woe; ++c) WO_CHUNK(c);
    {
      const __amdgpu_buffer_rsrc_t zr = MKRSRC(ws + OFF_ZP, NH * 2 * 16 * 128);
      const unsigned zo = (unsigned)((2 * nt + p) * 2048 + lane * 16);
      u32x4 zw[2];
      unsigned zm;
      (void)wait_ge<1>(misc + C_IDX + p, 1u);
#pragma unroll 1
      for (unsigned spins = 0; spins < (1u << 22) && !all_true(WFLAGS(W_KS) != 0u); ++spins) __builtin_amdgcn_s_sleep(1);
      CFENCE();
      const unsigned tk = (unsigned)__builtin_amdgcn_s_memrealtime();
#pragma unroll 1
      for (int c = woe; c < WO_M; ++c) WO_CHUNK(c);
      UNIT(ra, va, 0, nt3 * 128); UNIT(ra, va, 8192, nt3 * 128 + 64 * DM * 2);
      while ((unsigned)__builtin_amdgcn_s_memrealtime() - tk < (unsigned)G_T) __builtin_amdgcn_s_sleep(1);
#define MX4(v) max(max((v)[0], (v)[1]), max((v)[2], (v)[3]))
#pragma unroll 1
      for (unsigned spins = 0; spins <= SPIN_CAP; ++spins) {
        CFENCE();
        zw[0] = __builtin_amdgcn_raw_buffer_load_b128(zr, zo, 0, 16); zw[1] = __builtin_amdgcn_raw_buffer_load_b128(zr, zo + 1024, 0, 16);
        SB(); misc[C_GO + p] = 1u; SB();
        zm = max(MX4(zw[0]), MX4(zw[1]));
        if (all_true(zm != ~0u)) break;
        __builtin_amdgcn_s_sleep(2);
      }
      const bool plain = all_true((int)zm >= 0);
      if (__builtin_expect(!plain, 0)) { zw[0] &= 0x7FFFFFFFu; zw[1] &= 0x7FFFFFFFu; }
      float z0 = merge16(merge32(u2f(zw[0][0]), u2f(zw[0][2])), merge32(u2f(zw[1][0]), u2f(zw[1][2])));
      float z1 = merge16(merge32(u2f(zw[0][1]), u2f(zw[0][3])), merge32(u2f(zw[1][1]), u2f(zw[1][3])));
      z0 += dppf<0x128>(z0); z1 += dppf<0x128>(z1); z0 += dppf<0x124>(z0); z1 += dppf<0x124>(z1);
      *(unsigned*)(smem + L_ZF + p * 64 + (lane & 3) * 16 + ((lane >> 4) & 1) * 8 + (lane >> 5) * 4) = cvt_pk(z0, z1);
      CFENCE();
      misc[C_ZF + p] = plain ? 1u : 2u;
    }
#pragma unroll 1
    for (unsigned spins = 0; spins < (1u << 22) && !all_true(WFLAGS(W_AG) != 0u); ++spins) __builtin_amdgcn_s_sleep(2);
    CFENCE();
#pragma unroll 1
    for (int c = WO_M; c < 16; ++c) WO_CHUNK(c);
    WAIT_VM(0);
    if (lane == 0) misc[C_WO + p] = 1u;
#undef ISSUE
#undef PACED_BAR
    if (p == 0) return;
    const unsigned tag = rfl(misc[C_TAG]);
    gu32* of = (gu32*)(ws + OFF_OFLAG + (mt3 * 16 + CH(lane & 15)) * 128);
#pragma unroll 1
    for (unsigned spins = 0; spins < (1u << 22) && !all_true(WFLAGS(W_KS) == 2u); ++spins) __builtin_amdgcn_s_sleep(1);
    const unsigned t6 = (unsigned)__builtin_amdgcn_s_memrealtime();
    while ((unsigned)__builtin_amdgcn_s_memrealtime() - t6 < (unsigned)P5_T) __builtin_amdgcn_s_sleep(1);
#pragma unroll 1
    for (unsigned spins = 0;; ++spins) {
      const unsigned f = __hip_atomic_load(of, RLX_AGENT);
      const unsigned miss = (unsigned)__builtin_amdgcn_ballot_w64(f != tag) & 0xFFFEu;
      const unsigned m = (unsigned)__builtin_ctz((spins > SPIN_CAP ? 0u : miss) | 0x10000u);
      misc[C_ORDY] = m;
      if (m == 16u) break;
      __builtin_amdgcn_s_sleep(2);
    }
    return;
  }

  if (__builtin_expect(w >= 6, 0)) {
    const int pa = w - 6;
#define CODE_PF(on) __builtin_amdgcn_raw_buffer_load_b32(MKRSRC((pc0 & ~255ull) + (blockIdx.x >> 3) * (CPF_N * 64u), (on) ? CPF_N * 64 : 0), lane * 64, 0, 0)
    unsigned cpf = CODE_PF(w == 7), cpf2;
    SB();
    float* red = (float*)(smem + L_RED);
    const bool help = bid >= 224, gemv = bid < 252;
    const int gmat = bid & 1;
    const u16* wgu = gmat ? w_up : w_gate;
    const int sres = (int)(3u - (unsigned)((uintptr_t)wgu >> 7)) & 7;
    const int gfs = help ? sres + 8 * (((bid - 224) >> 1) % 7) : (bid >> 1) % 56;
    const int gdh = (help ? ((bid - 224) >> 1) / 7 : (bid >> 1) / 56) & 1;
    const bool act0 = !help, act1 = gemv && (help || (gfs & 7) != sres);
    const int gd0 = gdh * 512 + 256 * pa + 32 * (lane >> 3);
    const __amdgpu_buffer_rsrc_t rgu0 = MKRSRC(wgu, act0 ? DM * FF * 2 : 0), rgu1 = MKRSRC(wgu, act1 ? DM * FF * 2 : 0);
    const unsigned wpo = (unsigned)((gd0 * FF + gfs * 64 + 8 * (lane & 7)) * 2);
    u32x4 wv[32], wd[14];
    const int fg = bid >> 4, cg = bid & 15;
    const int f0 = fg * 224 + pa * 112;
    const __amdgpu_buffer_rsrc_t rdn = MKRSRC(w_down, DM * FF * 2);
    const unsigned wqo = (unsigned)(((f0 + 14 * (lane >> 3)) * DM + cg * 64 + 8 * (lane & 7)) * 2);
    asm volatile("" ::"s"(w_r), "s"(gate_w), "s"(ws), "s"(w_gate), "s"(w_up), "s"(w_down));
    unsigned pf;
#define GU_LD(i0, i1) do { SB(); _Pragma("unroll") for (int i = (i0); i < (i1); ++i) wv[i] = __builtin_amdgcn_raw_buffer_load_b128(i < 16 ? rgu0 : rgu1, wpo, i * (FF * 2), 2); SB(); } while (0)
#define DN_LD(i0, i1) do { SB(); _Pragma("unroll") for (int i = (i0); i < (i1); ++i) wd[i] = __builtin_amdgcn_raw_buffer_load_b128(rdn, wqo, i * (DM * 2), 2); SB(); } while (0)
    GU_LD(0, GU_E);
    {
#pragma unroll
      for (int t = 0; t <= R_AT; ++t) BAR();
      const int r0 = m0 + nt * 8 + 4 * pa;
      u32x4 xr[4][2], rv[2], gv[2];
#pragma unroll
      for (int i = 0; i < 2; ++i) {
        rv[i] = *(const u32x4*)(w_r + i * 512 + lane * 8);
        gv[i] = *(const u32x4*)(gate_w + i * 512 + lane * 8);
      }
#pragma unroll
      for (int r = 0; r < 4; ++r)
#pragma unroll
        for (int i = 0; i < 2; ++i) xr[r][i] = *(const u32x4*)(x + (size_t)(r0 + r) * DM + i * 512 + lane * 8);
      const unsigned ep = __hip_atomic_load((gu32*)(ws + OFF_EPOCH), RLX_AGENT);
      {
        const u32x4 nw = {~0u, ~0u, ~0u, ~0u};
        const __amdgpu_buffer_rsrc_t kvr = MKRSRC(ws + OFF_KVP, NH * 2 * 16 * 1024), zr = MKRSRC(ws + OFF_ZP, NH * 2 * 16 * 128);
        const unsigned rec = (unsigned)((2 * nt + pa) * 32 + mt);
        __builtin_amdgcn_raw_buffer_store_b128(nw, kvr, rec * 1024 + lane * 16, 0, 16);
        __builtin_amdgcn_raw_buffer_store_b128(nw, kvr, rec * 1024 + lane * 16 + 16384, 0, 16);
        if (lane < 10) __builtin_amdgcn_raw_buffer_store_b128(nw, zr, lane < 8 ? (rec + (lane >> 2) * 16) * 64 + (lane & 3) * 16 : ZSC + (rec + (lane & 1) * 16) * 16, 0, 16);
      }
#pragma unroll
      for (int t = R_AT + 1; t < R_AT + R_GAP; ++t) BAR();
      float racc[4], gacc[4];
#pragma unroll
      for (int r = 0; r < 4; ++r) {
        BAR();
        if (r == 1) { WAIT_VM(0); SB(); }
        racc[r] = 0.f; gacc[r] = 0.f;
#pragma unroll
        for (int i = 0; i < 2; ++i)
#pragma unroll
          for (int e = 0; e < 4; ++e) {
            const float x0 = blo(xr[r][i][e]), x1 = bhi(xr[r][i][e]);
            racc[r] += x0 * blo(rv[i][e]); racc[r] += x1 * bhi(rv[i][e]);
            const unsigned pr = cvt_pk(x0 * blo(gv[i][e]), x1 * bhi(gv[i][e]));
            gacc[r] += blo(pr); gacc[r] += bhi(pr);
          }
        PIN(racc[r]); PIN(gacc[r]);
        if (r == 3) { SB(); pf = *(const unsigned*)(wo + (size_t)(128 * ml + 64 * pa + lane) * DM + 64 * nt3); cpf2 = CODE_PF(w == 7); }
      }
      BAR();
      float rs = merge16(merge32(racc[0], racc[1]), merge32(racc[2], racc[3]));
      float gs = merge16(merge32(gacc[0], gacc[1]), merge32(gacc[2], gacc[3]));
      rs += dppf<0x128>(rs); gs += dppf<0x128>(gs); rs += dppf<0x124>(rs); gs += dppf<0x124>(gs);
      rs += dppf<0x122>(rs); gs += dppf<0x122>(gs); rs += dppf<0x121>(rs); gs += dppf<0x121>(gs);
      const int rr = r0 + ((lane >> 5) | ((lane >> 3) & 2));
      const unsigned rb = cvt_pk(rs, rs) & 0xFFFFu;
      unsigned key = (((rb & 0x8000u) ? (~rb & 0xFFFFu) : (rb | 0x8000u)) << 16) | (0xFFFFu - (unsigned)rr);
      { const auto t_ = __builtin_amdgcn_permlane16_swap(key, key, false, false); key = max(t_[0], t_[1]); }
      { const auto t_ = __builtin_amdgcn_permlane32_swap(key, key, false, false); key = max(t_[0], t_[1]); }
      const unsigned tg = rfl(ep) + 1u;
      if (lane == 0) misc[C_TAG + pa] = pa ? key : tg;
      WAIT_LGKM0();
      BAR();
      if (pa == 0 && lane == 0) __hip_atomic_store((gu64*)(ws + OFF_RG) + bid, ((u64)tg << 32) | max(key, (unsigned)misc[C_TAG + 1]), RLX_AGENT);
      if ((lane & 15) == 0) __hip_atomic_store((gu64*)(ws + OFF_GG) + rr, ((u64)tg << 32) | f2u(bfr(sigmoid_f(bfr(gs)))), RLX_AGENT);
      DN_LD(0, DN_E);
#pragma unroll
      for (int t = R_AT + R_GAP + 6; t < 13; ++t) BAR();
    }
    u32x4 rg[2];
#pragma unroll
    for (int i = 0; i < 2; ++i) rg[i] = __builtin_amdgcn_raw_buffer_load_b128(MKRSRC(ws + OFF_RG, 2048), lane * 16 + i * 1024, 0, 16);
    const gu64* ggp = (const gu64*)(ws + OFF_GG) + m0 + 64 * pa + lane;
    u64 gg = __hip_atomic_load(ggp, RLX_AGENT);
    BAR();
    const unsigned tag = rfl(misc[C_TAG]);
    unsigned idx;
    unsigned bestk;
    bool late;
    {
      unsigned best = 0;
      bool ok = (unsigned)(gg >> 32) == tag;
#pragma unroll
      for (int i = 0; i < 4; ++i) { ok = ok && rg[i >> 1][2 * (i & 1) + 1] == tag; best = max(best, rg[i >> 1][2 * (i & 1)]); }
      late = !all_true(ok);
      if (__builtin_expect(late, 0)) {
        BAR();
#pragma unroll 1
        for (unsigned spins = 0;; ++spins) {
          u64 rh[4];
#pragma unroll
          for (int i = 0; i < 4; ++i) rh[i] = __hip_atomic_load((gu64*)(ws + OFF_RG) + i * 64 + lane, RLX_AGENT);
          gg = __hip_atomic_load(ggp, RLX_AGENT);
          ok = (unsigned)(gg >> 32) == tag; best = 0;
#pragma unroll
          for (int i = 0; i < 4; ++i) { ok = ok && (unsigned)(rh[i] >> 32) == tag; best = max(best, (unsigned)rh[i]); }
          if (all_true(ok) || spins > SPIN_CAP) break;
          __builtin_amdgcn_s_sleep(8);
        }
      }
      best = wave_max(best);
      idx = (0xFFFFu - (best & 0xFFFFu)) & (SEQ - 1);
      bestk = best;
    }
    asm volatile("" ::"v"(pf), "v"(cpf), "v"(cpf2));
    u32x4 xs[4];
#pragma unroll
    for (int i = 0; i < 4; ++i) xs[i] = *(const u32x4*)(x + (size_t)idx * DM + gd0 + 8 * i);
    if (!late) BAR();
    __builtin_amdgcn_s_sleep(2);
    GU_LD(GU_E, 32);
    ((unsigned*)(smem + L_GATE))[64 * pa + lane] = (unsigned)gg;
    CFENCE();
    if (lane == 0) misc[C_IDX + pa] = idx + 1u;
    if (bid == 0 && pa == 0 && lane == 0) __hip_atomic_store((gu32*)(ws + OFF_EPOCH), tag, RLX_AGENT);
    unsigned sel[2][4];
#pragma unroll
    for (int i = 0; i < 8; ++i) {
      const unsigned pick = (i >> 2) ? 0x0302u : 0x0100u;
      sel[i >> 2][i & 3] = ((lane & 7) >> 1) == (i & 3) ? ((lane & 1) ? (pick << 16) | 0x0C0Cu : 0x0C0C0000u | pick) : 0x0C0C0C0Cu;
      PIN(sel[i >> 2][i & 3]);
    }
#define AFR(pr, i) FRAG(((u32x4){__builtin_amdgcn_perm(0u, pr, sel[(i) & 1][0]), __builtin_amdgcn_perm(0u, pr, sel[(i) & 1][1]), \
                                 __builtin_amdgcn_perm(0u, pr, sel[(i) & 1][2]), __builtin_amdgcn_perm(0u, pr, sel[(i) & 1][3])}))
    f32x4* rdw = (f32x4*)(red + pa * 128 + (lane >> 5) * 64 + (lane & 7) * 8 + ((lane >> 4) & 1) * 4);
    const bool rvalid = (lane >> 5) == ((lane >> 3) & 1);
    const auto rv = LP(volatile float, L_RED, lane * 4);
    {
      bf16x8 af[32];
      f32x4 a = {0.f, 0.f, 0.f, 0.f};
#pragma unroll
      for (int i = 0; i < GU_AH; ++i) { af[i] = AFR(xs[i >> 3][(i >> 1) & 3], i); PIN(af[i]); }
      SB();
#pragma unroll
      for (int i = 0; i < 32; ++i) {
        a = MFMA(af[i], FRAG(wv[i]), a);
        PIN(a);
        if (i + GU_AH < 32) { af[(i + GU_AH) & 31] = AFR(xs[((i + GU_AH) >> 3) & 3][((i + GU_AH) >> 1) & 3], i + GU_AH); PIN(af[(i + GU_AH) & 31]); }
        SB();
      }
      if (rvalid) *rdw = a;
      CFENCE();
      if (pa == 1) misc[C_GU] = 1u;
      else {
        float v;
#pragma unroll 1
        for (unsigned spins = 0; spins < (1u << 22); ++spins) {
          const unsigned f_ = misc[C_GU];
          CFENCE();
          v = (rv[0] + rv[64]) + (rv[128] + rv[192]);
          if (rfl(f_) != 0u) break;
          __builtin_amdgcn_s_sleep(1);
        }
        if (gemv) __hip_atomic_store((gu64*)(ws + OFF_GU) + ((help ? 4 : 0) + gdh * 2 + gmat) * FF + gfs * 64 + lane, ((u64)tag << 32) | f2u(v), RLX_AGENT);
      }
    }
    {
#pragma unroll 1
      for (unsigned spins = 0; spins < (1u << 22) && !all_true(WFLAGS(W_X2) != 0u); ++spins) __builtin_amdgcn_s_sleep(1);
      __builtin_amdgcn_s_sleep(10);
      DN_LD(DN_E, 14);
      __builtin_amdgcn_s_sleep(8);
      float hv[2];
      const int fl = f0 + 2 * (lane < 56 ? lane : 55);
      const __amdgpu_buffer_rsrc_t gr = MKRSRC(ws + OFF_GU, 8 * FF * 8);
      const bool ex[2] = {(((unsigned)((uintptr_t)w_gate >> 7) + (fl >> 6)) & 7u) == 3u, (((unsigned)((uintptr_t)w_up >> 7) + (fl >> 6)) & 7u) == 3u};
#pragma unroll 1
      for (unsigned spins = 0;; ++spins) {
        u32x4 g[8];
        CFENCE();
#pragma unroll
        for (int i = 0; i < 8; ++i) {
          g[i] = (u32x4){0u, tag, 0u, tag};
          if (i < 4 || ex[i & 1]) g[i] = __builtin_amdgcn_raw_buffer_load_b128(gr, fl * 8, i * (FF * 8), 16);
        }
        bool ok = true;
#pragma unroll
        for (int j = 0; j < 2; ++j) {
#pragma unroll
          for (int i = 0; i < 8; ++i) ok = ok && g[i][2 * j + 1] == tag;
          const float gt = bfr((u2f(g[0][2 * j]) + u2f(g[4][2 * j])) + (u2f(g[2][2 * j]) + u2f(g[6][2 * j])));
          const float up = bfr((u2f(g[1][2 * j]) + u2f(g[5][2 * j])) + (u2f(g[3][2 * j]) + u2f(g[7][2 * j])));
          hv[j] = bfr(gt * __builtin_amdgcn_rcpf(1.0f + __expf(-gt))) * up;
        }
        if (all_true(ok) || spins > SPIN_CAP) break;
        __builtin_amdgcn_s_sleep(4);
      }
      u32x4 hp[2];
      {
        char* hb = smem + L_H + pa * 256;
        if (lane < 56) *(unsigned*)(hb + (lane / 7) * 32 + (lane % 7) * 4) = cvt_pk(hv[0], hv[1]);
        CFENCE();
        hp[0] = *(volatile lu32x4*)(hb + (lane >> 3) * 32); hp[1] = *(volatile lu32x4*)(hb + (lane >> 3) * 32 + 16);
      }
      bf16x8 af[14];
#pragma unroll
      for (int i = 0; i < 14; ++i) { af[i] = AFR(hp[i >> 3][(i >> 1) & 3], i); PIN(af[i]); }
      SB();
      f32x4 a = {0.f, 0.f, 0.f, 0.f};
#pragma unroll
      for (int i = 0; i < 14; ++i) a = MFMA(af[i], FRAG(wd[i]), a);
      if (rvalid) rdw[64] = a;
      CFENCE();
      if (pa == 1) misc[C_DN] = 1u;
      else {
        float y;
#pragma unroll 1
        for (unsigned spins = 0; spins < (1u << 22); ++spins) {
          const unsigned f_ = misc[C_DN];
          CFENCE();
          y = (rv[256] + rv[320]) + (rv[384] + rv[448]);
          if (rfl(f_) != 0u) break;
          __builtin_amdgcn_s_sleep(1);
        }
        __hip_atomic_store((gu64*)(ws + OFF_YG) + fg * 1024 + cg * 64 + lane, ((u64)tag << 32) | f2u(y), RLX_AGENT);
      }
    }
    if (pa == 1 && (int)(idx >> 7) == mt3) {
      const float xe = u2f((unsigned)x[(size_t)idx * DM + 64 * nt3 + lane] << 16);
      const unsigned ob = bestk >> 16;
      const float rsel = bfr(sigmoid_f(u2f(((ob & 0x8000u) ? (ob & 0x7FFFu) : (~ob & 0xFFFFu)) << 16)));
      float y = 0.f, gt = 0.f;
      __builtin_amdgcn_s_sleep(30);
#pragma unroll 1
      for (unsigned spins = 0;; ++spins) {
        u64 g[17];
#pragma unroll
        for (int i = 0; i < 16; ++i) g[i] = __hip_atomic_load((gu64*)(ws + OFF_YG) + i * 1024 + 64 * nt3 + lane, RLX_AGENT);
        g[16] = __hip_atomic_load((gu64*)(ws + OFF_GG) + idx, RLX_AGENT);
        bool ok = (unsigned)(g[16] >> 32) == tag;
        gt = u2f((unsigned)g[16]); y = 0.f;
#pragma unroll
        for (int i = 0; i < 16; ++i) { ok = ok && (unsigned)(g[i] >> 32) == tag; y += u2f((unsigned)g[i]); }
        if (all_true(ok) || spins > SPIN_CAP) break;
        __builtin_amdgcn_s_sleep(8);
      }
      red[512 + lane] = bfr(bfr(1.0f - gt) * bfr(xe + bfr(bfr(y) * rsel))) - bfr(bfr(1.0f - gt) * xe);
      CFENCE();
      if (lane == 0) misc[C_Y] = 1u;
    }
    return;
  }

  if (w == 0) misc[lane & 31] = 0u;
  if (w == 1) *(u32x2*)(smem + L_CW + lane * 8) = (u32x2){0u, 0u};
  const int hh = w & 1, wr = w >> 1;
  const int c16 = lane & 15, g = lane >> 4;
  const int fsw = (c16 >> 3) | ((g & 1) << 1);
  const int head = 2 * nt + hh;
  int aoff[2], boff[2];
#pragma unroll
  for (int i = 0; i < 2; ++i) {
    aoff[i] = (wr * 64 + c16) * 128 + (((4 * i + g) ^ (c16 >> 1)) * 16);
    boff[i] = 16384 + (8 * g + (c16 >> 2)) * 128 + (((2 * hh + (i ^ wr)) ^ fsw) * 32) + (c16 & 3) * 8;
  }
  f32x4 acc[3][4][2];
#pragma unroll
  for (int i = 0; i < 24; ++i) { const float s0 = i < 16 ? 1.f : 0.f; acc[i / 8][(i >> 1) & 3][i & 1] = (f32x4){s0, s0, s0, s0}; PIN(acc[i / 8][(i >> 1) & 3][i & 1]); }
  bf16x8 a[2][4], b[2][6];
  s16x4 tlo;
#define A_AT(sl, h, mi) (*(const bf16x8*)(smem + (sl) * SLOT + aoff[h] + (mi) * 2048))
#define B_AT(sl, h, s, n) (smem + (sl) * SLOT + boff[n] + (s) * 8192 + (h) * 4096)
#define MM(s, mi, n, A, B) do { if ((s) == 0) AMFMA(acc[0][mi][n], B, A); else AMFMA(acc[s][mi][n], A, B); } while (0)
#define RD(sl, h, u)                                                                                       \
  do {                                                                                                     \
    if ((u) == 0) a[h][0] = A_AT(sl, h, 0);                                                                \
    else if ((u) >= 13) a[h][(u) - 12] = A_AT(sl, h, (u) - 12);                                            \
    else {                                                                                                 \
      const int j_ = (((u) - 1) / 2 + 2) % 6;                                                              \
      if ((u) & 1) tlo = TRD(B_AT(sl, h, j_ >> 1, j_ & 1));                                                \
      else b[h][j_] = CAT(tlo, TRD(B_AT(sl, h, j_ >> 1, j_ & 1) + 512));                                   \
    }                                                                                                      \
  } while (0)
#define O_M(n) ((((n) / 4) & 1) ? 3 - (n) % 4 : (n) % 4)
#define O_J(n) ((n) / 4)
#define STEP(h, rs)                                                                                        \
  _Pragma("unroll") for (int n_ = 0; n_ < 24; ++n_) {                                                      \
    const int jj_ = (O_J(n_) + 2) % 6, mi_ = O_M(n_);                                                      \
    MM(jj_ >> 1, mi_, jj_ & 1, a[h][mi_], b[h][jj_]); SB();                                                \
    if (n_ < 16) { RD(rs, 1 - (h), n_); }                                                                 \
    SB();                                                                                                  \
  }
#define LGKM0() __builtin_amdgcn_s_waitcnt(0xC07F)
#define STAGE(sl, nx) do { STEP(0, sl); LGKM0(); BAR(); STEP(1, nx); LGKM0(); SB(); } while (0)

  __amdgpu_buffer_rsrc_t wsr = MKRSRC(ws, OFF_OF + SEQ * DM * 2);
  asm volatile("" : "+s"(wsr));
  const unsigned rec = (unsigned)((head * 2 + wr) * 16 + mt);
  unsigned x1w = L_X1 + w * 2048 + lane * 16, cw = L_CW + w * 128 + g * 16, one = 1u;
  unsigned kvo = (unsigned)OFF_KVP + rec * 1024 + lane * 16;
  unsigned zo = c16 == 0 ? (unsigned)OFF_ZP + rec * 64 + g * 16 : lane == 1 ? (unsigned)OFF_ZP + ZSC + rec * 16 : 0x80000000u;
  PIN(x1w); PIN(cw); PIN(one); PIN(kvo); PIN(zo);
  WAIT_LGKM0();
  BAR();
#pragma unroll
  for (int u = 0; u < 16; ++u) RD(0, 0, u);
  LGKM0();
  SB();
#pragma unroll 1
  for (int it = 0; it < 4; ++it) { STAGE(0, 1); STAGE(1, 2); STAGE(2, 0); }
  STAGE(0, 1);
  STEP(0, 1); LGKM0(); BAR();
  bf16x8 ta0_0, ta0_1, ta0_2, ta0_3, ta1_0, ta1_1, ta1_2, ta1_3, ta2_0, ta2_1, ta2_2, ta2_3;
  bf16x8 ta3_0, ta3_1, ta3_2, ta3_3, tb0_0_0, tb0_0_1, tb0_1_0, tb0_1_1, tb0_2_0, tb0_2_1, tb0_3_0, tb0_3_1;
  bf16x8 tb1_0_0, tb1_0_1, tb1_1_0, tb1_1_1, tb1_2_0, tb1_2_1, tb1_3_0, tb1_3_1, tb2_0_0, tb2_0_1, tb2_1_0, tb2_1_1;
  bf16x8 tb2_2_0, tb2_2_1, tb2_3_0, tb2_3_1;
  s16x4 tb0_0_0_l, tb0_0_1_l, tb0_1_0_l, tb0_1_1_l, tb0_2_0_l, tb0_2_1_l, tb0_3_0_l, tb0_3_1_l, tb1_0_0_l, tb1_0_1_l, tb1_1_0_l, tb1_1_1_l;
  s16x4 tb1_2_0_l, tb1_2_1_l, tb1_3_0_l, tb1_3_1_l, tb2_0_0_l, tb2_0_1_l, tb2_1_0_l, tb2_1_1_l, tb2_2_0_l, tb2_2_1_l, tb2_3_0_l, tb2_3_1_l;
  unsigned kp[16], vp[16], x1r, cr;
  u32x2 lf15;
  float kmin = 0.f, qmin = 0.f;
  u32x4 ones;
  float ek[4];
#define PHI(src, dst, mn, k, st)                                                                           \
  do {                                                                                                     \
    float& e_ = ek[2 * (k) + ((st) & 1)];                                                                  \
    if ((st) < 2) e_ = __builtin_fmaf((src)[2 * (k) + ((st) & 1)], 1.44269504088896341f, -1.44269504088896341f); \
    else if ((st) < 4) e_ = __builtin_amdgcn_fmed3f(__builtin_amdgcn_exp2f(e_), 0.f, 1.f);                 \
    else if ((st) < 6) e_ = fmax_raw((src)[2 * (k) + ((st) & 1)], e_);                                     \
    else if ((st) == 6) dst = cvt_pk(ek[2 * (k)], ek[2 * (k) + 1]);                                        \
    else mn = __builtin_fminf(__builtin_fminf(mn, (src)[2 * (k)]), (src)[2 * (k) + 1]);                     \
    SB();                                                                                                  \
  } while (0)
#define PK(mi, n, k, st) PHI(acc[1][mi][n], kp[8 * (n) + 2 * (mi) + (k)], kmin, k, st)
#define CV(mi, n, k) do { vp[8 * (n) + 2 * (mi) + (k)] = cvt_pk(acc[2][mi][n][2 * (k)], acc[2][mi][n][2 * (k) + 1]); PIN(vp[8 * (n) + 2 * (mi) + (k)]); SB(); } while (0)
#define F15_RD lf15 = *(volatile lu32x2*)(smem + L_CTL + 4 * C_F15)
#define F15_CHK do { if (__builtin_expect(!all_true((lf15[0] & lf15[1]) != 0u), 0)) { (void)wait_ge<1>(misc + C_F15, 1u); (void)wait_ge<1>(misc + C_F15 + 1, 1u); } CFENCE(); } while (0)
#define PRE(i) do { if ((i) == 30) { x1r = x1w ^ 4096u; PIN(x1r); } if ((i) == 31) { cr = cw ^ 256u; PIN(cr); } \
                    if ((i) == 32) { ones = (u32x4){0x3F803F80u, 0x3F803F80u, 0x3F803F80u, 0x3F803F80u}; PIN(ones); } } while (0)
  MM(1, 0, 0, a[1][0], b[1][2]); SB(); tb1_0_1_l = TRD(B_AT(2, 0, 1, 1)); PRE(0); SB();
  MM(1, 0, 1, a[1][0], b[1][3]); SB(); tb1_0_1 = CAT(tb1_0_1_l, TRD(B_AT(2, 0, 1, 1) + 512)); PRE(1); SB();
  MM(2, 0, 0, a[1][0], b[1][4]); SB(); ta0_0 = A_AT(2, 0, 0); PRE(2); SB();
  MM(2, 0, 1, a[1][0], b[1][5]); SB(); ta0_1 = A_AT(2, 0, 1); PRE(3); SB();
  MM(0, 0, 0, a[1][0], b[1][0]); SB(); ta0_2 = A_AT(2, 0, 2); PRE(4); SB();
  MM(0, 0, 1, a[1][0], b[1][1]); SB(); ta0_3 = A_AT(2, 0, 3); PRE(5); SB();
  MM(1, 1, 0, a[1][1], b[1][2]); SB(); tb1_0_0_l = TRD(B_AT(2, 0, 1, 0)); PRE(6); SB();
  MM(1, 1, 1, a[1][1], b[1][3]); SB(); tb1_0_0 = CAT(tb1_0_0_l, TRD(B_AT(2, 0, 1, 0) + 512)); PRE(7); SB();
  MM(2, 1, 0, a[1][1], b[1][4]); SB(); tb1_1_1_l = TRD(B_AT(2, 1, 1, 1)); PRE(8); SB();
  MM(2, 1, 1, a[1][1], b[1][5]); SB(); tb1_1_1 = CAT(tb1_1_1_l, TRD(B_AT(2, 1, 1, 1) + 512)); PRE(9); SB();
  MM(0, 1, 0, a[1][1], b[1][0]); SB(); ta1_0 = A_AT(2, 1, 0); PRE(10); SB();
  MM(0, 1, 1, a[1][1], b[1][1]); SB(); ta1_1 = A_AT(2, 1, 1); PRE(11); SB();
  MM(1, 2, 0, a[1][2], b[1][2]); SB(); ta1_2 = A_AT(2, 1, 2); PRE(12); SB();
  MM(1, 2, 1, a[1][2], b[1][3]); SB(); ta1_3 = A_AT(2, 1, 3); PRE(13); SB();
  MM(2, 2, 0, a[1][2], b[1][4]); SB(); tb1_1_0_l = TRD(B_AT(2, 1, 1, 0)); PRE(14); SB();
  MM(2, 2, 1, a[1][2], b[1][5]); SB(); tb1_1_0 = CAT(tb1_1_0_l, TRD(B_AT(2, 1, 1, 0) + 512)); PRE(15); SB();
  MM(0, 2, 0, a[1][2], b[1][0]); SB(); PRE(16); SB();
  MM(0, 2, 1, a[1][2], b[1][1]); SB(); PRE(17); SB();
  MM(1, 3, 0, a[1][3], b[1][2]); SB(); PRE(18); SB();
  MM(1, 3, 1, a[1][3], b[1][3]); SB(); PRE(19); SB();
  MM(2, 3, 0, a[1][3], b[1][4]); SB(); F15_RD; PRE(20); SB();
  MM(2, 3, 1, a[1][3], b[1][5]); SB(); PRE(21); SB();
  MM(0, 3, 0, a[1][3], b[1][0]); SB(); tb2_0_1_l = TRD(B_AT(2, 0, 2, 1)); PRE(22); SB();
  MM(0, 3, 1, a[1][3], b[1][1]); SB(); tb2_0_1 = CAT(tb2_0_1_l, TRD(B_AT(2, 0, 2, 1) + 512)); PRE(23); SB();
  MM(1, 0, 1, ta0_0, tb1_0_1); SB(); PRE(24); SB();
  MM(1, 1, 1, ta0_1, tb1_0_1); SB(); PRE(25); SB();
  MM(1, 2, 1, ta0_2, tb1_0_1); SB(); F15_CHK; tb2_1_1_l = TRD(B_AT(2, 1, 2, 1)); PRE(26); SB();
  MM(1, 3, 1, ta0_3, tb1_0_1); SB(); tb2_1_1 = CAT(tb2_1_1_l, TRD(B_AT(2, 1, 2, 1) + 512)); PRE(27); SB();
  MM(1, 0, 0, ta0_0, tb1_0_0); SB(); tb1_2_1_l = TRD(B_AT(0, 0, 1, 1)); PRE(28); SB();
  MM(1, 1, 0, ta0_1, tb1_0_0); SB(); tb1_2_1 = CAT(tb1_2_1_l, TRD(B_AT(0, 0, 1, 1) + 512)); PRE(29); SB();
  MM(1, 2, 0, ta0_2, tb1_0_0); SB(); ta2_0 = A_AT(0, 0, 0); PRE(30); SB();
  MM(1, 3, 0, ta0_3, tb1_0_0); SB(); ta2_1 = A_AT(0, 0, 1); PRE(31); SB();
  MM(1, 0, 1, ta1_0, tb1_1_1); SB(); ta2_2 = A_AT(0, 0, 2); PRE(32); SB();
  MM(1, 1, 1, ta1_1, tb1_1_1); SB(); ta2_3 = A_AT(0, 0, 3); PRE(33); SB();
  MM(1, 2, 1, ta1_2, tb1_1_1); SB(); tb1_3_1_l = TRD(B_AT(0, 1, 1, 1)); PRE(34); SB();
  MM(1, 3, 1, ta1_3, tb1_1_1); SB(); tb1_3_1 = CAT(tb1_3_1_l, TRD(B_AT(0, 1, 1, 1) + 512)); PRE(35); SB();
  MM(1, 0, 0, ta1_0, tb1_1_0); SB(); ta3_0 = A_AT(0, 1, 0); PRE(36); SB();
  MM(1, 1, 0, ta1_1, tb1_1_0); SB(); ta3_1 = A_AT(0, 1, 1); PRE(37); SB();
  MM(1, 2, 0, ta1_2, tb1_1_0); SB(); ta3_2 = A_AT(0, 1, 2); PRE(38); SB();
  MM(1, 3, 0, ta1_3, tb1_1_0); SB(); ta3_3 = A_AT(0, 1, 3); PRE(39); SB();
  MM(1, 0, 1, ta2_0, tb1_2_1); SB(); tb1_2_0_l = TRD(B_AT(0, 0, 1, 0)); PRE(40); SB();
  MM(1, 1, 1, ta2_1, tb1_2_1); SB(); tb1_2_0 = CAT(tb1_2_0_l, TRD(B_AT(0, 0, 1, 0) + 512)); PRE(41); SB();
  MM(1, 2, 1, ta2_2, tb1_2_1); SB(); tb1_3_0_l = TRD(B_AT(0, 1, 1, 0)); PRE(42); SB();
  MM(1, 3, 1, ta2_3, tb1_2_1); SB(); tb1_3_0 = CAT(tb1_3_0_l, TRD(B_AT(0, 1, 1, 0) + 512)); PRE(43); SB();
  MM(1, 0, 1, ta3_0, tb1_3_1); SB(); tb2_2_1_l = TRD(B_AT(0, 0, 2, 1)); PRE(44); SB();
  MM(1, 1, 1, ta3_1, tb1_3_1); SB(); tb2_2_1 = CAT(tb2_2_1_l, TRD(B_AT(0, 0, 2, 1) + 512)); PRE(45); SB();
  MM(1, 2, 1, ta3_2, tb1_3_1); SB(); tb2_3_1_l = TRD(B_AT(0, 1, 2, 1)); PRE(46); SB();
  MM(1, 3, 1, ta3_3, tb1_3_1); SB(); tb2_3_1 = CAT(tb2_3_1_l, TRD(B_AT(0, 1, 2, 1) + 512)); PRE(47); SB();
  MM(1, 0, 0, ta2_0, tb1_2_0); SB(); tb2_0_0_l = TRD(B_AT(2, 0, 2, 0)); PK(0, 1, 0, 0); PK(0, 1, 0, 1); PK(0, 1, 1, 0); PK(0, 1, 1, 1); PK(0, 1, 0, 2); SB();
  MM(1, 1, 0, ta2_1, tb1_2_0); SB(); tb2_0_0 = CAT(tb2_0_0_l, TRD(B_AT(2, 0, 2, 0) + 512)); PK(0, 1, 0, 3); PK(0, 1, 0, 7); PK(0, 1, 1, 2); SB();
  MM(1, 2, 0, ta2_2, tb1_2_0); SB(); tb2_1_0_l = TRD(B_AT(2, 1, 2, 0)); PK(0, 1, 1, 3); PK(0, 1, 1, 7); PK(0, 1, 0, 4); PK(0, 1, 0, 5); SB();
  MM(1, 3, 0, ta2_3, tb1_2_0); SB(); tb2_1_0 = CAT(tb2_1_0_l, TRD(B_AT(2, 1, 2, 0) + 512)); PK(0, 1, 1, 4); PK(0, 1, 1, 5); PK(0, 1, 0, 6); PK(0, 1, 1, 6); PK(1, 1, 0, 0); SB();
  MM(1, 0, 0, ta3_0, tb1_3_0); SB(); tb2_2_0_l = TRD(B_AT(0, 0, 2, 0)); PK(1, 1, 0, 1); PK(1, 1, 1, 0); PK(1, 1, 1, 1); PK(1, 1, 0, 2); SB();
  MM(1, 1, 0, ta3_1, tb1_3_0); SB(); tb2_2_0 = CAT(tb2_2_0_l, TRD(B_AT(0, 0, 2, 0) + 512)); PK(1, 1, 0, 3); PK(1, 1, 0, 7); PK(1, 1, 1, 2); SB();
  MM(1, 2, 0, ta3_2, tb1_3_0); SB(); tb2_3_0_l = TRD(B_AT(0, 1, 2, 0)); PK(1, 1, 1, 3); PK(1, 1, 1, 7); PK(1, 1, 0, 4); PK(1, 1, 0, 5); SB();
  MM(1, 3, 0, ta3_3, tb1_3_0); SB(); tb2_3_0 = CAT(tb2_3_0_l, TRD(B_AT(0, 1, 2, 0) + 512)); PK(1, 1, 1, 4); PK(1, 1, 1, 5); PK(1, 1, 0, 6); PK(1, 1, 1, 6); SB();
  MM(2, 0, 1, ta0_0, tb2_0_1); SB(); PK(2, 1, 0, 0); PK(2, 1, 0, 1); PK(2, 1, 1, 0); PK(2, 1, 1, 1); SB();
  MM(2, 1, 1, ta0_1, tb2_0_1); SB(); PK(2, 1, 0, 2); PK(2, 1, 0, 3); SB();
  MM(2, 2, 1, ta0_2, tb2_0_1); SB(); PK(2, 1, 0, 7); PK(2, 1, 1, 2); PK(2, 1, 1, 3); SB();
  MM(2, 3, 1, ta0_3, tb2_0_1); SB(); PK(2, 1, 1, 7); PK(2, 1, 0, 4); PK(2, 1, 0, 5); PK(2, 1, 1, 4); SB();
  MM(2, 0, 1, ta1_0, tb2_1_1); SB(); PK(2, 1, 1, 5); PK(2, 1, 0, 6); PK(2, 1, 1, 6); PK(3, 1, 0, 0); SB();
  MM(2, 1, 1, ta1_1, tb2_1_1); SB(); PK(3, 1, 0, 1); PK(3, 1, 1, 0); PK(3, 1, 1, 1); PK(3, 1, 0, 2); SB();
  MM(2, 2, 1, ta1_2, tb2_1_1); SB(); PK(3, 1, 0, 3); PK(3, 1, 0, 7); PK(3, 1, 1, 2); SB();
  MM(2, 3, 1, ta1_3, tb2_1_1); SB(); PK(3, 1, 1, 3); PK(3, 1, 1, 7); PK(3, 1, 0, 4); SB();
  MM(2, 0, 1, ta2_0, tb2_2_1); SB(); PK(3, 1, 0, 5); PK(3, 1, 1, 4); PK(3, 1, 1, 5); PK(3, 1, 0, 6); SB();
  MM(2, 1, 1, ta2_1, tb2_2_1); SB(); PK(3, 1, 1, 6); PK(0, 0, 0, 0); PK(0, 0, 0, 1); PK(0, 0, 1, 0); SB();
  MM(2, 2, 1, ta2_2, tb2_2_1); SB(); PK(0, 0, 1, 1); PK(0, 0, 0, 2); PK(0, 0, 0, 3); SB();
  MM(2, 3, 1, ta2_3, tb2_2_1); SB(); PK(0, 0, 0, 7); PK(0, 0, 1, 2); PK(0, 0, 1, 3); SB();
  MM(2, 0, 1, ta3_0, tb2_3_1); SB(); PK(0, 0, 1, 7); PK(0, 0, 0, 4); PK(0, 0, 0, 5); PK(0, 0, 1, 4); SB();
  MM(2, 1, 1, ta3_1, tb2_3_1); SB(); PK(0, 0, 1, 5); PK(0, 0, 0, 6); PK(0, 0, 1, 6); PK(1, 0, 0, 0); SB();
  MM(2, 2, 1, ta3_2, tb2_3_1); SB(); PK(1, 0, 0, 1); PK(1, 0, 1, 0); PK(1, 0, 1, 1); PK(1, 0, 0, 2); SB();
  MM(2, 3, 1, ta3_3, tb2_3_1); SB(); PK(1, 0, 0, 3); PK(1, 0, 0, 7); PK(1, 0, 1, 2); SB();
  MM(2, 0, 0, ta0_0, tb2_0_0); SB(); PK(1, 0, 1, 3); PK(1, 0, 1, 7); PK(1, 0, 0, 4); SB();
  MM(2, 1, 0, ta0_1, tb2_0_0); SB(); PK(1, 0, 0, 5); PK(1, 0, 1, 4); PK(1, 0, 1, 5); PK(1, 0, 0, 6); SB();
  MM(2, 2, 0, ta0_2, tb2_0_0); SB(); PK(1, 0, 1, 6); PK(2, 0, 0, 0); PK(2, 0, 0, 1); PK(2, 0, 1, 0); SB();
  MM(2, 3, 0, ta0_3, tb2_0_0); SB(); PK(2, 0, 1, 1); PK(2, 0, 0, 2); PK(2, 0, 0, 3); SB();
  MM(2, 0, 0, ta1_0, tb2_1_0); SB(); PK(2, 0, 0, 7); PK(2, 0, 1, 2); PK(2, 0, 1, 3); SB();
  MM(2, 1, 0, ta1_1, tb2_1_0); SB(); PK(2, 0, 1, 7); PK(2, 0, 0, 4); PK(2, 0, 0, 5); PK(2, 0, 1, 4); SB();
  MM(2, 2, 0, ta1_2, tb2_1_0); SB(); PK(2, 0, 1, 5); PK(2, 0, 0, 6); PK(2, 0, 1, 6); PK(3, 0, 0, 0); SB();
  MM(2, 3, 0, ta1_3, tb2_1_0); SB(); PK(3, 0, 0, 1); PK(3, 0, 1, 0); PK(3, 0, 1, 1); SB();
  MM(2, 0, 0, ta2_0, tb2_2_0); SB(); PK(3, 0, 0, 2); PK(3, 0, 0, 3); SB();
  MM(2, 1, 0, ta2_1, tb2_2_0); SB(); PK(3, 0, 0, 7); PK(3, 0, 1, 2); SB();
  MM(2, 2, 0, ta2_2, tb2_2_0); SB(); PK(3, 0, 1, 3); PK(3, 0, 1, 7); SB();
  MM(2, 3, 0, ta2_3, tb2_2_0); SB(); PK(3, 0, 0, 4); PK(3, 0, 0, 5); PK(3, 0, 1, 4); SB();
  MM(2, 0, 0, ta3_0, tb2_3_0); SB(); PK(3, 0, 1, 5); PK(3, 0, 0, 6); PK(3, 0, 1, 6); SB();
  MM(2, 1, 0, ta3_1, tb2_3_0); SB(); CV(0, 1, 0); CV(0, 1, 1); CV(1, 1, 0); SB();
  MM(2, 2, 0, ta3_2, tb2_3_0); SB(); CV(1, 1, 1); CV(2, 1, 0); CV(2, 1, 1); CV(3, 1, 0); SB();
  MM(2, 3, 0, ta3_3, tb2_3_0); SB(); CV(3, 1, 1); SB();
  const f32x4 zero = {0.f, 0.f, 0.f, 0.f};
#define TAKE(flag, READS)                                                                                  \
  _Pragma("unroll 1") for (unsigned spins = 0; spins < (1u << 22); ++spins) {                              \
    const unsigned f_ = (flag);                                                                            \
    CFENCE();                                                                                              \
    READS;                                                                                                 \
    if (__builtin_expect(rfl(f_) != 0u, 1)) break;                                                         \
  }
  unsigned lidx, ltag;
  {
    phi_fix(kp, kmin);
#define KF(j, n) FRAG(((u32x4){kp[8 * (n) + 4 * (j)], kp[8 * (n) + 4 * (j) + 1], kp[8 * (n) + 4 * (j) + 2], kp[8 * (n) + 4 * (j) + 3]}))
#define VF(j, n) FRAG(((u32x4){vp[8 * (n) + 4 * (j)], vp[8 * (n) + 4 * (j) + 1], vp[8 * (n) + 4 * (j) + 2], vp[8 * (n) + 4 * (j) + 3]}))
    f32x4 s1 = MFMA(KF(0, 1), VF(0, 1), zero), s0 = MFMA(KF(0, 0), VF(0, 1), zero), zs = MFMA(KF(0, 1), FRAG(ones), zero);
    s1 = MFMA(KF(1, 1), VF(1, 1), s1); s0 = MFMA(KF(1, 0), VF(1, 1), s0); zs = MFMA(KF(1, 1), FRAG(ones), zs);
    f32x4 z = MFMA(KF(1, 0), FRAG(ones), MFMA(KF(0, 0), FRAG(ones), zero));
    SB();
    CV(0, 0, 0); CV(0, 0, 1); CV(1, 0, 0); CV(1, 0, 1);
    *LP(f32x4, x1w, 0) = s1; *LP(f32x4, x1w, 1024) = s0; *LP(f32x4, cw, 0) = zs;
    CFENCE();
    *LP(volatile unsigned, cw, W_X1) = one;
    SB();
    CV(2, 0, 0); CV(2, 0, 1); CV(3, 0, 0); CV(3, 0, 1);
    f32x4 kv0 = MFMA(KF(0, 0), VF(0, 0), zero), kv1 = MFMA(KF(0, 1), VF(0, 0), zero);
    kv0 = MFMA(KF(1, 0), VF(1, 0), kv0); kv1 = MFMA(KF(1, 1), VF(1, 0), kv1);
    SB();
    f32x4 p0, p1, pz;
#define X1_RD { p0 = *LP(volatile f32x4, x1r, 0); p1 = *LP(volatile f32x4, x1r, 1024); pz = *LP(volatile f32x4, cr, 0); }
    const unsigned f1 = *LP(volatile unsigned, cr, W_X1);
    CFENCE();
    X1_RD
    SB();
    tb0_0_0_l = TRD(B_AT(2, 0, 0, 0)); tb0_0_0 = CAT(tb0_0_0_l, TRD(B_AT(2, 0, 0, 0) + 512));
    tb0_0_1_l = TRD(B_AT(2, 0, 0, 1)); tb0_0_1 = CAT(tb0_0_1_l, TRD(B_AT(2, 0, 0, 1) + 512));
    tb0_1_0_l = TRD(B_AT(2, 1, 0, 0)); tb0_1_0 = CAT(tb0_1_0_l, TRD(B_AT(2, 1, 0, 0) + 512));
    tb0_1_1_l = TRD(B_AT(2, 1, 0, 1)); tb0_1_1 = CAT(tb0_1_1_l, TRD(B_AT(2, 1, 0, 1) + 512));
    tb0_2_0_l = TRD(B_AT(0, 0, 0, 0)); tb0_2_0 = CAT(tb0_2_0_l, TRD(B_AT(0, 0, 0, 0) + 512));
    tb0_2_1_l = TRD(B_AT(0, 0, 0, 1)); tb0_2_1 = CAT(tb0_2_1_l, TRD(B_AT(0, 0, 0, 1) + 512));
    tb0_3_0_l = TRD(B_AT(0, 1, 0, 0)); tb0_3_0 = CAT(tb0_3_0_l, TRD(B_AT(0, 1, 0, 0) + 512));
    tb0_3_1_l = TRD(B_AT(0, 1, 0, 1)); tb0_3_1 = CAT(tb0_3_1_l, TRD(B_AT(0, 1, 0, 1) + 512));
    SB();
    if (__builtin_expect(!all_true(f1 != 0u), 0)) { TAKE(*LP(volatile unsigned, cr, W_X1), X1_RD); LGKM0(); }
    const f32x4 m0_ = kv0 + p0, m1_ = kv1 + p1;
    float am = fmax_raw(fabsf(m0_[0]), fabsf(m1_[0]));
#pragma unroll
    for (int e = 1; e < 4; ++e) am = fmax_raw(fmax_raw(am, fabsf(m0_[e])), fabsf(m1_[e]));
#define PKH(a_, b_) __builtin_bit_cast(unsigned, __builtin_convertvector((f32x2){(a_), (b_)}, f16x2_t))
    u32x4 hv = {PKH(m0_[0], m0_[1]), PKH(m0_[2], m0_[3]), PKH(m1_[0], m1_[1]), PKH(m1_[2], m1_[3])};
#define Z_STORE(scl) do { unsigned z0_ = zv[0]; asm("v_writelane_b32 %0, %1, 1" : "+v"(z0_) : "s"(scl)); zv[0] = z0_; __builtin_amdgcn_raw_buffer_store_b128(zv, wsr, zo, 0, 16); } while (0)
    PIN(hv);
    if (__builtin_expect(all_true(f2u(am) - 0x3C800000u <= 0x47000000u - 0x3C800000u), 1)) {
      __builtin_amdgcn_raw_buffer_store_b128(hv, wsr, kvo, 0, 16);
      SB();
      u32x4 zv = __builtin_bit_cast(u32x4, z + pz);
      Z_STORE(0x3F800000u);
    } else {
      unsigned ex = max(wave_max(f2u(am) & 0x7FFFFFFFu) >> 23, 15u);
      if (ex >= 121u && ex < 142u) ex = 141u;
      const float inv = u2f((268u - ex) << 23);
      u32x4 hs = {PKH(m0_[0] * inv, m0_[1] * inv), PKH(m0_[2] * inv, m0_[3] * inv), PKH(m1_[0] * inv, m1_[1] * inv), PKH(m1_[2] * inv, m1_[3] * inv)};
      u32x4 zv = __builtin_bit_cast(u32x4, z + pz);
#pragma unroll
      for (int e = 0; e < 4; ++e) { hs[e] = min(hs[e], ~1u); zv[e] = min(zv[e] | (ex != 141u ? 0x80000000u : 0u), ~1u); }
      CFENCE();
      __builtin_amdgcn_raw_buffer_store_b128(hs, wsr, kvo, 0, 16);
      Z_STORE((ex - 14u) << 23);
      CFENCE();
    }
  }
  *LP(volatile unsigned, cw, W_KS) = one;
  unsigned ko, oo, x2w, zfa, zfb, zfp, gta;
  u32x2 eye, ov[4][2];
  u32x4 t[16];
  float gq[4];
  unsigned pq[16];
#define PQ(mi, n, k, st) PHI(acc[0][mi][n], pq[4 * (mi) + 2 * (n) + (k)], qmin, k, st)
#define NOPQ() asm volatile("s_nop 4" : "+v"(acc[0][3][0]), "+v"(acc[0][3][1]))
#define PRQ(i)                                                                                             \
  do {                                                                                                     \
    if ((i) == 0) { lidx = misc[C_IDX + wr]; ltag = misc[C_TAG]; }                                         \
    if ((i) == 2) { zfp = L_CTL + 4 * (C_ZF + hh); PIN(zfp); }                                             \
    if ((i) == 3) { ko = kvo - mt * 1024; PIN(ko); }                                                       \
    if ((i) == 4) { gta = L_GATE + wr * 256 + c16 * 4; PIN(gta); }                                         \
    if ((i) == 5) { zfa = L_ZF + hh * 64 + wr * 8 + g * 16; PIN(zfa); }                                    \
    if ((i) == 6) { zfb = zfa ^ 8u; PIN(zfb); }                                                            \
    if ((i) == 7) { const unsigned o_ = 0x3C00u << (16 * (lane & 1)); eye = (u32x2){(lane & 2) ? 0u : o_, (lane & 2) ? o_ : 0u}; PIN(eye); } \
    if ((i) == 8) { x2w = L_X2 + w * 1024 + lane * 16; PIN(x2w); }                                         \
    if ((i) == 9) { oo = (unsigned)OFF_OF + (unsigned)(((mt * 8 + wr * 4) * NH + head) * 1024) + lane * 16; PIN(oo); } \
  } while (0)
#define SHOT_GATE                                                                                          \
  do {                                                                                                     \
    phi_fix(pq, qmin);                                                                                     \
    _Pragma("unroll") for (int i = 0; i < 16; ++i) PIN(pq[i]);                                             \
    if (__builtin_expect(!all_true(lidx != 0u), 0)) (void)wait_ge<1>(misc + C_IDX + wr, 1u);        \
    (void)wait_ge<1>(misc + C_GO + hh, 1u);                                                                \
    CFENCE();                                                                                              \
    _Pragma("unroll") for (int i = 0; i < 4; ++i) gq[i] = *LP(volatile float, gta, i * 64);                \
  } while (0)
#define T_LD(i) t[i] = __builtin_amdgcn_raw_buffer_load_b128(wsr, ko + ((i) & 3) * 1024, ((i) >> 2) * 4096, 16)
  SB();
  MM(0, 0, 0, ta0_0, tb0_0_0); SB(); PRQ(0); SB();
  MM(0, 0, 1, ta0_0, tb0_0_1); SB(); PRQ(1); SB();
  MM(0, 0, 0, ta1_0, tb0_1_0); SB(); PRQ(2); SB();
  MM(0, 0, 1, ta1_0, tb0_1_1); SB(); PRQ(3); SB();
  MM(0, 0, 0, ta2_0, tb0_2_0); SB(); PRQ(4); SB();
  MM(0, 0, 1, ta2_0, tb0_2_1); SB(); PRQ(5); SB();
  MM(0, 0, 0, ta3_0, tb0_3_0); SB(); PRQ(6); SB();
  MM(0, 0, 1, ta3_0, tb0_3_1); SB(); PRQ(7); SB();
  MM(0, 1, 0, ta0_1, tb0_0_0); SB(); PRQ(8); SB();
  MM(0, 1, 1, ta0_1, tb0_0_1); SB(); PRQ(9); SB();
  MM(0, 1, 0, ta1_1, tb0_1_0); SB(); PQ(0, 0, 0, 0); PQ(0, 0, 0, 1); PQ(0, 0, 1, 0); SB();
  MM(0, 1, 1, ta1_1, tb0_1_1); SB(); PQ(0, 0, 1, 1); PQ(0, 0, 0, 2); SB();
  MM(0, 1, 0, ta2_1, tb0_2_0); SB(); PQ(0, 0, 0, 3); PQ(0, 0, 0, 7); SB();
  MM(0, 1, 1, ta2_1, tb0_2_1); SB(); PQ(0, 0, 1, 2); PQ(0, 0, 1, 3); SB();
  MM(0, 1, 0, ta3_1, tb0_3_0); SB(); PQ(0, 0, 1, 7); PQ(0, 0, 0, 4); PQ(0, 0, 0, 5); SB();
  MM(0, 1, 1, ta3_1, tb0_3_1); SB(); PQ(0, 0, 1, 4); PQ(0, 0, 1, 5); PQ(0, 0, 0, 6); SB();
  MM(0, 2, 0, ta0_2, tb0_0_0); SB(); PQ(0, 0, 1, 6); PQ(0, 1, 0, 0); PQ(0, 1, 0, 1); SB();
  MM(0, 2, 1, ta0_2, tb0_0_1); SB(); PQ(0, 1, 1, 0); PQ(0, 1, 1, 1); PQ(0, 1, 0, 2); SB();
  MM(0, 2, 0, ta1_2, tb0_1_0); SB(); PQ(0, 1, 0, 3); PQ(0, 1, 0, 7); SB();
  MM(0, 2, 1, ta1_2, tb0_1_1); SB(); PQ(0, 1, 1, 2); PQ(0, 1, 1, 3); SB();
  MM(0, 2, 0, ta2_2, tb0_2_0); SB(); PQ(0, 1, 1, 7); PQ(0, 1, 0, 4); PQ(0, 1, 0, 5); SB();
  MM(0, 2, 1, ta2_2, tb0_2_1); SB(); PQ(0, 1, 1, 4); PQ(0, 1, 1, 5); PQ(0, 1, 0, 6); SB();
  MM(0, 2, 0, ta3_2, tb0_3_0); SB(); PQ(0, 1, 1, 6); PQ(1, 0, 0, 0); PQ(1, 0, 0, 1); SB();
  MM(0, 2, 1, ta3_2, tb0_3_1); SB(); PQ(1, 0, 1, 0); PQ(1, 0, 1, 1); PQ(1, 0, 0, 2); SB();
  MM(0, 3, 0, ta0_3, tb0_0_0); SB(); PQ(1, 0, 0, 3); PQ(1, 0, 0, 7); SB();
  MM(0, 3, 1, ta0_3, tb0_0_1); SB(); PQ(1, 0, 1, 2); PQ(1, 0, 1, 3); SB();
  MM(0, 3, 0, ta1_3, tb0_1_0); SB(); PQ(1, 0, 1, 7); PQ(1, 0, 0, 4); PQ(1, 0, 0, 5); SB();
  MM(0, 3, 1, ta1_3, tb0_1_1); SB(); PQ(1, 0, 1, 4); PQ(1, 0, 1, 5); PQ(1, 0, 0, 6); SB();
  MM(0, 3, 0, ta2_3, tb0_2_0); SB(); PQ(1, 0, 1, 6); PQ(1, 1, 0, 0); PQ(1, 1, 0, 1); SB();
  MM(0, 3, 1, ta2_3, tb0_2_1); SB(); PQ(1, 1, 1, 0); PQ(1, 1, 1, 1); PQ(1, 1, 0, 2); SB();
  MM(0, 3, 0, ta3_3, tb0_3_0); SB(); PQ(1, 1, 0, 3); PQ(1, 1, 0, 7); SB();
  MM(0, 3, 1, ta3_3, tb0_3_1); SB(); PQ(1, 1, 1, 2); PQ(1, 1, 1, 3); SB();
  PQ(1, 1, 1, 7); SB();
  PQ(1, 1, 0, 4); SB();
  PQ(1, 1, 0, 5); SB();
  PQ(1, 1, 1, 4); SB();
  PQ(1, 1, 1, 5); SB();
  PQ(1, 1, 0, 6); SB();
  PQ(1, 1, 1, 6); SB();
  PQ(2, 0, 0, 0); SB();
  PQ(2, 0, 0, 1); SB();
  PQ(2, 0, 1, 0); SB();
  PQ(2, 0, 1, 1); SB();
  PQ(2, 0, 0, 2); SB();
  PQ(2, 0, 0, 3); SB();
  PQ(2, 0, 0, 7); SB();
  PQ(2, 0, 1, 2); SB();
  PQ(2, 0, 1, 3); SB();
  PQ(2, 0, 1, 7); SB();
  PQ(2, 0, 0, 4); SB();
  PQ(2, 0, 0, 5); SB();
  PQ(2, 0, 1, 4); SB();
  PQ(2, 0, 1, 5); SB();
  PQ(2, 0, 0, 6); SB();
  PQ(2, 0, 1, 6); SB();
  PQ(2, 1, 0, 0); SB();
  PQ(2, 1, 0, 1); SB();
  PQ(2, 1, 1, 0); SB();
  PQ(2, 1, 1, 1); SB();
  PQ(2, 1, 0, 2); SB();
  PQ(2, 1, 0, 3); SB();
  PQ(2, 1, 0, 7); SB();
  PQ(2, 1, 1, 2); SB();
  PQ(2, 1, 1, 3); SB();
  PQ(2, 1, 1, 7); SB();
  PQ(2, 1, 0, 4); SB();
  PQ(2, 1, 0, 5); SB();
  PQ(2, 1, 1, 4); SB();
  PQ(2, 1, 1, 5); SB();
  PQ(2, 1, 0, 6); SB();
  PQ(2, 1, 1, 6); SB();
  NOPQ();
  PQ(3, 0, 0, 0); SB();
  PQ(3, 0, 0, 1); SB();
  PQ(3, 0, 1, 0); SB();
  PQ(3, 0, 1, 1); SB();
  PQ(3, 0, 0, 2); SB();
  PQ(3, 0, 0, 3); SB();
  PQ(3, 0, 0, 7); SB();
  PQ(3, 0, 1, 2); SB();
  PQ(3, 0, 1, 3); SB();
  PQ(3, 0, 1, 7); SB();
  PQ(3, 0, 0, 4); SB();
  PQ(3, 0, 0, 5); SB();
  PQ(3, 0, 1, 4); SB();
  PQ(3, 0, 1, 5); SB();
  PQ(3, 0, 0, 6); SB();
  PQ(3, 0, 1, 6); SB();
  PQ(3, 1, 0, 0); SB();
  PQ(3, 1, 0, 1); SB();
  PQ(3, 1, 1, 0); SB();
  PQ(3, 1, 1, 1); SB();
  PQ(3, 1, 0, 2); SB();
  PQ(3, 1, 0, 3); SB();
  PQ(3, 1, 0, 7); SB();
  PQ(3, 1, 1, 2); SB();
  PQ(3, 1, 1, 3); SB();
  PQ(3, 1, 1, 7); SB();
  PQ(3, 1, 0, 4); SB();
  PQ(3, 1, 0, 5); SB();
  PQ(3, 1, 1, 4); SB();
  PQ(3, 1, 1, 5); SB();
  PQ(3, 1, 0, 6); SB();
  PQ(3, 1, 1, 6); SB();
  SHOT_GATE;
  T_LD(0);
  T_LD(1);
  T_LD(2);
  T_LD(3);
  T_LD(4);
  T_LD(5);
  T_LD(6);
  T_LD(7);
  T_LD(8);
  T_LD(9);
  T_LD(10);
  T_LD(11);
  T_LD(12);
  T_LD(13);
  T_LD(14);
  T_LD(15);
  *LP(volatile unsigned, cw, W_AG) = one;
  SB();
  {
#define PF(mi) FRAG(((u32x4){pq[4 * (mi)], pq[4 * (mi) + 1], pq[4 * (mi) + 2], pq[4 * (mi) + 3]}))
#define H4(v, k) __builtin_bit_cast(f16x4_t, (u32x2){(v)[2 * (k)], (v)[2 * (k) + 1]})
    f32x4 s0 = zero, s1 = zero;
#define KV_MM(i0, i1)                                                                                      \
  _Pragma("unroll") for (int i = (i0); i < (i1); ++i) {                                                    \
    s0 = __builtin_amdgcn_mfma_f32_4x4x4f16(__builtin_bit_cast(f16x4_t, eye), H4(t[i], 0), s0, 0, 0, 0);   \
    s1 = __builtin_amdgcn_mfma_f32_4x4x4f16(__builtin_bit_cast(f16x4_t, eye), H4(t[i], 1), s1, 0, 0, 0);   \
  }
    float rc[4];
    u32x2 z0, z1;
#define Z_RD { z0 = *LP(volatile u32x2, zfa, 0); z1 = *LP(volatile u32x2, zfb, 0); }
    f32x4 d[4];
#define DEN1() do { const u32x4 zf = {z0[0], z0[1], z1[0], z1[1]}; _Pragma("unroll") for (int mi = 0; mi < 4; ++mi) d[mi] = MFMA(FRAG(zf), PF(mi), zero); } while (0)
#define DEN2()                                                                                             \
  do {                                                                                                     \
    _Pragma("unroll") for (int mi = 0; mi < 4; ++mi) rc[mi] = __builtin_amdgcn_rcpf(d[mi][0] + 1e-6f);     \
    _Pragma("unroll") for (int mi = 0; mi < 4; ++mi) { rc[mi] *= gq[mi]; PIN(rc[mi]); }                    \
  } while (0)
    KV_MM(0, DENAT - 4);
    SB();
    unsigned zfl = *LP(volatile unsigned, zfp, 0);
    CFENCE();
    Z_RD
    SB();
    KV_MM(DENAT - 4, DENAT);
    SB();
    DEN1();
    SB();
    KV_MM(DENAT, DENAT + 2);
    SB();
    DEN2();
    const u64 zbad = __builtin_amdgcn_ballot_w64(zfl != 1u);
    SB();
    KV_MM(DENAT + 2, 16);
    const u64 kbad = __builtin_amdgcn_ballot_w64(__builtin_isunordered(s0[0], s1[0]));
    if (__builtin_expect((kbad | zbad) != 0ull, 0)) {
      if (kbad != 0ull) {
        unsigned todo = 15u;
#pragma unroll 1
        for (unsigned spins = 0; spins <= SPIN_CAP; ++spins) {
          unsigned miss = 0;
#pragma unroll
          for (int q = 0; q < 4; ++q)
            if (todo & (1u << q)) { if (!all_true(max(max(MX4(t[4 * q]), MX4(t[4 * q + 1])), max(MX4(t[4 * q + 2]), MX4(t[4 * q + 3]))) != ~0u)) miss |= 1u << q; }
          todo = miss;
          if (todo == 0u) break;
          __builtin_amdgcn_s_sleep(2);
          CFENCE();
#pragma unroll
          for (int q = 0; q < 4; ++q)
            if (todo & (1u << q)) { T_LD(4 * q); T_LD(4 * q + 1); T_LD(4 * q + 2); T_LD(4 * q + 3); }
        }
        s0 = zero; s1 = zero;
        KV_MM(0, 16);
      }
      zfl = rfl(zfl);
      if (zfl == 0u) { TAKE(zfl = misc[C_ZF + hh], Z_RD); zfl = rfl(zfl); DEN1(); DEN2(); }
      if (zfl != 1u) {
        unsigned sc;
#pragma unroll 1
        for (unsigned spins = 0; spins <= SPIN_CAP; ++spins) {
          CFENCE();
          sc = __builtin_amdgcn_raw_buffer_load_b32(wsr, (unsigned)OFF_ZP + ZSC + (rec - mt + c16) * 16, 0, 16);
          if (all_true(sc != ~0u)) break;
        }
        s0 = zero; s1 = zero;
#pragma unroll
        for (int i = 0; i < 16; ++i) {
          const float s_ = u2f((unsigned)__builtin_amdgcn_readlane((int)sc, i));
#pragma unroll
          for (int e = 0; e < 4; ++e) {
            const unsigned tw = t[i][e];
            const f16x2_t h = __builtin_bit_cast(f16x2_t, tw);
            f32x4& d = e < 2 ? s0 : s1;
            d[2 * (e & 1)] = __builtin_fmaf((float)h[0], s_, d[2 * (e & 1)]); d[2 * (e & 1) + 1] = __builtin_fmaf((float)h[1], s_, d[2 * (e & 1) + 1]);
          }
        }
      }
    }
    const u32x4 kown = {cvt_pk(s0[0], s0[1]), cvt_pk(s0[2], s0[3]), cvt_pk(s1[0], s1[1]), cvt_pk(s1[2], s1[3])};
    *LP(u32x2, x2w, 8) = (u32x2){kown[0], kown[1]}; *LP(u32x2, x2w, 0) = (u32x2){kown[2], kown[3]};
    CFENCE();
    *LP(volatile unsigned, cw, W_X2) = one;
    SB();
    f32x4 o[4];
    u32x4 koth;
#pragma unroll
    for (int mi = 0; mi < 4; ++mi) o[mi] = MFMA(FRAG(kown), PF(mi), zero);
    const unsigned x2r = x2w ^ 2048u;
    SB();
#define O_CVT(mi, h) do { ov[mi][h] = (u32x2){cvt_pk(o[mi][0] * rc[mi], o[mi][1] * rc[mi]), cvt_pk(o[mi][2] * rc[mi], o[mi][3] * rc[mi])}; if ((h) == 0) PIN(ov[mi][0]); } while (0)
    O_CVT(0, 0); SB();
    const unsigned f2 = *LP(volatile unsigned, cr, W_X2);
    CFENCE();
    koth = *LP(volatile u32x4, x2r, 0);
    SB();
    O_CVT(1, 0); O_CVT(2, 0); O_CVT(3, 0);
    SB();
    if (__builtin_expect(!all_true(f2 != 0u), 0)) TAKE(*LP(volatile unsigned, cr, W_X2), koth = *LP(volatile u32x4, x2r, 0));
#pragma unroll
    for (int mi = 0; mi < 4; ++mi) o[mi] = MFMA(FRAG(koth), PF(mi), zero);
    SB();
    unsigned oom[4];
#pragma unroll
    for (int mi = 0; mi < 4; ++mi) { oom[mi] = oo + mi * (NH * 1024); PIN(oom[mi]); }
    SB();
#pragma unroll
    for (int mi = 0; mi < 4; ++mi) { O_CVT(mi, 1); __builtin_amdgcn_raw_buffer_store_b128((u32x4){ov[mi][0][0], ov[mi][0][1], ov[mi][1][0], ov[mi][1][1]}, wsr, oom[mi], 0, 16); SB(); }
  }
  SB();
  f32x4 oc[4][2];
  unsigned xa[4], xb[2], so[4];
  unsigned rlo, rhi;
  {
    u32x2 xl[8];
#pragma unroll
    for (int n = 0; n < 4; ++n) { xa[n] = (unsigned)(((32 * w + c16) * 128 + (((g >> 1) ^ (c16 >> 1)) * 16) + (g & 1) * 8) ^ (n * 32)); PIN(xa[n]); }
#pragma unroll
    for (int i = 0; i < 8; ++i) xl[i] = *LP(volatile u32x2, xa[i & 3], (i >> 2) * 2048);
    const unsigned kw = w * 4096 + lane * 16, wlo = hh ? x1w : kw, whi = hh ? kw : x1w;
    rlo = hh ? x1w ^ 2048u : kw; rhi = hh ? kw : x1w ^ 2048u;
#pragma unroll
    for (int mi = 0; mi < 4; ++mi) *LP(volatile u32x4, mi < 2 ? wlo : whi, (mi & 1) * 1024) = (u32x4){ov[mi][0][0], ov[mi][0][1], ov[mi][1][0], ov[mi][1][1]};
    *LP(volatile unsigned, cw, W_X1) = 2u;
    unsigned fo = lane == 0 ? (unsigned)OFF_OFLAG + (unsigned)(mt * 16 + nt) * 128u : 0x80000000u, fa = L_CW + (lane & 3) * 128, two = 2u;
    PIN(fo); PIN(fa); PIN(two); PIN(ltag);
    SB();
    WAIT_VM(0);
    *LP(volatile unsigned, cw, W_KS) = two;
    if (all_true(*LP(volatile unsigned, fa, W_KS) == 2u)) __builtin_amdgcn_raw_buffer_store_b32(ltag, wsr, fo, 0, 16);
    CFENCE();
    SB();
#pragma unroll
    for (int i = 0; i < 8; ++i) {
      const int tt = i >> 2;
      const float omg = bfr(1.0f - (tt ? (hh ? gq[3] : gq[1]) : (hh ? gq[2] : gq[0])));
#pragma unroll
      for (int k = 0; k < 2; ++k) {
        const unsigned pr = cvt_pk(omg * blo(xl[i][k]), omg * bhi(xl[i][k]));
        oc[i & 3][tt][2 * k] = blo(pr); oc[i & 3][tt][2 * k + 1] = bhi(pr);
      }
      PIN(oc[i & 3][tt]);
    }
#pragma unroll
    for (int i = 0; i < 2; ++i) { xb[i] = (unsigned)((32 * w + 8 * i + (lane >> 3)) * 128 + (((lane & 7) ^ (4 * i + g)) * 16)); PIN(xb[i]); }
#pragma unroll
    for (int i = 0; i < 4; ++i) { so[i] = (unsigned)(((m0 + 32 * w + 8 * i + (lane >> 3)) * DM + 64 * nt3) * 2 + (lane & 7) * 16); PIN(so[i]); }
  }
  int wof[2][2][4];
#pragma unroll
  for (int n = 0; n < 16; ++n) {
    wof[n >> 3][(n >> 2) & 1][n & 3] = 16384 + ((n >> 2) & 1) * 65536 + (((n >> 3) ^ wr) * 16 + 4 * g + (c16 >> 2)) * 128 + (((n & 3) ^ fsw) * 32) + (c16 & 3) * 8;
    PIN(wof[n >> 3][(n >> 2) & 1][n & 3]);
  }
  unsigned ob0 = (unsigned)OFF_OF + (unsigned)(((mt3 * 8 + 2 * w) * NH) * 1024 + lane * 16), ob1 = ob0 + NH * 1024;
  int cs1 = CH(1) * 2048, cs2 = CH(2) * 2048, cs3 = CH(3) * 2048;
  bf16x8 wf[2][4], wl[4];
  s16x4 tl2;
#define W_AT(hd, n, r) (smem + wof[r][WOPOS((hd) >> 1) >> 3][n] + (WOPOS((hd) >> 1) & 7) * 8192 + ((hd) & 1) * 4096)
  const unsigned lw = min(misc[C_WO], misc[C_WO + 1]);
  PIN(ob0); PIN(ob1); asm volatile("" : "+s"(cs1), "+s"(cs2), "+s"(cs3));
  SB();

  if (__builtin_expect(rfl(lw) == 0u, 0)) { (void)wait_ge<1>(misc + C_WO, 1u); (void)wait_ge<1>(misc + C_WO + 1, 1u); }
  CFENCE();
#pragma unroll
  for (int n = 0; n < 4; ++n) wf[0][n] = CAT(TRD(W_AT(0, n, 0)), TRD(W_AT(0, n, 1)));
  u32x4 of[OPF + 1][4];
  unsigned rdy = 0;
#define O_READY(c) do { if (__builtin_expect(rdy <= (unsigned)(c), 0)) rdy = wait_ge<0>(misc + C_ORDY, (unsigned)(c) + 1u); CFENCE(); } while (0)
#define O_LD1(c, k4) of[(c) % (OPF + 1)][k4] = __builtin_amdgcn_raw_buffer_load_b128(wsr, (((k4) & 1) ? ob1 : ob0) + ((k4) >> 1) * 1024, (c) == 1 ? cs1 : (c) == 2 ? cs2 : (c) == 3 ? cs3 : CH(c) * 2048, 16)
  TAKE(*LP(volatile unsigned, cw ^ 128u, W_X1) >> 1, { _Pragma("unroll") for (int k = 0; k < 4; ++k) of[0][k] = *LP(volatile u32x4, k < 2 ? rlo : rhi, (k & 1) * 1024); });
  unsigned li = 0;
#define P3_MM(c, j)                                                                                        \
  do {                                                                                                     \
    constexpr int hd = 2 * (c) + ((j) >> 3), n = ((j) >> 1) & 3;                                           \
    AMFMA(oc[n][(j) & 1], wf[(j) >> 3][n], of[(c) % (OPF + 1)][2 * ((j) >> 3) + ((j) & 1)]); SB();         \
    if (((j) & 1) == 0) tlo = TRD(W_AT(hd + 1, n, 0)); else wf[1 - ((j) >> 3)][n] = CAT(tlo, TRD(W_AT(hd + 1, n, 1))); \
    if ((c) > 0 && (c) + OPF < 16) {                                                                       \
      if ((j) == 0) O_READY((c) + OPF);                                                                    \
      if (((j) & 3) == 1) O_LD1((c) + OPF, (c) + OPF == 15 ? (((j) >> 3) | (((j) >> 1) & 2)) : (j) >> 2);  \
    }                                                                                                      \
    if ((c) == 14 && (j) < 8) { if (((j) & 1) == 0) tl2 = TRD(W_AT(31, n, 0)); else wl[n] = CAT(tl2, TRD(W_AT(31, n, 1))); } \
    if ((c) == 14 && (j) == 8) li = misc[C_IDX];                                                           \
    SB();                                                                                                  \
  } while (0)
#define P3_4(c, j) P3_MM(c, j); P3_MM(c, (j) + 1); P3_MM(c, (j) + 2); P3_MM(c, (j) + 3)
#define P3_C(c) P3_4(c, 0); P3_4(c, 4); P3_4(c, 8); P3_4(c, 12)
  P3_C(0);
#pragma unroll
  for (int c = 1; c <= OPF; ++c) { O_READY(c); O_LD1(c, 0); SB(); O_LD1(c, 1); SB(); O_LD1(c, 2); SB(); O_LD1(c, 3); SB(); }
  P3_C(1); P3_C(2); P3_C(3); P3_C(4); P3_C(5); P3_C(6); P3_C(7);
  P3_C(8); P3_C(9); P3_C(10); P3_C(11); P3_C(12); P3_C(13); P3_C(14);
  li = rfl(li);
  if (__builtin_expect(li == 0u, 0)) li = wait_ge<1>(misc + C_IDX, 1u);
  const int idx = (int)li - 1;
  if ((idx >> 5) == mt3 * 4 + w) {
    f32x4 dl[4];
    NOP8(oc);
    TAKE(misc[C_Y], { _Pragma("unroll") for (int n = 0; n < 4; ++n) dl[n] = *(volatile lf32x4*)(smem + L_RED + 2048 + n * 64 + g * 16); });
    const bool mine = c16 == (idx & 15);
    if (idx & 16) {
#pragma unroll
      for (int n = 0; n < 4; ++n) oc[n][1] += mine ? dl[n] : zero;
    } else {
#pragma unroll
      for (int n = 0; n < 4; ++n) oc[n][0] += mine ? dl[n] : zero;
    }
    NOP8(oc);
  }
  u32x4 ot[4];
  const __amdgpu_buffer_rsrc_t outr = MKRSRC(out, SEQ * DM * 2);
#define E_MM(tt, h, n) do { AMFMA(oc[n][tt], (h) ? wl[n] : wf[0][n], of[15 % (OPF + 1)][2 * (h) + (tt)]); SB(); } while (0)
#define E_WR(tt, n) do { *LP(volatile u32x2, xa[n], (tt) * 2048) = (u32x2){cvt_pk(oc[n][tt][0], oc[n][tt][1]), cvt_pk(oc[n][tt][2], oc[n][tt][3])}; SB(); } while (0)
#define E_RD(tt) do { ot[2 * (tt)] = *LP(volatile u32x4, xb[0], (tt) * 2048); ot[2 * (tt) + 1] = *LP(volatile u32x4, xb[1], (tt) * 2048); SB(); } while (0)
#define E_ST(i) do { __builtin_amdgcn_raw_buffer_store_b128(ot[i], outr, so[i], 0, 17); SB(); } while (0)
#define E_NOP(tt) asm volatile("s_nop 4" : "+v"(oc[0][tt]))
  SB();
  E_MM(0, 0, 0); E_MM(0, 0, 1); E_MM(0, 0, 2); E_MM(0, 0, 3); E_MM(0, 1, 0); E_MM(0, 1, 1); E_MM(0, 1, 2); E_MM(0, 1, 3);
  E_NOP(0); E_WR(0, 0); E_WR(0, 1);
  E_MM(1, 0, 0); E_WR(0, 2); E_MM(1, 0, 1); E_WR(0, 3); E_MM(1, 0, 2); E_RD(0); E_MM(1, 0, 3);
  E_MM(1, 1, 0); E_MM(1, 1, 1); E_MM(1, 1, 2); E_MM(1, 1, 3);
  E_ST(0); E_ST(1);
  E_NOP(1); E_WR(1, 0); E_WR(1, 1); E_WR(1, 2); E_WR(1, 3); E_RD(1);
  E_ST(2); E_ST(3);
}

extern "C" void kernel_launch(void* const* d_in, const int* in_sizes, int n_in,
                              void* d_out, int out_size, void* d_ws, size_t ws_size,
                              hipStream_t stream) {
  char* ws = (char*)d_ws;
  (void)hipFuncSetAttribute((const void*)fused_kernel, hipFuncAttributeMaxDynamicSharedMemorySize, L_TOTAL);
  fused_kernel<<<256, 512, L_TOTAL, stream>>>((const u16*)d_in[0], (const u16*)d_in[1], (const u16*)d_in[2], ws, (u16*)d_out,
                                              (const u16*)d_in[4], (const u16*)d_in[8], (const u16*)d_in[5], (const u16*)d_in[6],
                                              (const u16*)d_in[7]);
}
```

```cpp
#include <hip/hip_runtime.h>
#include <hip/hip_bf16.h>
#include <stdint.h>

typedef unsigned short u16;
typedef unsigned long long u64;
typedef __attribute__((ext_vector_type(8))) short bf16x8;
typedef __attribute__((ext_vector_type(4))) short s16x4;
typedef __attribute__((ext_vector_type(4))) float f32x4;
typedef __attribute__((ext_vector_type(2))) float f32x2;
typedef __attribute__((ext_vector_type(2))) __bf16 bf16x2_t;
typedef __attribute__((ext_vector_type(2))) _Float16 f16x2_t;
typedef __attribute__((ext_vector_type(4))) _Float16 f16x4_t;
typedef __attribute__((ext_vector_type(4))) unsigned int u32x4;
typedef __attribute__((ext_vector_type(2))) unsigned int u32x2;
typedef __attribute__((ext_vector_type(8))) unsigned int u32x8;
typedef __attribute__((address_space(3))) s16x4* lds_s16x4_ptr;
typedef __attribute__((address_space(1))) unsigned gu32;
typedef __attribute__((address_space(1))) u64 gu64;
typedef __attribute__((address_space(3))) unsigned lu32;
typedef __attribute__((address_space(3))) u32x2 lu32x2;
typedef __attribute__((address_space(3))) u32x4 lu32x4;
typedef __attribute__((address_space(3))) f32x4 lf32x4;
#define RLX_AGENT __ATOMIC_RELAXED, __HIP_MEMORY_SCOPE_AGENT

#if defined(__HIP_DEVICE_COMPILE__)
#define NOPK __attribute__((target("no-packed-fp32-ops")))
#else
#define NOPK
#endif

#define SEQ 2048
#define DM 1024
#define N1 3072
#define FF 3584
#define NH 32

#define OFF_EPOCH  ((size_t)0)
#define OFF_RG     ((size_t)4096)
#define OFF_GG     ((size_t)16384)
#define OFF_GU     ((size_t)262144)
#define OFF_YG     ((size_t)524288)
#define OFF_KVP    ((size_t)1 << 20)
#define OFF_ZP     ((size_t)2 << 20)
#define ZSC        65536u
#define OFF_OFLAG  (((size_t)3 << 20) + 65536)
#define OFF_OF     ((size_t)4 << 20)

#define SPIN_CAP (1u << 18)

__device__ __forceinline__ float u2f(unsigned u) { return __builtin_bit_cast(float, u); }
__device__ __forceinline__ unsigned f2u(float f) { return __builtin_bit_cast(unsigned, f); }
__device__ __forceinline__ bool all_true(bool ok) { return __builtin_amdgcn_ballot_w64(!ok) == 0ull; }
__device__ __forceinline__ unsigned cvt_pk(float a, float b) {
  f32x2 v = {a, b};
  return __builtin_bit_cast(unsigned, __builtin_convertvector(v, bf16x2_t));
}
__device__ __forceinline__ float blo(unsigned u) { return u2f(u << 16); }
__device__ __forceinline__ float bhi(unsigned u) { return u2f(u & 0xFFFF0000u); }
__device__ __forceinline__ float bfr(float f) { return blo(cvt_pk(f, f)); }
__device__ __forceinline__ float fmax_raw(float a, float b) { return __builtin_elementwise_maximum(a, b); }
__device__ __forceinline__ unsigned eps2(unsigned p) { return cvt_pk(blo(p) + 1e-6f, bhi(p) + 1e-6f); }
template <int N>
__device__ __forceinline__ void phi_fix(unsigned (&p)[N], float umin) {
  if (__builtin_expect(__builtin_amdgcn_ballot_w64(umin < -5.9f) != 0ull, 0)) {
#pragma unroll
    for (int i = 0; i < N; ++i) p[i] = eps2(p[i]);
  }
}
__device__ __forceinline__ float sigmoid_f(float x) { return 1.0f / (1.0f + __expf(-x)); }

template <int CTRL, int RM = 0xf> __device__ __forceinline__ int dppi(int old, int v) {
  return __builtin_amdgcn_update_dpp(old, v, CTRL, RM, 0xf, false);
}
template <int CTRL, int RM = 0xf> __device__ __forceinline__ float dppf(float v) {
  return __builtin_bit_cast(float, dppi<CTRL, RM>(0, __builtin_bit_cast(int, v)));
}
__device__ __forceinline__ unsigned wave_max(unsigned v) {
  v = max(v, (unsigned)dppi<0x111>(0, (int)v)); v = max(v, (unsigned)dppi<0x112>(0, (int)v));
  v = max(v, (unsigned)dppi<0x114>(0, (int)v)); v = max(v, (unsigned)dppi<0x118>(0, (int)v));
  v = max(v, (unsigned)dppi<0x142, 0xa>(0, (int)v)); v = max(v, (unsigned)dppi<0x143, 0xc>(0, (int)v));
  return (unsigned)__builtin_amdgcn_readlane((int)v, 63);
}
__device__ __forceinline__ float merge32(float a, float b) {
  const auto r = __builtin_amdgcn_permlane32_swap(f2u(a), f2u(b), false, false);
  return u2f(r[0]) + u2f(r[1]);
}
__device__ __forceinline__ float merge16(float a, float b) {
  const auto r = __builtin_amdgcn_permlane16_swap(f2u(a), f2u(b), false, false);
  return u2f(r[0]) + u2f(r[1]);
}
typedef __attribute__((address_space(3))) void* ldsp;
template <int IMM>
__device__ __forceinline__ void bufl16(__amdgpu_buffer_rsrc_t r, unsigned l, unsigned voff, unsigned soff) {
#if defined(__HIP_DEVICE_COMPILE__)
  __builtin_amdgcn_raw_ptr_buffer_load_lds(r, (ldsp)(uintptr_t)l, 16, voff, soff, IMM, 0);
#endif
}
#define LP(T, base, imm) ((__attribute__((address_space(3))) T*)(uintptr_t)(unsigned)((base) + (imm)))
#define MKRSRC(p, n) __builtin_amdgcn_make_buffer_rsrc((void*)(p), 0, (n), 0x00020000)
#define WAIT_VM(n) asm volatile("s_waitcnt vmcnt(%0)" ::"n"(n) : "memory")
#define WAIT_LGKM0() asm volatile("s_waitcnt lgkmcnt(0)" ::: "memory")
#define BAR() do { __builtin_amdgcn_sched_barrier(0); asm volatile("s_barrier" ::: "memory"); __builtin_amdgcn_sched_barrier(0); } while (0)
#define CFENCE() asm volatile("" ::: "memory")
#define MFMA(a, b, c) __builtin_amdgcn_mfma_f32_16x16x32_bf16((a), (b), (c), 0, 0, 0)
#define PIN(v) asm volatile("" : "+v"(v))
#define AMFMA(acc, a, b) asm volatile("v_mfma_f32_16x16x32_bf16 %0, %1, %2, %0" : "+v"(acc) : "v"(a), "v"(b))
#define NOP8(t) asm volatile("s_nop 7" : "+v"(t[0][0]), "+v"(t[0][1]), "+v"(t[1][0]), "+v"(t[1][1]), "+v"(t[2][0]), "+v"(t[2][1]), "+v"(t[3][0]), "+v"(t[3][1]))
#define SB() __builtin_amdgcn_sched_barrier(0)
#define TRD(p) ((s16x4)__builtin_amdgcn_ds_read_tr16_b64_v4i16((lds_s16x4_ptr)(p)))
#define CAT(lo, hi) __builtin_shufflevector(lo, hi, 0, 1, 2, 3, 4, 5, 6, 7)
#define FRAG(x) __builtin_bit_cast(bf16x8, x)

#define PACE   57
#define BURST  30
#define R_AT   2
#define R_GAP  2
#define CPF_N  20
#define WO_E   6
#define WO_ES  4
#define WO_M   9
#define G_T    35
#define P5_T   30
#define GU_AH  8
#define DN_E   8
#define GU_E   4
#define DENAT  12
#define OPF    3

__device__ __forceinline__ unsigned rfl(unsigned v) { return (unsigned)__builtin_amdgcn_readfirstlane((int)v); }
template <int SLEEP>
__device__ __forceinline__ unsigned wait_ge(volatile lu32* p, unsigned need) {
  unsigned v = 0;
#pragma unroll 1
  for (unsigned spins = 0; spins < (1u << 22); ++spins) {
    v = rfl(*p);
    if (v >= need) break;
    if (SLEEP) __builtin_amdgcn_s_sleep(SLEEP);
  }
  CFENCE();
  return v;
}

#define SLOT     40960
#define L_X1     147456
#define L_X2     155648
#define L_CW     159744
#define L_ZF     160256
#define L_GATE   160384
#define L_RED    160896
#define L_H      163200
#define L_CTL    163712
#define L_TOTAL  163840
#define W_X1   64
#define W_X2   68
#define W_AG   72
#define W_KS   76
#define WFLAGS(f) (*(volatile lu32*)(smem + L_CW + (lane & 3) * 128 + (f)))
#define WOPOS(c) ((c) < 3 ? 13 + (c) : (c) < 8 ? (c) : (c) < 11 ? (c) - 8 : (c) - 3)
#define C_F15  0
#define C_WO   2
#define C_ZF   4
#define C_TAG  8
#define C_IDX  10
#define C_GO   12
#define C_ORDY 18
#define C_GU   19
#define C_DN   20
#define C_Y    23

NOPK __global__ __launch_bounds__(512, 2) void fused_kernel(const u16* __restrict__ x, const u16* __restrict__ wqkv,
                                                            const u16* __restrict__ wo, char* ws, u16* __restrict__ out,
                                                            const u16* __restrict__ w_r, const u16* __restrict__ gate_w,
                                                            const u16* __restrict__ w_gate, const u16* __restrict__ w_up,
                                                            const u16* __restrict__ w_down) {
  extern __shared__ __attribute__((aligned(16))) char smem[];
  asm volatile("s_load_dwordx2 s[100:101], %0, 60" :: "s"(__builtin_amdgcn_kernarg_segment_ptr()) : "s100", "s101");
  SB();
  const int tid = threadIdx.x, lane = tid & 63;
  const int w = __builtin_amdgcn_readfirstlane(tid >> 6);
  u64 pc0;
  asm volatile("s_getpc_b64 %0" : "=s"(pc0));
  const int bid = blockIdx.x, xcd = bid & 7, ix = bid >> 3;
  const int ml = ix >> 2, nl = ix & 3;
  const int mt = (xcd >> 2) * 8 + ml, nt = (xcd & 3) * 4 + nl;
  const int m0 = mt * 128;
  const int mt3 = mt, nt3 = nt ^ 1;
#define CH(c) (((4 * (xcd & 3) + (c)) & 15) ^ ((c) < 4 ? nl : 0))
  volatile lu32* misc = (volatile lu32*)(smem + L_CTL);

  if (__builtin_expect((w >> 1) == 2, 1)) {
    u32x8 ka;
    asm volatile("s_load_dwordx8 %0, %1, 0x0" : "=s"(ka) : "s"(__builtin_amdgcn_kernarg_segment_ptr()));
    SB();
    const int p = w - 4;
    unsigned va[4], vb[4], vw[4];
#pragma unroll
    for (int j = 0; j < 4; ++j) {
      const int row = 32 * (w & 1) + 8 * j + (lane >> 3);
      va[j] = (unsigned)((m0 + row) * (DM * 2) + (((lane & 7) ^ ((row >> 1) & 7)) * 16) - 1024 * j);
      vb[j] = (unsigned)(row * (N1 * 2) + 128 * nt + (((lane & 7) ^ ((row & 2) | ((j & 1) << 2))) * 16) - 1024 * j);
      PIN(va[j]); PIN(vb[j]);
    }
    unsigned l0 = (unsigned)(uintptr_t)(ldsp)smem + p * 4096;
    int kf = (4 * ml + 4 * (xcd & 3)) & 15, ks = (4 * ml - 2 * (ml >> 2) + 4 * (xcd & 3)) & 15, sres = (3 - nt) & 7;
    asm volatile("" : "+s"(l0), "+s"(kf), "+s"(ks), "+s"(sres));
    SB();
    asm volatile("s_waitcnt lgkmcnt(0)" : "+s"(ka));
    const u16* x = (const u16*)(((u64)ka[1] << 32) | ka[0]);
    const u16* wqkv = (const u16*)(((u64)ka[3] << 32) | ka[2]);
    const u16* wo = (const u16*)(((u64)ka[5] << 32) | ka[4]);
    char* ws = (char*)(((u64)ka[7] << 32) | ka[6]);
    int ko_ = (int)((ka[2] >> 7) & 7u) == sres ? ks : kf;
    const __amdgpu_buffer_rsrc_t ra = MKRSRC(x, SEQ * DM * 2), rb = MKRSRC(wqkv, DM * N1 * 2), rw = MKRSRC(wo, DM * DM * 2);
#define UNIT(r, v, off, so) do { bufl16<0>(r, l0 + (off), v[0], so); bufl16<1024>(r, l0 + (off), v[1], so); bufl16<2048>(r, l0 + (off), v[2], so); bufl16<3072>(r, l0 + (off), v[3], so); } while (0)
#define ISSUE(t)                                                                                           \
  do {                                                                                                     \
    const int k_ = (ko_ + (t)) & 15;                                                                       \
    UNIT(ra, va, ((t) % 3) * SLOT, k_ * 128); UNIT(ra, va, ((t) % 3) * SLOT + 8192, k_ * 128 + 64 * DM * 2); \
    _Pragma("unroll") for (int s_ = 0; s_ < 3; ++s_) UNIT(rb, vb, ((t) % 3) * SLOT + 16384 + s_ * 8192, k_ * (64 * N1 * 2) + s_ * (DM * 2)); \
  } while (0)
#define PACED_BAR()                                                                                        \
  do {                                                                                                     \
    if (p == 0) {                                                                                          \
      unsigned now;                                                                                        \
      do { now = (unsigned)__builtin_amdgcn_s_memrealtime(); } while ((int)(now - tp) < 0);                \
      tp = ((int)(now - tp) > BURST ? now - BURST : tp) + PACE;                                            \
    }                                                                                                      \
    BAR();                                                                                                 \
  } while (0)
    ISSUE(0); ISSUE(1);
    WAIT_VM(20);
    BAR();
    ISSUE(2);
    unsigned tp = (unsigned)__builtin_amdgcn_s_memrealtime() - PACE;
#pragma unroll
    for (int j = 0; j < 4; ++j) vw[j] = (unsigned)((32 * p + 8 * j + (lane >> 3)) * (DM * 2) + 128 * nt3 + (((lane & 7) ^ ((lane >> 3) & 6)) * 16) - 1024 * j);
#define WO_CHUNK(c) UNIT(rw, vw, 16384 + WOPOS(c) * 8192, CH(c) * (64 * DM * 2))
#pragma unroll
    for (int t = 0; t < 13; ++t) {
      WAIT_VM(20);
      PACED_BAR();
      asm volatile("" : "+s"(l0), "+s"(ko_));
      ISSUE(t + 3);
    }
    WO_CHUNK(0); WO_CHUNK(1); WO_CHUNK(2);
    SB();
    const bool wslow = ((((uintptr_t)wo >> 7) + nt3) & 7) == 3;
    const int woe = wslow ? WO_ES : WO_E, wom = wslow ? WO_M + 1 : WO_M;
    WAIT_VM(32);
    PACED_BAR();
    WAIT_VM(12);
    if (lane == 0) misc[C_F15 + p] = 1u;
#pragma unroll 1
    for (int c = 3; c < woe; ++c) WO_CHUNK(c);
    {
      const __amdgpu_buffer_rsrc_t zr = MKRSRC(ws + OFF_ZP, NH * 2 * 16 * 128);
      const unsigned zo = (unsigned)((2 * nt + p) * 2048 + lane * 16);
      u32x4 zw[2];
      unsigned zm;
      (void)wait_ge<1>(misc + C_IDX + p, 1u);
#pragma unroll 1
      for (unsigned spins = 0; spins < (1u << 22) && !all_true(WFLAGS(W_KS) != 0u); ++spins) __builtin_amdgcn_s_sleep(1);
      CFENCE();
      const unsigned tk = (unsigned)__builtin_amdgcn_s_memrealtime();
#pragma unroll 1
      for (int c = woe; c < wom; ++c) WO_CHUNK(c);
      UNIT(ra, va, 0, nt3 * 128); UNIT(ra, va, 8192, nt3 * 128 + 64 * DM * 2);
      while ((unsigned)__builtin_amdgcn_s_memrealtime() - tk < (unsigned)G_T) __builtin_amdgcn_s_sleep(1);
#define MX4(v) max(max((v)[0], (v)[1]), max((v)[2], (v)[3]))
#pragma unroll 1
      for (unsigned spins = 0; spins <= SPIN_CAP; ++spins) {
        CFENCE();
        zw[0] = __builtin_amdgcn_raw_buffer_load_b128(zr, zo, 0, 16); zw[1] = __builtin_amdgcn_raw_buffer_load_b128(zr, zo + 1024, 0, 16);
        SB(); misc[C_GO + p] = 1u; SB();
        zm = max(MX4(zw[0]), MX4(zw[1]));
        if (all_true(zm != ~0u)) break;
        __builtin_amdgcn_s_sleep(2);
      }
      const bool plain = all_true((int)zm >= 0);
      if (__builtin_expect(!plain, 0)) { zw[0] &= 0x7FFFFFFFu; zw[1] &= 0x7FFFFFFFu; }
      float z0 = merge16(merge32(u2f(zw[0][0]), u2f(zw[0][2])), merge32(u2f(zw[1][0]), u2f(zw[1][2])));
      float z1 = merge16(merge32(u2f(zw[0][1]), u2f(zw[0][3])), merge32(u2f(zw[1][1]), u2f(zw[1][3])));
      z0 += dppf<0x128>(z0); z1 += dppf<0x128>(z1); z0 += dppf<0x124>(z0); z1 += dppf<0x124>(z1);
      *(unsigned*)(smem + L_ZF + p * 64 + (lane & 3) * 16 + ((lane >> 4) & 1) * 8 + (lane >> 5) * 4) = cvt_pk(z0, z1);
      CFENCE();
      misc[C_ZF + p] = plain ? 1u : 2u;
    }
#pragma unroll 1
    for (unsigned spins = 0; spins < (1u << 22) && !all_true(WFLAGS(W_AG) != 0u); ++spins) __builtin_amdgcn_s_sleep(2);
    CFENCE();
#pragma unroll 1
    for (int c = wom; c < 16; ++c) WO_CHUNK(c);
    WAIT_VM(0);
    if (lane == 0) misc[C_WO + p] = 1u;
#undef ISSUE
#undef PACED_BAR
    if (p == 0) return;
    const unsigned tag = rfl(misc[C_TAG]);
    gu32* of = (gu32*)(ws + OFF_OFLAG + (mt3 * 16 + CH(lane & 15)) * 128);
#pragma unroll 1
    for (unsigned spins = 0; spins < (1u << 22) && !all_true(WFLAGS(W_KS) == 2u); ++spins) __builtin_amdgcn_s_sleep(1);
    const unsigned t6 = (unsigned)__builtin_amdgcn_s_memrealtime();
    while ((unsigned)__builtin_amdgcn_s_memrealtime() - t6 < (unsigned)P5_T) __builtin_amdgcn_s_sleep(1);
#pragma unroll 1
    for (unsigned spins = 0;; ++spins) {
      const unsigned f = __hip_atomic_load(of, RLX_AGENT);
      const unsigned miss = (unsigned)__builtin_amdgcn_ballot_w64(f != tag) & 0xFFFEu;
      const unsigned m = (unsigned)__builtin_ctz((spins > SPIN_CAP ? 0u : miss) | 0x10000u);
      misc[C_ORDY] = m;
      if (m == 16u) break;
      __builtin_amdgcn_s_sleep(2);
    }
    return;
  }

  if (__builtin_expect(w >= 6, 0)) {
    const int pa = w - 6;
#define CODE_PF(on) __builtin_amdgcn_raw_buffer_load_b32(MKRSRC((pc0 & ~255ull) + (blockIdx.x >> 3) * (CPF_N * 64u), (on) ? CPF_N * 64 : 0), lane * 64, 0, 0)
    unsigned cpf = CODE_PF(w == 7), cpf2;
    SB();
    float* red = (float*)(smem + L_RED);
    const bool help = bid >= 224, gemv = bid < 252;
    const int gmat = bid & 1;
    const u16* wgu = gmat ? w_up : w_gate;
    const int sres = (int)(3u - (unsigned)((uintptr_t)wgu >> 7)) & 7;
    const int gfs = help ? sres + 8 * (((bid - 224) >> 1) % 7) : (bid >> 1) % 56;
    const int gdh = (help ? ((bid - 224) >> 1) / 7 : (bid >> 1) / 56) & 1;
    const bool act0 = !help, act1 = gemv && (help || (gfs & 7) != sres);
    const int gd0 = gdh * 512 + 256 * pa + 32 * (lane >> 3);
    const __amdgpu_buffer_rsrc_t rgu0 = MKRSRC(wgu, act0 ? DM * FF * 2 : 0), rgu1 = MKRSRC(wgu, act1 ? DM * FF * 2 : 0);
    const unsigned wpo = (unsigned)((gd0 * FF + gfs * 64 + 8 * (lane & 7)) * 2);
    u32x4 wv[32], wd[14];
    const int fg = bid >> 4, cg = bid & 15;
    const int f0 = fg * 224 + pa * 112;
    const __amdgpu_buffer_rsrc_t rdn = MKRSRC(w_down, DM * FF * 2);
    const unsigned wqo = (unsigned)(((f0 + 14 * (lane >> 3)) * DM + cg * 64 + 8 * (lane & 7)) * 2);
    asm volatile("" ::"s"(w_r), "s"(gate_w), "s"(ws), "s"(w_gate), "s"(w_up), "s"(w_down));
    unsigned pf;
#define GU_LD(i0, i1) do { SB(); _Pragma("unroll") for (int i = (i0); i < (i1); ++i) wv[i] = __builtin_amdgcn_raw_buffer_load_b128(i < 16 ? rgu0 : rgu1, wpo, i * (FF * 2), 2); SB(); } while (0)
#define DN_LD(i0, i1) do { SB(); _Pragma("unroll") for (int i = (i0); i < (i1); ++i) wd[i] = __builtin_amdgcn_raw_buffer_load_b128(rdn, wqo, i * (DM * 2), 2); SB(); } while (0)
    DN_LD(0, DN_E);
    {
#pragma unroll
      for (int t = 0; t <= R_AT; ++t) BAR();
      const int r0 = m0 + nt * 8 + 4 * pa;
      u32x4 xr[4][2], rv[2], gv[2];
#pragma unroll
      for (int i = 0; i < 2; ++i) {
        rv[i] = *(const u32x4*)(w_r + i * 512 + lane * 8);
        gv[i] = *(const u32x4*)(gate_w + i * 512 + lane * 8);
      }
#pragma unroll
      for (int r = 0; r < 4; ++r)
#pragma unroll
        for (int i = 0; i < 2; ++i) xr[r][i] = *(const u32x4*)(x + (size_t)(r0 + r) * DM + i * 512 + lane * 8);
      const unsigned ep = __hip_atomic_load((gu32*)(ws + OFF_EPOCH), RLX_AGENT);
      {
        const u32x4 nw = {~0u, ~0u, ~0u, ~0u};
        const __amdgpu_buffer_rsrc_t kvr = MKRSRC(ws + OFF_KVP, NH * 2 * 16 * 1024), zr = MKRSRC(ws + OFF_ZP, NH * 2 * 16 * 128);
        const unsigned rec = (unsigned)((2 * nt + pa) * 32 + mt);
        __builtin_amdgcn_raw_buffer_store_b128(nw, kvr, rec * 1024 + lane * 16, 0, 16);
        __builtin_amdgcn_raw_buffer_store_b128(nw, kvr, rec * 1024 + lane * 16 + 16384, 0, 16);
        if (lane < 10) __builtin_amdgcn_raw_buffer_store_b128(nw, zr, lane < 8 ? (rec + (lane >> 2) * 16) * 64 + (lane & 3) * 16 : ZSC + (rec + (lane & 1) * 16) * 16, 0, 16);
      }
#pragma unroll
      for (int t = R_AT + 1; t < R_AT + R_GAP; ++t) BAR();
      float racc[4], gacc[4];
#pragma unroll
      for (int r = 0; r < 4; ++r) {
        BAR();
        if (r == 1) { WAIT_VM(0); SB(); }
        racc[r] = 0.f; gacc[r] = 0.f;
#pragma unroll
        for (int i = 0; i < 2; ++i)
#pragma unroll
          for (int e = 0; e < 4; ++e) {
            const float x0 = blo(xr[r][i][e]), x1 = bhi(xr[r][i][e]);
            racc[r] += x0 * blo(rv[i][e]); racc[r] += x1 * bhi(rv[i][e]);
            const unsigned pr = cvt_pk(x0 * blo(gv[i][e]), x1 * bhi(gv[i][e]));
            gacc[r] += blo(pr); gacc[r] += bhi(pr);
          }
        PIN(racc[r]); PIN(gacc[r]);
        if (r == 3) { SB(); pf = *(const unsigned*)(wo + (size_t)(128 * ml + 64 * pa + lane) * DM + 64 * nt3); cpf2 = CODE_PF(w == 7); }
      }
      BAR();
      float rs = merge16(merge32(racc[0], racc[1]), merge32(racc[2], racc[3]));
      float gs = merge16(merge32(gacc[0], gacc[1]), merge32(gacc[2], gacc[3]));
      rs += dppf<0x128>(rs); gs += dppf<0x128>(gs); rs += dppf<0x124>(rs); gs += dppf<0x124>(gs);
      rs += dppf<0x122>(rs); gs += dppf<0x122>(gs); rs += dppf<0x121>(rs); gs += dppf<0x121>(gs);
      const int rr = r0 + ((lane >> 5) | ((lane >> 3) & 2));
      const unsigned rb = cvt_pk(rs, rs) & 0xFFFFu;
      unsigned key = (((rb & 0x8000u) ? (~rb & 0xFFFFu) : (rb | 0x8000u)) << 16) | (0xFFFFu - (unsigned)rr);
      { const auto t_ = __builtin_amdgcn_permlane16_swap(key, key, false, false); key = max(t_[0], t_[1]); }
      { const auto t_ = __builtin_amdgcn_permlane32_swap(key, key, false, false); key = max(t_[0], t_[1]); }
      const unsigned tg = rfl(ep) + 1u;
      if (lane == 0) misc[C_TAG + pa] = pa ? key : tg;
      WAIT_LGKM0();
      BAR();
      if (pa == 0 && lane == 0) __hip_atomic_store((gu64*)(ws + OFF_RG) + bid, ((u64)tg << 32) | max(key, (unsigned)misc[C_TAG + 1]), RLX_AGENT);
      if ((lane & 15) == 0) __hip_atomic_store((gu64*)(ws + OFF_GG) + rr, ((u64)tg << 32) | f2u(bfr(sigmoid_f(bfr(gs)))), RLX_AGENT);
      GU_LD(0, GU_E);
#pragma unroll
      for (int t = R_AT + R_GAP + 6; t < 13; ++t) BAR();
    }
    unsigned sel[2][4];
#pragma unroll
    for (int i = 0; i < 8; ++i) {
      const unsigned pick = (i >> 2) ? 0x0302u : 0x0100u;
      sel[i >> 2][i & 3] = ((lane & 7) >> 1) == (i & 3) ? ((lane & 1) ? (pick << 16) | 0x0C0Cu : 0x0C0C0000u | pick) : 0x0C0C0C0Cu;
      PIN(sel[i >> 2][i & 3]);
    }
    u32x4 rg[2];
#pragma unroll
    for (int i = 0; i < 2; ++i) rg[i] = __builtin_amdgcn_raw_buffer_load_b128(MKRSRC(ws + OFF_RG, 2048), lane * 16 + i * 1024, 0, 16);
    const gu64* ggp = (const gu64*)(ws + OFF_GG) + m0 + 64 * pa + lane;
    u64 gg = __hip_atomic_load(ggp, RLX_AGENT);
    BAR();
    const unsigned tag = rfl(misc[C_TAG]);
    unsigned idx;
    unsigned bestk;
    bool late;
    {
      unsigned best = 0;
      bool ok = (unsigned)(gg >> 32) == tag;
#pragma unroll
      for (int i = 0; i < 4; ++i) { ok = ok && rg[i >> 1][2 * (i & 1) + 1] == tag; best = max(best, rg[i >> 1][2 * (i & 1)]); }
      late = !all_true(ok);
      if (__builtin_expect(late, 0)) {
        BAR();
#pragma unroll 1
        for (unsigned spins = 0;; ++spins) {
          u64 rh[4];
#pragma unroll
          for (int i = 0; i < 4; ++i) rh[i] = __hip_atomic_load((gu64*)(ws + OFF_RG) + i * 64 + lane, RLX_AGENT);
          gg = __hip_atomic_load(ggp, RLX_AGENT);
          ok = (unsigned)(gg >> 32) == tag; best = 0;
#pragma unroll
          for (int i = 0; i < 4; ++i) { ok = ok && (unsigned)(rh[i] >> 32) == tag; best = max(best, (unsigned)rh[i]); }
          if (all_true(ok) || spins > SPIN_CAP) break;
          __builtin_amdgcn_s_sleep(8);
        }
      }
      best = wave_max(best);
      idx = (0xFFFFu - (best & 0xFFFFu)) & (SEQ - 1);
      bestk = best;
    }
    asm volatile("" ::"v"(pf), "v"(cpf), "v"(cpf2));
    u32x4 xs[4];
#pragma unroll
    for (int i = 0; i < 4; ++i) xs[i] = *(const u32x4*)(x + (size_t)idx * DM + gd0 + 8 * i);
    if (!late) BAR();
    __builtin_amdgcn_s_sleep(2);
    GU_LD(GU_E, 32);
    ((unsigned*)(smem + L_GATE))[64 * pa + lane] = (unsigned)gg;
    CFENCE();
    if (lane == 0) misc[C_IDX + pa] = idx + 1u;
    if (bid == 0 && pa == 0 && lane == 0) __hip_atomic_store((gu32*)(ws + OFF_EPOCH), tag, RLX_AGENT);
#define AFR(pr, i) FRAG(((u32x4){__builtin_amdgcn_perm(0u, pr, sel[(i) & 1][0]), __builtin_amdgcn_perm(0u, pr, sel[(i) & 1][1]), \
                                 __builtin_amdgcn_perm(0u, pr, sel[(i) & 1][2]), __builtin_amdgcn_perm(0u, pr, sel[(i) & 1][3])}))
    f32x4* rdw = (f32x4*)(red + pa * 128 + (lane >> 5) * 64 + (lane & 7) * 8 + ((lane >> 4) & 1) * 4);
    const bool rvalid = (lane >> 5) == ((lane >> 3) & 1);
    const auto rv = LP(volatile float, L_RED, lane * 4);
    {
      bf16x8 af[32];
      f32x4 a = {0.f, 0.f, 0.f, 0.f};
#pragma unroll
      for (int i = 0; i < GU_AH; ++i) { af[i] = AFR(xs[i >> 3][(i >> 1) & 3], i); PIN(af[i]); }
      SB();
#pragma unroll
      for (int i = 0; i < 32; ++i) {
        a = MFMA(af[i], FRAG(wv[i]), a);
        PIN(a);
        if (i + GU_AH < 32) { af[(i + GU_AH) & 31] = AFR(xs[((i + GU_AH) >> 3) & 3][((i + GU_AH) >> 1) & 3], i + GU_AH); PIN(af[(i + GU_AH) & 31]); }
        SB();
      }
      if (rvalid) *rdw = a;
      CFENCE();
      if (pa == 1) misc[C_GU] = 1u;
      else {
        float v;
#pragma unroll 1
        for (unsigned spins = 0; spins < (1u << 22); ++spins) {
          const unsigned f_ = misc[C_GU];
          CFENCE();
          v = (rv[0] + rv[64]) + (rv[128] + rv[192]);
          if (rfl(f_) != 0u) break;
          __builtin_amdgcn_s_sleep(1);
        }
        if (gemv) __hip_atomic_store((gu64*)(ws + OFF_GU) + ((help ? 4 : 0) + gdh * 2 + gmat) * FF + gfs * 64 + lane, ((u64)tag << 32) | f2u(v), RLX_AGENT);
      }
    }
    {
#pragma unroll 1
      for (unsigned spins = 0; spins < (1u << 22) && !all_true(WFLAGS(W_X2) != 0u); ++spins) __builtin_amdgcn_s_sleep(1);
      __builtin_amdgcn_s_sleep(10);
      DN_LD(DN_E, 14);
      __builtin_amdgcn_s_sleep(8);
      float hv[2];
      const int fl = f0 + 2 * (lane < 56 ? lane : 55);
      const __amdgpu_buffer_rsrc_t gr = MKRSRC(ws + OFF_GU, 8 * FF * 8);
      const bool ex[2] = {(((unsigned)((uintptr_t)w_gate >> 7) + (fl >> 6)) & 7u) == 3u, (((unsigned)((uintptr_t)w_up >> 7) + (fl >> 6)) & 7u) == 3u};
#pragma unroll 1
      for (unsigned spins = 0;; ++spins) {
        u32x4 g[8];
        CFENCE();
#pragma unroll
        for (int i = 0; i < 8; ++i) {
          g[i] = (u32x4){0u, tag, 0u, tag};
          if (i < 4 || ex[i & 1]) g[i] = __builtin_amdgcn_raw_buffer_load_b128(gr, fl * 8, i * (FF * 8), 16);
        }
        bool ok = true;
#pragma unroll
        for (int j = 0; j < 2; ++j) {
#pragma unroll
          for (int i = 0; i < 8; ++i) ok = ok && g[i][2 * j + 1] == tag;
          const float gt = bfr((u2f(g[0][2 * j]) + u2f(g[4][2 * j])) + (u2f(g[2][2 * j]) + u2f(g[6][2 * j])));
          const float up = bfr((u2f(g[1][2 * j]) + u2f(g[5][2 * j])) + (u2f(g[3][2 * j]) + u2f(g[7][2 * j])));
          hv[j] = bfr(gt * __builtin_amdgcn_rcpf(1.0f + __expf(-gt))) * up;
        }
        if (all_true(ok) || spins > SPIN_CAP) break;
        __builtin_amdgcn_s_sleep(4);
      }
      u32x4 hp[2];
      {
        char* hb = smem + L_H + pa * 256;
        if (lane < 56) *(unsigned*)(hb + (lane / 7) * 32 + (lane % 7) * 4) = cvt_pk(hv[0], hv[1]);
        CFENCE();
        hp[0] = *(volatile lu32x4*)(hb + (lane >> 3) * 32); hp[1] = *(volatile lu32x4*)(hb + (lane >> 3) * 32 + 16);
      }
      bf16x8 af[14];
#pragma unroll
      for (int i = 0; i < 14; ++i) { af[i] = AFR(hp[i >> 3][(i >> 1) & 3], i); PIN(af[i]); }
      SB();
      f32x4 a = {0.f, 0.f, 0.f, 0.f};
#pragma unroll
      for (int i = 0; i < 14; ++i) a = MFMA(af[i], FRAG(wd[i]), a);
      if (rvalid) rdw[64] = a;
      CFENCE();
      if (pa == 1) misc[C_DN] = 1u;
      else {
        float y;
#pragma unroll 1
        for (unsigned spins = 0; spins < (1u << 22); ++spins) {
          const unsigned f_ = misc[C_DN];
          CFENCE();
          y = (rv[256] + rv[320]) + (rv[384] + rv[448]);
          if (rfl(f_) != 0u) break;
          __builtin_amdgcn_s_sleep(1);
        }
        __hip_atomic_store((gu64*)(ws + OFF_YG) + fg * 1024 + cg * 64 + lane, ((u64)tag << 32) | f2u(y), RLX_AGENT);
      }
    }
    if (pa == 1 && (int)(idx >> 7) == mt3) {
      const float xe = u2f((unsigned)x[(size_t)idx * DM + 64 * nt3 + lane] << 16);
      const unsigned ob = bestk >> 16;
      const float rsel = bfr(sigmoid_f(u2f(((ob & 0x8000u) ? (ob & 0x7FFFu) : (~ob & 0xFFFFu)) << 16)));
      float y = 0.f, gt = 0.f;
      __builtin_amdgcn_s_sleep(30);
#pragma unroll 1
      for (unsigned spins = 0;; ++spins) {
        u64 g[17];
#pragma unroll
        for (int i = 0; i < 16; ++i) g[i] = __hip_atomic_load((gu64*)(ws + OFF_YG) + i * 1024 + 64 * nt3 + lane, RLX_AGENT);
        g[16] = __hip_atomic_load((gu64*)(ws + OFF_GG) + idx, RLX_AGENT);
        bool ok = (unsigned)(g[16] >> 32) == tag;
        gt = u2f((unsigned)g[16]); y = 0.f;
#pragma unroll
        for (int i = 0; i < 16; ++i) { ok = ok && (unsigned)(g[i] >> 32) == tag; y += u2f((unsigned)g[i]); }
        if (all_true(ok) || spins > SPIN_CAP) break;
        __builtin_amdgcn_s_sleep(8);
      }
      red[512 + lane] = bfr(bfr(1.0f - gt) * bfr(xe + bfr(bfr(y) * rsel))) - bfr(bfr(1.0f - gt) * xe);
      CFENCE();
      if (lane == 0) misc[C_Y] = 1u;
    }
    return;
  }

  if (w == 0) misc[lane & 31] = 0u;
  if (w == 1) *(u32x2*)(smem + L_CW + lane * 8) = (u32x2){0u, 0u};
  const int hh = w & 1, wr = w >> 1;
  const int c16 = lane & 15, g = lane >> 4;
  const int fsw = (c16 >> 3) | ((g & 1) << 1);
  const int head = 2 * nt + hh;
  int aoff[2], boff[2];
#pragma unroll
  for (int i = 0; i < 2; ++i) {
    aoff[i] = (wr * 64 + c16) * 128 + (((4 * i + g) ^ (c16 >> 1)) * 16);
    boff[i] = 16384 + (8 * g + (c16 >> 2)) * 128 + (((2 * hh + (i ^ wr)) ^ fsw) * 32) + (c16 & 3) * 8;
  }
  f32x4 acc[3][4][2];
#pragma unroll
  for (int i = 0; i < 24; ++i) { const float s0 = i < 16 ? 1.f : 0.f; acc[i / 8][(i >> 1) & 3][i & 1] = (f32x4){s0, s0, s0, s0}; PIN(acc[i / 8][(i >> 1) & 3][i & 1]); }
  bf16x8 a[2][4], b[2][6];
  s16x4 tlo;
#define A_AT(sl, h, mi) (*(const bf16x8*)(smem + (sl) * SLOT + aoff[h] + (mi) * 2048))
#define B_AT(sl, h, s, n) (smem + (sl) * SLOT + boff[n] + (s) * 8192 + (h) * 4096)
#define MM(s, mi, n, A, B) do { if ((s) == 0) AMFMA(acc[0][mi][n], B, A); else AMFMA(acc[s][mi][n], A, B); } while (0)
#define RD(sl, h, u)                                                                                       \
  do {                                                                                                     \
    if ((u) == 0) a[h][0] = A_AT(sl, h, 0);                                                                \
    else if ((u) >= 13) a[h][(u) - 12] = A_AT(sl, h, (u) - 12);                                            \
    else {                                                                                                 \
      const int j_ = (((u) - 1) / 2 + 2) % 6;                                                              \
      if ((u) & 1) tlo = TRD(B_AT(sl, h, j_ >> 1, j_ & 1));                                                \
      else b[h][j_] = CAT(tlo, TRD(B_AT(sl, h, j_ >> 1, j_ & 1) + 512));                                   \
    }                                                                                                      \
  } while (0)
#define O_M(n) ((((n) / 4) & 1) ? 3 - (n) % 4 : (n) % 4)
#define O_J(n) ((n) / 4)
#define STEP(h, rs)                                                                                        \
  _Pragma("unroll") for (int n_ = 0; n_ < 24; ++n_) {                                                      \
    const int jj_ = (O_J(n_) + 2) % 6, mi_ = O_M(n_);                                                      \
    MM(jj_ >> 1, mi_, jj_ & 1, a[h][mi_], b[h][jj_]); SB();                                                \
    if (n_ < 16) { RD(rs, 1 - (h), n_); }                                                                 \
    SB();                                                                                                  \
  }
#define LGKM0() __builtin_amdgcn_s_waitcnt(0xC07F)
#define STAGE(sl, nx) do { STEP(0, sl); LGKM0(); BAR(); STEP(1, nx); LGKM0(); SB(); } while (0)

  __amdgpu_buffer_rsrc_t wsr = MKRSRC(ws, OFF_OF + SEQ * DM * 2);
  asm volatile("" : "+s"(wsr));
  const unsigned rec = (unsigned)((head * 2 + wr) * 16 + mt);
  unsigned x1w = L_X1 + w * 2048 + lane * 16, cw = L_CW + w * 128 + g * 16, one = 1u;
  unsigned kvo = (unsigned)OFF_KVP + rec * 1024 + lane * 16;
  unsigned zo = c16 == 0 ? (unsigned)OFF_ZP + rec * 64 + g * 16 : lane == 1 ? (unsigned)OFF_ZP + ZSC + rec * 16 : 0x80000000u;
  PIN(x1w); PIN(cw); PIN(one); PIN(kvo); PIN(zo);
  WAIT_LGKM0();
  BAR();
#pragma unroll
  for (int u = 0; u < 16; ++u) RD(0, 0, u);
  LGKM0();
  SB();
#pragma unroll 1
  for (int it = 0; it < 4; ++it) { STAGE(0, 1); STAGE(1, 2); STAGE(2, 0); }
  STAGE(0, 1);
  STEP(0, 1); LGKM0(); BAR();
  bf16x8 ta0_0, ta0_1, ta0_2, ta0_3, ta1_0, ta1_1, ta1_2, ta1_3, ta2_0, ta2_1, ta2_2, ta2_3;
  bf16x8 ta3_0, ta3_1, ta3_2, ta3_3, tb0_0_0, tb0_0_1, tb0_1_0, tb0_1_1, tb0_2_0, tb0_2_1, tb0_3_0, tb0_3_1;
  bf16x8 tb1_0_0, tb1_0_1, tb1_1_0, tb1_1_1, tb1_2_0, tb1_2_1, tb1_3_0, tb1_3_1, tb2_0_0, tb2_0_1, tb2_1_0, tb2_1_1;
  bf16x8 tb2_2_0, tb2_2_1, tb2_3_0, tb2_3_1;
  s16x4 tb0_0_0_l, tb0_0_1_l, tb0_1_0_l, tb0_1_1_l, tb0_2_0_l, tb0_2_1_l, tb0_3_0_l, tb0_3_1_l, tb1_0_0_l, tb1_0_1_l, tb1_1_0_l, tb1_1_1_l;
  s16x4 tb1_2_0_l, tb1_2_1_l, tb1_3_0_l, tb1_3_1_l, tb2_0_0_l, tb2_0_1_l, tb2_1_0_l, tb2_1_1_l, tb2_2_0_l, tb2_2_1_l, tb2_3_0_l, tb2_3_1_l;
  unsigned kp[16], vp[16], x1r, cr;
  u32x2 lf15;
  float kmin = 0.f, qmin = 0.f;
  u32x4 ones;
  float ek[4];
#define PHI(src, dst, mn, k, st)                                                                           \
  do {                                                                                                     \
    float& e_ = ek[2 * (k) + ((st) & 1)];                                                                  \
    if ((st) < 2) e_ = __builtin_fmaf((src)[2 * (k) + ((st) & 1)], 1.44269504088896341f, -1.44269504088896341f); \
    else if ((st) < 4) e_ = __builtin_amdgcn_fmed3f(__builtin_amdgcn_exp2f(e_), 0.f, 1.f);                 \
    else if ((st) < 6) e_ = fmax_raw((src)[2 * (k) + ((st) & 1)], e_);                                     \
    else if ((st) == 6) dst = cvt_pk(ek[2 * (k)], ek[2 * (k) + 1]);                                        \
    else mn = __builtin_fminf(__builtin_fminf(mn, (src)[2 * (k)]), (src)[2 * (k) + 1]);                     \
    SB();                                                                                                  \
  } while (0)
#define PK(mi, n, k, st) PHI(acc[1][mi][n], kp[8 * (n) + 2 * (mi) + (k)], kmin, k, st)
#define CV(mi, n, k) do { vp[8 * (n) + 2 * (mi) + (k)] = cvt_pk(acc[2][mi][n][2 * (k)], acc[2][mi][n][2 * (k) + 1]); PIN(vp[8 * (n) + 2 * (mi) + (k)]); SB(); } while (0)
#define F15_RD lf15 = *(volatile lu32x2*)(smem + L_CTL + 4 * C_F15)
#define F15_CHK do { if (__builtin_expect(!all_true((lf15[0] & lf15[1]) != 0u), 0)) { (void)wait_ge<1>(misc + C_F15, 1u); (void)wait_ge<1>(misc + C_F15 + 1, 1u); } CFENCE(); } while (0)
#define PRE(i) do { if ((i) == 30) { x1r = x1w ^ 4096u; PIN(x1r); } if ((i) == 31) { cr = cw ^ 256u; PIN(cr); } \
                    if ((i) == 32) { ones = (u32x4){0x3F803F80u, 0x3F803F80u, 0x3F803F80u, 0x3F803F80u}; PIN(ones); } } while (0)
  MM(1, 0, 0, a[1][0], b[1][2]); SB(); tb1_0_1_l = TRD(B_AT(2, 0, 1, 1)); PRE(0); SB();
  MM(1, 0, 1, a[1][0], b[1][3]); SB(); tb1_0_1 = CAT(tb1_0_1_l, TRD(B_AT(2, 0, 1, 1) + 512)); PRE(1); SB();
  MM(2, 0, 0, a[1][0], b[1][4]); SB(); ta0_0 = A_AT(2, 0, 0); PRE(2); SB();
  MM(2, 0, 1, a[1][0], b[1][5]); SB(); ta0_1 = A_AT(2, 0, 1); PRE(3); SB();
  MM(0, 0, 0, a[1][0], b[1][0]); SB(); ta0_2 = A_AT(2, 0, 2); PRE(4); SB();
  MM(0, 0, 1, a[1][0], b[1][1]); SB(); ta0_3 = A_AT(2, 0, 3); PRE(5); SB();
  MM(1, 1, 0, a[1][1], b[1][2]); SB(); tb1_0_0_l = TRD(B_AT(2, 0, 1, 0)); PRE(6); SB();
  MM(1, 1, 1, a[1][1], b[1][3]); SB(); tb1_0_0 = CAT(tb1_0_0_l, TRD(B_AT(2, 0, 1, 0) + 512)); PRE(7); SB();
  MM(2, 1, 0, a[1][1], b[1][4]); SB(); tb1_1_1_l = TRD(B_AT(2, 1, 1, 1)); PRE(8); SB();
  MM(2, 1, 1, a[1][1], b[1][5]); SB(); tb1_1_1 = CAT(tb1_1_1_l, TRD(B_AT(2, 1, 1, 1) + 512)); PRE(9); SB();
  MM(0, 1, 0, a[1][1], b[1][0]); SB(); ta1_0 = A_AT(2, 1, 0); PRE(10); SB();
  MM(0, 1, 1, a[1][1], b[1][1]); SB(); ta1_1 = A_AT(2, 1, 1); PRE(11); SB();
  MM(1, 2, 0, a[1][2], b[1][2]); SB(); ta1_2 = A_AT(2, 1, 2); PRE(12); SB();
  MM(1, 2, 1, a[1][2], b[1][3]); SB(); ta1_3 = A_AT(2, 1, 3); PRE(13); SB();
  MM(2, 2, 0, a[1][2], b[1][4]); SB(); tb1_1_0_l = TRD(B_AT(2, 1, 1, 0)); PRE(14); SB();
  MM(2, 2, 1, a[1][2], b[1][5]); SB(); tb1_1_0 = CAT(tb1_1_0_l, TRD(B_AT(2, 1, 1, 0) + 512)); PRE(15); SB();
  MM(0, 2, 0, a[1][2], b[1][0]); SB(); PRE(16); SB();
  MM(0, 2, 1, a[1][2], b[1][1]); SB(); PRE(17); SB();
  MM(1, 3, 0, a[1][3], b[1][2]); SB(); PRE(18); SB();
  MM(1, 3, 1, a[1][3], b[1][3]); SB(); PRE(19); SB();
  MM(2, 3, 0, a[1][3], b[1][4]); SB(); F15_RD; PRE(20); SB();
  MM(2, 3, 1, a[1][3], b[1][5]); SB(); PRE(21); SB();
  MM(0, 3, 0, a[1][3], b[1][0]); SB(); tb2_0_1_l = TRD(B_AT(2, 0, 2, 1)); PRE(22); SB();
  MM(0, 3, 1, a[1][3], b[1][1]); SB(); tb2_0_1 = CAT(tb2_0_1_l, TRD(B_AT(2, 0, 2, 1) + 512)); PRE(23); SB();
  MM(1, 0, 1, ta0_0, tb1_0_1); SB(); PRE(24); SB();
  MM(1, 1, 1, ta0_1, tb1_0_1); SB(); PRE(25); SB();
  MM(1, 2, 1, ta0_2, tb1_0_1); SB(); F15_CHK; tb2_1_1_l = TRD(B_AT(2, 1, 2, 1)); PRE(26); SB();
  MM(1, 3, 1, ta0_3, tb1_0_1); SB(); tb2_1_1 = CAT(tb2_1_1_l, TRD(B_AT(2, 1, 2, 1) + 512)); PRE(27); SB();
  MM(1, 0, 0, ta0_0, tb1_0_0); SB(); tb1_2_1_l = TRD(B_AT(0, 0, 1, 1)); PRE(28); SB();
  MM(1, 1, 0, ta0_1, tb1_0_0); SB(); tb1_2_1 = CAT(tb1_2_1_l, TRD(B_AT(0, 0, 1, 1) + 512)); PRE(29); SB();
  MM(1, 2, 0, ta0_2, tb1_0_0); SB(); ta2_0 = A_AT(0, 0, 0); PRE(30); SB();
  MM(1, 3, 0, ta0_3, tb1_0_0); SB(); ta2_1 = A_AT(0, 0, 1); PRE(31); SB();
  MM(1, 0, 1, ta1_0, tb1_1_1); SB(); ta2_2 = A_AT(0, 0, 2); PRE(32); SB();
  MM(1, 1, 1, ta1_1, tb1_1_1); SB(); ta2_3 = A_AT(0, 0, 3); PRE(33); SB();
  MM(1, 2, 1, ta1_2, tb1_1_1); SB(); tb1_3_1_l = TRD(B_AT(0, 1, 1, 1)); PRE(34); SB();
  MM(1, 3, 1, ta1_3, tb1_1_1); SB(); tb1_3_1 = CAT(tb1_3_1_l, TRD(B_AT(0, 1, 1, 1) + 512)); PRE(35); SB();
  MM(1, 0, 0, ta1_0, tb1_1_0); SB(); ta3_0 = A_AT(0, 1, 0); PRE(36); SB();
  MM(1, 1, 0, ta1_1, tb1_1_0); SB(); ta3_1 = A_AT(0, 1, 1); PRE(37); SB();
  MM(1, 2, 0, ta1_2, tb1_1_0); SB(); ta3_2 = A_AT(0, 1, 2); PRE(38); SB();
  MM(1, 3, 0, ta1_3, tb1_1_0); SB(); ta3_3 = A_AT(0, 1, 3); PRE(39); SB();
  MM(1, 0, 1, ta2_0, tb1_2_1); SB(); tb1_2_0_l = TRD(B_AT(0, 0, 1, 0)); PRE(40); SB();
  MM(1, 1, 1, ta2_1, tb1_2_1); SB(); tb1_2_0 = CAT(tb1_2_0_l, TRD(B_AT(0, 0, 1, 0) + 512)); PRE(41); SB();
  MM(1, 2, 1, ta2_2, tb1_2_1); SB(); tb1_3_0_l = TRD(B_AT(0, 1, 1, 0)); PRE(42); SB();
  MM(1, 3, 1, ta2_3, tb1_2_1); SB(); tb1_3_0 = CAT(tb1_3_0_l, TRD(B_AT(0, 1, 1, 0) + 512)); PRE(43); SB();
  MM(1, 0, 1, ta3_0, tb1_3_1); SB(); tb2_2_1_l = TRD(B_AT(0, 0, 2, 1)); PRE(44); SB();
  MM(1, 1, 1, ta3_1, tb1_3_1); SB(); tb2_2_1 = CAT(tb2_2_1_l, TRD(B_AT(0, 0, 2, 1) + 512)); PRE(45); SB();
  MM(1, 2, 1, ta3_2, tb1_3_1); SB(); tb2_3_1_l = TRD(B_AT(0, 1, 2, 1)); PRE(46); SB();
  MM(1, 3, 1, ta3_3, tb1_3_1); SB(); tb2_3_1 = CAT(tb2_3_1_l, TRD(B_AT(0, 1, 2, 1) + 512)); PRE(47); SB();
  MM(1, 0, 0, ta2_0, tb1_2_0); SB(); tb2_0_0_l = TRD(B_AT(2, 0, 2, 0)); PK(0, 1, 0, 0); PK(0, 1, 0, 1); PK(0, 1, 1, 0); PK(0, 1, 1, 1); PK(0, 1, 0, 2); SB();
  MM(1, 1, 0, ta2_1, tb1_2_0); SB(); tb2_0_0 = CAT(tb2_0_0_l, TRD(B_AT(2, 0, 2, 0) + 512)); PK(0, 1, 0, 3); PK(0, 1, 0, 7); PK(0, 1, 1, 2); PK(0, 1, 1, 3); SB();
  MM(1, 2, 0, ta2_2, tb1_2_0); SB(); tb2_1_0_l = TRD(B_AT(2, 1, 2, 0)); PK(0, 1, 1, 7); PK(0, 1, 0, 4); PK(0, 1, 0, 5); PK(0, 1, 1, 4); PK(0, 1, 1, 5); PK(0, 1, 0, 6); SB();
  MM(1, 3, 0, ta2_3, tb1_2_0); SB(); tb2_1_0 = CAT(tb2_1_0_l, TRD(B_AT(2, 1, 2, 0) + 512)); PK(0, 1, 1, 6); PK(1, 1, 0, 0); PK(1, 1, 0, 1); PK(1, 1, 1, 0); PK(1, 1, 1, 1); PK(1, 1, 0, 2); SB();
  MM(1, 0, 0, ta3_0, tb1_3_0); SB(); tb2_2_0_l = TRD(B_AT(0, 0, 2, 0)); PK(1, 1, 0, 3); PK(1, 1, 0, 7); PK(1, 1, 1, 2); PK(1, 1, 1, 3); SB();
  MM(1, 1, 0, ta3_1, tb1_3_0); SB(); tb2_2_0 = CAT(tb2_2_0_l, TRD(B_AT(0, 0, 2, 0) + 512)); PK(1, 1, 1, 7); PK(1, 1, 0, 4); PK(1, 1, 0, 5); PK(1, 1, 1, 4); PK(1, 1, 1, 5); SB();
  MM(1, 2, 0, ta3_2, tb1_3_0); SB(); tb2_3_0_l = TRD(B_AT(0, 1, 2, 0)); PK(1, 1, 0, 6); PK(1, 1, 1, 6); PK(2, 1, 0, 0); PK(2, 1, 0, 1); PK(2, 1, 1, 0); SB();
  MM(1, 3, 0, ta3_3, tb1_3_0); SB(); tb2_3_0 = CAT(tb2_3_0_l, TRD(B_AT(0, 1, 2, 0) + 512)); PK(2, 1, 1, 1); PK(2, 1, 0, 2); PK(2, 1, 0, 3); SB();
  MM(2, 0, 1, ta0_0, tb2_0_1); SB(); PK(2, 1, 0, 7); PK(2, 1, 1, 2); PK(2, 1, 1, 3); SB();
  MM(2, 1, 1, ta0_1, tb2_0_1); SB(); PK(2, 1, 1, 7); PK(2, 1, 0, 4); PK(2, 1, 0, 5); PK(2, 1, 1, 4); PK(2, 1, 1, 5); SB();
  MM(2, 2, 1, ta0_2, tb2_0_1); SB(); PK(2, 1, 0, 6); PK(2, 1, 1, 6); PK(3, 1, 0, 0); PK(3, 1, 0, 1); PK(3, 1, 1, 0); SB();
  MM(2, 3, 1, ta0_3, tb2_0_1); SB(); PK(3, 1, 1, 1); PK(3, 1, 0, 2); PK(3, 1, 0, 3); SB();
  MM(2, 0, 1, ta1_0, tb2_1_1); SB(); PK(3, 1, 0, 7); PK(3, 1, 1, 2); PK(3, 1, 1, 3); SB();
  MM(2, 1, 1, ta1_1, tb2_1_1); SB(); PK(3, 1, 1, 7); PK(3, 1, 0, 4); PK(3, 1, 0, 5); PK(3, 1, 1, 4); PK(3, 1, 1, 5); SB();
  MM(2, 2, 1, ta1_2, tb2_1_1); SB(); PK(3, 1, 0, 6); PK(3, 1, 1, 6); PK(0, 0, 0, 0); PK(0, 0, 0, 1); PK(0, 0, 1, 0); SB();
  MM(2, 3, 1, ta1_3, tb2_1_1); SB(); PK(0, 0, 1, 1); PK(0, 0, 0, 2); PK(0, 0, 0, 3); SB();
  MM(2, 0, 1, ta2_0, tb2_2_1); SB(); PK(0, 0, 0, 7); PK(0, 0, 1, 2); PK(0, 0, 1, 3); SB();
  MM(2, 1, 1, ta2_1, tb2_2_1); SB(); PK(0, 0, 1, 7); PK(0, 0, 0, 4); PK(0, 0, 0, 5); PK(0, 0, 1, 4); PK(0, 0, 1, 5); SB();
  MM(2, 2, 1, ta2_2, tb2_2_1); SB(); PK(0, 0, 0, 6); PK(0, 0, 1, 6); PK(1, 0, 0, 0); PK(1, 0, 0, 1); PK(1, 0, 1, 0); SB();
  MM(2, 3, 1, ta2_3, tb2_2_1); SB(); PK(1, 0, 1, 1); PK(1, 0, 0, 2); PK(1, 0, 0, 3); SB();
  MM(2, 0, 1, ta3_0, tb2_3_1); SB(); PK(1, 0, 0, 7); PK(1, 0, 1, 2); PK(1, 0, 1, 3); SB();
  MM(2, 1, 1, ta3_1, tb2_3_1); SB(); PK(1, 0, 1, 7); PK(1, 0, 0, 4); PK(1, 0, 0, 5); PK(1, 0, 1, 4); PK(1, 0, 1, 5); SB();
  MM(2, 2, 1, ta3_2, tb2_3_1); SB(); PK(1, 0, 0, 6); PK(1, 0, 1, 6); PK(2, 0, 0, 0); PK(2, 0, 0, 1); PK(2, 0, 1, 0); SB();
  MM(2, 3, 1, ta3_3, tb2_3_1); SB(); PK(2, 0, 1, 1); PK(2, 0, 0, 2); PK(2, 0, 0, 3); SB();
  MM(2, 0, 0, ta0_0, tb2_0_0); SB(); PK(2, 0, 0, 7); PK(2, 0, 1, 2); PK(2, 0, 1, 3); SB();
  MM(2, 1, 0, ta0_1, tb2_0_0); SB(); PK(2, 0, 1, 7); PK(2, 0, 0, 4); PK(2, 0, 0, 5); PK(2, 0, 1, 4); PK(2, 0, 1, 5); SB();
  MM(2, 2, 0, ta0_2, tb2_0_0); SB(); PK(2, 0, 0, 6); PK(2, 0, 1, 6); PK(3, 0, 0, 0); PK(3, 0, 0, 1); PK(3, 0, 1, 0); SB();
  MM(2, 3, 0, ta0_3, tb2_0_0); SB(); PK(3, 0, 1, 1); PK(3, 0, 0, 2); PK(3, 0, 0, 3); SB();
  MM(2, 0, 0, ta1_0, tb2_1_0); SB(); PK(3, 0, 0, 7); PK(3, 0, 1, 2); PK(3, 0, 1, 3); SB();
  MM(2, 1, 0, ta1_1, tb2_1_0); SB(); PK(3, 0, 1, 7); PK(3, 0, 0, 4); PK(3, 0, 0, 5); PK(3, 0, 1, 4); PK(3, 0, 1, 5); SB();
  MM(2, 2, 0, ta1_2, tb2_1_0); SB(); PK(3, 0, 0, 6); PK(3, 0, 1, 6); CV(0, 1, 0); CV(0, 1, 1); CV(1, 1, 0); SB();
  MM(2, 3, 0, ta1_3, tb2_1_0); SB(); CV(1, 1, 1); CV(2, 1, 0); CV(2, 1, 1); CV(3, 1, 0); CV(3, 1, 1); SB();
  const f32x4 zero = {0.f, 0.f, 0.f, 0.f};
#define TAKE(flag, READS)                                                                                  \
  _Pragma("unroll 1") for (unsigned spins = 0; spins < (1u << 22); ++spins) {                              \
    const unsigned f_ = (flag);                                                                            \
    CFENCE();                                                                                              \
    READS;                                                                                                 \
    if (__builtin_expect(rfl(f_) != 0u, 1)) break;                                                         \
  }
  unsigned lidx, ltag;
  {
    phi_fix(kp, kmin);
#define KF(j, n) FRAG(((u32x4){kp[8 * (n) + 4 * (j)], kp[8 * (n) + 4 * (j) + 1], kp[8 * (n) + 4 * (j) + 2], kp[8 * (n) + 4 * (j) + 3]}))
#define VF(j, n) FRAG(((u32x4){vp[8 * (n) + 4 * (j)], vp[8 * (n) + 4 * (j) + 1], vp[8 * (n) + 4 * (j) + 2], vp[8 * (n) + 4 * (j) + 3]}))
    f32x4 s1 = MFMA(KF(0, 1), VF(0, 1), zero), s0 = MFMA(KF(0, 0), VF(0, 1), zero), zs = MFMA(KF(0, 1), FRAG(ones), zero);
    s1 = MFMA(KF(1, 1), VF(1, 1), s1); s0 = MFMA(KF(1, 0), VF(1, 1), s0); zs = MFMA(KF(1, 1), FRAG(ones), zs);
#define QR(u, n) do { tb0_##u##_##n##_l = TRD(B_AT((u) < 2 ? 2 : 0, (u) & 1, 0, n)); tb0_##u##_##n = CAT(tb0_##u##_##n##_l, TRD(B_AT((u) < 2 ? 2 : 0, (u) & 1, 0, n) + 512)); SB(); } while (0)
#define X1_RD { p0 = *LP(volatile f32x4, x1r, 0); p1 = *LP(volatile f32x4, x1r, 1024); pz = *LP(volatile f32x4, cr, 0); }
    SB();
    MM(2, 0, 0, ta2_0, tb2_2_0); SB(); QR(0, 0); MM(2, 1, 0, ta2_1, tb2_2_0); SB(); QR(0, 1);
    *LP(f32x4, x1w, 0) = s1; *LP(f32x4, x1w, 1024) = s0; *LP(f32x4, cw, 0) = zs;
    CFENCE();
    *LP(volatile unsigned, cw, W_X1) = one;
    SB();
    MM(2, 0, 0, ta3_0, tb2_3_0); SB(); QR(1, 0); MM(2, 1, 0, ta3_1, tb2_3_0); SB(); QR(1, 1);
    MM(2, 2, 0, ta2_2, tb2_2_0); SB(); QR(2, 0); MM(2, 3, 0, ta2_3, tb2_2_0); SB(); CV(0, 0, 0); CV(0, 0, 1); QR(2, 1);
    MM(2, 2, 0, ta3_2, tb2_3_0); SB(); CV(1, 0, 0); CV(1, 0, 1); QR(3, 0); MM(2, 3, 0, ta3_3, tb2_3_0); SB(); QR(3, 1);
    f32x4 p0, p1, pz;
    const unsigned f1 = *LP(volatile unsigned, cr, W_X1);
    CFENCE();
    X1_RD
    SB();
    f32x4 kv0 = MFMA(KF(0, 0), VF(0, 0), zero), kv1 = MFMA(KF(0, 1), VF(0, 0), zero);
    SB();
    CV(2, 0, 0); CV(2, 0, 1);
    f32x4 z = MFMA(KF(0, 0), FRAG(ones), zero);
    SB();
    CV(3, 0, 0); CV(3, 0, 1);
    kv0 = MFMA(KF(1, 0), VF(1, 0), kv0); kv1 = MFMA(KF(1, 1), VF(1, 0), kv1);
    z = MFMA(KF(1, 0), FRAG(ones), z);
    SB();
    if (__builtin_expect(!all_true(f1 != 0u), 0)) { TAKE(*LP(volatile unsigned, cr, W_X1), X1_RD); LGKM0(); }
    const f32x4 m0_ = kv0 + p0, m1_ = kv1 + p1;
    float am = fmax_raw(fabsf(m0_[0]), fabsf(m1_[0]));
#pragma unroll
    for (int e = 1; e < 4; ++e) am = fmax_raw(fmax_raw(am, fabsf(m0_[e])), fabsf(m1_[e]));
#define PKH(a_, b_) __builtin_bit_cast(unsigned, __builtin_convertvector((f32x2){(a_), (b_)}, f16x2_t))
    u32x4 hv = {PKH(m0_[0], m0_[1]), PKH(m0_[2], m0_[3]), PKH(m1_[0], m1_[1]), PKH(m1_[2], m1_[3])};
#define Z_STORE(scl) do { unsigned z0_ = zv[0]; asm("v_writelane_b32 %0, %1, 1" : "+v"(z0_) : "s"(scl)); zv[0] = z0_; __builtin_amdgcn_raw_buffer_store_b128(zv, wsr, zo, 0, 16); } while (0)
    PIN(hv);
    if (__builtin_expect(all_true(f2u(am) - 0x3C800000u <= 0x47000000u - 0x3C800000u), 1)) {
      __builtin_amdgcn_raw_buffer_store_b128(hv, wsr, kvo, 0, 16);
      SB();
      u32x4 zv = __builtin_bit_cast(u32x4, z + pz);
      Z_STORE(0x3F800000u);
    } else {
      unsigned ex = max(wave_max(f2u(am) & 0x7FFFFFFFu) >> 23, 15u);
      if (ex >= 121u && ex < 142u) ex = 141u;
      const float inv = u2f((268u - ex) << 23);
      u32x4 hs = {PKH(m0_[0] * inv, m0_[1] * inv), PKH(m0_[2] * inv, m0_[3] * inv), PKH(m1_[0] * inv, m1_[1] * inv), PKH(m1_[2] * inv, m1_[3] * inv)};
      u32x4 zv = __builtin_bit_cast(u32x4, z + pz);
#pragma unroll
      for (int e = 0; e < 4; ++e) { hs[e] = min(hs[e], ~1u); zv[e] = min(zv[e] | (ex != 141u ? 0x80000000u : 0u), ~1u); }
      CFENCE();
      __builtin_amdgcn_raw_buffer_store_b128(hs, wsr, kvo, 0, 16);
      Z_STORE((ex - 14u) << 23);
      CFENCE();
    }
  }
  *LP(volatile unsigned, cw, W_KS) = one;
  unsigned ko, oo, x2w, zfa, zfb, zfp, gta;
  u32x2 eye, ov[4][2];
  u32x4 t[16];
  float gq[4];
  unsigned pq[16];
#define PQ(mi, n, k, st) PHI(acc[0][mi][n], pq[4 * (mi) + 2 * (n) + (k)], qmin, k, st)
#define NOPQ() asm volatile("s_nop 4" : "+v"(acc[0][3][0]), "+v"(acc[0][3][1]))
#define PRQ(i)                                                                                             \
  do {                                                                                                     \
    if ((i) == 0) { lidx = misc[C_IDX + wr]; ltag = misc[C_TAG]; }                                         \
    if ((i) == 2) { zfp = L_CTL + 4 * (C_ZF + hh); PIN(zfp); }                                             \
    if ((i) == 3) { ko = kvo - mt * 1024; PIN(ko); }                                                       \
    if ((i) == 4) { gta = L_GATE + wr * 256 + c16 * 4; PIN(gta); }                                         \
    if ((i) == 5) { zfa = L_ZF + hh * 64 + wr * 8 + g * 16; PIN(zfa); }                                    \
    if ((i) == 6) { zfb = zfa ^ 8u; PIN(zfb); }                                                            \
    if ((i) == 7) { const unsigned o_ = 0x3C00u << (16 * (lane & 1)); eye = (u32x2){(lane & 2) ? 0u : o_, (lane & 2) ? o_ : 0u}; PIN(eye); } \
    if ((i) == 8) { x2w = L_X2 + w * 1024 + lane * 16; PIN(x2w); }                                         \
    if ((i) == 9) { oo = (unsigned)OFF_OF + (unsigned)(((mt * 8 + wr * 4) * NH + head) * 1024) + lane * 16; PIN(oo); } \
  } while (0)
#define SHOT_GATE                                                                                          \
  do {                                                                                                     \
    phi_fix(pq, qmin);                                                                                     \
    _Pragma("unroll") for (int i = 0; i < 16; ++i) PIN(pq[i]);                                             \
    if (__builtin_expect(!all_true(lidx != 0u), 0)) (void)wait_ge<1>(misc + C_IDX + wr, 1u);        \
    (void)wait_ge<1>(misc + C_GO + hh, 1u);                                                                \
    CFENCE();                                                                                              \
    _Pragma("unroll") for (int i = 0; i < 4; ++i) gq[i] = *LP(volatile float, gta, i * 64);                \
  } while (0)
#define T_LD(i) t[i] = __builtin_amdgcn_raw_buffer_load_b128(wsr, ko + ((i) & 3) * 1024, ((i) >> 2) * 4096, 16)
  SB();
  MM(0, 0, 0, ta0_0, tb0_0_0); SB(); PRQ(0); SB();
  MM(0, 0, 1, ta0_0, tb0_0_1); SB(); PRQ(1); SB();
  MM(0, 0, 0, ta1_0, tb0_1_0); SB(); PRQ(2); SB();
  MM(0, 0, 1, ta1_0, tb0_1_1); SB(); PRQ(3); SB();
  MM(0, 0, 0, ta2_0, tb0_2_0); SB(); PRQ(4); SB();
  MM(0, 0, 1, ta2_0, tb0_2_1); SB(); PRQ(5); SB();
  MM(0, 0, 0, ta3_0, tb0_3_0); SB(); PRQ(6); SB();
  MM(0, 0, 1, ta3_0, tb0_3_1); SB(); PRQ(7); SB();
  MM(0, 1, 0, ta0_1, tb0_0_0); SB(); PRQ(8); SB();
  MM(0, 1, 1, ta0_1, tb0_0_1); SB(); PRQ(9); SB();
  MM(0, 1, 0, ta1_1, tb0_1_0); SB(); PQ(0, 0, 0, 0); PQ(0, 0, 0, 1); PQ(0, 0, 1, 0); SB();
  MM(0, 1, 1, ta1_1, tb0_1_1); SB(); PQ(0, 0, 1, 1); PQ(0, 0, 0, 2); SB();
  MM(0, 1, 0, ta2_1, tb0_2_0); SB(); PQ(0, 0, 0, 3); PQ(0, 0, 0, 7); SB();
  MM(0, 1, 1, ta2_1, tb0_2_1); SB(); PQ(0, 0, 1, 2); PQ(0, 0, 1, 3); SB();
  MM(0, 1, 0, ta3_1, tb0_3_0); SB(); PQ(0, 0, 1, 7); PQ(0, 0, 0, 4); PQ(0, 0, 0, 5); SB();
  MM(0, 1, 1, ta3_1, tb0_3_1); SB(); PQ(0, 0, 1, 4); PQ(0, 0, 1, 5); PQ(0, 0, 0, 6); SB();
  MM(0, 2, 0, ta0_2, tb0_0_0); SB(); PQ(0, 0, 1, 6); PQ(0, 1, 0, 0); PQ(0, 1, 0, 1); SB();
  MM(0, 2, 1, ta0_2, tb0_0_1); SB(); PQ(0, 1, 1, 0); PQ(0, 1, 1, 1); PQ(0, 1, 0, 2); SB();
  MM(0, 2, 0, ta1_2, tb0_1_0); SB(); PQ(0, 1, 0, 3); PQ(0, 1, 0, 7); SB();
  MM(0, 2, 1, ta1_2, tb0_1_1); SB(); PQ(0, 1, 1, 2); PQ(0, 1, 1, 3); SB();
  MM(0, 2, 0, ta2_2, tb0_2_0); SB(); PQ(0, 1, 1, 7); PQ(0, 1, 0, 4); PQ(0, 1, 0, 5); SB();
  MM(0, 2, 1, ta2_2, tb0_2_1); SB(); PQ(0, 1, 1, 4); PQ(0, 1, 1, 5); PQ(0, 1, 0, 6); SB();
  MM(0, 2, 0, ta3_2, tb0_3_0); SB(); PQ(0, 1, 1, 6); PQ(1, 0, 0, 0); PQ(1, 0, 0, 1); SB();
  MM(0, 2, 1, ta3_2, tb0_3_1); SB(); PQ(1, 0, 1, 0); PQ(1, 0, 1, 1); PQ(1, 0, 0, 2); SB();
  MM(0, 3, 0, ta0_3, tb0_0_0); SB(); PQ(1, 0, 0, 3); PQ(1, 0, 0, 7); SB();
  MM(0, 3, 1, ta0_3, tb0_0_1); SB(); PQ(1, 0, 1, 2); PQ(1, 0, 1, 3); SB();
  MM(0, 3, 0, ta1_3, tb0_1_0); SB(); PQ(1, 0, 1, 7); PQ(1, 0, 0, 4); PQ(1, 0, 0, 5); SB();
  MM(0, 3, 1, ta1_3, tb0_1_1); SB(); PQ(1, 0, 1, 4); PQ(1, 0, 1, 5); PQ(1, 0, 0, 6); SB();
  MM(0, 3, 0, ta2_3, tb0_2_0); SB(); PQ(1, 0, 1, 6); PQ(1, 1, 0, 0); PQ(1, 1, 0, 1); SB();
  MM(0, 3, 1, ta2_3, tb0_2_1); SB(); PQ(1, 1, 1, 0); PQ(1, 1, 1, 1); PQ(1, 1, 0, 2); SB();
  MM(0, 3, 0, ta3_3, tb0_3_0); SB(); PQ(1, 1, 0, 3); PQ(1, 1, 0, 7); SB();
  MM(0, 3, 1, ta3_3, tb0_3_1); SB(); PQ(1, 1, 1, 2); PQ(1, 1, 1, 3); SB();
  PQ(1, 1, 1, 7); SB();
  PQ(1, 1, 0, 4); SB();
  PQ(1, 1, 0, 5); SB();
  PQ(1, 1, 1, 4); SB();
  PQ(1, 1, 1, 5); SB();
  PQ(1, 1, 0, 6); SB();
  PQ(1, 1, 1, 6); SB();
  PQ(2, 0, 0, 0); SB();
  PQ(2, 0, 0, 1); SB();
  PQ(2, 0, 1, 0); SB();
  PQ(2, 0, 1, 1); SB();
  PQ(2, 0, 0, 2); SB();
  PQ(2, 0, 0, 3); SB();
  PQ(2, 0, 0, 7); SB();
  PQ(2, 0, 1, 2); SB();
  PQ(2, 0, 1, 3); SB();
  PQ(2, 0, 1, 7); SB();
  PQ(2, 0, 0, 4); SB();
  PQ(2, 0, 0, 5); SB();
  PQ(2, 0, 1, 4); SB();
  PQ(2, 0, 1, 5); SB();
  PQ(2, 0, 0, 6); SB();
  PQ(2, 0, 1, 6); SB();
  PQ(2, 1, 0, 0); SB();
  PQ(2, 1, 0, 1); SB();
  PQ(2, 1, 1, 0); SB();
  PQ(2, 1, 1, 1); SB();
  PQ(2, 1, 0, 2); SB();
  PQ(2, 1, 0, 3); SB();
  PQ(2, 1, 0, 7); SB();
  PQ(2, 1, 1, 2); SB();
  PQ(2, 1, 1, 3); SB();
  PQ(2, 1, 1, 7); SB();
  PQ(2, 1, 0, 4); SB();
  PQ(2, 1, 0, 5); SB();
  PQ(2, 1, 1, 4); SB();
  PQ(2, 1, 1, 5); SB();
  PQ(2, 1, 0, 6); SB();
  PQ(2, 1, 1, 6); SB();
  NOPQ();
  PQ(3, 0, 0, 0); SB();
  PQ(3, 0, 0, 1); SB();
  PQ(3, 0, 1, 0); SB();
  PQ(3, 0, 1, 1); SB();
  PQ(3, 0, 0, 2); SB();
  PQ(3, 0, 0, 3); SB();
  PQ(3, 0, 0, 7); SB();
  PQ(3, 0, 1, 2); SB();
  PQ(3, 0, 1, 3); SB();
  PQ(3, 0, 1, 7); SB();
  PQ(3, 0, 0, 4); SB();
  PQ(3, 0, 0, 5); SB();
  PQ(3, 0, 1, 4); SB();
  PQ(3, 0, 1, 5); SB();
  PQ(3, 0, 0, 6); SB();
  PQ(3, 0, 1, 6); SB();
  PQ(3, 1, 0, 0); SB();
  PQ(3, 1, 0, 1); SB();
  PQ(3, 1, 1, 0); SB();
  PQ(3, 1, 1, 1); SB();
  PQ(3, 1, 0, 2); SB();
  PQ(3, 1, 0, 3); SB();
  PQ(3, 1, 0, 7); SB();
  PQ(3, 1, 1, 2); SB();
  PQ(3, 1, 1, 3); SB();
  PQ(3, 1, 1, 7); SB();
  PQ(3, 1, 0, 4); SB();
  PQ(3, 1, 0, 5); SB();
  PQ(3, 1, 1, 4); SB();
  PQ(3, 1, 1, 5); SB();
  PQ(3, 1, 0, 6); SB();
  PQ(3, 1, 1, 6); SB();
  SHOT_GATE;
  T_LD(0);
  T_LD(1);
  T_LD(2);
  T_LD(3);
  T_LD(4);
  T_LD(5);
  T_LD(6);
  T_LD(7);
  T_LD(8);
  T_LD(9);
  T_LD(10);
  T_LD(11);
  T_LD(12);
  T_LD(13);
  T_LD(14);
  T_LD(15);
  *LP(volatile unsigned, cw, W_AG) = one;
  SB();
  {
#define PF(mi) FRAG(((u32x4){pq[4 * (mi)], pq[4 * (mi) + 1], pq[4 * (mi) + 2], pq[4 * (mi) + 3]}))
#define H4(v, k) __builtin_bit_cast(f16x4_t, (u32x2){(v)[2 * (k)], (v)[2 * (k) + 1]})
    f32x4 s0 = zero, s1 = zero;
#define KV_MM(i0, i1)                                                                                      \
  _Pragma("unroll") for (int i = (i0); i < (i1); ++i) {                                                    \
    s0 = __builtin_amdgcn_mfma_f32_4x4x4f16(__builtin_bit_cast(f16x4_t, eye), H4(t[i], 0), s0, 0, 0, 0);   \
    s1 = __builtin_amdgcn_mfma_f32_4x4x4f16(__builtin_bit_cast(f16x4_t, eye), H4(t[i], 1), s1, 0, 0, 0);   \
  }
    float rc[4];
    u32x2 z0, z1;
#define Z_RD { z0 = *LP(volatile u32x2, zfa, 0); z1 = *LP(volatile u32x2, zfb, 0); }
    f32x4 d[4];
#define DEN1() do { const u32x4 zf = {z0[0], z0[1], z1[0], z1[1]}; _Pragma("unroll") for (int mi = 0; mi < 4; ++mi) d[mi] = MFMA(FRAG(zf), PF(mi), zero); } while (0)
#define DEN2()                                                                                             \
  do {                                                                                                     \
    _Pragma("unroll") for (int mi = 0; mi < 4; ++mi) rc[mi] = __builtin_amdgcn_rcpf(d[mi][0] + 1e-6f);     \
    _Pragma("unroll") for (int mi = 0; mi < 4; ++mi) { rc[mi] *= gq[mi]; PIN(rc[mi]); }                    \
  } while (0)
    KV_MM(0, DENAT - 4);
    SB();
    unsigned zfl = *LP(volatile unsigned, zfp, 0);
    CFENCE();
    Z_RD
    SB();
    KV_MM(DENAT - 4, DENAT);
    SB();
    DEN1();
    SB();
    KV_MM(DENAT, DENAT + 2);
    SB();
    DEN2();
    const u64 zbad = __builtin_amdgcn_ballot_w64(zfl != 1u);
    SB();
    KV_MM(DENAT + 2, 16);
    const u64 kbad = __builtin_amdgcn_ballot_w64(__builtin_isunordered(s0[0], s1[0]));
    if (__builtin_expect((kbad | zbad) != 0ull, 0)) {
      if (kbad != 0ull) {
        unsigned todo = 15u;
#pragma unroll 1
        for (unsigned spins = 0; spins <= SPIN_CAP; ++spins) {
          unsigned miss = 0;
#pragma unroll
          for (int q = 0; q < 4; ++q)
            if (todo & (1u << q)) { if (!all_true(max(max(MX4(t[4 * q]), MX4(t[4 * q + 1])), max(MX4(t[4 * q + 2]), MX4(t[4 * q + 3]))) != ~0u)) miss |= 1u << q; }
          todo = miss;
          if (todo == 0u) break;
          __builtin_amdgcn_s_sleep(2);
          CFENCE();
#pragma unroll
          for (int q = 0; q < 4; ++q)
            if (todo & (1u << q)) { T_LD(4 * q); T_LD(4 * q + 1); T_LD(4 * q + 2); T_LD(4 * q + 3); }
        }
        s0 = zero; s1 = zero;
        KV_MM(0, 16);
      }
      zfl = rfl(zfl);
      if (zfl == 0u) { TAKE(zfl = misc[C_ZF + hh], Z_RD); zfl = rfl(zfl); DEN1(); DEN2(); }
      if (zfl != 1u) {
        unsigned sc;
#pragma unroll 1
        for (unsigned spins = 0; spins <= SPIN_CAP; ++spins) {
          CFENCE();
          sc = __builtin_amdgcn_raw_buffer_load_b32(wsr, (unsigned)OFF_ZP + ZSC + (rec - mt + c16) * 16, 0, 16);
          if (all_true(sc != ~0u)) break;
        }
        s0 = zero; s1 = zero;
#pragma unroll
        for (int i = 0; i < 16; ++i) {
          const float s_ = u2f((unsigned)__builtin_amdgcn_readlane((int)sc, i));
#pragma unroll
          for (int e = 0; e < 4; ++e) {
            const unsigned tw = t[i][e];
            const f16x2_t h = __builtin_bit_cast(f16x2_t, tw);
            f32x4& d = e < 2 ? s0 : s1;
            d[2 * (e & 1)] = __builtin_fmaf((float)h[0], s_, d[2 * (e & 1)]); d[2 * (e & 1) + 1] = __builtin_fmaf((float)h[1], s_, d[2 * (e & 1) + 1]);
          }
        }
      }
    }
    const u32x4 kown = {cvt_pk(s0[0], s0[1]), cvt_pk(s0[2], s0[3]), cvt_pk(s1[0], s1[1]), cvt_pk(s1[2], s1[3])};
    *LP(u32x2, x2w, 8) = (u32x2){kown[0], kown[1]}; *LP(u32x2, x2w, 0) = (u32x2){kown[2], kown[3]};
    CFENCE();
    *LP(volatile unsigned, cw, W_X2) = one;
    SB();
    f32x4 o[4];
    u32x4 koth;
#pragma unroll
    for (int mi = 0; mi < 4; ++mi) o[mi] = MFMA(FRAG(kown), PF(mi), zero);
    const unsigned x2r = x2w ^ 2048u;
    SB();
#define O_CVT(mi, h) do { ov[mi][h] = (u32x2){cvt_pk(o[mi][0] * rc[mi], o[mi][1] * rc[mi]), cvt_pk(o[mi][2] * rc[mi], o[mi][3] * rc[mi])}; if ((h) == 0) PIN(ov[mi][0]); } while (0)
    O_CVT(0, 0); SB();
    const unsigned f2 = *LP(volatile unsigned, cr, W_X2);
    CFENCE();
    koth = *LP(volatile u32x4, x2r, 0);
    SB();
    O_CVT(1, 0); O_CVT(2, 0); O_CVT(3, 0);
    SB();
    if (__builtin_expect(!all_true(f2 != 0u), 0)) TAKE(*LP(volatile unsigned, cr, W_X2), koth = *LP(volatile u32x4, x2r, 0));
#pragma unroll
    for (int mi = 0; mi < 4; ++mi) o[mi] = MFMA(FRAG(koth), PF(mi), zero);
    SB();
    unsigned oom[4];
#pragma unroll
    for (int mi = 0; mi < 4; ++mi) { oom[mi] = oo + mi * (NH * 1024); PIN(oom[mi]); }
    SB();
#pragma unroll
    for (int mi = 0; mi < 4; ++mi) { O_CVT(mi, 1); __builtin_amdgcn_raw_buffer_store_b128((u32x4){ov[mi][0][0], ov[mi][0][1], ov[mi][1][0], ov[mi][1][1]}, wsr, oom[mi], 0, 16); SB(); }
  }
  SB();
  f32x4 oc[4][2];
  unsigned xa[4], xb[2], so[4];
  unsigned rlo, rhi;
  {
    u32x2 xl[8];
#pragma unroll
    for (int n = 0; n < 4; ++n) { xa[n] = (unsigned)(((32 * w + c16) * 128 + (((g >> 1) ^ (c16 >> 1)) * 16) + (g & 1) * 8) ^ (n * 32)); PIN(xa[n]); }
#pragma unroll
    for (int i = 0; i < 8; ++i) xl[i] = *LP(volatile u32x2, xa[i & 3], (i >> 2) * 2048);
    const unsigned kw = w * 4096 + lane * 16, wlo = hh ? x1w : kw, whi = hh ? kw : x1w;
    rlo = hh ? x1w ^ 2048u : kw; rhi = hh ? kw : x1w ^ 2048u;
#pragma unroll
    for (int mi = 0; mi < 4; ++mi) *LP(volatile u32x4, mi < 2 ? wlo : whi, (mi & 1) * 1024) = (u32x4){ov[mi][0][0], ov[mi][0][1], ov[mi][1][0], ov[mi][1][1]};
    *LP(volatile unsigned, cw, W_X1) = 2u;
    unsigned fo = lane == 0 ? (unsigned)OFF_OFLAG + (unsigned)(mt * 16 + nt) * 128u : 0x80000000u, fa = L_CW + (lane & 3) * 128, two = 2u;
    PIN(fo); PIN(fa); PIN(two); PIN(ltag);
    SB();
    WAIT_VM(0);
    *LP(volatile unsigned, cw, W_KS) = two;
    if (all_true(*LP(volatile unsigned, fa, W_KS) == 2u)) __builtin_amdgcn_raw_buffer_store_b32(ltag, wsr, fo, 0, 16);
    CFENCE();
    SB();
#pragma unroll
    for (int i = 0; i < 8; ++i) {
      const int tt = i >> 2;
      const float omg = bfr(1.0f - (tt ? (hh ? gq[3] : gq[1]) : (hh ? gq[2] : gq[0])));
#pragma unroll
      for (int k = 0; k < 2; ++k) {
        const unsigned pr = cvt_pk(omg * blo(xl[i][k]), omg * bhi(xl[i][k]));
        oc[i & 3][tt][2 * k] = blo(pr); oc[i & 3][tt][2 * k + 1] = bhi(pr);
      }
      PIN(oc[i & 3][tt]);
    }
#pragma unroll
    for (int i = 0; i < 2; ++i) { xb[i] = (unsigned)((32 * w + 8 * i + (lane >> 3)) * 128 + (((lane & 7) ^ (4 * i + g)) * 16)); PIN(xb[i]); }
#pragma unroll
    for (int i = 0; i < 4; ++i) { so[i] = (unsigned)(((m0 + 32 * w + 8 * i + (lane >> 3)) * DM + 64 * nt3) * 2 + (lane & 7) * 16); PIN(so[i]); }
  }
  int wof[2][2][4];
#pragma unroll
  for (int n = 0; n < 16; ++n) {
    wof[n >> 3][(n >> 2) & 1][n & 3] = 16384 + ((n >> 2) & 1) * 65536 + (((n >> 3) ^ wr) * 16 + 4 * g + (c16 >> 2)) * 128 + (((n & 3) ^ fsw) * 32) + (c16 & 3) * 8;
    PIN(wof[n >> 3][(n >> 2) & 1][n & 3]);
  }
  unsigned ob0 = (unsigned)OFF_OF + (unsigned)(((mt3 * 8 + 2 * w) * NH) * 1024 + lane * 16), ob1 = ob0 + NH * 1024;
  int cs1 = CH(1) * 2048, cs2 = CH(2) * 2048, cs3 = CH(3) * 2048;
  bf16x8 wf[2][4], wl[4];
  s16x4 tl2;
#define W_AT(hd, n, r) (smem + wof[r][WOPOS((hd) >> 1) >> 3][n] + (WOPOS((hd) >> 1) & 7) * 8192 + ((hd) & 1) * 4096)
  const unsigned lw = min(misc[C_WO], misc[C_WO + 1]);
  PIN(ob0); PIN(ob1); asm volatile("" : "+s"(cs1), "+s"(cs2), "+s"(cs3));
  SB();

  if (__builtin_expect(rfl(lw) == 0u, 0)) { (void)wait_ge<1>(misc + C_WO, 1u); (void)wait_ge<1>(misc + C_WO + 1, 1u); }
  CFENCE();
#pragma unroll
  for (int n = 0; n < 4; ++n) wf[0][n] = CAT(TRD(W_AT(0, n, 0)), TRD(W_AT(0, n, 1)));
  u32x4 of[OPF + 1][4];
  unsigned rdy = 0;
#define O_READY(c) do { if (__builtin_expect(rdy <= (unsigned)(c), 0)) rdy = wait_ge<0>(misc + C_ORDY, (unsigned)(c) + 1u); CFENCE(); } while (0)
#define O_LD1(c, k4) of[(c) % (OPF + 1)][k4] = __builtin_amdgcn_raw_buffer_load_b128(wsr, (((k4) & 1) ? ob1 : ob0) + ((k4) >> 1) * 1024, (c) == 1 ? cs1 : (c) == 2 ? cs2 : (c) == 3 ? cs3 : CH(c) * 2048, 16)
  TAKE(*LP(volatile unsigned, cw ^ 128u, W_X1) >> 1, { _Pragma("unroll") for (int k = 0; k < 4; ++k) of[0][k] = *LP(volatile u32x4, k < 2 ? rlo : rhi, (k & 1) * 1024); });
  unsigned li = 0;
#define P3_MM(c, j)                                                                                        \
  do {                                                                                                     \
    constexpr int hd = 2 * (c) + ((j) >> 3), n = ((j) >> 1) & 3;                                           \
    AMFMA(oc[n][(j) & 1], wf[(j) >> 3][n], of[(c) % (OPF + 1)][2 * ((j) >> 3) + ((j) & 1)]); SB();         \
    if (((j) & 1) == 0) tlo = TRD(W_AT(hd + 1, n, 0)); else wf[1 - ((j) >> 3)][n] = CAT(tlo, TRD(W_AT(hd + 1, n, 1))); \
    if ((c) > 0 && (c) + OPF < 16) {                                                                       \
      if ((j) == 0) O_READY((c) + OPF);                                                                    \
      if (((j) & 3) == 1) O_LD1((c) + OPF, (c) + OPF == 15 ? (((j) >> 3) | (((j) >> 1) & 2)) : (j) >> 2);  \
    }                                                                                                      \
    if ((c) == 14 && (j) < 8) { if (((j) & 1) == 0) tl2 = TRD(W_AT(31, n, 0)); else wl[n] = CAT(tl2, TRD(W_AT(31, n, 1))); } \
    if ((c) == 14 && (j) == 8) li = misc[C_IDX];                                                           \
    SB();                                                                                                  \
  } while (0)
#define P3_4(c, j) P3_MM(c, j); P3_MM(c, (j) + 1); P3_MM(c, (j) + 2); P3_MM(c, (j) + 3)
#define P3_C(c) P3_4(c, 0); P3_4(c, 4); P3_4(c, 8); P3_4(c, 12)
  P3_C(0);
#pragma unroll
  for (int c = 1; c <= OPF; ++c) { O_READY(c); O_LD1(c, 0); SB(); O_LD1(c, 1); SB(); O_LD1(c, 2); SB(); O_LD1(c, 3); SB(); }
  P3_C(1); P3_C(2); P3_C(3); P3_C(4); P3_C(5); P3_C(6); P3_C(7);
  P3_C(8); P3_C(9); P3_C(10); P3_C(11); P3_C(12); P3_C(13); P3_C(14);
  li = rfl(li);
  if (__builtin_expect(li == 0u, 0)) li = wait_ge<1>(misc + C_IDX, 1u);
  const int idx = (int)li - 1;
  if ((idx >> 5) == mt3 * 4 + w) {
    f32x4 dl[4];
    NOP8(oc);
    TAKE(misc[C_Y], { _Pragma("unroll") for (int n = 0; n < 4; ++n) dl[n] = *(volatile lf32x4*)(smem + L_RED + 2048 + n * 64 + g * 16); });
    const bool mine = c16 == (idx & 15);
    if (idx & 16) {
#pragma unroll
      for (int n = 0; n < 4; ++n) oc[n][1] += mine ? dl[n] : zero;
    } else {
#pragma unroll
      for (int n = 0; n < 4; ++n) oc[n][0] += mine ? dl[n] : zero;
    }
    NOP8(oc);
  }
  u32x4 ot[4];
  const __amdgpu_buffer_rsrc_t outr = MKRSRC(out, SEQ * DM * 2);
#define E_MM(tt, h, n) do { AMFMA(oc[n][tt], (h) ? wl[n] : wf[0][n], of[15 % (OPF + 1)][2 * (h) + (tt)]); SB(); } while (0)
#define E_WR(tt, n) do { *LP(volatile u32x2, xa[n], (tt) * 2048) = (u32x2){cvt_pk(oc[n][tt][0], oc[n][tt][1]), cvt_pk(oc[n][tt][2], oc[n][tt][3])}; SB(); } while (0)
#define E_RD(tt) do { ot[2 * (tt)] = *LP(volatile u32x4, xb[0], (tt) * 2048); ot[2 * (tt) + 1] = *LP(volatile u32x4, xb[1], (tt) * 2048); SB(); } while (0)
#define E_ST(i) do { __builtin_amdgcn_raw_buffer_store_b128(ot[i], outr, so[i], 0, 17); SB(); } while (0)
#define E_NOP(tt) asm volatile("s_nop 4" : "+v"(oc[0][tt]))
  SB();
  E_MM(0, 0, 0); E_MM(0, 0, 1); E_MM(0, 0, 2); E_MM(0, 0, 3); E_MM(0, 1, 0); E_MM(0, 1, 1); E_MM(0, 1, 2); E_MM(0, 1, 3);
  E_NOP(0); E_WR(0, 0); E_WR(0, 1);
  E_MM(1, 0, 0); E_WR(0, 2); E_MM(1, 0, 1); E_WR(0, 3); E_MM(1, 0, 2); E_RD(0); E_MM(1, 0, 3);
  E_MM(1, 1, 0); E_MM(1, 1, 1); E_MM(1, 1, 2); E_MM(1, 1, 3);
  E_ST(0); E_ST(1);
  E_NOP(1); E_WR(1, 0); E_WR(1, 1); E_WR(1, 2); E_WR(1, 3); E_RD(1);
  E_ST(2); E_ST(3);
}

extern "C" void kernel_launch(void* const* d_in, const int* in_sizes, int n_in,
                              void* d_out, int out_size, void* d_ws, size_t ws_size,
                              hipStream_t stream) {
  char* ws = (char*)d_ws;
  (void)hipFuncSetAttribute((const void*)fused_kernel, hipFuncAttributeMaxDynamicSharedMemorySize, L_TOTAL);
  fused_kernel<<<256, 512, L_TOTAL, stream>>>((const u16*)d_in[0], (const u16*)d_in[1], (const u16*)d_in[2], ws, (u16*)d_out,
                                              (const u16*)d_in[4], (const u16*)d_in[8], (const u16*)d_in[5], (const u16*)d_in[6],
                                              (const u16*)d_in[7]);
}
```

```cpp
#define STAMPS
#define MINSTAMP 1
#define NOSTG 1
#define DG_B 9
#define GE_B -1
#define DN_A 7
#define KX2 0
#define KB 0
#define KAS 4
#define R_NAP 0
#define RG_E 0
#define CPF 3
#define CPF_N 20
#define NOX 0
#include <hip/hip_runtime.h>
#include <hip/hip_bf16.h>
#include <stdint.h>

typedef unsigned short u16;
typedef unsigned long long u64;
typedef __attribute__((ext_vector_type(8))) short bf16x8;
typedef __attribute__((ext_vector_type(4))) short s16x4;
typedef __attribute__((ext_vector_type(16))) float f32x16;
typedef __attribute__((ext_vector_type(4))) float f32x4;
typedef __attribute__((ext_vector_type(2))) float f32x2;
typedef __attribute__((ext_vector_type(2))) __bf16 bf16x2_t;
typedef __attribute__((ext_vector_type(2))) _Float16 f16x2_t;
typedef __attribute__((ext_vector_type(4))) _Float16 f16x4_t;
typedef __attribute__((ext_vector_type(4))) unsigned int u32x4;
typedef __attribute__((ext_vector_type(2))) unsigned int u32x2;
typedef __attribute__((ext_vector_type(2))) unsigned short u16x2;
typedef __attribute__((ext_vector_type(8))) unsigned int u32x8;
typedef __attribute__((address_space(3))) s16x4* lds_s16x4_ptr;
typedef __attribute__((address_space(1))) unsigned gu32;
typedef __attribute__((address_space(1))) u64 gu64;
typedef __attribute__((address_space(3))) unsigned lu32;
typedef __attribute__((address_space(3))) u32x2 lu32x2;
typedef __attribute__((address_space(3))) u32x4 lu32x4;
typedef __attribute__((address_space(3))) f32x4 lf32x4;
#define RLX_AGENT __ATOMIC_RELAXED, __HIP_MEMORY_SCOPE_AGENT
#define RLX_WG __ATOMIC_RELAXED, __HIP_MEMORY_SCOPE_WORKGROUP

#if defined(__HIP_DEVICE_COMPILE__)
#define NOPK __attribute__((target("no-packed-fp32-ops")))
#else
#define NOPK
#endif

#define SEQ 2048
#define DM 1024
#define N1 3072
#define FF 3584
#define NH 32

#define OFF_EPOCH  ((size_t)0)
#define OFF_RG     ((size_t)4096)
#define OFF_GG     ((size_t)16384)
#define OFF_MISC   ((size_t)32768)
#define OFF_GU     ((size_t)262144)
#define OFF_YG     ((size_t)524288)
#define OFF_KVP    ((size_t)1 << 20)
#define OFF_ZP     ((size_t)2 << 20)
#define OFF_KVFLAG ((size_t)3 << 20)
#define OFF_OFLAG  (((size_t)3 << 20) + 65536)
#define OFF_OF     ((size_t)4 << 20)

#define SPIN_CAP (1u << 18)
#define OFF_TS ((size_t)128 << 20)
__device__ __forceinline__ u64 rt_() { u64 t; asm volatile("s_memrealtime %0\n\ts_waitcnt lgkmcnt(0)" : "=s"(t)::"memory"); return t; }
__device__ __forceinline__ u64 ct_() { u64 t; asm volatile("s_memtime %0\n\ts_waitcnt lgkmcnt(0)" : "=s"(t)::"memory"); return t; }
#define STAMP(kern, slot) do { if ((!MINSTAMP || (slot) == 0 || (slot) == 10 || ((slot) == 3 && w >= 6)) && lane == 0) ((gu64*)(ws + OFF_TS))[(((kern) * 256 + bid) * 16 + w) * 16 + (slot)] = rt_(); } while (0)
#define CSTAMP(slot) do { if (!MINSTAMP && lane == 0) ((gu64*)(ws + OFF_TS))[((256 + bid) * 16 + w) * 16 + (slot)] = ct_(); } while (0)
#define STG(k) do { } while (0)
#define NST0 1
#define T00() asm volatile("s_memrealtime s[98:99]" ::: "s98", "s99")
#define STAMP0() do { u64 t_; asm volatile("s_waitcnt lgkmcnt(0)\n\ts_mov_b64 %0, s[98:99]" : "=s"(t_)); if (lane == 0) ((gu64*)(ws + OFF_TS))[(bid * 16 + w) * 16] = t_; } while (0)
#define TSAVE(v) const u64 v = rt_()
#define TSOUT(slot, v) do { if (lane == 0) ((gu64*)(ws + OFF_TS))[(bid * 16 + w) * 16 + (slot)] = v; } while (0)
#define TW0() const u64 tw_ = MINSTAMP ? 0 : ct_()
#define TW1(i) do { if (!MINSTAMP) { twacc[i] += ct_() - tw_; twn[i] += 1; } } while (0)
#define TWOUT(kern) do { if (!MINSTAMP && lane == 0) { u64* q_ = ((u64*)(ws + OFF_TS)) + (((kern) * 256 + bid) * 16 + w) * 16; q_[12] = twacc[0] | (1ull << 62); q_[14] = twacc[1] | (1ull << 62); q_[15] = twn[0] | (1ull << 62); } } while (0)
#define TWDECL() u64 twacc[2] = {0, 0}; unsigned twn[2] = {0, 0}

__device__ __forceinline__ float u2f(unsigned u) { return __builtin_bit_cast(float, u); }
__device__ __forceinline__ unsigned f2u(float f) { return __builtin_bit_cast(unsigned, f); }
__device__ __forceinline__ bool all_true(bool ok) { return __builtin_amdgcn_ballot_w64(!ok) == 0ull; }
__device__ __forceinline__ float shfl_f(float v, int src) { return u2f((unsigned)__builtin_amdgcn_ds_bpermute(src << 2, (int)f2u(v))); }
__device__ __forceinline__ unsigned cvt_pk(float a, float b) {
  f32x2 v = {a, b};
  return __builtin_bit_cast(unsigned, __builtin_convertvector(v, bf16x2_t));
}
__device__ __forceinline__ float blo(unsigned u) { return u2f(u << 16); }
__device__ __forceinline__ float bhi(unsigned u) { return u2f(u & 0xFFFF0000u); }
__device__ __forceinline__ float bfr(float f) { return blo(cvt_pk(f, f)); }
__device__ __forceinline__ float phi_e(float u) {
  return __builtin_amdgcn_fmed3f(__builtin_amdgcn_exp2f(__builtin_fmaf(u, 1.44269504088896341f, -1.44269504088896341f)), 0.f, 1.f);
}
__device__ __forceinline__ float fmax_raw(float a, float b) { return __builtin_elementwise_maximum(a, b); }
__device__ __forceinline__ unsigned phi2a(float a, float b) { return cvt_pk(fmax_raw(a, phi_e(a)), fmax_raw(b, phi_e(b))); }
__device__ __forceinline__ unsigned eps2(unsigned p) { return cvt_pk(blo(p) + 1e-6f, bhi(p) + 1e-6f); }
template <int N>
__device__ __forceinline__ void phi_fix(unsigned (&p)[N], float umin) {
  if (__builtin_expect(__builtin_amdgcn_ballot_w64(umin < -5.9f) != 0ull, 0)) {
#pragma unroll
    for (int i = 0; i < N; ++i) p[i] = eps2(p[i]);
  }
}
__device__ __forceinline__ float sigmoid_f(float x) { return 1.0f / (1.0f + __expf(-x)); }

template <int CTRL, int RM = 0xf> __device__ __forceinline__ int dppi(int old, int v) {
  return __builtin_amdgcn_update_dpp(old, v, CTRL, RM, 0xf, false);
}
template <int CTRL, int RM = 0xf> __device__ __forceinline__ float dppf(float v) {
  return __builtin_bit_cast(float, dppi<CTRL, RM>(0, __builtin_bit_cast(int, v)));
}
__device__ __forceinline__ float wave_sum(float v) {
  v += dppf<0x111>(v); v += dppf<0x112>(v); v += dppf<0x114>(v); v += dppf<0x118>(v);
  v += dppf<0x142, 0xa>(v); v += dppf<0x143, 0xc>(v);
  return __builtin_bit_cast(float, __builtin_amdgcn_readlane(__builtin_bit_cast(int, v), 63));
}
__device__ __forceinline__ unsigned wave_max(unsigned v) {
  v = max(v, (unsigned)dppi<0x111>(0, (int)v)); v = max(v, (unsigned)dppi<0x112>(0, (int)v));
  v = max(v, (unsigned)dppi<0x114>(0, (int)v)); v = max(v, (unsigned)dppi<0x118>(0, (int)v));
  v = max(v, (unsigned)dppi<0x142, 0xa>(0, (int)v)); v = max(v, (unsigned)dppi<0x143, 0xc>(0, (int)v));
  return (unsigned)__builtin_amdgcn_readlane((int)v, 63);
}
__device__ __forceinline__ float sum_x16(float v) {
  const unsigned u = f2u(v);
  const auto r = __builtin_amdgcn_permlane16_swap(u, u, false, false);
  return u2f(r[0]) + u2f(r[1]);
}
__device__ __forceinline__ float sum_x32(float v) {
  const unsigned u = f2u(v);
  const auto r = __builtin_amdgcn_permlane32_swap(u, u, false, false);
  return u2f(r[0]) + u2f(r[1]);
}
__device__ __forceinline__ float merge32(float a, float b) {
  const auto r = __builtin_amdgcn_permlane32_swap(f2u(a), f2u(b), false, false);
  return u2f(r[0]) + u2f(r[1]);
}
__device__ __forceinline__ float merge16(float a, float b) {
  const auto r = __builtin_amdgcn_permlane16_swap(f2u(a), f2u(b), false, false);
  return u2f(r[0]) + u2f(r[1]);
}
__device__ __forceinline__ float sum_hi3(float v) {
  v += dppf<0x128>(v);
  return sum_x32(sum_x16(v));
}
#define AUXD 0
typedef __attribute__((address_space(3))) void* ldsp;
template <int IMM>
__device__ __forceinline__ void bufl16(__amdgpu_buffer_rsrc_t r, unsigned l, unsigned voff, unsigned soff) {
#if defined(__HIP_DEVICE_COMPILE__)
  __builtin_amdgcn_raw_ptr_buffer_load_lds(r, (ldsp)(uintptr_t)l, 16, voff, soff, IMM, AUXD);
#endif
}
#define LP(T, base, imm) ((__attribute__((address_space(3))) T*)(uintptr_t)(unsigned)((base) + (imm)))
#define MKRSRC(p, n) __builtin_amdgcn_make_buffer_rsrc((void*)(p), 0, (n), 0x00020000)
#define WAIT_VM(n) asm volatile("s_waitcnt vmcnt(%0)" ::"n"(n) : "memory")
#define WAIT_LGKM0() asm volatile("s_waitcnt lgkmcnt(0)" ::: "memory")
#define BAR() do { __builtin_amdgcn_sched_barrier(0); asm volatile("s_barrier" ::: "memory"); __builtin_amdgcn_sched_barrier(0); } while (0)
#define CFENCE() asm volatile("" ::: "memory")
#define MFMA(a, b, c) __builtin_amdgcn_mfma_f32_16x16x32_bf16((a), (b), (c), 0, 0, 0)
#define PIN(v) asm volatile("" : "+v"(v))
#define AMFMA(acc, a, b) asm volatile("v_mfma_f32_16x16x32_bf16 %0, %1, %2, %0" : "+v"(acc) : "v"(a), "v"(b))
#define NOP8(t) asm volatile("s_nop 7" : "+v"(t[0][0]), "+v"(t[0][1]), "+v"(t[1][0]), "+v"(t[1][1]), "+v"(t[2][0]), "+v"(t[2][1]), "+v"(t[3][0]), "+v"(t[3][1]))
#define SB() __builtin_amdgcn_sched_barrier(0)
#define TRD(p) ((s16x4)__builtin_amdgcn_ds_read_tr16_b64_v4i16((lds_s16x4_ptr)(p)))
#define CAT(lo, hi) __builtin_shufflevector(lo, hi, 0, 1, 2, 3, 4, 5, 6, 7)
#define FRAG(x) __builtin_bit_cast(bf16x8, x)

#define STG7(k) do { } while (0)
#define UG_N 8
#define WO_M 9
#define WM_NAP 0
#define G_T 35
#define D_PRIO 0
#define WO_ES 4
#define WO_MS 10
#define HL_NAP 4
#define DN_G W_X2
#define NB14 0
#define WO_0 3
#define PC14 10
#define GU_NAP 2
#define GU_AH 8
#define WR 0
#define WR_B0 6
#define WR_P 5
#define WR_AT 0
#define KWARM 1
#define MORD 2
#define TG 0
#define SH_DR 1
#define SH_T 0
#define SH_N 3
#define SH_NAP 4
#define X_AT 3
#define XM 0
#define DNE 0
#define NORDY 0
#define KO 0
#define Z_D 0
#define DG_P 14
#define DG_NAP 0
#define ENDW 1
#define OPAT 0
#define XL 3
#define XL_AT 1
#define NOSW 0
#define TA 27
#define DN2 0
#define OSF 0
#define SPOLL 0
#define SP_NAP 2
#define OWN 16
#define DW 0
#define GW 0
#define DN3 14
#define DL_NAP 4
#define DL_W 4
#define DG_E 0
#define GE 4
#define GF GE
#define GF_B 5
#define GE_NAP 0
#define WS_C 3
#define WS_A 0
#define TR 7
#define ES 1
#define ES2 0
#define TW 0
#define TW_N 64
#define KH 0
#define KY 0
#define SG_N 0
#define SG_0 5
#define SG_W 0
#define SG_H 0
#define RID 0
#define LPO 0
#define LP_T 5
#define LP_N 3
#define LP_NAP 0
#define FW 0
#define PUB 1
#define NF15 0
#define WO_L 0
#define WL_NAP 8
#define QP 1
#define ZS 1
#define Z_E 0
#define G1_Z 0
#define G1_NAP 0
#define HE 0
#define HE_NAP 8
#define Z2 0
#define ZC 0
#define PRI 0
#define MV_NAP 0
#define SELE 1
#define WROT 0
#define OV 1
#define RP_N 0
#define RP_P 62
#define RP_B 0
#define WU 0
#define WU_G 0
#define WU_R 0
#define SL0 0
#define WO2 1
#define OMK 0
#define NODRAIN TG
#define DN_Z 0
#define PRELD 0
#define WS_B0 13
#define WS_D 0
#define GU_F15 0
#define P3ORD 0
#define KB1 0
#define KB0 0
#define SP0 0
#define SPD SP0
#define SPG 0
#define SP1 0
#define EP 0
#define PRB 0
#define PR_NAP 0
#define G_T0 40
#define DN_IN 99
#define GU_W 32
#define W2 0
#define W2_Q 10
#define W2_AT 1
#define GU1 32
#define DN_S 0
#define DS_NAP 8
#define DMAP 0
#define MK_R 1
#define CP_R 3
#define PACE 57
#define BURST 30
#define KA 4
#define KX 4
#define SLD 2
#define R_AT 2
#define R_GAP 2
#define WPF 4
#define G_NAP 24
#define GL_NAP 2
#define KL_NAP 2
#define K_NAP 0
#define UGATE 0
#define ZGATE 2
#define WO_Q 40
#define WO_E 6
#define WO_NAP 0
#define H_NAP 8
#define DN_NAP 10
#define Y_NAP 30
#define P5_T 30
#define PL_NAP 2
#define OPF 3
#define OAUX 17
#define DN_FIRST 1
#define AG_AT 0
#define DENAT 12

__device__ __forceinline__ unsigned rfl(unsigned v) { return (unsigned)__builtin_amdgcn_readfirstlane((int)v); }
template <int SLEEP>
__device__ __forceinline__ unsigned wait_ge(volatile lu32* p, unsigned need) {
  unsigned v = 0;
#pragma unroll 1
  for (unsigned spins = 0; spins < (1u << 22); ++spins) {
    v = rfl(*p);
    if (v >= need) break;
    if (SLEEP) __builtin_amdgcn_s_sleep(SLEEP);
  }
  CFENCE();
  return v;
}

#define SLOT     40960
#define L_X1     147456
#define L_X2     155648
#define L_CW     159744
#define L_ZF     160256
#define L_GATE   160384
#define L_RED    160896
#define L_H      163200
#define L_CTL    163712
#define L_TOTAL  163840
#define W_X1   64
#define W_X2   68
#define W_AG   72
#define W_KS   76
#define WFLAGS(f) (*(volatile lu32*)(smem + L_CW + (lane & 3) * 128 + (f)))
#define WOPOS(c) ((c) < 3 ? 13 + (c) : (c) < 8 ? (c) : (c) < 11 ? (c) - 8 : (c) - 3)
#define C_F15  0
#define C_WO   2
#define C_ZF   4
#define C_UD   6
#define C_TAG  8
#define C_IDX  10
#define C_GO   12
#define C_F14  14
#define C_OARR 17
#define C_ORDY 18
#define C_GU   19
#define C_DN   20
#define C_T6   21
#define C_Y    23

NOPK __global__ __launch_bounds__(512, 2) void fused_kernel(const u16* __restrict__ x, const u16* __restrict__ wqkv,
                                                            const u16* __restrict__ wo, char* ws, u16* __restrict__ out,
                                                            const u16* __restrict__ w_r, const u16* __restrict__ gate_w,
                                                            const u16* __restrict__ w_gate, const u16* __restrict__ w_up,
                                                            const u16* __restrict__ w_down) {
  extern __shared__ __attribute__((aligned(16))) char smem[];
  T00();
  if (KWARM) asm volatile("s_load_dwordx2 s[100:101], %0, 60" :: "s"(__builtin_amdgcn_kernarg_segment_ptr()) : "s100", "s101");
  SB();
  const int tid = threadIdx.x, lane = tid & 63;
  const int w = __builtin_amdgcn_readfirstlane(tid >> 6);
  u64 pc0;
  asm volatile("s_getpc_b64 %0" : "=s"(pc0));
  const int bid = blockIdx.x, xcd = bid & 7, ix = bid >> 3;
  const int ml = ix >> 2, nl = ix & 3;
  const int mt = (xcd >> 2) * 8 + ml, nt = (xcd & 3) * 4 + nl;
  const int m0 = mt * 128;
  const int mt3 = mt, nt3 = nt ^ 1;
  const int rot = 4 * (xcd & 3);
#define CH(c) (((rot + (c)) & 15) ^ (OWN && (c) < 4 ? nl : 0))
  volatile lu32* misc = (volatile lu32*)(smem + L_CTL);

  if (__builtin_expect((w >> 1) == 2, 1)) {
    u32x8 ka;
    asm volatile("s_load_dwordx8 %0, %1, 0x0" : "=s"(ka) : "s"(__builtin_amdgcn_kernarg_segment_ptr()));
    SB();
    const int p = w - 4;
    unsigned va[4], vb[4], vw[4];
#pragma unroll
    for (int j = 0; j < 4; ++j) {
      const int row = 32 * (w & 1) + 8 * j + (lane >> 3);
      va[j] = (unsigned)((m0 + row) * (DM * 2) + (((lane & 7) ^ ((NOSW & 1) ? 0 : (row >> 1) & 7)) * 16) - 1024 * j);
      vb[j] = (unsigned)(row * (N1 * 2) + 128 * nt + (((lane & 7) ^ ((NOSW & 2) ? 0 : (row & 2) | ((j & 1) << 2))) * 16) - 1024 * j);
      PIN(va[j]); PIN(vb[j]);
    }
    unsigned l0 = (unsigned)(uintptr_t)(ldsp)smem + p * 4096;
    int kf = (KA * ml + KX * (xcd & 3) + KH * (xcd >> 2) + KY * (xcd & 1) + KB1 * (nl >> 1) + KB0 * (nl & 1)) & 15, ks = (KAS * ml - SLD * (ml >> 2) + KX * (xcd & 3) + KH * (xcd >> 2) + KY * (xcd & 1) + KB1 * (nl >> 1) + KB0 * (nl & 1)) & 15, sres = (3 - nt) & 7;
    asm volatile("" : "+s"(l0), "+s"(kf), "+s"(ks), "+s"(sres));
    SB();
    asm volatile("s_waitcnt lgkmcnt(0)" : "+s"(ka));
    const u16* x = (const u16*)(((u64)ka[1] << 32) | ka[0]);
    const u16* wqkv = (const u16*)(((u64)ka[3] << 32) | ka[2]);
    const u16* wo = (const u16*)(((u64)ka[5] << 32) | ka[4]);
    char* ws = (char*)(((u64)ka[7] << 32) | ka[6]);
    const int koff = !(NOX & 16384) && (int)((ka[2] >> 7) & 7u) == sres ? ks : kf;
    const __amdgpu_buffer_rsrc_t ra = MKRSRC(x, SEQ * DM * 2), rb = MKRSRC(wqkv + (((NOX & 16384) && (nt & 7) == 3) ? -64 : 0), DM * N1 * 2), rw = MKRSRC(wo + (((NOX & 8192) && (nt3 & 7) == 3) ? 64 : 0), (NOX & 4) ? 0 : DM * DM * 2);
    int ko_ = koff;
#define UNIT(r, v, off, so) do { bufl16<0>(r, l0 + (off), v[0], so); bufl16<1024>(r, l0 + (off), v[1], so); bufl16<2048>(r, l0 + (off), v[2], so); bufl16<3072>(r, l0 + (off), v[3], so); } while (0)
#define ISSUE(t)                                                                                           \
  do {                                                                                                     \
    const int k_ = (ko_ + (t)) & 15;                                                                       \
    UNIT(ra, va, ((t) % 3) * SLOT, k_ * 128); UNIT(ra, va, ((t) % 3) * SLOT + 8192, k_ * 128 + 64 * DM * 2); \
    _Pragma("unroll") for (int s_ = 0; s_ < 3; ++s_) UNIT(rb, vb, ((t) % 3) * SLOT + 16384 + s_ * 8192, k_ * (64 * N1 * 2) + s_ * (DM * 2)); \
  } while (0)
#define PACED_BAR(t)                                                                                       \
  do {                                                                                                     \
    if (p == 0) {                                                                                          \
      unsigned now;                                                                                        \
      do { now = (unsigned)__builtin_amdgcn_s_memrealtime(); } while ((int)(now - tp) < 0);                \
      tp = ((int)(now - tp) > ((t) < RP_N ? RP_B : BURST) ? now - ((t) < RP_N ? RP_B : BURST) : tp) + ((t) < RP_N ? RP_P : PACE);                                            \
    }                                                                                                      \
    BAR();                                                                                                 \
  } while (0)
    ISSUE(0); ISSUE(1);
    STAMP0();
#define LB(i)
    WAIT_VM(20 + NST0);
    BAR();
    LB(0);
    ISSUE(2);
    unsigned tp = (unsigned)__builtin_amdgcn_s_memrealtime() - PACE;
#pragma unroll
    for (int j = 0; j < 4; ++j) vw[j] = (unsigned)((32 * p + 8 * j + (lane >> 3)) * (DM * 2) + 128 * nt3 + (((lane & 7) ^ ((NOSW & 4) ? 0 : (lane >> 3) & 6)) * 16) - 1024 * j);
#define WO_CHUNK(c) UNIT(rw, vw, 16384 + WOPOS(c) * 8192, (WROT ? (2 * ml + (c)) & 15 : CH(c)) * (64 * DM * 2))
    u32x4 wr_[4 * WR + 1];
#define WR_N(b) ((b) < WR_B0 ? 0 : ((b) - WR_B0 + 1) * WR_P > 4 * WR ? 4 * WR : ((b) - WR_B0 + 1) * WR_P)
#define WS_N(b) (4 * ((WS_C > 0 && (b) >= WS_B0) + (WS_C > 1 && (b) >= WS_B0 + WS_D) + (WS_C > 2 && (b) >= WS_B0 + 2 * WS_D)) + WR_N(b))
#pragma unroll
    for (int t = 0; t < 13; ++t) {
      WAIT_VM(20 + WS_N(t) - WS_N(t - 2) + (t == 0 ? NST0 : 0));
      PACED_BAR(t);
      LB(t + 1);
      asm volatile("" : "+s"(l0), "+s"(ko_));
      ISSUE(t + 3);
#pragma unroll
      for (int c = 0; c < WS_C; ++c) if (t + 1 == WS_B0 + c * WS_D) WO_CHUNK(c);
      SB();
#pragma unroll
      for (int i = WR_N(t); i < WR_N(t + 1); ++i) wr_[i] = __builtin_amdgcn_raw_buffer_load_b128(rw, vw[i & 3] + 1024 * (i & 3), ((rot + 16 - WR + (i >> 2)) & 15) * (64 * DM * 2), 0);
      SB();
    }
    static_assert(WR_N(13) == 4 * WR, "");
    const bool wslow = ((((uintptr_t)wo >> 7) + nt3) & 7) == 3;
    const int woe = wslow ? WO_ES : WO_E, wom = wslow ? WO_MS : WO_M;
    static_assert(WS_B0 + 2 * WS_D <= 13, "");
    LB(2);
    WAIT_VM(NF15 ? WS_N(13) - WS_N(12) : 20 + WS_N(13) - WS_N(11));
    LB(3);
    PACED_BAR(13);
    LB(14);
#pragma unroll
    for (int c = WS_C; c < WS_C + WS_A; ++c) WO_CHUNK(c);
    WAIT_VM(WS_N(13) - WS_N(12) + 4 * WS_A);
    LB(0);
    if (!NF15 && lane == 0) misc[C_F15 + p] = 1u;
    STAMP(0, 1);
#pragma unroll 1
    for (int c = WS_C + WS_A; c < woe; ++c) { WO_CHUNK(c); if (W2) WAIT_VM(W2_Q); }
    u32x4 w2_[4 * W2 + 1];
#pragma unroll
    for (int i = 0; i < 4 * W2; ++i) { w2_[i] = __builtin_amdgcn_raw_buffer_load_b128(rw, vw[i & 3] + 1024 * (i & 3), ((rot + 16 - W2 + (i >> 2)) & 15) * (64 * DM * 2), 0); SB(); WAIT_VM(W2_Q); SB(); }
    {
      const __amdgpu_buffer_rsrc_t zr = MKRSRC(ws + OFF_ZP, NH * 2 * 16 * 128);
      const unsigned r0 = (unsigned)((2 * nt + p) * 32);
      const unsigned zo0 = (r0 + (lane & 31)) * 128 + 64, zo1 = (r0 + (lane >> 2)) * 128 + (lane & 3) * 16;
      u32x4 zw[2];
      unsigned sc;
      LB(7);
      (void)wait_ge<1>(misc + C_IDX + p, 1u);
#pragma unroll 1
      for (unsigned spins = 0; spins < (1u << 22) && !all_true(WFLAGS(W_KS) != 0u); ++spins) __builtin_amdgcn_s_sleep(1);
      CFENCE();
      LB(1);
      const unsigned tk = (unsigned)__builtin_amdgcn_s_memrealtime();
      __builtin_amdgcn_s_sleep(WM_NAP);
      unsigned gt = G_T;
      if (PRB == 1) sc = __builtin_amdgcn_raw_buffer_load_b32(zr, zo0, 0, 16);
      SB();
#define X_TILE() do { UNIT(ra, va, 0, nt3 * 128); UNIT(ra, va, 8192, nt3 * 128 + 64 * DM * 2); } while (0)
      if ((XL & 2) && XL_AT == 0) X_TILE();
#pragma unroll 1
      for (int c = woe; c < wom; ++c) WO_CHUNK(c);
      if ((XL & 2) && XL_AT == 1) X_TILE();
      LB(4);
      if (PRB == 2) { __builtin_amdgcn_s_sleep(PR_NAP); sc = __builtin_amdgcn_raw_buffer_load_b32(zr, zo0, 0, 16); }
      if (PRB && all_true(sc != ~0u || (lane & 15) == mt)) gt = G_T0;
#define WR_PUT() _Pragma("unroll") for (int i = 0; i < 4 * WR; ++i) *LP(u32x4, l0 + lane * 16, 16384 + WOPOS(16 - WR + (i >> 2)) * 8192 + (i & 3) * 1024) = wr_[i]
      if (WR_AT == 0) { WR_PUT(); }
#define W2_PUT() _Pragma("unroll") for (int i = 0; i < 4 * W2; ++i) *LP(u32x4, l0 + lane * 16, 16384 + WOPOS(16 - W2 + (i >> 2)) * 8192 + (i & 3) * 1024) = w2_[i]
      if (W2_AT == 0) { W2_PUT(); }
      while ((unsigned)__builtin_amdgcn_s_memrealtime() - tk < gt) __builtin_amdgcn_s_sleep(1);
      if (UGATE & 2) { (void)wait_ge<1>(misc + C_UD, 1u); (void)wait_ge<1>(misc + C_UD + 1, 1u); }
      LB(5);
      STAMP(0, 2);
      if (Z_D) { misc[C_GO + p] = 1u; __builtin_amdgcn_s_sleep(Z_D); }
#define MX4(v) max(max((v)[0], (v)[1]), max((v)[2], (v)[3]))
#pragma unroll 1
      for (unsigned spins = 0; spins <= SPIN_CAP; ++spins) {
        CFENCE();
        zw[0] = __builtin_amdgcn_raw_buffer_load_b128(zr, r0 * 64 + lane * 16, 0, 16); zw[1] = __builtin_amdgcn_raw_buffer_load_b128(zr, r0 * 64 + lane * 16 + 1024, 0, 16);
        if (ZGATE == 2) { SB(); if (Z_E) __builtin_amdgcn_s_sleep(Z_E); misc[C_GO + p] = 1u; SB(); }
        sc = max(MX4(zw[0]), MX4(zw[1]));
        if (all_true(sc != ~0u)) break;
        if (ZC == 1) { TSOUT(11, rt_()); }
        __builtin_amdgcn_s_sleep(GL_NAP);
      }
      const bool plain = all_true((int)sc >= 0);
      if (__builtin_expect(!plain, 0)) { zw[0] &= 0x7FFFFFFFu; zw[1] &= 0x7FFFFFFFu; }
      LB(6);
      float z0 = merge16(merge32(u2f(zw[0][0]), u2f(zw[0][2])), merge32(u2f(zw[1][0]), u2f(zw[1][2])));
      float z1 = merge16(merge32(u2f(zw[0][1]), u2f(zw[0][3])), merge32(u2f(zw[1][1]), u2f(zw[1][3])));
      z0 += dppf<0x128>(z0); z1 += dppf<0x128>(z1); z0 += dppf<0x124>(z0); z1 += dppf<0x124>(z1);
      *(unsigned*)(smem + L_ZF + p * 64 + (lane & 3) * 16 + ((lane >> 4) & 1) * 8 + (lane >> 5) * 4) = cvt_pk(z0, z1);
      CFENCE();
      misc[C_ZF + p] = plain ? 1u : 2u;
      STAMP(0, 3);
    }
#pragma unroll 1
    for (unsigned spins = 0; spins < (1u << 22) && !all_true(WFLAGS(W_AG) != 0u); ++spins) __builtin_amdgcn_s_sleep(2);
    LB(8);
    __builtin_amdgcn_s_sleep(WO_NAP);
    CFENCE();
#pragma unroll 1
    for (int c = wom; c < 16 - WR - W2 - WO_L; ++c) { WO_CHUNK(c); WAIT_VM(WO_Q); }
    if (WO_L) {
#pragma unroll 1
      for (unsigned spins = 0; spins < (1u << 22) && !all_true(WFLAGS(W_X2) != 0u); ++spins) __builtin_amdgcn_s_sleep(1);
      __builtin_amdgcn_s_sleep(WL_NAP);
#pragma unroll 1
      for (int c = 16 - WO_L; c < 16; ++c) WO_CHUNK(c);
    }
    if (!EP || p == 0) {
      WAIT_VM(0);
      if (WR_AT == 1) { WR_PUT(); }
      if (W2_AT == 1) { W2_PUT(); }
      CFENCE();
      if (lane == 0) misc[C_WO + p] = 1u;
    }
    LB(9);
    STAMP(0, 5);
#undef ISSUE
#undef PACED_BAR
    if ((TW & 1) && p == 0) { const unsigned t_ = __builtin_amdgcn_raw_buffer_load_b32(MKRSRC(out, SEQ * DM * 2), lane < TW_N ? (unsigned)(((m0 + lane * (128 / TW_N)) * DM + 64 * nt3) * 2) : 0x80000000u, 0, 0); asm volatile("" :: "v"(t_)); }
    if (p == 0 || NORDY) return;
    const unsigned tag = rfl(misc[C_TAG]);
    gu32* of = (gu32*)(ws + OFF_OFLAG + mt3 * 2048 + CH(lane & 15) * ((TA & 4) ? 4 : 128));
    if ((TA & 32) && lane >= 16) return;
#pragma unroll 1
    for (unsigned spins = 0; spins < (1u << 22) && !all_true(WFLAGS(W_KS) == 2u); ++spins) __builtin_amdgcn_s_sleep(1);
    const unsigned t6 = (unsigned)__builtin_amdgcn_s_memrealtime();
    while ((unsigned)__builtin_amdgcn_s_memrealtime() - t6 < (unsigned)P5_T) __builtin_amdgcn_s_sleep(1);
#pragma unroll 1
    for (unsigned spins = 0;; ++spins) {
      const unsigned f = __hip_atomic_load(of, RLX_AGENT);
      const unsigned miss = (unsigned)__builtin_amdgcn_ballot_w64(f != tag) & (OWN ? 0xFFFEu : 0xFFFFu);
      const unsigned m = (TR & 4) ? (unsigned)__builtin_ctz((spins > SPIN_CAP ? 0u : miss) | 0x10000u) : (miss == 0u || spins > SPIN_CAP) ? 16u : (unsigned)__builtin_ctz(miss);
      if (EP) WAIT_VM(0);
      if ((TR & 4) || lane == 0) { misc[C_ORDY] = m; if (EP) misc[C_WO + 1] = 1u; }
      if (m == 16u) break;
      __builtin_amdgcn_s_sleep(PL_NAP);
    }
    STAMP(0, 6);
    return;
  }

  if (__builtin_expect(w >= 6, 0)) {
    const int pa = w - 6;
    STAMP0();
#define CODE_PF(on) __builtin_amdgcn_raw_buffer_load_b32(MKRSRC((pc0 & ~255ull) - 256 * PRELD + (blockIdx.x >> 3) * (CPF_N * 64u), (on) ? CPF_N * 64 : 0), lane * 64, 0, 0)
    unsigned cpf = 0, cpf2 = 0;
    if (CPF & 1) cpf = CODE_PF(w == 7);
    SB();
    float* red = (float*)(smem + L_RED);
    const int snt = (int)(3u - (unsigned)((uintptr_t)wqkv >> 7)) & 7;
    const int rid = !RID ? bid : (nt & 7) == snt ? 224 + mt * 2 + (nt >> 3) : mt * 14 + (nt >> 3) * 7 + (nt & 7) - ((nt & 7) > snt ? 1 : 0);
    const bool help = rid >= 224, gemv = rid < 252;
    const int gmat = rid & 1;
    const u16* wgu = gmat ? w_up : w_gate;
    const int sres = (int)(3u - (unsigned)((uintptr_t)wgu >> 7)) & 7;
    const int gfs = help ? sres + 8 * (((rid - 224) >> 1) % 7) : (rid >> 1) % 56;
    const int gdh = (help ? ((rid - 224) >> 1) / 7 : (rid >> 1) / 56) & 1;
    const bool act0 = !help, act1 = GW ? !help : gemv && (help || (gfs & 7) != sres);
    const int gd0 = gdh * 512 + 256 * pa + 32 * (lane >> 3);
    const __amdgpu_buffer_rsrc_t rgu0 = MKRSRC(wgu, (act0 && !(NOX & 1)) ? DM * FF * 2 : 0), rgu1 = MKRSRC(wgu, (act1 && !(NOX & 9)) ? DM * FF * 2 : 0);
    const unsigned wpo = GW ? (unsigned)((((((bid >> 1) / (56 >> GW)) * 2 + pa) * (256 >> GW) + 32 * (lane >> (3 + GW))) * FF + ((bid >> 1) % (56 >> GW)) * (64 << GW) + 8 * (lane & ((8 << GW) - 1))) * 2) : (unsigned)((gd0 * FF + gfs * 64 + 8 * (lane & 7)) * 2);
    u32x4 wv[32], wd[14];
    const int fg = DMAP == 0 ? bid >> 4 : DMAP == 1 ? nt : bid & 15, cg = DMAP == 0 ? bid & 15 : DMAP == 1 ? mt : ((bid >> 4) + bid) & 15;
    const int f0 = fg * 224 + pa * 112;
    const bool dslow = (((unsigned)((uintptr_t)w_down >> 7) + cg) & 7u) == 3u;
    const __amdgpu_buffer_rsrc_t rdn = MKRSRC(w_down, ((NOX & 2) || ((NOX & 512) && dslow) || ((NOX & 1024) && !dslow) || ((NOX & 2048) && pa)) ? 0 : DM * FF * 2);
    const unsigned wqo = DW ? (unsigned)(((((bid >> (4 - DW)) * 2 + pa) * (112 >> DW) + 14 * (lane >> (3 + DW))) * DM + (bid & ((16 >> DW) - 1)) * (64 << DW) + 8 * (lane & ((8 << DW) - 1))) * 2) : (unsigned)(((f0 + 14 * (lane >> 3)) * DM + cg * 64 + 8 * (lane & 7)) * 2);
    asm volatile("" ::"s"(w_r), "s"(gate_w), "s"(ws), "s"(w_gate), "s"(w_up), "s"(w_down));
    unsigned pf0, pf1;
#define WO_PREFETCH()                                                                                      \
  do {                                                                                                     \
    const u16* pp_ = wo + (size_t)(128 * ml + 64 * pa + (lane >> 1)) * DM + 64 * nt3 + (lane & 1) * 32;    \
    pf0 = pf1 = 0; if (TA & 1) pf0 = *(const unsigned*)(wo + (size_t)(128 * ml + 64 * pa + lane) * DM + 64 * nt3); else if (!(NOX & 256)) { pf0 = *(const unsigned*)pp_; pf1 = *(const unsigned*)(pp_ + 32 * DM); }                             \
  } while (0)
    const int sgk = SG_N ? (SG_H == 0 ? ix % (SG_N ? SG_N : 1) : SG_H == 1 ? ml % (SG_N ? SG_N : 1) : (ix + xcd) % (SG_N ? SG_N : 1)) : 0;
    const int sgb = SG_0 + sgk, sgw = SG_0 + (sgk + SG_W) % (SG_N ? SG_N : 1);
    const u16* pfp = wo + (size_t)(128 * ml + 64 * pa + lane) * DM + 64 * nt3;
    if (SG_N) { pf0 = pf1 = 0; _Pragma("unroll") for (int k = 0; k < DN_A; ++k) asm volatile("" : "=v"(wd[k])); }
#define SGH(b) do { if (SG_N && (b) >= SG_0 && (b) < SG_0 + SG_N) { SB(); \
      if (WPF == 99 && (b) == sgw) asm volatile("global_load_dword %0, %1, off" : "+v"(pf0) : "v"(pfp) : "memory"); \
      if ((b) == sgb) { _Pragma("unroll") for (int k = 0; k < DN_A; ++k) asm volatile("buffer_load_dwordx4 %0, %1, %2, %3 offen nt" : "+v"(wd[k]) : "v"(wqo), "s"(rdn), "s"(k * (DM * 2)) : "memory"); } SB(); } } while (0)
#define DNH(b) do { SGH(b); if ((b) == GF_B) { SB(); _Pragma("unroll") for (int k = GE; k < GF; ++k) wv[k] = __builtin_amdgcn_raw_buffer_load_b128(rgu0, wpo, k * (FF * 2), 2); SB(); } if ((b) == GE_B) { SB(); __builtin_amdgcn_s_sleep(GE_NAP); _Pragma("unroll") for (int k = 0; k < GE; ++k) wv[k] = __builtin_amdgcn_raw_buffer_load_b128(rgu0, wpo, k * (FF * 2), 2); SB(); } if (!SG_N && (b) >= DG_B && ((b) - DG_B) * DG_P < DN_A) { SB(); __builtin_amdgcn_s_sleep(DG_NAP); _Pragma("unroll") for (int k = ((b) - DG_B) * DG_P; k < ((b) - DG_B + 1) * DG_P; ++k) if (k < DN_A) wd[k] = __builtin_amdgcn_raw_buffer_load_b128(rdn, wqo, k * (DM * 2), 2); SB(); } } while (0)
    if (WPF == -1) WO_PREFETCH();
    DNH(-1);
    if (WPF == -2) WO_PREFETCH();
    {
#pragma unroll
      for (int t = 0; t <= R_AT; ++t) { BAR(); STG7(t); if (t < R_AT) DNH(t); }
      __builtin_amdgcn_s_sleep(R_NAP);
      const int r0 = m0 + nt * 8 + 4 * pa;
      u32x4 xr[4][2], rv[2], gv[2];
#pragma unroll
      for (int i = 0; i < 2; ++i) {
        rv[i] = *(const u32x4*)(w_r + i * 512 + lane * 8);
        gv[i] = *(const u32x4*)(gate_w + i * 512 + lane * 8);
      }
#pragma unroll
      for (int r = 0; r < 4; ++r)
#pragma unroll
        for (int i = 0; i < 2; ++i) xr[r][i] = (NOX & 32) ? rv[i] : *(const u32x4*)(x + (size_t)(r0 + r) * DM + i * 512 + lane * 8);
      const unsigned ep = __hip_atomic_load((gu32*)(ws + OFF_EPOCH), RLX_AGENT);
      if (WPF == 0) WO_PREFETCH();
      unsigned tw0 = 0, tw1 = 0;
      if (TW & 2) tw0 = __builtin_amdgcn_raw_buffer_load_b32(MKRSRC(ws + OFF_OF, SEQ * DM * 2), lane < 8 ? (unsigned)(((mt * 8 + lane) * NH + 2 * nt + pa) * 1024) : 0x80000000u, 0, 0);
      if (TW & 4) tw1 = __builtin_amdgcn_raw_buffer_load_b32(MKRSRC(out, SEQ * DM * 2), lane < TW_N / 2 ? (unsigned)(((m0 + 64 * pa + lane * (128 / TW_N)) * DM + 64 * nt3) * 2) : 0x80000000u, 0, 0);
      {
        const u32x4 nw = {~0u, ~0u, ~0u, ~0u};
        const __amdgpu_buffer_rsrc_t kvr = MKRSRC(ws + OFF_KVP, NH * 2 * 16 * 1024), zr = MKRSRC(ws + OFF_ZP, NH * 2 * 16 * 128);
        const unsigned rec = (unsigned)((2 * nt + pa) * 32 + mt);
        if (!(NOX & 64)) {
        __builtin_amdgcn_raw_buffer_store_b128(nw, kvr, rec * 1024 + lane * 16, 0, 16);
        __builtin_amdgcn_raw_buffer_store_b128(nw, kvr, rec * 1024 + lane * 16 + 16384, 0, 16);
        }
#define O_MARK(i) __builtin_amdgcn_raw_buffer_store_b128(nw, MKRSRC(ws + OFF_OF, SEQ * DM * 2), (unsigned)(((mt * 8 + (i)) * NH + 2 * nt + pa) * 1024 + lane * 16), 0, 16)
        if (OMK == 1) { _Pragma("unroll") for (int i = 0; i < 8; ++i) O_MARK(i); }
        if (lane < 10) __builtin_amdgcn_raw_buffer_store_b128(nw, zr, lane < 8 ? (rec + (lane >> 2) * 16) * 64 + (lane & 3) * 16 : 65536 + (rec + (lane & 1) * 16) * 16, 0, 16);
      }
      DNH(R_AT);
#pragma unroll
      for (int t = R_AT + 1; t < R_AT + R_GAP; ++t) { BAR(); STG7(t); DNH(t); }
      float racc[4], gacc[4];
#pragma unroll
      for (int r = 0; r < 4; ++r) {
        BAR(); STG7(R_AT + R_GAP + r);
        if (r == MK_R) { WAIT_VM(0); if (SG_N) __builtin_amdgcn_s_waitcnt(0x0F70); SB(); }
        if (!DG_E) DNH(R_AT + R_GAP + r);
        racc[r] = 0.f; gacc[r] = 0.f;
#pragma unroll
        for (int i = 0; i < ((NOX & 128) ? 1 : 2); ++i)
#pragma unroll
          for (int e = 0; e < ((NOX & 128) ? 1 : 4); ++e) {
            const float x0 = blo(xr[r][i][e]), x1 = bhi(xr[r][i][e]);
            racc[r] += x0 * blo(rv[i][e]); racc[r] += x1 * bhi(rv[i][e]);
            const unsigned pr = cvt_pk(x0 * blo(gv[i][e]), x1 * bhi(gv[i][e]));
            gacc[r] += blo(pr); gacc[r] += bhi(pr);
          }
        PIN(racc[r]); PIN(gacc[r]);
        if (OMK == 2) { const u32x4 nw = {~0u, ~0u, ~0u, ~0u}; O_MARK(2 * r); O_MARK(2 * r + 1); }
        if (WPF == r + 1) { SB(); WO_PREFETCH(); }
        if (r == CP_R && (CPF & 2)) cpf2 = CODE_PF(w == 7);
        if (DG_E) DNH(R_AT + R_GAP + r);
      }
      BAR(); STG7(R_AT + R_GAP + 4);
      DNH(R_AT + R_GAP + 4);
      float rs = merge16(merge32(racc[0], racc[1]), merge32(racc[2], racc[3]));
      float gs = merge16(merge32(gacc[0], gacc[1]), merge32(gacc[2], gacc[3]));
      rs += dppf<0x128>(rs); gs += dppf<0x128>(gs); rs += dppf<0x124>(rs); gs += dppf<0x124>(gs);
      rs += dppf<0x122>(rs); gs += dppf<0x122>(gs); rs += dppf<0x121>(rs); gs += dppf<0x121>(gs);
      const int rr = r0 + ((lane >> 5) | ((lane >> 3) & 2));
      const unsigned rb = cvt_pk(rs, rs) & 0xFFFFu;
      unsigned key = (((rb & 0x8000u) ? (~rb & 0xFFFFu) : (rb | 0x8000u)) << 16) | (0xFFFFu - (unsigned)rr);
      { const auto t_ = __builtin_amdgcn_permlane16_swap(key, key, false, false); key = max(t_[0], t_[1]); }
      { const auto t_ = __builtin_amdgcn_permlane32_swap(key, key, false, false); key = max(t_[0], t_[1]); }
      const unsigned tg = rfl(ep) + 1u;
      asm volatile("" :: "v"(tw0), "v"(tw1));
      if (lane == 0) misc[C_TAG + pa] = pa ? key : tg;
      if (MK_R == 9) WAIT_VM(0);
      WAIT_LGKM0();
      BAR(); STG7(R_AT + R_GAP + 5);
      if (pa == 0 && lane == 0) __hip_atomic_store((gu64*)(ws + OFF_RG) + bid, ((u64)tg << 32) | max(key, (unsigned)misc[C_TAG + 1]), RLX_AGENT);
      if (FW == 1 && pa == 1 && lane == 0) __hip_atomic_store((gu32*)(ws + OFF_OFLAG + (mt * 16 + nt) * 128), ~tg, RLX_AGENT);
      if (FW == 2 && pa == 1 && lane < 32) __hip_atomic_store((gu32*)(ws + OFF_OFLAG + (mt * 16 + nt) * 128) + lane, ~tg, RLX_AGENT);
      if ((lane & 15) == 0) __hip_atomic_store((gu64*)(ws + OFF_GG) + rr, ((u64)tg << 32) | f2u(bfr(sigmoid_f(bfr(gs)))), RLX_AGENT);
      if (WPF == 19) { SB(); WO_PREFETCH(); if (CP_R == 9) cpf2 = CODE_PF(w == 7); }
      DNH(R_AT + R_GAP + 5);
#pragma unroll
      for (int t = R_AT + R_GAP + 6; t < 13 - NB14 - RG_E; ++t) { BAR(); STG7(t); if (WPF == t + 10) WO_PREFETCH(); DNH(t); }
    }
    if (CPF & 4) cpf2 = CODE_PF(w == 7);
    unsigned sel[2][4];
#pragma unroll
    for (int i = 0; i < 8; ++i) {
      const unsigned pick = (i >> 2) ? 0x0302u : 0x0100u;
      sel[i >> 2][i & 3] = ((lane & 7) >> 1) == (i & 3) ? ((lane & 1) ? (pick << 16) | 0x0C0Cu : 0x0C0C0000u | pick) : 0x0C0C0C0Cu;
      PIN(sel[i >> 2][i & 3]);
    }
    u64 rg[4];
#pragma unroll
    for (int i = 0; i < 2; ++i) { const u32x4 t_ = __builtin_amdgcn_raw_buffer_load_b128(MKRSRC(ws + OFF_RG, 2048), lane * 16 + i * 1024, 0, 16); rg[2 * i] = ((u64)t_[1] << 32) | t_[0]; rg[2 * i + 1] = ((u64)t_[3] << 32) | t_[2]; }
    const gu64* ggp = (const gu64*)(ws + OFF_GG) + m0 + 64 * pa + lane;
    u64 gg = __hip_atomic_load(ggp, RLX_AGENT);
#pragma unroll
    for (int t = 13 - NB14 - RG_E; t <= 13 - NB14; ++t) { BAR(); STG7(t); }
    const unsigned tag = rfl(misc[C_TAG]);
    unsigned idx;
    unsigned bestk;
    bool late;
    {
      unsigned best = 0;
      bool ok = (unsigned)(gg >> 32) == tag;
#pragma unroll
      for (int i = 0; i < 4; ++i) { ok = ok && (unsigned)(rg[i] >> 32) == tag; best = max(best, (unsigned)rg[i]); }
      late = !all_true(ok);
      if (__builtin_expect(late, 0)) {
        BAR();
#pragma unroll 1
        for (unsigned spins = 0;; ++spins) {
          u64 rh[4];
#pragma unroll
          for (int i = 0; i < 4; ++i) rh[i] = __hip_atomic_load((gu64*)(ws + OFF_RG) + i * 64 + lane, RLX_AGENT);
          gg = __hip_atomic_load(ggp, RLX_AGENT);
          ok = (unsigned)(gg >> 32) == tag; best = 0;
#pragma unroll
          for (int i = 0; i < 4; ++i) { ok = ok && (unsigned)(rh[i] >> 32) == tag; best = max(best, (unsigned)rh[i]); }
          if (all_true(ok) || spins > SPIN_CAP) break;
          __builtin_amdgcn_s_sleep(8);
        }
      }
      best = wave_max(best);
      idx = (0xFFFFu - (best & 0xFFFFu)) & (SEQ - 1);
      bestk = best;
    }
    if (SG_N) WAIT_VM(0);
    asm volatile("" ::"v"(pf0), "v"(pf1), "v"(cpf), "v"(cpf2));
    u32x4 xs[4];
#pragma unroll
    for (int i = 0; i < 4; ++i) xs[i] = *(const u32x4*)(x + (size_t)idx * DM + gd0 + 8 * i);
    STAMP(0, 9);
    if (!late) BAR();
    STG7(14 - NB14);
    if (GU_F15) { (void)wait_ge<1>(misc + C_F15, 1u); (void)wait_ge<1>(misc + C_F15 + 1, 1u); }
    __builtin_amdgcn_s_sleep(GU_NAP);
#pragma unroll
    for (int i = GF; i < (GU_W < GU1 ? GU_W : GU1); ++i) wv[i] = __builtin_amdgcn_raw_buffer_load_b128(i < 16 ? rgu0 : rgu1, wpo, i * (FF * 2), 2);
    SB();
#pragma unroll
    for (int k = 0; k < DNE; ++k) wd[k] = __builtin_amdgcn_raw_buffer_load_b128(rdn, wqo, k * (DM * 2), 2);
    SB();
    ((unsigned*)(smem + L_GATE))[64 * pa + lane] = (unsigned)gg;
    CFENCE();
    if (lane == 0) misc[C_IDX + pa] = idx + 1u;
    if (UGATE) { WAIT_VM(UG_N); if (lane == 0) misc[C_UD + pa] = 1u; }
    if (bid == 0 && pa == 0 && lane == 0) __hip_atomic_store((gu32*)(ws + OFF_EPOCH), tag, RLX_AGENT);
    if (MV_NAP) __builtin_amdgcn_s_sleep(MV_NAP);
#define AFR(pr, i) FRAG(((u32x4){__builtin_amdgcn_perm(0u, pr, sel[(i) & 1][0]), __builtin_amdgcn_perm(0u, pr, sel[(i) & 1][1]), \
                                 __builtin_amdgcn_perm(0u, pr, sel[(i) & 1][2]), __builtin_amdgcn_perm(0u, pr, sel[(i) & 1][3])}))
#define SPL(n) "s_load_dwordx2 s[100:101], %0, %" #n "\n\t"
#define SPF8(base, step) asm volatile(SPL(1) SPL(2) SPL(3) SPL(4) SPL(5) SPL(6) SPL(7) SPL(8) :: "s"(base), \
    "n"(60), "n"(60 + (step)), "n"(60 + 2 * (step)), "n"(60 + 3 * (step)), "n"(60 + 4 * (step)), "n"(60 + 5 * (step)), "n"(60 + 6 * (step)), "n"(60 + 7 * (step)) : "s100", "s101")
    const char* s_db = (const char*)w_down + ((size_t)f0 * DM + cg * 64) * 2;
#define SPDN(u) do { const char* b_ = s_db + (size_t)(u) * (DM * 2); SPF8(b_, 14 * DM * 2); } while (0)
    f32x4* rdw = (f32x4*)(red + pa * 128 + (lane >> 5) * 64 + (lane & 7) * 8 + ((lane >> 4) & 1) * 4);
    const bool rvalid = (lane >> 5) == ((lane >> 3) & 1);
    const auto rv = LP(volatile float, L_RED, lane * 4);
    {
      bf16x8 af[32];
      f32x4 a = {0.f, 0.f, 0.f, 0.f};
#pragma unroll
      for (int i = 0; i < GU_AH; ++i) { af[i] = AFR(xs[i >> 3][(i >> 1) & 3], i); PIN(af[i]); }
      SB();
#pragma unroll
      for (int u = 0; u < SP0; ++u) SPDN(u);
#pragma unroll
      for (int i = 0; i < 32; ++i) {
        if (SPG && i % SPG == SPG - 1 && SP0 + i / SPG < SPD) SPDN(SP0 + i / (SPG ? SPG : 1));
        if (i == GU1) {
#pragma unroll 1
          for (unsigned spins = 0; spins < (1u << 22) && !all_true(G1_Z ? misc[C_GO + (lane & 1)] != 0u : WFLAGS(W_AG) != 0u); ++spins) __builtin_amdgcn_s_sleep(1);
          __builtin_amdgcn_s_sleep(G1_NAP);
#pragma unroll
          for (int k = GU1; k < 32; ++k) wv[k] = __builtin_amdgcn_raw_buffer_load_b128(k < 16 ? rgu0 : rgu1, wpo, k * (FF * 2), 2);
          SB();
        }
        a = MFMA(af[i], FRAG(wv[i]), a);
        PIN(a);
        if (i + GU_W < 32) { SB(); wv[(i + GU_W) & 31] = __builtin_amdgcn_raw_buffer_load_b128(i + GU_W < 16 ? rgu0 : rgu1, wpo, (i + GU_W) * (FF * 2), 2); SB(); }
        if (i + 1 == DN_IN) { SB(); _Pragma("unroll") for (int k = 0; k < DN_A; ++k) wd[k] = __builtin_amdgcn_raw_buffer_load_b128(rdn, wqo, k * (DM * 2), 2); }
        if (i + GU_AH < 32) { af[(i + GU_AH) & 31] = AFR(xs[((i + GU_AH) >> 3) & 3][((i + GU_AH) >> 1) & 3], i + GU_AH); PIN(af[(i + GU_AH) & 31]); }
        SB();
      }
      if (rvalid) *rdw = a;
      CFENCE();
      if (pa == 1) misc[C_GU] = 1u;
      else {
        float v;
#pragma unroll 1
        for (unsigned spins = 0; spins < (1u << 22); ++spins) {
          const unsigned f_ = misc[C_GU];
          CFENCE();
          v = (rv[0] + rv[64]) + (rv[128] + rv[192]);
          if (rfl(f_) != 0u) break;
          __builtin_amdgcn_s_sleep(1);
        }
        if (gemv) __hip_atomic_store((gu64*)(ws + OFF_GU) + ((help ? 4 : 0) + gdh * 2 + gmat) * FF + gfs * 64 + lane, ((u64)tag << 32) | f2u(v), RLX_AGENT);
      }
    }
    STAMP(0, 10);
    __builtin_amdgcn_s_setprio(D_PRIO);
    {
#pragma unroll
      for (int u = SPD; u < SPD + SP1; ++u) SPDN(u);
      if (DN2) {
#pragma unroll 1
        for (unsigned spins = 0; spins < (1u << 22) && !all_true(WFLAGS(W_AG) != 0u); ++spins) __builtin_amdgcn_s_sleep(1);
#pragma unroll
        for (int i = 0; i < DN_A; ++i) wd[i] = __builtin_amdgcn_raw_buffer_load_b128(rdn, wqo, i * (DM * 2), 2);
        SB();
      }
      if (OSF) {
#pragma unroll 1
        for (unsigned spins = 0; spins < (1u << 22) && !all_true(WFLAGS(W_AG) == 2u); ++spins) __builtin_amdgcn_s_sleep(1);
      }
#pragma unroll 1
      for (unsigned spins = 0; spins < (1u << 22) && !all_true(DN_Z == 2 ? misc[C_GO + (lane & 1)] != 0u : WFLAGS(DN_G) != 0u && (!DN_Z || misc[C_ZF + (lane & 1)] != 0u)); ++spins) __builtin_amdgcn_s_sleep(1);
      __builtin_amdgcn_s_sleep(DN_NAP);
      if (DN_S && ((((unsigned)((uintptr_t)w_down >> 7) + cg) & 7u) == 3u) == (DN_S == 1)) {
#pragma unroll 1
        for (unsigned spins = 0; spins < (1u << 22) && !all_true(WFLAGS(W_X2) != 0u); ++spins) __builtin_amdgcn_s_sleep(1);
        __builtin_amdgcn_s_sleep(DS_NAP);
      }
#pragma unroll
      for (int i = DN_A; i < DN3; ++i) wd[i] = __builtin_amdgcn_raw_buffer_load_b128(rdn, wqo, i * (DM * 2), 2);
      SB();
      __builtin_amdgcn_s_sleep(H_NAP);
      float hv[2];
      const int fl = f0 + 2 * (lane < 56 ? lane : 55);
      const __amdgpu_buffer_rsrc_t gr = MKRSRC(ws + OFF_GU, 8 * FF * 8);
      const bool ex[2] = {(((unsigned)((uintptr_t)w_gate >> 7) + (fl >> 6)) & 7u) == 3u, (((unsigned)((uintptr_t)w_up >> 7) + (fl >> 6)) & 7u) == 3u};
#pragma unroll 1
      for (unsigned spins = 0;; ++spins) {
        u32x4 g[8];
        CFENCE();
#pragma unroll
        for (int i = 0; i < 8; ++i) {
          g[i] = (u32x4){0u, tag, 0u, tag};
          if (i < 4 || ex[i & 1]) g[i] = __builtin_amdgcn_raw_buffer_load_b128(gr, fl * 8, i * (FF * 8), 16);
        }
        bool ok = true;
#pragma unroll
        for (int j = 0; j < 2; ++j) {
#pragma unroll
          for (int i = 0; i < 8; ++i) ok = ok && g[i][2 * j + 1] == tag;
          const float gt = bfr((u2f(g[0][2 * j]) + u2f(g[4][2 * j])) + (u2f(g[2][2 * j]) + u2f(g[6][2 * j])));
          const float up = bfr((u2f(g[1][2 * j]) + u2f(g[5][2 * j])) + (u2f(g[3][2 * j]) + u2f(g[7][2 * j])));
          hv[j] = bfr(gt * __builtin_amdgcn_rcpf(1.0f + __expf(-gt))) * up;
        }
        if (all_true(ok) || spins > SPIN_CAP) break;
        __builtin_amdgcn_s_sleep(HL_NAP);
      }
      STAMP(0, 11);
      u32x4 hp[2];
      {
        char* hb = smem + L_H + pa * 256;
        const unsigned pr = cvt_pk(hv[0], hv[1]);
        if (TA & 8) { if (lane < 56) *(unsigned*)(hb + (lane / 7) * 32 + (lane % 7) * 4) = pr; }
        else if (lane < 56) { *(u16*)(hb + (lane / 14) * 32 + (lane % 14) * 2) = (u16)pr; *(u16*)(hb + 128 + (lane / 14) * 32 + (lane % 14) * 2) = (u16)(pr >> 16); }
        CFENCE();
        hp[0] = *(volatile lu32x4*)(hb + (lane >> 3) * 32); hp[1] = *(volatile lu32x4*)(hb + (lane >> 3) * 32 + 16);
      }
      if (DN3 < 14) {
        if (DL_W) (void)wait_ge<1>(misc + C_ORDY, DL_W);
        __builtin_amdgcn_s_sleep(DL_NAP);
#pragma unroll
        for (int i = DN3; i < 14; ++i) wd[i] = __builtin_amdgcn_raw_buffer_load_b128(rdn, wqo, i * (DM * 2), 2);
        SB();
      }
      bf16x8 af[14];
#pragma unroll
      for (int i = 0; i < 14; ++i) { af[i] = AFR(hp[i >> 3][(i >> 1) & 3], i); PIN(af[i]); }
      SB();
      f32x4 a = {0.f, 0.f, 0.f, 0.f};
#pragma unroll
      for (int i = 0; i < 14; ++i) a = MFMA(af[i], FRAG(wd[i]), a);
      if (rvalid) rdw[64] = a;
      CFENCE();
      if (pa == 1) misc[C_DN] = 1u;
      else {
        float y;
#pragma unroll 1
        for (unsigned spins = 0; spins < (1u << 22); ++spins) {
          const unsigned f_ = misc[C_DN];
          CFENCE();
          y = (rv[256] + rv[320]) + (rv[384] + rv[448]);
          if (rfl(f_) != 0u) break;
          __builtin_amdgcn_s_sleep(1);
        }
        __hip_atomic_store((gu64*)(ws + OFF_YG) + fg * 1024 + cg * 64 + lane, ((u64)tag << 32) | f2u(y), RLX_AGENT);
      }
    }
    STAMP(0, 3);
    if (pa == 1 && (int)(idx >> 7) == mt3) {
      const float xe = u2f((unsigned)x[(size_t)idx * DM + 64 * nt3 + lane] << 16);
      const unsigned ob = bestk >> 16;
      const float rsel = bfr(sigmoid_f(u2f(((ob & 0x8000u) ? (ob & 0x7FFFu) : (~ob & 0xFFFFu)) << 16)));
      float y = 0.f, gt = 0.f;
      __builtin_amdgcn_s_sleep(Y_NAP);
#pragma unroll 1
      for (unsigned spins = 0;; ++spins) {
        u64 g[17];
#pragma unroll
        for (int i = 0; i < 16; ++i) g[i] = __hip_atomic_load((gu64*)(ws + OFF_YG) + i * 1024 + 64 * nt3 + lane, RLX_AGENT);
        g[16] = __hip_atomic_load((gu64*)(ws + OFF_GG) + idx, RLX_AGENT);
        bool ok = (unsigned)(g[16] >> 32) == tag;
        gt = u2f((unsigned)g[16]); y = 0.f;
#pragma unroll
        for (int i = 0; i < 16; ++i) { ok = ok && (unsigned)(g[i] >> 32) == tag; y += u2f((unsigned)g[i]); }
        if (all_true(ok) || spins > SPIN_CAP) break;
        __builtin_amdgcn_s_sleep(8);
      }
      red[512 + lane] = bfr(bfr(1.0f - gt) * bfr(xe + bfr(bfr(y) * rsel))) - bfr(bfr(1.0f - gt) * xe);
      CFENCE();
      if (lane == 0) misc[C_Y] = 1u;
    }
    return;
  }

  STAMP0();
  if (w == 0) misc[lane & 31] = 0u;
  if (w == 1) *(u32x2*)(smem + L_CW + lane * 8) = (u32x2){0u, 0u};
  const int hh = w & 1, wr = w >> 1;
  const int c16 = lane & 15, g = lane >> 4;
  const int fsw = (c16 >> 3) | ((g & 1) << 1);
  const int head = 2 * nt + hh;
  int aoff[2], boff[2];
#pragma unroll
  for (int i = 0; i < 2; ++i) {
    aoff[i] = (wr * 64 + c16) * 128 + (((4 * i + g) ^ (c16 >> 1)) * 16);
    boff[i] = 16384 + (8 * g + (c16 >> 2)) * 128 + (((2 * hh + (i ^ wr)) ^ fsw) * 32) + (c16 & 3) * 8;
  }
  f32x4 acc[3][4][2];
#pragma unroll
  for (int i = 0; i < 24; ++i) { const float s0 = i < 16 ? 1.f : 0.f; acc[i / 8][(i >> 1) & 3][i & 1] = (f32x4){s0, s0, s0, s0}; PIN(acc[i / 8][(i >> 1) & 3][i & 1]); }
  bf16x8 a[2][4], b[2][6];
  s16x4 tlo;
#define A_AT(sl, h, mi) (*(const bf16x8*)(smem + (sl) * SLOT + aoff[h] + (mi) * 2048))
#define B_AT(sl, h, s, n) (smem + (sl) * SLOT + boff[n] + (s) * 8192 + (h) * 4096)
#define MM(s, mi, n, A, B) do { if ((s) == 0) AMFMA(acc[0][mi][n], B, A); else AMFMA(acc[s][mi][n], A, B); } while (0)
#define RD(sl, h, u)                                                                                       \
  do {                                                                                                     \
    if ((u) == 0) a[h][0] = A_AT(sl, h, 0);                                                                \
    else if ((u) >= 13) a[h][(u) - 12] = A_AT(sl, h, (u) - 12);                                            \
    else {                                                                                                 \
      const int j_ = (((u) - 1) / 2 + 2) % 6;                                                              \
      if ((u) & 1) tlo = TRD(B_AT(sl, h, j_ >> 1, j_ & 1));                                                \
      else b[h][j_] = CAT(tlo, TRD(B_AT(sl, h, j_ >> 1, j_ & 1) + 512));                                   \
    }                                                                                                      \
  } while (0)
#define O_M(n) ((((n) / 4) & 1) ? 3 - (n) % 4 : (n) % 4)
#define O_J(n) ((n) / 4)
#define STEP(h, rs, LATE)                                                                                  \
  _Pragma("unroll") for (int n_ = 0; n_ < 24; ++n_) {                                                      \
    const int jj_ = (O_J(n_) + 2) % 6, mi_ = O_M(n_);                                                      \
    MM(jj_ >> 1, mi_, jj_ & 1, a[h][mi_], b[h][jj_]); SB();                                                \
    if (n_ < 16) { RD(rs, 1 - (h), n_); } else { LATE; }                                                   \
    SB();                                                                                                  \
  }
#define LGKM0() __builtin_amdgcn_s_waitcnt(0xC07F)
#define STAGE(sl, nx, LATE, END) do { STEP(0, sl, (void)0); LGKM0(); BAR(); STEP(1, nx, LATE); END; SB(); } while (0)

  __amdgpu_buffer_rsrc_t wsr = MKRSRC(ws, OFF_OF + SEQ * DM * 2), xr = MKRSRC(x, SEQ * DM * 2);
  asm volatile("" : "+s"(wsr), "+s"(xr));
  const unsigned rec = (unsigned)((head * 2 + wr) * 16 + mt);
  unsigned x1w = L_X1 + w * 2048 + lane * 16, cw = L_CW + w * 128 + g * 16, one = 1u;
  unsigned kvo = (unsigned)OFF_KVP + rec * 1024 + lane * 16;
  unsigned zo = c16 == 0 ? (unsigned)OFF_ZP + rec * 64 + g * 16 : lane == 1 ? (unsigned)OFF_ZP + 65536 + rec * 16 : 0x80000000u;
  PIN(x1w); PIN(cw); PIN(one); PIN(kvo); PIN(zo);
  if (PRI == 2) __builtin_amdgcn_s_setprio(3);
  WAIT_LGKM0();
  BAR();
  STAMP(0, 8); CSTAMP(8);
#pragma unroll
  for (int u = 0; u < 16; ++u) RD(0, 0, u);
  if (SL0) { for (int i = 0; i < (int)((ix + 2 * w) & 7); ++i) __builtin_amdgcn_s_sleep(SL0); }
  LGKM0();
  SB();
#pragma unroll 1
  for (int it = 0; it < 4; ++it) { STAGE(0, 1, (void)0, LGKM0()); STAGE(1, 2, (void)0, LGKM0()); STAGE(2, 0, (void)0, LGKM0()); }
  STAGE(0, 1, (void)0, LGKM0());
  STEP(0, 1, (void)0); LGKM0(); BAR();
  if (PRI == 1 || PRI == 3) __builtin_amdgcn_s_setprio(3);
  bf16x8 ta0_0, ta0_1, ta0_2, ta0_3, ta1_0, ta1_1, ta1_2, ta1_3, ta2_0, ta2_1, ta2_2, ta2_3;
  bf16x8 ta3_0, ta3_1, ta3_2, ta3_3, tb0_0_0, tb0_0_1, tb0_1_0, tb0_1_1, tb0_2_0, tb0_2_1, tb0_3_0, tb0_3_1;
  bf16x8 tb1_0_0, tb1_0_1, tb1_1_0, tb1_1_1, tb1_2_0, tb1_2_1, tb1_3_0, tb1_3_1, tb2_0_0, tb2_0_1, tb2_1_0, tb2_1_1;
  bf16x8 tb2_2_0, tb2_2_1, tb2_3_0, tb2_3_1;
  s16x4 tb0_0_0_l, tb0_0_1_l, tb0_1_0_l, tb0_1_1_l, tb0_2_0_l, tb0_2_1_l, tb0_3_0_l, tb0_3_1_l, tb1_0_0_l, tb1_0_1_l, tb1_1_0_l, tb1_1_1_l;
  s16x4 tb1_2_0_l, tb1_2_1_l, tb1_3_0_l, tb1_3_1_l, tb2_0_0_l, tb2_0_1_l, tb2_1_0_l, tb2_1_1_l, tb2_2_0_l, tb2_2_1_l, tb2_3_0_l, tb2_3_1_l;
  unsigned kp[16], vp[16], x1r, cr;
  u32x2 lf15;
  float kmin = 0.f, qmin = 0.f;
  u32x4 ones;
  float ek[4];
#define PHI(src, dst, mn, k, st)                                                                           \
  do {                                                                                                     \
    float& e_ = ek[2 * (k) + ((st) & 1)];                                                                  \
    if ((st) < 2) e_ = __builtin_fmaf((src)[2 * (k) + ((st) & 1)], 1.44269504088896341f, -1.44269504088896341f); \
    else if ((st) < 4) e_ = __builtin_amdgcn_fmed3f(__builtin_amdgcn_exp2f(e_), 0.f, 1.f);                 \
    else if ((st) < 6) e_ = fmax_raw((src)[2 * (k) + ((st) & 1)], e_);                                     \
    else if ((st) == 6) dst = cvt_pk(ek[2 * (k)], ek[2 * (k) + 1]);                                        \
    else mn = __builtin_fminf(__builtin_fminf(mn, (src)[2 * (k)]), (src)[2 * (k) + 1]);                     \
    SB();                                                                                                  \
  } while (0)
#define PK(mi, n, k, st) PHI(acc[1][mi][n], kp[8 * (n) + 2 * (mi) + (k)], kmin, k, st)
#define CV(mi, n, k) do { vp[8 * (n) + 2 * (mi) + (k)] = cvt_pk(acc[2][mi][n][2 * (k)], acc[2][mi][n][2 * (k) + 1]); PIN(vp[8 * (n) + 2 * (mi) + (k)]); SB(); } while (0)
#define F15_RD if (!NF15) lf15 = *(volatile lu32x2*)(smem + L_CTL + 4 * C_F15)
#define F15_CHK do { if (!NF15 && __builtin_expect(!all_true((lf15[0] & lf15[1]) != 0u), 0)) { (void)wait_ge<1>(misc + C_F15, 1u); (void)wait_ge<1>(misc + C_F15 + 1, 1u); } CFENCE(); } while (0)
#define PRE(i) do { if ((i) == 30) { x1r = x1w ^ 4096u; PIN(x1r); } if ((i) == 31) { cr = cw ^ 256u; PIN(cr); } \
                    if ((i) == 32) { ones = (u32x4){0x3F803F80u, 0x3F803F80u, 0x3F803F80u, 0x3F803F80u}; PIN(ones); } } while (0)
#define HOOK_K0
#define HOOK_V0 STAMP(0, 1); CSTAMP(1);
  MM(1, 0, 0, a[1][0], b[1][2]); SB(); tb1_0_1_l = TRD(B_AT(2, 0, 1, 1)); PRE(0); SB();
  MM(1, 0, 1, a[1][0], b[1][3]); SB(); tb1_0_1 = CAT(tb1_0_1_l, TRD(B_AT(2, 0, 1, 1) + 512)); PRE(1); SB();
  MM(2, 0, 0, a[1][0], b[1][4]); SB(); ta0_0 = A_AT(2, 0, 0); PRE(2); SB();
  MM(2, 0, 1, a[1][0], b[1][5]); SB(); ta0_1 = A_AT(2, 0, 1); PRE(3); SB();
  MM(0, 0, 0, a[1][0], b[1][0]); SB(); ta0_2 = A_AT(2, 0, 2); PRE(4); SB();
  MM(0, 0, 1, a[1][0], b[1][1]); SB(); ta0_3 = A_AT(2, 0, 3); PRE(5); SB();
  MM(1, 1, 0, a[1][1], b[1][2]); SB(); tb1_0_0_l = TRD(B_AT(2, 0, 1, 0)); PRE(6); SB();
  MM(1, 1, 1, a[1][1], b[1][3]); SB(); tb1_0_0 = CAT(tb1_0_0_l, TRD(B_AT(2, 0, 1, 0) + 512)); PRE(7); SB();
  MM(2, 1, 0, a[1][1], b[1][4]); SB(); tb1_1_1_l = TRD(B_AT(2, 1, 1, 1)); PRE(8); SB();
  MM(2, 1, 1, a[1][1], b[1][5]); SB(); tb1_1_1 = CAT(tb1_1_1_l, TRD(B_AT(2, 1, 1, 1) + 512)); PRE(9); SB();
  MM(0, 1, 0, a[1][1], b[1][0]); SB(); ta1_0 = A_AT(2, 1, 0); PRE(10); SB();
  MM(0, 1, 1, a[1][1], b[1][1]); SB(); ta1_1 = A_AT(2, 1, 1); PRE(11); SB();
  MM(1, 2, 0, a[1][2], b[1][2]); SB(); ta1_2 = A_AT(2, 1, 2); PRE(12); SB();
  MM(1, 2, 1, a[1][2], b[1][3]); SB(); ta1_3 = A_AT(2, 1, 3); PRE(13); SB();
  MM(2, 2, 0, a[1][2], b[1][4]); SB(); tb1_1_0_l = TRD(B_AT(2, 1, 1, 0)); PRE(14); SB();
  MM(2, 2, 1, a[1][2], b[1][5]); SB(); tb1_1_0 = CAT(tb1_1_0_l, TRD(B_AT(2, 1, 1, 0) + 512)); PRE(15); SB();
  MM(0, 2, 0, a[1][2], b[1][0]); SB(); PRE(16); SB();
  MM(0, 2, 1, a[1][2], b[1][1]); SB(); PRE(17); SB();
  MM(1, 3, 0, a[1][3], b[1][2]); SB(); PRE(18); SB();
  MM(1, 3, 1, a[1][3], b[1][3]); SB(); PRE(19); SB();
  MM(2, 3, 0, a[1][3], b[1][4]); SB(); F15_RD; PRE(20); SB();
  MM(2, 3, 1, a[1][3], b[1][5]); SB(); PRE(21); SB();
  MM(0, 3, 0, a[1][3], b[1][0]); SB(); tb2_0_1_l = TRD(B_AT(2, 0, 2, 1)); PRE(22); SB();
  MM(0, 3, 1, a[1][3], b[1][1]); SB(); tb2_0_1 = CAT(tb2_0_1_l, TRD(B_AT(2, 0, 2, 1) + 512)); PRE(23); SB();
  HOOK_K0 MM(1, 0, 1, ta0_0, tb1_0_1); SB(); PRE(24); SB();
  MM(1, 1, 1, ta0_1, tb1_0_1); SB(); PRE(25); SB();
  MM(1, 2, 1, ta0_2, tb1_0_1); SB(); F15_CHK; tb2_1_1_l = TRD(B_AT(2, 1, 2, 1)); PRE(26); SB();
  MM(1, 3, 1, ta0_3, tb1_0_1); SB(); tb2_1_1 = CAT(tb2_1_1_l, TRD(B_AT(2, 1, 2, 1) + 512)); PRE(27); SB();
  MM(1, 0, 0, ta0_0, tb1_0_0); SB(); tb1_2_1_l = TRD(B_AT(0, 0, 1, 1)); PRE(28); SB();
  MM(1, 1, 0, ta0_1, tb1_0_0); SB(); tb1_2_1 = CAT(tb1_2_1_l, TRD(B_AT(0, 0, 1, 1) + 512)); PRE(29); SB();
  MM(1, 2, 0, ta0_2, tb1_0_0); SB(); ta2_0 = A_AT(0, 0, 0); PRE(30); SB();
  MM(1, 3, 0, ta0_3, tb1_0_0); SB(); ta2_1 = A_AT(0, 0, 1); PRE(31); SB();
  MM(1, 0, 1, ta1_0, tb1_1_1); SB(); ta2_2 = A_AT(0, 0, 2); PRE(32); SB();
  MM(1, 1, 1, ta1_1, tb1_1_1); SB(); ta2_3 = A_AT(0, 0, 3); PRE(33); SB();
  MM(1, 2, 1, ta1_2, tb1_1_1); SB(); tb1_3_1_l = TRD(B_AT(0, 1, 1, 1)); PRE(34); SB();
  MM(1, 3, 1, ta1_3, tb1_1_1); SB(); tb1_3_1 = CAT(tb1_3_1_l, TRD(B_AT(0, 1, 1, 1) + 512)); PRE(35); SB();
  MM(1, 0, 0, ta1_0, tb1_1_0); SB(); ta3_0 = A_AT(0, 1, 0); PRE(36); SB();
  MM(1, 1, 0, ta1_1, tb1_1_0); SB(); ta3_1 = A_AT(0, 1, 1); PRE(37); SB();
  MM(1, 2, 0, ta1_2, tb1_1_0); SB(); ta3_2 = A_AT(0, 1, 2); PRE(38); SB();
  MM(1, 3, 0, ta1_3, tb1_1_0); SB(); ta3_3 = A_AT(0, 1, 3); PRE(39); SB();
  MM(1, 0, 1, ta2_0, tb1_2_1); SB(); tb1_2_0_l = TRD(B_AT(0, 0, 1, 0)); PRE(40); SB();
  MM(1, 1, 1, ta2_1, tb1_2_1); SB(); tb1_2_0 = CAT(tb1_2_0_l, TRD(B_AT(0, 0, 1, 0) + 512)); PRE(41); SB();
  MM(1, 2, 1, ta2_2, tb1_2_1); SB(); tb1_3_0_l = TRD(B_AT(0, 1, 1, 0)); PRE(42); SB();
  MM(1, 3, 1, ta2_3, tb1_2_1); SB(); tb1_3_0 = CAT(tb1_3_0_l, TRD(B_AT(0, 1, 1, 0) + 512)); PRE(43); SB();
  MM(1, 0, 1, ta3_0, tb1_3_1); SB(); tb2_2_1_l = TRD(B_AT(0, 0, 2, 1)); PRE(44); SB();
  MM(1, 1, 1, ta3_1, tb1_3_1); SB(); tb2_2_1 = CAT(tb2_2_1_l, TRD(B_AT(0, 0, 2, 1) + 512)); PRE(45); SB();
  MM(1, 2, 1, ta3_2, tb1_3_1); SB(); tb2_3_1_l = TRD(B_AT(0, 1, 2, 1)); PRE(46); SB();
  MM(1, 3, 1, ta3_3, tb1_3_1); SB(); tb2_3_1 = CAT(tb2_3_1_l, TRD(B_AT(0, 1, 2, 1) + 512)); PRE(47); SB();
  MM(1, 0, 0, ta2_0, tb1_2_0); SB(); tb2_0_0_l = TRD(B_AT(2, 0, 2, 0)); PK(0, 1, 0, 0); PK(0, 1, 0, 1); PK(0, 1, 1, 0); PK(0, 1, 1, 1); PK(0, 1, 0, 2); SB();
  MM(1, 1, 0, ta2_1, tb1_2_0); SB(); tb2_0_0 = CAT(tb2_0_0_l, TRD(B_AT(2, 0, 2, 0) + 512)); PK(0, 1, 0, 3); PK(0, 1, 0, 7); PK(0, 1, 1, 2); PK(0, 1, 1, 3); SB();
  MM(1, 2, 0, ta2_2, tb1_2_0); SB(); tb2_1_0_l = TRD(B_AT(2, 1, 2, 0)); PK(0, 1, 1, 7); PK(0, 1, 0, 4); PK(0, 1, 0, 5); PK(0, 1, 1, 4); PK(0, 1, 1, 5); PK(0, 1, 0, 6); SB();
  MM(1, 3, 0, ta2_3, tb1_2_0); SB(); tb2_1_0 = CAT(tb2_1_0_l, TRD(B_AT(2, 1, 2, 0) + 512)); PK(0, 1, 1, 6); PK(1, 1, 0, 0); PK(1, 1, 0, 1); PK(1, 1, 1, 0); PK(1, 1, 1, 1); PK(1, 1, 0, 2); SB();
  MM(1, 0, 0, ta3_0, tb1_3_0); SB(); tb2_2_0_l = TRD(B_AT(0, 0, 2, 0)); PK(1, 1, 0, 3); PK(1, 1, 0, 7); PK(1, 1, 1, 2); PK(1, 1, 1, 3); SB();
  MM(1, 1, 0, ta3_1, tb1_3_0); SB(); tb2_2_0 = CAT(tb2_2_0_l, TRD(B_AT(0, 0, 2, 0) + 512)); PK(1, 1, 1, 7); PK(1, 1, 0, 4); PK(1, 1, 0, 5); PK(1, 1, 1, 4); PK(1, 1, 1, 5); SB();
  MM(1, 2, 0, ta3_2, tb1_3_0); SB(); tb2_3_0_l = TRD(B_AT(0, 1, 2, 0)); PK(1, 1, 0, 6); PK(1, 1, 1, 6); PK(2, 1, 0, 0); PK(2, 1, 0, 1); PK(2, 1, 1, 0); SB();
  MM(1, 3, 0, ta3_3, tb1_3_0); SB(); tb2_3_0 = CAT(tb2_3_0_l, TRD(B_AT(0, 1, 2, 0) + 512)); PK(2, 1, 1, 1); PK(2, 1, 0, 2); PK(2, 1, 0, 3); SB();
  HOOK_V0 MM(2, 0, 1, ta0_0, tb2_0_1); SB(); PK(2, 1, 0, 7); PK(2, 1, 1, 2); PK(2, 1, 1, 3); SB();
  MM(2, 1, 1, ta0_1, tb2_0_1); SB(); PK(2, 1, 1, 7); PK(2, 1, 0, 4); PK(2, 1, 0, 5); PK(2, 1, 1, 4); PK(2, 1, 1, 5); SB();
  MM(2, 2, 1, ta0_2, tb2_0_1); SB(); PK(2, 1, 0, 6); PK(2, 1, 1, 6); PK(3, 1, 0, 0); PK(3, 1, 0, 1); PK(3, 1, 1, 0); SB();
  MM(2, 3, 1, ta0_3, tb2_0_1); SB(); PK(3, 1, 1, 1); PK(3, 1, 0, 2); PK(3, 1, 0, 3); SB();
  MM(2, 0, 1, ta1_0, tb2_1_1); SB(); PK(3, 1, 0, 7); PK(3, 1, 1, 2); PK(3, 1, 1, 3); SB();
  MM(2, 1, 1, ta1_1, tb2_1_1); SB(); PK(3, 1, 1, 7); PK(3, 1, 0, 4); PK(3, 1, 0, 5); PK(3, 1, 1, 4); PK(3, 1, 1, 5); SB();
  MM(2, 2, 1, ta1_2, tb2_1_1); SB(); PK(3, 1, 0, 6); PK(3, 1, 1, 6); PK(0, 0, 0, 0); PK(0, 0, 0, 1); PK(0, 0, 1, 0); SB();
  MM(2, 3, 1, ta1_3, tb2_1_1); SB(); PK(0, 0, 1, 1); PK(0, 0, 0, 2); PK(0, 0, 0, 3); SB();
  MM(2, 0, 1, ta2_0, tb2_2_1); SB(); PK(0, 0, 0, 7); PK(0, 0, 1, 2); PK(0, 0, 1, 3); SB();
  MM(2, 1, 1, ta2_1, tb2_2_1); SB(); PK(0, 0, 1, 7); PK(0, 0, 0, 4); PK(0, 0, 0, 5); PK(0, 0, 1, 4); PK(0, 0, 1, 5); SB();
  MM(2, 2, 1, ta2_2, tb2_2_1); SB(); PK(0, 0, 0, 6); PK(0, 0, 1, 6); PK(1, 0, 0, 0); PK(1, 0, 0, 1); PK(1, 0, 1, 0); SB();
  MM(2, 3, 1, ta2_3, tb2_2_1); SB(); PK(1, 0, 1, 1); PK(1, 0, 0, 2); PK(1, 0, 0, 3); SB();
  MM(2, 0, 1, ta3_0, tb2_3_1); SB(); PK(1, 0, 0, 7); PK(1, 0, 1, 2); PK(1, 0, 1, 3); SB();
  MM(2, 1, 1, ta3_1, tb2_3_1); SB(); PK(1, 0, 1, 7); PK(1, 0, 0, 4); PK(1, 0, 0, 5); PK(1, 0, 1, 4); PK(1, 0, 1, 5); SB();
  MM(2, 2, 1, ta3_2, tb2_3_1); SB(); PK(1, 0, 0, 6); PK(1, 0, 1, 6); PK(2, 0, 0, 0); PK(2, 0, 0, 1); PK(2, 0, 1, 0); SB();
  MM(2, 3, 1, ta3_3, tb2_3_1); SB(); PK(2, 0, 1, 1); PK(2, 0, 0, 2); PK(2, 0, 0, 3); SB();
  MM(2, 0, 0, ta0_0, tb2_0_0); SB(); PK(2, 0, 0, 7); PK(2, 0, 1, 2); PK(2, 0, 1, 3); SB();
  MM(2, 1, 0, ta0_1, tb2_0_0); SB(); PK(2, 0, 1, 7); PK(2, 0, 0, 4); PK(2, 0, 0, 5); PK(2, 0, 1, 4); PK(2, 0, 1, 5); SB();
  MM(2, 2, 0, ta0_2, tb2_0_0); SB(); PK(2, 0, 0, 6); PK(2, 0, 1, 6); PK(3, 0, 0, 0); PK(3, 0, 0, 1); PK(3, 0, 1, 0); SB();
  MM(2, 3, 0, ta0_3, tb2_0_0); SB(); PK(3, 0, 1, 1); PK(3, 0, 0, 2); PK(3, 0, 0, 3); SB();
  MM(2, 0, 0, ta1_0, tb2_1_0); SB(); PK(3, 0, 0, 7); PK(3, 0, 1, 2); PK(3, 0, 1, 3); SB();
  MM(2, 1, 0, ta1_1, tb2_1_0); SB(); PK(3, 0, 1, 7); PK(3, 0, 0, 4); PK(3, 0, 0, 5); PK(3, 0, 1, 4); PK(3, 0, 1, 5); SB();
  MM(2, 2, 0, ta1_2, tb2_1_0); SB(); PK(3, 0, 0, 6); PK(3, 0, 1, 6); CV(0, 1, 0); CV(0, 1, 1); CV(1, 1, 0); SB();
  MM(2, 3, 0, ta1_3, tb2_1_0); SB(); CV(1, 1, 1); CV(2, 1, 0); CV(2, 1, 1); CV(3, 1, 0); CV(3, 1, 1); SB();
  const f32x4 zero = {0.f, 0.f, 0.f, 0.f};
#define TAKE(flag, READS)                                                                                  \
  _Pragma("unroll 1") for (unsigned spins = 0; spins < (1u << 22); ++spins) {                              \
    const unsigned f_ = (flag);                                                                            \
    CFENCE();                                                                                              \
    READS;                                                                                                 \
    if (__builtin_expect(rfl(f_) != 0u, 1)) break;                                                         \
  }
  unsigned lidx, ltag;
  {
    phi_fix(kp, kmin);
#define KF(j, n) FRAG(((u32x4){kp[8 * (n) + 4 * (j)], kp[8 * (n) + 4 * (j) + 1], kp[8 * (n) + 4 * (j) + 2], kp[8 * (n) + 4 * (j) + 3]}))
#define VF(j, n) FRAG(((u32x4){vp[8 * (n) + 4 * (j)], vp[8 * (n) + 4 * (j) + 1], vp[8 * (n) + 4 * (j) + 2], vp[8 * (n) + 4 * (j) + 3]}))
    f32x4 s1 = MFMA(KF(0, 1), VF(0, 1), zero), s0 = MFMA(KF(0, 0), VF(0, 1), zero), zs = MFMA(KF(0, 1), FRAG(ones), zero);
    s1 = MFMA(KF(1, 1), VF(1, 1), s1); s0 = MFMA(KF(1, 0), VF(1, 1), s0); zs = MFMA(KF(1, 1), FRAG(ones), zs);
#define QR(u, n) do { tb0_##u##_##n##_l = TRD(B_AT((u) < 2 ? 2 : 0, (u) & 1, 0, n)); tb0_##u##_##n = CAT(tb0_##u##_##n##_l, TRD(B_AT((u) < 2 ? 2 : 0, (u) & 1, 0, n) + 512)); SB(); } while (0)
#define X1_RD { p0 = *LP(volatile f32x4, x1r, 0); p1 = *LP(volatile f32x4, x1r, 1024); pz = *LP(volatile f32x4, cr, 0); }
    SB();
    MM(2, 0, 0, ta2_0, tb2_2_0); SB(); QR(0, 0); MM(2, 1, 0, ta2_1, tb2_2_0); SB(); QR(0, 1);
    *LP(f32x4, x1w, 0) = s1; *LP(f32x4, x1w, 1024) = s0; *LP(f32x4, cw, 0) = zs;
    CFENCE();
    *LP(volatile unsigned, cw, W_X1) = one;
    SB();
    MM(2, 0, 0, ta3_0, tb2_3_0); SB(); QR(1, 0); MM(2, 1, 0, ta3_1, tb2_3_0); SB(); QR(1, 1);
    MM(2, 2, 0, ta2_2, tb2_2_0); SB(); QR(2, 0); MM(2, 3, 0, ta2_3, tb2_2_0); SB(); CV(0, 0, 0); CV(0, 0, 1); QR(2, 1);
    MM(2, 2, 0, ta3_2, tb2_3_0); SB(); CV(1, 0, 0); CV(1, 0, 1); QR(3, 0); MM(2, 3, 0, ta3_3, tb2_3_0); SB(); QR(3, 1);
    f32x4 p0, p1, pz;
    unsigned f1;
    if (OV == 1) { f1 = *LP(volatile unsigned, cr, W_X1); CFENCE(); X1_RD SB(); }
    f32x4 kv0 = MFMA(KF(0, 0), VF(0, 0), zero), kv1 = MFMA(KF(0, 1), VF(0, 0), zero);
    SB();
    CV(2, 0, 0); CV(2, 0, 1);
    f32x4 z = MFMA(KF(0, 0), FRAG(ones), zero);
    SB();
    if (OV == 2) { f1 = *LP(volatile unsigned, cr, W_X1); CFENCE(); X1_RD SB(); }
    CV(3, 0, 0); CV(3, 0, 1);
    kv0 = MFMA(KF(1, 0), VF(1, 0), kv0); kv1 = MFMA(KF(1, 1), VF(1, 0), kv1);
    z = MFMA(KF(1, 0), FRAG(ones), z);
    SB();
    if (__builtin_expect(!all_true(f1 != 0u), 0)) { TAKE(*LP(volatile unsigned, cr, W_X1), X1_RD); LGKM0(); }
    const f32x4 m0_ = kv0 + p0, m1_ = kv1 + p1;
    float am = fmax_raw(fabsf(m0_[0]), fabsf(m1_[0]));
#pragma unroll
    for (int e = 1; e < 4; ++e) am = fmax_raw(fmax_raw(am, fabsf(m0_[e])), fabsf(m1_[e]));
#define PKH(a_, b_) __builtin_bit_cast(unsigned, __builtin_convertvector((f32x2){(a_), (b_)}, f16x2_t))
    u32x4 hv = {PKH(m0_[0], m0_[1]), PKH(m0_[2], m0_[3]), PKH(m1_[0], m1_[1]), PKH(m1_[2], m1_[3])};
#define Z_STORE(scl) do { unsigned z0_ = zv[0]; asm("v_writelane_b32 %0, %1, 1" : "+v"(z0_) : "s"(scl)); zv[0] = z0_; __builtin_amdgcn_raw_buffer_store_b128(zv, wsr, zo, 0, 16); } while (0)
    PIN(hv);
    if (__builtin_expect(all_true(f2u(am) - 0x3C800000u <= 0x47000000u - 0x3C800000u), 1)) {
      __builtin_amdgcn_raw_buffer_store_b128(hv, wsr, kvo, 0, 16);
      SB();
      u32x4 zv = __builtin_bit_cast(u32x4, z + pz);
      Z_STORE(0x3F800000u);
    } else {
      unsigned ex = max(wave_max(f2u(am) & 0x7FFFFFFFu) >> 23, 15u);
      if (ex >= 121u && ex < 142u) ex = 141u;
      const float inv = u2f((268u - ex) << 23);
      u32x4 hs = {PKH(m0_[0] * inv, m0_[1] * inv), PKH(m0_[2] * inv, m0_[3] * inv), PKH(m1_[0] * inv, m1_[1] * inv), PKH(m1_[2] * inv, m1_[3] * inv)};
      u32x4 zv = __builtin_bit_cast(u32x4, z + pz);
#pragma unroll
      for (int e = 0; e < 4; ++e) { hs[e] = min(hs[e], ~1u); zv[e] = min(zv[e] | (((TA & 2) && ex != 141u) ? 0x80000000u : 0u), ~1u); }
      CFENCE();
      __builtin_amdgcn_raw_buffer_store_b128(hs, wsr, kvo, 0, 16);
      Z_STORE((ex - 14u) << 23);
      CFENCE();
    }
  }
  *LP(volatile unsigned, cw, W_KS) = one;
  if (PRI == 3) __builtin_amdgcn_s_setprio(0);
  STAMP(0, 2); CSTAMP(2);
  unsigned eoff, ko, oo, x2w, zfa, zfb, zfp, gta;
  u32x2 eye, ov[4][2];
  u32x4 xv[4], t[16];
  float gq[4];
  unsigned pq[16];
#define PQ(mi, n, k, st) do { PHI(acc[0][mi][n], pq[4 * (mi) + 2 * (n) + (k)], qmin, k, st); if (QP == 2 && (st) == 6) { PIN(pq[4 * (mi) + 2 * (n) + (k)]); if ((k) == 1) PIN(qmin); SB(); } } while (0)
#define NOPQ() asm volatile("s_nop 4" : "+v"(acc[0][3][0]), "+v"(acc[0][3][1]))
#define PRQ(i)                                                                                             \
  do {                                                                                                     \
    if ((i) == 0) { lidx = misc[C_IDX + wr]; ltag = misc[C_TAG]; }                                         \
    if ((i) == 1 && XL != 3) { eoff = (unsigned)(((m0 + 32 * w + c16) * DM + 64 * nt3 + 16 * (g & 1) + 8 * (g >> 1)) * 2); PIN(eoff); } \
    if ((i) >= 3 && (i) < 7 && !(XL & 2)) xv[((i) - 3) & 3] = (OPAT & 2) ? __builtin_amdgcn_raw_buffer_load_b128(xr, (unsigned)(((m0 + 32 * w + (lane >> 3)) * DM + 64 * nt3) * 2 + (lane & 7) * 16), ((i) - 3) * (16 * DM), 0) : __builtin_amdgcn_raw_buffer_load_b128(xr, eoff + (((i) - 3) & 1) * 64, (((i) - 3) >> 1) * (32 * DM), 0); \
    if ((i) == 2) { zfp = L_CTL + 4 * (C_ZF + hh); PIN(zfp); }                                             \
    if ((i) == 3) { ko = kvo - mt * 1024; PIN(ko); }                                                       \
    if ((i) == 4) { gta = L_GATE + wr * 256 + c16 * 4; PIN(gta); }                                         \
    if ((i) == 5) { zfa = L_ZF + hh * 64 + wr * 8 + g * 16; PIN(zfa); }                                    \
    if ((i) == 6) { zfb = zfa ^ 8u; PIN(zfb); }                                                            \
    if ((i) == 7) { const unsigned o_ = 0x3C00u << (16 * (lane & 1)); eye = (u32x2){(lane & 2) ? 0u : o_, (lane & 2) ? o_ : 0u}; PIN(eye); } \
    if ((i) == 8) { x2w = L_X2 + w * 1024 + lane * 16; PIN(x2w); }                                         \
    if ((i) == 9) { oo = (unsigned)OFF_OF + (unsigned)(((mt * 8 + wr * 4) * NH + head) * 1024) + lane * 16; PIN(oo); } \
  } while (0)
#define SHOT_GATE                                                                                          \
  do {                                                                                                     \
    if (QP == 1) { phi_fix(pq, qmin); _Pragma("unroll") for (int i = 0; i < 16; ++i) PIN(pq[i]); }              \
    if (__builtin_expect(!all_true(lidx != 0u), 0)) (void)wait_ge<1>(misc + C_IDX + wr, 1u);        \
    if (K_NAP) __builtin_amdgcn_s_sleep(K_NAP);                                                            \
    if (UGATE & 1) { (void)wait_ge<1>(misc + C_UD, 1u); (void)wait_ge<1>(misc + C_UD + 1, 1u); }           \
    if (ZGATE) (void)wait_ge<1>(misc + (ZGATE == 2 ? C_GO : C_ZF) + hh, 1u);                                                     \
    CFENCE();                                                                                              \
    _Pragma("unroll") for (int i = 0; i < 4; ++i) gq[i] = *LP(volatile float, gta, i * 64);                \
  } while (0)
  const int kx = KO == 1 ? (mt & 8) : KO == 2 ? (mt & 8) ^ 8 : KO == 3 ? (mt & 12) : 0;
#define T_LD(i) t[i] = __builtin_amdgcn_raw_buffer_load_b128(wsr, ko + ((i) & 3) * 1024, (((i) >> 2) * 4096) ^ (kx * 1024), 16)
  SB();
  MM(0, 0, 0, ta0_0, tb0_0_0); SB(); PRQ(0); SB();
  MM(0, 0, 1, ta0_0, tb0_0_1); SB(); PRQ(1); SB();
  MM(0, 0, 0, ta1_0, tb0_1_0); SB(); PRQ(2); SB();
  MM(0, 0, 1, ta1_0, tb0_1_1); SB(); PRQ(3); SB();
  MM(0, 0, 0, ta2_0, tb0_2_0); SB(); PRQ(4); SB();
  MM(0, 0, 1, ta2_0, tb0_2_1); SB(); PRQ(5); SB();
  MM(0, 0, 0, ta3_0, tb0_3_0); SB(); PRQ(6); SB();
  MM(0, 0, 1, ta3_0, tb0_3_1); SB(); PRQ(7); SB();
  MM(0, 1, 0, ta0_1, tb0_0_0); SB(); PRQ(8); SB();
  MM(0, 1, 1, ta0_1, tb0_0_1); SB(); PRQ(9); SB();
  MM(0, 1, 0, ta1_1, tb0_1_0); SB(); PQ(0, 0, 0, 0); PQ(0, 0, 0, 1); PQ(0, 0, 1, 0); SB();
  MM(0, 1, 1, ta1_1, tb0_1_1); SB(); PQ(0, 0, 1, 1); PQ(0, 0, 0, 2); SB();
  MM(0, 1, 0, ta2_1, tb0_2_0); SB(); PQ(0, 0, 0, 3); PQ(0, 0, 0, 7); SB();
  MM(0, 1, 1, ta2_1, tb0_2_1); SB(); PQ(0, 0, 1, 2); PQ(0, 0, 1, 3); SB();
  MM(0, 1, 0, ta3_1, tb0_3_0); SB(); PQ(0, 0, 1, 7); PQ(0, 0, 0, 4); PQ(0, 0, 0, 5); SB();
  MM(0, 1, 1, ta3_1, tb0_3_1); SB(); PQ(0, 0, 1, 4); PQ(0, 0, 1, 5); PQ(0, 0, 0, 6); SB();
  MM(0, 2, 0, ta0_2, tb0_0_0); SB(); PQ(0, 0, 1, 6); PQ(0, 1, 0, 0); PQ(0, 1, 0, 1); SB();
  MM(0, 2, 1, ta0_2, tb0_0_1); SB(); PQ(0, 1, 1, 0); PQ(0, 1, 1, 1); PQ(0, 1, 0, 2); SB();
  MM(0, 2, 0, ta1_2, tb0_1_0); SB(); PQ(0, 1, 0, 3); PQ(0, 1, 0, 7); SB();
  MM(0, 2, 1, ta1_2, tb0_1_1); SB(); PQ(0, 1, 1, 2); PQ(0, 1, 1, 3); SB();
  MM(0, 2, 0, ta2_2, tb0_2_0); SB(); PQ(0, 1, 1, 7); PQ(0, 1, 0, 4); PQ(0, 1, 0, 5); SB();
  MM(0, 2, 1, ta2_2, tb0_2_1); SB(); PQ(0, 1, 1, 4); PQ(0, 1, 1, 5); PQ(0, 1, 0, 6); SB();
  MM(0, 2, 0, ta3_2, tb0_3_0); SB(); PQ(0, 1, 1, 6); PQ(1, 0, 0, 0); PQ(1, 0, 0, 1); SB();
  MM(0, 2, 1, ta3_2, tb0_3_1); SB(); PQ(1, 0, 1, 0); PQ(1, 0, 1, 1); PQ(1, 0, 0, 2); SB();
  MM(0, 3, 0, ta0_3, tb0_0_0); SB(); PQ(1, 0, 0, 3); PQ(1, 0, 0, 7); SB();
  MM(0, 3, 1, ta0_3, tb0_0_1); SB(); PQ(1, 0, 1, 2); PQ(1, 0, 1, 3); SB();
  MM(0, 3, 0, ta1_3, tb0_1_0); SB(); PQ(1, 0, 1, 7); PQ(1, 0, 0, 4); PQ(1, 0, 0, 5); SB();
  MM(0, 3, 1, ta1_3, tb0_1_1); SB(); PQ(1, 0, 1, 4); PQ(1, 0, 1, 5); PQ(1, 0, 0, 6); SB();
  MM(0, 3, 0, ta2_3, tb0_2_0); SB(); PQ(1, 0, 1, 6); PQ(1, 1, 0, 0); PQ(1, 1, 0, 1); SB();
  MM(0, 3, 1, ta2_3, tb0_2_1); SB(); PQ(1, 1, 1, 0); PQ(1, 1, 1, 1); PQ(1, 1, 0, 2); SB();
  MM(0, 3, 0, ta3_3, tb0_3_0); SB(); PQ(1, 1, 0, 3); PQ(1, 1, 0, 7); SB();
  MM(0, 3, 1, ta3_3, tb0_3_1); SB(); PQ(1, 1, 1, 2); PQ(1, 1, 1, 3); SB();
  PQ(1, 1, 1, 7); SB();
  PQ(1, 1, 0, 4); SB();
  PQ(1, 1, 0, 5); SB();
  PQ(1, 1, 1, 4); SB();
  PQ(1, 1, 1, 5); SB();
  PQ(1, 1, 0, 6); SB();
  PQ(1, 1, 1, 6); SB();
  PQ(2, 0, 0, 0); SB();
  PQ(2, 0, 0, 1); SB();
  PQ(2, 0, 1, 0); SB();
  PQ(2, 0, 1, 1); SB();
  PQ(2, 0, 0, 2); SB();
  PQ(2, 0, 0, 3); SB();
  PQ(2, 0, 0, 7); SB();
  PQ(2, 0, 1, 2); SB();
  PQ(2, 0, 1, 3); SB();
  PQ(2, 0, 1, 7); SB();
  PQ(2, 0, 0, 4); SB();
  PQ(2, 0, 0, 5); SB();
  PQ(2, 0, 1, 4); SB();
  PQ(2, 0, 1, 5); SB();
  PQ(2, 0, 0, 6); SB();
  PQ(2, 0, 1, 6); SB();
  PQ(2, 1, 0, 0); SB();
  PQ(2, 1, 0, 1); SB();
  PQ(2, 1, 1, 0); SB();
  PQ(2, 1, 1, 1); SB();
  PQ(2, 1, 0, 2); SB();
  PQ(2, 1, 0, 3); SB();
  PQ(2, 1, 0, 7); SB();
  PQ(2, 1, 1, 2); SB();
  PQ(2, 1, 1, 3); SB();
  PQ(2, 1, 1, 7); SB();
  PQ(2, 1, 0, 4); SB();
  PQ(2, 1, 0, 5); SB();
  PQ(2, 1, 1, 4); SB();
  PQ(2, 1, 1, 5); SB();
  PQ(2, 1, 0, 6); SB();
  PQ(2, 1, 1, 6); SB();
  NOPQ();
  PQ(3, 0, 0, 0); SB();
  PQ(3, 0, 0, 1); SB();
  PQ(3, 0, 1, 0); SB();
  PQ(3, 0, 1, 1); SB();
  PQ(3, 0, 0, 2); SB();
  PQ(3, 0, 0, 3); SB();
  PQ(3, 0, 0, 7); SB();
  PQ(3, 0, 1, 2); SB();
  PQ(3, 0, 1, 3); SB();
  PQ(3, 0, 1, 7); SB();
  PQ(3, 0, 0, 4); SB();
  PQ(3, 0, 0, 5); SB();
  PQ(3, 0, 1, 4); SB();
  PQ(3, 0, 1, 5); SB();
  PQ(3, 0, 0, 6); SB();
  PQ(3, 0, 1, 6); SB();
  PQ(3, 1, 0, 0); SB();
  PQ(3, 1, 0, 1); SB();
  PQ(3, 1, 1, 0); SB();
  PQ(3, 1, 1, 1); SB();
  PQ(3, 1, 0, 2); SB();
  PQ(3, 1, 0, 3); SB();
  PQ(3, 1, 0, 7); SB();
  PQ(3, 1, 1, 2); SB();
  PQ(3, 1, 1, 3); SB();
  PQ(3, 1, 1, 7); SB();
  PQ(3, 1, 0, 4); SB();
  PQ(3, 1, 0, 5); SB();
  PQ(3, 1, 1, 4); SB();
  PQ(3, 1, 1, 5); SB();
  PQ(3, 1, 0, 6); SB();
  PQ(3, 1, 1, 6); SB();
  SHOT_GATE;
  T_LD(0);
  T_LD(1);
  T_LD(2);
  T_LD(3);
  T_LD(4);
  T_LD(5);
  T_LD(6);
  T_LD(7);
  T_LD(8);
  T_LD(9);
  T_LD(10);
  T_LD(11);
  T_LD(12);
  T_LD(13);
  T_LD(14);
  T_LD(15);
  if (AG_AT == 0) *LP(volatile unsigned, cw, W_AG) = one;
  SB();
  if (QP != 1) phi_fix(pq, qmin);
  STAMP(0, 3); CSTAMP(3);
  const unsigned tag = rfl(ltag);
  {
#define PF(mi) FRAG(((u32x4){pq[4 * (mi)], pq[4 * (mi) + 1], pq[4 * (mi) + 2], pq[4 * (mi) + 3]}))
#define H4(v, k) __builtin_bit_cast(f16x4_t, (u32x2){(v)[2 * (k)], (v)[2 * (k) + 1]})
    f32x4 s0 = zero, s1 = zero;
#define KV_MM(i0, i1)                                                                                      \
  _Pragma("unroll") for (int i = (i0); i < (i1); ++i) {                                                    \
    s0 = __builtin_amdgcn_mfma_f32_4x4x4f16(__builtin_bit_cast(f16x4_t, eye), H4(t[i], 0), s0, 0, 0, 0);   \
    s1 = __builtin_amdgcn_mfma_f32_4x4x4f16(__builtin_bit_cast(f16x4_t, eye), H4(t[i], 1), s1, 0, 0, 0);   \
  }
    float rc[4];
    u32x2 z0, z1;
#define Z_RD { z0 = *LP(volatile u32x2, zfa, 0); z1 = *LP(volatile u32x2, zfb, 0); }
    f32x4 d[4];
#define DEN1() do { const u32x4 zf = {z0[0], z0[1], z1[0], z1[1]}; _Pragma("unroll") for (int mi = 0; mi < 4; ++mi) d[mi] = MFMA(FRAG(zf), PF(mi), zero); } while (0)
#define DEN2()                                                                                             \
  do {                                                                                                     \
    _Pragma("unroll") for (int mi = 0; mi < 4; ++mi) rc[mi] = __builtin_amdgcn_rcpf(d[mi][0] + 1e-6f);     \
    _Pragma("unroll") for (int mi = 0; mi < 4; ++mi) { rc[mi] *= gq[mi]; PIN(rc[mi]); }                    \
  } while (0)
    KV_MM(0, DENAT - 4);
    SB();
    unsigned zfl = *LP(volatile unsigned, zfp, 0);
    CFENCE();
    Z_RD
    SB();
    KV_MM(DENAT - 4, DENAT);
    SB();
    DEN1();
    SB();
    KV_MM(DENAT, DENAT + 2);
    SB();
    DEN2();
    const u64 zbad = __builtin_amdgcn_ballot_w64(zfl != 1u);
    SB();
    KV_MM(DENAT + 2, 16);
    const u64 kbad = __builtin_amdgcn_ballot_w64(__builtin_isunordered(s0[0], s1[0]));
    if (__builtin_expect((kbad | zbad) != 0ull, 0)) {
      if (!ZS || kbad != 0ull) {
      if (ZS && !ZC) { TSOUT(11, rt_()); }
      unsigned todo = 15u;
#pragma unroll 1
      for (unsigned spins = 0; spins <= SPIN_CAP; ++spins) {
        unsigned miss = 0;
#pragma unroll
        for (int q = 0; q < 4; ++q)
          if (todo & (1u << q)) { if (!all_true(max(max(MX4(t[4 * q]), MX4(t[4 * q + 1])), max(MX4(t[4 * q + 2]), MX4(t[4 * q + 3]))) != ~0u)) miss |= 1u << q; }
        todo = miss;
        if (todo == 0u) break;
        __builtin_amdgcn_s_sleep(KL_NAP);
        CFENCE();
#pragma unroll
        for (int q = 0; q < 4; ++q)
          if (todo & (1u << q)) { T_LD(4 * q); T_LD(4 * q + 1); T_LD(4 * q + 2); T_LD(4 * q + 3); }
      }
      s0 = zero; s1 = zero;
      KV_MM(0, 16);
      }
      zfl = rfl(zfl);
      if (zfl == 0u) { TAKE(zfl = misc[C_ZF + hh], Z_RD); zfl = rfl(zfl); DEN1(); DEN2(); }
      if (zfl != 1u) {
        unsigned sc;
#pragma unroll 1
        for (unsigned spins = 0; spins <= SPIN_CAP; ++spins) {
          CFENCE();
          sc = __builtin_amdgcn_raw_buffer_load_b32(wsr, (unsigned)OFF_ZP + 65536 + (rec - mt + (c16 ^ kx)) * 16, 0, 16);
          if (all_true(sc != ~0u)) break;
        }
        s0 = zero; s1 = zero;
#pragma unroll
        for (int i = 0; i < 16; ++i) {
          const float s_ = u2f((unsigned)__builtin_amdgcn_readlane((int)sc, i));
#pragma unroll
          for (int e = 0; e < 4; ++e) {
            const unsigned tw = t[i][e];
            const f16x2_t h = __builtin_bit_cast(f16x2_t, tw);
            f32x4& d = e < 2 ? s0 : s1;
            d[2 * (e & 1)] = __builtin_fmaf((float)h[0], s_, d[2 * (e & 1)]); d[2 * (e & 1) + 1] = __builtin_fmaf((float)h[1], s_, d[2 * (e & 1) + 1]);
          }
        }
      }
      if (!TG && !ZS) { TSOUT(11, rt_()); }
    }
    STAMP(0, 4); CSTAMP(4);
    const u32x4 kown = {cvt_pk(s0[0], s0[1]), cvt_pk(s0[2], s0[3]), cvt_pk(s1[0], s1[1]), cvt_pk(s1[2], s1[3])};
    *LP(u32x2, x2w, 8) = (u32x2){kown[0], kown[1]}; *LP(u32x2, x2w, 0) = (u32x2){kown[2], kown[3]};
    CFENCE();
    *LP(volatile unsigned, cw, W_X2) = one;
    if (AG_AT == 1) *LP(volatile unsigned, cw, W_AG) = one;
    SB();
    f32x4 o[4];
    u32x4 koth;
#pragma unroll
    for (int mi = 0; mi < 4; ++mi) o[mi] = MFMA(FRAG(kown), PF(mi), zero);
    const unsigned x2r = x2w ^ 2048u;
    SB();
#define O_CVT(mi, h) do { ov[mi][h] = (u32x2){cvt_pk(o[mi][0] * rc[mi], o[mi][1] * rc[mi]), cvt_pk(o[mi][2] * rc[mi], o[mi][3] * rc[mi])}; if ((h) == 0) PIN(ov[mi][0]); } while (0)
    O_CVT(0, 0); SB();
    const unsigned f2 = *LP(volatile unsigned, cr, W_X2);
    CFENCE();
    koth = *LP(volatile u32x4, x2r, 0);
    SB();
    O_CVT(1, 0); O_CVT(2, 0); O_CVT(3, 0);
    SB();
    if (__builtin_expect(!all_true(f2 != 0u), 0)) TAKE(*LP(volatile unsigned, cr, W_X2), koth = *LP(volatile u32x4, x2r, 0));
    STAMP(0, 5); CSTAMP(5);
#pragma unroll
    for (int mi = 0; mi < 4; ++mi) o[mi] = MFMA(FRAG(koth), PF(mi), zero);
    SB();
    unsigned oom[4];
#pragma unroll
    for (int mi = 0; mi < 4; ++mi) { oom[mi] = oo + mi * (NH * 1024); PIN(oom[mi]); }
    SB();
#pragma unroll
    for (int mi = 0; mi < 4; ++mi) { O_CVT(mi, 1); __builtin_amdgcn_raw_buffer_store_b128((u32x4){ov[mi][0][0], ov[mi][0][1], ov[mi][1][0], ov[mi][1][1]}, wsr, oom[mi], 0, 16); SB(); if (OSF == mi + 1) { *LP(volatile unsigned, cw, W_AG) = 2u; SB(); } }
  }
  SB();
#define CTP15
#define CTP16
  f32x4 oc[4][2];
  unsigned xa[4], ra_[2], so_[4], rlo, rhi;
#pragma unroll
  for (int n = 0; n < 4; ++n) { xa[n] = (unsigned)(((32 * w + c16) * 128 + (((g >> 1) ^ (c16 >> 1)) * 16) + (g & 1) * 8) ^ (n * 32)); if (XL) PIN(xa[n]); }
  if (XL & 2) {
    u32x2 xl[8];
#define XL_RD { _Pragma("unroll") for (int i = 0; i < 8; ++i) xl[i] = *LP(volatile u32x2, xa[i & 3], (i >> 2) * 2048); }
    XL_RD
    const unsigned kw = w * 4096 + lane * 16, wlo = hh ? x1w : kw, whi = hh ? kw : x1w;
    rlo = hh ? x1w ^ 2048u : kw; rhi = hh ? kw : x1w ^ 2048u;
#pragma unroll
    for (int mi = 0; mi < 4; ++mi) *LP(volatile u32x4, mi < 2 ? wlo : whi, (mi & 1) * 1024) = (u32x4){ov[mi][0][0], ov[mi][0][1], ov[mi][1][0], ov[mi][1][1]};
    *LP(volatile unsigned, cw, W_X1) = 2u;
    unsigned fo = lane == 0 ? (unsigned)OFF_OFLAG + (unsigned)(mt * 16 + nt) * 128u : 0x80000000u, fa = L_CW + (lane & 3) * 128, two = 2u;
    PIN(fo); PIN(fa); PIN(two); PIN(ltag);
#pragma unroll
    for (int i = 0; i < 8; ++i) {
      if (i == PUB - 1) {
        SB();
        CTP15
        WAIT_VM(0);
        *LP(volatile unsigned, cw, W_KS) = two;
        if (all_true(*LP(volatile unsigned, fa, W_KS) == 2u)) __builtin_amdgcn_raw_buffer_store_b32(ltag, wsr, fo, 0, 16);
        CFENCE();
        STAMP(0, 6);
        CTP16
        SB();
      }
      const int tt = i >> 2;
      const float omg = bfr(1.0f - (tt ? (hh ? gq[3] : gq[1]) : (hh ? gq[2] : gq[0])));
#pragma unroll
      for (int k = 0; k < 2; ++k) {
        const unsigned pr = cvt_pk(omg * blo(xl[i][k]), omg * bhi(xl[i][k]));
        oc[i & 3][tt][2 * k] = blo(pr); oc[i & 3][tt][2 * k + 1] = bhi(pr);
      }
      PIN(oc[i & 3][tt]);
    }
  }
  if (XL & 1) {
#pragma unroll
    for (int i = 0; i < 2; ++i) { ra_[i] = (unsigned)((32 * w + 8 * i + (lane >> 3)) * 128 + (((lane & 7) ^ (4 * i + g)) * 16)); PIN(ra_[i]); }
#pragma unroll
    for (int i = 0; i < 4; ++i) { so_[i] = (unsigned)(((m0 + 32 * w + 8 * i + (lane >> 3)) * DM + 64 * nt3) * 2 + (lane & 7) * 16); PIN(so_[i]); }
  }
  if (!(XL & 2))
#pragma unroll
  for (int i = 0; i < 4; ++i) {
    const int tt = i >> 1, n = 2 * (i & 1);
    const float omg = bfr(1.0f - (tt ? (hh ? gq[3] : gq[1]) : (hh ? gq[2] : gq[0])));
#pragma unroll
    for (int k = 0; k < 2; ++k) {
      const auto sw = __builtin_amdgcn_permlane16_swap(cvt_pk(omg * blo(xv[i][k]), omg * bhi(xv[i][k])),
                                                       cvt_pk(omg * blo(xv[i][2 + k]), omg * bhi(xv[i][2 + k])), false, false);
      oc[n][tt][2 * k] = blo(sw[0]); oc[n][tt][2 * k + 1] = bhi(sw[0]);
      oc[n + 1][tt][2 * k] = blo(sw[1]); oc[n + 1][tt][2 * k + 1] = bhi(sw[1]);
    }
    PIN(oc[n][tt]); PIN(oc[n + 1][tt]);
  }
  int wof[2][2][4];
#pragma unroll
  for (int n = 0; n < 16; ++n) {
    wof[n >> 3][(n >> 2) & 1][n & 3] = 16384 + ((n >> 2) & 1) * 65536 + (((n >> 3) ^ wr) * 16 + 4 * g + (c16 >> 2)) * 128 + (((n & 3) ^ fsw) * 32) + (c16 & 3) * 8;
    PIN(wof[n >> 3][(n >> 2) & 1][n & 3]);
  }
  const unsigned obase = (unsigned)OFF_OF + (unsigned)(((mt3 * 8 + 2 * w) * NH) * 1024 + lane * 16);
  bf16x8 wf[2][4], wl[4];
  s16x4 tl2;
#define W_AT(hd, n, r) (smem + wof[r][WOPOS((hd) >> 1) >> 3][n] + (WOPOS((hd) >> 1) & 7) * 8192 + ((hd) & 1) * 4096)
  unsigned lw = min(misc[C_WO], misc[C_WO + 1]);
  unsigned ob0 = obase, ob1 = obase + NH * 1024;
  int cs1 = CH(1) * 2048, cs2 = CH(2) * 2048, cs3 = CH(3) * 2048;
  PIN(ob0); PIN(ob1); asm volatile("" : "+s"(cs1), "+s"(cs2), "+s"(cs3));
  SB();
  if (!PUB && (!TG || SH_DR)) WAIT_VM(0);
  if (!NODRAIN && !(TR & 1) && lane == 0) {
    const unsigned old = __hip_atomic_fetch_add((lu32*)(smem + L_CTL) + C_OARR, 1u, RLX_WG);
    if (old == 3u) {
      __hip_atomic_store((gu32*)(ws + OFF_OFLAG + mt * 2048 + nt * ((TA & 4) ? 4 : 128)), tag, RLX_AGENT);
      misc[C_T6] = (unsigned)__builtin_amdgcn_s_memrealtime() | 1u;
    }
  }
  CFENCE();
  if (!PUB) { STAMP(0, 6); CSTAMP(6); }

  if (__builtin_expect(rfl(lw) == 0u, 0)) { (void)wait_ge<1>(misc + C_WO, 1u); (void)wait_ge<1>(misc + C_WO + 1, 1u); }
  CFENCE();
#pragma unroll
  for (int n = 0; n < 4; ++n) wf[0][n] = CAT(TRD(W_AT(0, n, 0)), TRD(W_AT(0, n, 1)));
  u32x4 of[OPF + 1][4];
  unsigned rdy = 0;
#define O_READY(c) do { if (__builtin_expect(rdy <= (unsigned)(c), 0)) rdy = wait_ge<(TR & 4) ? 0 : 1>(misc + C_ORDY, (unsigned)(c) + 1u); CFENCE(); } while (0)
#define O_LD1(c, k4) of[(c) % (OPF + 1)][k4] = __builtin_amdgcn_raw_buffer_load_b128(wsr, (((k4) & 1) ? ob1 : ob0) + ((k4) >> 1) * 1024, (c) == 1 ? cs1 : (c) == 2 ? cs2 : (c) == 3 ? cs3 : CH(c) * 2048, 16)
  TAKE(*LP(volatile unsigned, cw ^ 128u, W_X1) >> 1, { _Pragma("unroll") for (int k = 0; k < 4; ++k) of[0][k] = *LP(volatile u32x4, k < 2 ? rlo : rhi, (k & 1) * 1024); });
  STAMP(0, 7); CSTAMP(7);
  unsigned li = 0;
#define P3_MM(c, j)                                                                                        \
  do {                                                                                                     \
    constexpr int hd = 2 * (c) + ((j) >> 3), n = ((j) >> 1) & 3;                                           \
    constexpr int tt_ = P3ORD ? (((j) ^ ((j) >> 1)) & 1) : ((j) & 1);                                     \
    AMFMA(oc[n][tt_], wf[(j) >> 3][n], of[(c) % (OPF + 1)][2 * ((j) >> 3) + tt_]); SB();                   \
    if (hd + 1 < 32) {                                                                                     \
      if (((j) & 1) == 0) tlo = TRD(W_AT((hd + 1) & 31, n, 0)); else wf[1 - ((j) >> 3)][n] = CAT(tlo, TRD(W_AT((hd + 1) & 31, n, 1))); \
    }                                                                                                      \
    if ((c) + OPF < 16 && !(OWN && (c) == 0)) {                                                            \
      if ((j) == 0) O_READY((c) + OPF);                                                                    \
      if (((j) & 3) == 1) O_LD1((c) + OPF, (ES && (c) + OPF == 15) ? (((j) >> 3) | (((j) >> 1) & 2)) : (j) >> 2);                                                      \
    }                                                                                                      \
    if (OWN && (c) == 0 && (j) == OWN - 1) { _Pragma("unroll") for (int c_ = 1; c_ <= OPF; ++c_) { O_READY(c_); O_LD1(c_, 0); SB(); O_LD1(c_, 1); SB(); O_LD1(c_, 2); SB(); O_LD1(c_, 3); SB(); } STAMP(0, 7); } \
    if (ES && (c) == 14 && (j) < 8) { if (((j) & 1) == 0) tl2 = TRD(W_AT(31, n, 0)); else wl[n] = CAT(tl2, TRD(W_AT(31, n, 1))); } \
    if ((c) == 15 - ES && (j) == 8) li = misc[C_IDX];                                                           \
    SB();                                                                                                  \
  } while (0)
#define P3_4(c, j) P3_MM(c, j); P3_MM(c, (j) + 1); P3_MM(c, (j) + 2); P3_MM(c, (j) + 3)
#define P3_C(c) P3_4(c, 0); P3_4(c, 4); P3_4(c, 8); P3_4(c, 12)
  P3_C(0); P3_C(1); P3_C(2); P3_C(3); P3_C(4); P3_C(5); P3_C(6); P3_C(7);
  P3_C(8); P3_C(9); P3_C(10); P3_C(11); P3_C(12); P3_C(13); P3_C(14);
  STAMP(0, 9); CSTAMP(9);
  li = rfl(li);
  if (__builtin_expect(li == 0u, 0)) li = wait_ge<1>(misc + C_IDX, 1u);
  const int idx = (int)li - 1;
  if ((idx >> 5) == mt3 * 4 + w) {
    f32x4 dl[4];
    if (ES) NOP8(oc);
    TAKE(misc[C_Y], { _Pragma("unroll") for (int n = 0; n < 4; ++n) dl[n] = *(volatile lf32x4*)(smem + L_RED + 2048 + n * 64 + g * 16); });
    const bool mine = c16 == (idx & 15);
    if (idx & 16) {
#pragma unroll
      for (int n = 0; n < 4; ++n) oc[n][1] += mine ? dl[n] : zero;
    } else {
#pragma unroll
      for (int n = 0; n < 4; ++n) oc[n][0] += mine ? dl[n] : zero;
    }
    if (ES) NOP8(oc);
  }
  {
    u32x4 o[4];
    const __amdgpu_buffer_rsrc_t outr = MKRSRC(((NOX & 4096) && (nt3 & 7) == 3) ? ws + (32 << 20) + 128 : (char*)out, SEQ * DM * 2);
#define E_MM(tt, h, n) do { AMFMA(oc[n][tt], (h) ? wl[n] : wf[0][n], of[15 % (OPF + 1)][2 * (h) + (tt)]); SB(); } while (0)
#define E_WR(tt, n) do { *LP(volatile u32x2, xa[n], (tt) * 2048) = (u32x2){cvt_pk(oc[n][tt][0], oc[n][tt][1]), cvt_pk(oc[n][tt][2], oc[n][tt][3])}; SB(); } while (0)
#define E_RD(tt) do { o[2 * (tt)] = *LP(volatile u32x4, ra_[0], (tt) * 2048); o[2 * (tt) + 1] = *LP(volatile u32x4, ra_[1], (tt) * 2048); SB(); } while (0)
#define E_ST(i) do { __builtin_amdgcn_raw_buffer_store_b128(o[i], outr, so_[i], 0, OAUX); SB(); } while (0)
#define E_NOP(k, tt, n) asm volatile("s_nop " #k : "+v"(oc[n][tt]))
    SB();
    E_MM(0, 0, 0); E_MM(0, 0, 1); E_MM(0, 0, 2); E_MM(0, 0, 3); E_MM(0, 1, 0); E_MM(0, 1, 1); E_MM(0, 1, 2); E_MM(0, 1, 3);
    E_NOP(4, 0, 0); E_WR(0, 0); E_WR(0, 1);
    E_MM(1, 0, 0); E_WR(0, 2); E_MM(1, 0, 1); E_WR(0, 3); E_MM(1, 0, 2); E_RD(0); E_MM(1, 0, 3);
    E_MM(1, 1, 0); E_MM(1, 1, 1); E_MM(1, 1, 2); E_MM(1, 1, 3);
    if (!ES2) { E_ST(0); E_ST(1); E_NOP(2, 1, 0); } else E_NOP(4, 1, 0);
    E_WR(1, 0); E_WR(1, 1); if (ES2 == 2) E_ST(0); E_WR(1, 2); E_WR(1, 3); E_RD(1);
    if (ES2 == 1) E_ST(0);
    if (ES2) E_ST(1);
    E_ST(2); E_ST(3);
  }
  if (ES) { } else if (XL & 1) {
    u32x4 o[4];
#pragma unroll
    for (int tt = 0; tt < 2; ++tt) {
#pragma unroll
      for (int n = 0; n < 4; ++n) *LP(volatile u32x2, xa[n], tt * 2048) = (u32x2){cvt_pk(oc[n][tt][0], oc[n][tt][1]), cvt_pk(oc[n][tt][2], oc[n][tt][3])};
      o[2 * tt] = *LP(volatile u32x4, ra_[0], tt * 2048); o[2 * tt + 1] = *LP(volatile u32x4, ra_[1], tt * 2048);
    }
#pragma unroll
    for (int i = 0; i < 4; ++i) __builtin_amdgcn_raw_buffer_store_b128(o[i], MKRSRC(out, SEQ * DM * 2), so_[i], 0, OAUX);
  } else
#pragma unroll
  for (int i = 0; i < 4; ++i) {
    const int tt = i >> 1, n = 2 * (i & 1);
    u32x4 o;
#pragma unroll
    for (int k = 0; k < 2; ++k) {
      const auto sw = __builtin_amdgcn_permlane16_swap(cvt_pk(oc[n][tt][2 * k], oc[n][tt][2 * k + 1]),
                                                       cvt_pk(oc[n + 1][tt][2 * k], oc[n + 1][tt][2 * k + 1]), false, false);
      o[k] = sw[0]; o[2 + k] = sw[1];
    }
    if (OPAT & 1) __builtin_amdgcn_raw_buffer_store_b128(o, MKRSRC(out, SEQ * DM * 2), (unsigned)(((m0 + 32 * w + 8 * i + (lane >> 3)) * DM + 64 * nt3) * 2 + (lane & 7) * 16), 0, OAUX);
    else if (OPAT & 4) __builtin_amdgcn_raw_buffer_store_b128(o, MKRSRC(out, SEQ * DM * 2), (unsigned)(((m0 + 32 * w + 8 * i + (lane & 7)) * DM + 64 * nt3) * 2 + (lane >> 3) * 16), 0, OAUX);
    else
    __builtin_amdgcn_raw_buffer_store_b128(o, MKRSRC(out, SEQ * DM * 2), eoff + tt * (32 * DM) + (i & 1) * 64, 0, OAUX);
  }
  if (ENDW) WAIT_VM(0);
  STAMP(0, 10); CSTAMP(10);
}

__global__ void probe_delay(const u64* ts) {
  __shared__ unsigned v[1024];
  __shared__ u64 t0s[256];
  const int t = threadIdx.x;
  if (t < 256) { u64 t0 = ~0ull; for (int i = 0; i < 8; ++i) { const u64 a = ts[(t * 16 + i) * 16]; if (a < t0) t0 = a; } t0s[t] = t0; }
  __syncthreads();
  u64 t0 = ~0ull; for (int i = 0; i < 256; ++i) t0 = t0s[i] < t0 ? t0s[i] : t0;
  const int b = t >> 2, w = 0 + (t & 3) % 4;
  u64 a = ts[(b * 16 + w) * 16 + 10];
  v[t] = (a >= t0 && a < t0 + 20000) ? (unsigned)(a - t0) : 0xFFFFFFFFu;
  __syncthreads();
  if (t == 0) {
    unsigned mn = 0xFFFFFFFFu, mx = 0; u64 sum = 0; int n = 0;
    for (int i = 0; i < 1024; ++i) if (v[i] != 0xFFFFFFFFu) { mn = v[i] < mn ? v[i] : mn; mx = v[i] > mx ? v[i] : mx; sum += v[i]; ++n; }
    const u64 val = 2 == 0 ? (u64)mn * 20 : (2 == 1 ? sum * 20 / (n ? n : 1) : (u64)mx * 20);
    const u64 end = t0 + 8000 + val;
    const u64 s0 = wall_clock64();
    while (wall_clock64() < end && wall_clock64() - s0 < 100000) { }
  }
}

extern "C" void kernel_launch(void* const* d_in, const int* in_sizes, int n_in,
                              void* d_out, int out_size, void* d_ws, size_t ws_size,
                              hipStream_t stream) {
  char* ws = (char*)d_ws;
  (void)hipFuncSetAttribute((const void*)fused_kernel, hipFuncAttributeMaxDynamicSharedMemorySize, L_TOTAL);
  fused_kernel<<<256, 512, L_TOTAL, stream>>>((const u16*)d_in[0], (const u16*)d_in[1], (const u16*)d_in[2], ws, (u16*)d_out,
                                              (const u16*)d_in[4], (const u16*)d_in[8], (const u16*)d_in[5], (const u16*)d_in[6],
                                              (const u16*)d_in[7]);
  probe_delay<<<1, 1024, 0, stream>>>((const u64*)(ws + OFF_TS));
}
```

```cpp
#include <hip/hip_runtime.h>
#include <hip/hip_bf16.h>
#include <stdint.h>

typedef unsigned short u16;
typedef unsigned long long u64;
typedef __attribute__((ext_vector_type(8))) short bf16x8;
typedef __attribute__((ext_vector_type(4))) short s16x4;
typedef __attribute__((ext_vector_type(4))) float f32x4;
typedef __attribute__((ext_vector_type(2))) float f32x2;
typedef __attribute__((ext_vector_type(2))) __bf16 bf16x2_t;
typedef __attribute__((ext_vector_type(2))) _Float16 f16x2_t;
typedef __attribute__((ext_vector_type(4))) _Float16 f16x4_t;
typedef __attribute__((ext_vector_type(4))) unsigned int u32x4;
typedef __attribute__((ext_vector_type(2))) unsigned int u32x2;
typedef __attribute__((ext_vector_type(8))) unsigned int u32x8;
typedef __attribute__((address_space(3))) s16x4* lds_s16x4_ptr;
typedef __attribute__((address_space(1))) unsigned gu32;
typedef __attribute__((address_space(1))) u64 gu64;
typedef __attribute__((address_space(3))) unsigned lu32;
typedef __attribute__((address_space(3))) u32x2 lu32x2;
typedef __attribute__((address_space(3))) u32x4 lu32x4;
typedef __attribute__((address_space(3))) f32x4 lf32x4;
#define RLX_AGENT __ATOMIC_RELAXED, __HIP_MEMORY_SCOPE_AGENT

#if defined(__HIP_DEVICE_COMPILE__)
#define NOPK __attribute__((target("no-packed-fp32-ops")))
#else
#define NOPK
#endif

#define SEQ 2048
#define DM 1024
#define N1 3072
#define FF 3584
#define NH 32

#define OFF_EPOCH  ((size_t)0)
#define OFF_RG     ((size_t)4096)
#define OFF_GG     ((size_t)16384)
#define OFF_GU     ((size_t)262144)
#define OFF_YG     ((size_t)524288)
#define OFF_KVP    ((size_t)1 << 20)
#define OFF_ZP     ((size_t)2 << 20)
#define ZSC        65536u
#define OFF_OFLAG  (((size_t)3 << 20) + 65536)
#define OFF_OF     ((size_t)4 << 20)

#define SPIN_CAP (1u << 18)

__device__ __forceinline__ float u2f(unsigned u) { return __builtin_bit_cast(float, u); }
__device__ __forceinline__ unsigned f2u(float f) { return __builtin_bit_cast(unsigned, f); }
__device__ __forceinline__ bool all_true(bool ok) { return __builtin_amdgcn_ballot_w64(!ok) == 0ull; }
__device__ __forceinline__ unsigned cvt_pk(float a, float b) {
  f32x2 v = {a, b};
  return __builtin_bit_cast(unsigned, __builtin_convertvector(v, bf16x2_t));
}
__device__ __forceinline__ float blo(unsigned u) { return u2f(u << 16); }
__device__ __forceinline__ float bhi(unsigned u) { return u2f(u & 0xFFFF0000u); }
__device__ __forceinline__ float bfr(float f) { return blo(cvt_pk(f, f)); }
__device__ __forceinline__ float fmax_raw(float a, float b) { return __builtin_elementwise_maximum(a, b); }
__device__ __forceinline__ unsigned eps2(unsigned p) { return cvt_pk(blo(p) + 1e-6f, bhi(p) + 1e-6f); }
template <int N>
__device__ __forceinline__ void phi_fix(unsigned (&p)[N], float umin) {
  if (__builtin_expect(__builtin_amdgcn_ballot_w64(umin < -5.9f) != 0ull, 0)) {
#pragma unroll
    for (int i = 0; i < N; ++i) p[i] = eps2(p[i]);
  }
}
__device__ __forceinline__ float sigmoid_f(float x) { return 1.0f / (1.0f + __expf(-x)); }

template <int CTRL, int RM = 0xf> __device__ __forceinline__ int dppi(int old, int v) {
  return __builtin_amdgcn_update_dpp(old, v, CTRL, RM, 0xf, false);
}
template <int CTRL, int RM = 0xf> __device__ __forceinline__ float dppf(float v) {
  return __builtin_bit_cast(float, dppi<CTRL, RM>(0, __builtin_bit_cast(int, v)));
}
__device__ __forceinline__ unsigned wave_max(unsigned v) {
  v = max(v, (unsigned)dppi<0x111>(0, (int)v)); v = max(v, (unsigned)dppi<0x112>(0, (int)v));
  v = max(v, (unsigned)dppi<0x114>(0, (int)v)); v = max(v, (unsigned)dppi<0x118>(0, (int)v));
  v = max(v, (unsigned)dppi<0x142, 0xa>(0, (int)v)); v = max(v, (unsigned)dppi<0x143, 0xc>(0, (int)v));
  return (unsigned)__builtin_amdgcn_readlane((int)v, 63);
}
__device__ __forceinline__ float merge32(float a, float b) {
  const auto r = __builtin_amdgcn_permlane32_swap(f2u(a), f2u(b), false, false);
  return u2f(r[0]) + u2f(r[1]);
}
__device__ __forceinline__ float merge16(float a, float b) {
  const auto r = __builtin_amdgcn_permlane16_swap(f2u(a), f2u(b), false, false);
  return u2f(r[0]) + u2f(r[1]);
}
typedef __attribute__((address_space(3))) void* ldsp;
template <int IMM>
__device__ __forceinline__ void bufl16(__amdgpu_buffer_rsrc_t r, unsigned l, unsigned voff, unsigned soff) {
#if defined(__HIP_DEVICE_COMPILE__)
  __builtin_amdgcn_raw_ptr_buffer_load_lds(r, (ldsp)(uintptr_t)l, 16, voff, soff, IMM, 0);
#endif
}
#define LP(T, base, imm) ((__attribute__((address_space(3))) T*)(uintptr_t)(unsigned)((base) + (imm)))
#define MKRSRC(p, n) __builtin_amdgcn_make_buffer_rsrc((void*)(p), 0, (n), 0x00020000)
#define WAIT_VM(n) asm volatile("s_waitcnt vmcnt(%0)" ::"n"(n) : "memory")
#define WAIT_LGKM0() asm volatile("s_waitcnt lgkmcnt(0)" ::: "memory")
#define BAR() do { __builtin_amdgcn_sched_barrier(0); asm volatile("s_barrier" ::: "memory"); __builtin_amdgcn_sched_barrier(0); } while (0)
#define CFENCE() asm volatile("" ::: "memory")
#define MFMA(a, b, c) __builtin_amdgcn_mfma_f32_16x16x32_bf16((a), (b), (c), 0, 0, 0)
#define PIN(v) asm volatile("" : "+v"(v))
#define AMFMA(acc, a, b) asm volatile("v_mfma_f32_16x16x32_bf16 %0, %1, %2, %0" : "+v"(acc) : "v"(a), "v"(b))
#define NOP8(t) asm volatile("s_nop 7" : "+v"(t[0][0]), "+v"(t[0][1]), "+v"(t[1][0]), "+v"(t[1][1]), "+v"(t[2][0]), "+v"(t[2][1]), "+v"(t[3][0]), "+v"(t[3][1]))
#define SB() __builtin_amdgcn_sched_barrier(0)
#define TRD(p) ((s16x4)__builtin_amdgcn_ds_read_tr16_b64_v4i16((lds_s16x4_ptr)(p)))
#define CAT(lo, hi) __builtin_shufflevector(lo, hi, 0, 1, 2, 3, 4, 5, 6, 7)
#define FRAG(x) __builtin_bit_cast(bf16x8, x)

#define PACE   57
#define BURST  30
#define R_AT   2
#define R_GAP  2
#define CPF_N  20
#define WO_E   6
#define WO_ES  4
#define WO_M   9
#define G_T    35
#define P5_T   30
#define GU_AH  8
#define GU_E   4
#define DN_E   7
#define DENAT  12
#define OPF    3

__device__ __forceinline__ unsigned rfl(unsigned v) { return (unsigned)__builtin_amdgcn_readfirstlane((int)v); }
template <int SLEEP>
__device__ __forceinline__ unsigned wait_ge(volatile lu32* p, unsigned need) {
  unsigned v = 0;
#pragma unroll 1
  for (unsigned spins = 0; spins < (1u << 22); ++spins) {
    v = rfl(*p);
    if (v >= need) break;
    if (SLEEP) __builtin_amdgcn_s_sleep(SLEEP);
  }
  CFENCE();
  return v;
}

#define SLOT     40960
#define L_X1     147456
#define L_X2     155648
#define L_CW     159744
#define L_ZF     160256
#define L_GATE   160384
#define L_RED    160896
#define L_H      163200
#define L_CTL    163712
#define L_TOTAL  163840
#define W_X1   64
#define W_X2   68
#define W_AG   72
#define W_KS   76
#define WFLAGS(f) (*(volatile lu32*)(smem + L_CW + (lane & 3) * 128 + (f)))
#define WOPOS(c) ((c) < 3 ? 13 + (c) : (c) < 8 ? (c) : (c) < 11 ? (c) - 8 : (c) - 3)
#define C_F15  0
#define C_WO   2
#define C_ZF   4
#define C_TAG  8
#define C_IDX  10
#define C_GO   12
#define C_ORDY 18
#define C_GU   19
#define C_DN   20
#define C_Y    23

NOPK __global__ __launch_bounds__(512, 2) void fused_kernel(const u16* __restrict__ x, const u16* __restrict__ wqkv,
                                                            const u16* __restrict__ wo, char* ws, u16* __restrict__ out,
                                                            const u16* __restrict__ w_r, const u16* __restrict__ gate_w,
                                                            const u16* __restrict__ w_gate, const u16* __restrict__ w_up,
                                                            const u16* __restrict__ w_down) {
  extern __shared__ __attribute__((aligned(16))) char smem[];
  asm volatile("s_load_dwordx2 s[100:101], %0, 60" :: "s"(__builtin_amdgcn_kernarg_segment_ptr()) : "s100", "s101");
  SB();
  const int tid = threadIdx.x, lane = tid & 63;
  const int w = __builtin_amdgcn_readfirstlane(tid >> 6);
  u64 pc0;
  asm volatile("s_getpc_b64 %0" : "=s"(pc0));
  const int bid = blockIdx.x, xcd = bid & 7, ix = bid >> 3;
  const int ml = ix >> 2, nl = ix & 3;
  const int mt = (xcd >> 2) * 8 + ml, nt = (xcd & 3) * 4 + nl;
  const int m0 = mt * 128;
  const int mt3 = mt, nt3 = nt ^ 1;
#define CH(c) (((4 * (xcd & 3) + (c)) & 15) ^ ((c) < 4 ? nl : 0))
  volatile lu32* misc = (volatile lu32*)(smem + L_CTL);

  if (__builtin_expect((w >> 1) == 2, 1)) {
    u32x8 ka;
    asm volatile("s_load_dwordx8 %0, %1, 0x0" : "=s"(ka) : "s"(__builtin_amdgcn_kernarg_segment_ptr()));
    SB();
    const int p = w - 4;
    unsigned va[4], vb[4], vw[4];
#pragma unroll
    for (int j = 0; j < 4; ++j) {
      const int row = 32 * (w & 1) + 8 * j + (lane >> 3);
      va[j] = (unsigned)((m0 + row) * (DM * 2) + (((lane & 7) ^ ((row >> 1) & 7)) * 16) - 1024 * j);
      vb[j] = (unsigned)(row * (N1 * 2) + 128 * nt + (((lane & 7) ^ ((row & 2) | ((j & 1) << 2))) * 16) - 1024 * j);
      PIN(va[j]); PIN(vb[j]);
    }
    unsigned l0 = (unsigned)(uintptr_t)(ldsp)smem + p * 4096;
    int kf = (4 * ml + 4 * (xcd & 3)) & 15, ks = (4 * ml - 2 * (ml >> 2) + 4 * (xcd & 3)) & 15, sres = (3 - nt) & 7;
    asm volatile("" : "+s"(l0), "+s"(kf), "+s"(ks), "+s"(sres));
    SB();
    asm volatile("s_waitcnt lgkmcnt(0)" : "+s"(ka));
    const u16* x = (const u16*)(((u64)ka[1] << 32) | ka[0]);
    const u16* wqkv = (const u16*)(((u64)ka[3] << 32) | ka[2]);
    const u16* wo = (const u16*)(((u64)ka[5] << 32) | ka[4]);
    char* ws = (char*)(((u64)ka[7] << 32) | ka[6]);
    int ko_ = (int)((ka[2] >> 7) & 7u) == sres ? ks : kf;
    const __amdgpu_buffer_rsrc_t ra = MKRSRC(x, SEQ * DM * 2), rb = MKRSRC(wqkv, DM * N1 * 2), rw = MKRSRC(wo, DM * DM * 2);
#define UNIT(r, v, off, so) do { bufl16<0>(r, l0 + (off), v[0], so); bufl16<1024>(r, l0 + (off), v[1], so); bufl16<2048>(r, l0 + (off), v[2], so); bufl16<3072>(r, l0 + (off), v[3], so); } while (0)
#define ISSUE(t)                                                                                           \
  do {                                                                                                     \
    const int k_ = (ko_ + (t)) & 15;                                                                       \
    UNIT(ra, va, ((t) % 3) * SLOT, k_ * 128); UNIT(ra, va, ((t) % 3) * SLOT + 8192, k_ * 128 + 64 * DM * 2); \
    _Pragma("unroll") for (int s_ = 0; s_ < 3; ++s_) UNIT(rb, vb, ((t) % 3) * SLOT + 16384 + s_ * 8192, k_ * (64 * N1 * 2) + s_ * (DM * 2)); \
  } while (0)
#define PACED_BAR()                                                                                        \
  do {                                                                                                     \
    if (p == 0) {                                                                                          \
      unsigned now;                                                                                        \
      do { now = (unsigned)__builtin_amdgcn_s_memrealtime(); } while ((int)(now - tp) < 0);                \
      tp = ((int)(now - tp) > BURST ? now - BURST : tp) + PACE;                                            \
    }                                                                                                      \
    BAR();                                                                                                 \
  } while (0)
    ISSUE(0); ISSUE(1);
    WAIT_VM(20);
    BAR();
    ISSUE(2);
    unsigned tp = (unsigned)__builtin_amdgcn_s_memrealtime() - PACE;
#pragma unroll
    for (int j = 0; j < 4; ++j) vw[j] = (unsigned)((32 * p + 8 * j + (lane >> 3)) * (DM * 2) + 128 * nt3 + (((lane & 7) ^ ((lane >> 3) & 6)) * 16) - 1024 * j);
#define WO_CHUNK(c) UNIT(rw, vw, 16384 + WOPOS(c) * 8192, CH(c) * (64 * DM * 2))
#pragma unroll
    for (int t = 0; t < 13; ++t) {
      WAIT_VM(20);
      PACED_BAR();
      asm volatile("" : "+s"(l0), "+s"(ko_));
      ISSUE(t + 3);
    }
    WO_CHUNK(0); WO_CHUNK(1); WO_CHUNK(2);
    SB();
    const bool wslow = ((((uintptr_t)wo >> 7) + nt3) & 7) == 3;
    const int woe = wslow ? WO_ES : WO_E, wom = wslow ? WO_M + 1 : WO_M;
    WAIT_VM(32);
    PACED_BAR();
    WAIT_VM(12);
    if (lane == 0) misc[C_F15 + p] = 1u;
#pragma unroll 1
    for (int c = 3; c < woe; ++c) WO_CHUNK(c);
    {
      const __amdgpu_buffer_rsrc_t zr = MKRSRC(ws + OFF_ZP, NH * 2 * 16 * 128);
      const unsigned zo = (unsigned)((2 * nt + p) * 2048 + lane * 16);
      u32x4 zw[2];
      unsigned zm;
      (void)wait_ge<1>(misc + C_IDX + p, 1u);
#pragma unroll 1
      for (unsigned spins = 0; spins < (1u << 22) && !all_true(WFLAGS(W_KS) != 0u); ++spins) __builtin_amdgcn_s_sleep(1);
      CFENCE();
      const unsigned tk = (unsigned)__builtin_amdgcn_s_memrealtime();
#pragma unroll 1
      for (int c = woe; c < wom; ++c) WO_CHUNK(c);
      UNIT(ra, va, 0, nt3 * 128); UNIT(ra, va, 8192, nt3 * 128 + 64 * DM * 2);
      while ((unsigned)__builtin_amdgcn_s_memrealtime() - tk < (unsigned)G_T) __builtin_amdgcn_s_sleep(1);
#define MX4(v) max(max((v)[0], (v)[1]), max((v)[2], (v)[3]))
#pragma unroll 1
      for (unsigned spins = 0; spins <= SPIN_CAP; ++spins) {
        CFENCE();
        zw[0] = __builtin_amdgcn_raw_buffer_load_b128(zr, zo, 0, 16); zw[1] = __builtin_amdgcn_raw_buffer_load_b128(zr, zo + 1024, 0, 16);
        SB(); misc[C_GO + p] = 1u; SB();
        zm = max(MX4(zw[0]), MX4(zw[1]));
        if (all_true(zm != ~0u)) break;
        __builtin_amdgcn_s_sleep(2);
      }
      const bool plain = all_true((int)zm >= 0);
      if (__builtin_expect(!plain, 0)) { zw[0] &= 0x7FFFFFFFu; zw[1] &= 0x7FFFFFFFu; }
      float z0 = merge16(merge32(u2f(zw[0][0]), u2f(zw[0][2])), merge32(u2f(zw[1][0]), u2f(zw[1][2])));
      float z1 = merge16(merge32(u2f(zw[0][1]), u2f(zw[0][3])), merge32(u2f(zw[1][1]), u2f(zw[1][3])));
      z0 += dppf<0x128>(z0); z1 += dppf<0x128>(z1); z0 += dppf<0x124>(z0); z1 += dppf<0x124>(z1);
      *(unsigned*)(smem + L_ZF + p * 64 + (lane & 3) * 16 + ((lane >> 4) & 1) * 8 + (lane >> 5) * 4) = cvt_pk(z0, z1);
      CFENCE();
      misc[C_ZF + p] = plain ? 1u : 2u;
    }
#pragma unroll 1
    for (unsigned spins = 0; spins < (1u << 22) && !all_true(WFLAGS(W_AG) != 0u); ++spins) __builtin_amdgcn_s_sleep(2);
    CFENCE();
#pragma unroll 1
    for (int c = wom; c < 16; ++c) WO_CHUNK(c);
    WAIT_VM(0);
    if (lane == 0) misc[C_WO + p] = 1u;
#undef ISSUE
#undef PACED_BAR
    if (p == 0) return;
    const unsigned tag = rfl(misc[C_TAG]);
    gu32* of = (gu32*)(ws + OFF_OFLAG + (mt3 * 16 + CH(lane & 15)) * 128);
#pragma unroll 1
    for (unsigned spins = 0; spins < (1u << 22) && !all_true(WFLAGS(W_KS) == 2u); ++spins) __builtin_amdgcn_s_sleep(1);
    const unsigned t6 = (unsigned)__builtin_amdgcn_s_memrealtime();
    while ((unsigned)__builtin_amdgcn_s_memrealtime() - t6 < (unsigned)P5_T) __builtin_amdgcn_s_sleep(1);
#pragma unroll 1
    for (unsigned spins = 0;; ++spins) {
      const unsigned f = __hip_atomic_load(of, RLX_AGENT);
      const unsigned miss = (unsigned)__builtin_amdgcn_ballot_w64(f != tag) & 0xFFFEu;
      const unsigned m = (unsigned)__builtin_ctz((spins > SPIN_CAP ? 0u : miss) | 0x10000u);
      misc[C_ORDY] = m;
      if (m == 16u) break;
      __builtin_amdgcn_s_sleep(2);
    }
    return;
  }

  if (__builtin_expect(w >= 6, 0)) {
    const int pa = w - 6;
#define CODE_PF(on) __builtin_amdgcn_raw_buffer_load_b32(MKRSRC((pc0 & ~255ull) + (blockIdx.x >> 3) * (CPF_N * 64u), (on) ? CPF_N * 64 : 0), lane * 64, 0, 0)
    unsigned cpf = CODE_PF(w == 7), cpf2;
    SB();
    float* red = (float*)(smem + L_RED);
    const bool help = bid >= 224, gemv = bid < 252;
    const int gmat = bid & 1;
    const u16* wgu = gmat ? w_up : w_gate;
    const int sres = (int)(3u - (unsigned)((uintptr_t)wgu >> 7)) & 7;
    const int gfs = help ? sres + 8 * (((bid - 224) >> 1) % 7) : (bid >> 1) % 56;
    const int gdh = (help ? ((bid - 224) >> 1) / 7 : (bid >> 1) / 56) & 1;
    const bool act0 = !help, act1 = gemv && (help || (gfs & 7) != sres);
    const int gd0 = gdh * 512 + 256 * pa + 32 * (lane >> 3);
    const __amdgpu_buffer_rsrc_t rgu0 = MKRSRC(wgu, act0 ? DM * FF * 2 : 0), rgu1 = MKRSRC(wgu, act1 ? DM * FF * 2 : 0);
    const unsigned wpo = (unsigned)((gd0 * FF + gfs * 64 + 8 * (lane & 7)) * 2);
    u32x4 wv[32], wd[14];
    const int fg = bid >> 4, cg = bid & 15;
    const int f0 = fg * 224 + pa * 112;
    const __amdgpu_buffer_rsrc_t rdn = MKRSRC(w_down, DM * FF * 2);
    const unsigned wqo = (unsigned)(((f0 + 14 * (lane >> 3)) * DM + cg * 64 + 8 * (lane & 7)) * 2);
    asm volatile("" ::"s"(w_r), "s"(gate_w), "s"(ws), "s"(w_gate), "s"(w_up), "s"(w_down));
    unsigned pf;
#define GU_LD(i0, i1) do { SB(); _Pragma("unroll") for (int i = (i0); i < (i1); ++i) wv[i] = __builtin_amdgcn_raw_buffer_load_b128(i < 16 ? rgu0 : rgu1, wpo, i * (FF * 2), 2); SB(); } while (0)
#define DN_LD(i0, i1) do { SB(); _Pragma("unroll") for (int i = (i0); i < (i1); ++i) wd[i] = __builtin_amdgcn_raw_buffer_load_b128(rdn, wqo, i * (DM * 2), 2); SB(); } while (0)
    GU_LD(0, GU_E);
    {
#pragma unroll
      for (int t = 0; t <= R_AT; ++t) BAR();
      const int r0 = m0 + nt * 8 + 4 * pa;
      u32x4 xr[4][2], rv[2], gv[2];
#pragma unroll
      for (int i = 0; i < 2; ++i) {
        rv[i] = *(const u32x4*)(w_r + i * 512 + lane * 8);
        gv[i] = *(const u32x4*)(gate_w + i * 512 + lane * 8);
      }
#pragma unroll
      for (int r = 0; r < 4; ++r)
#pragma unroll
        for (int i = 0; i < 2; ++i) xr[r][i] = *(const u32x4*)(x + (size_t)(r0 + r) * DM + i * 512 + lane * 8);
      const unsigned ep = __hip_atomic_load((gu32*)(ws + OFF_EPOCH), RLX_AGENT);
      {
        const u32x4 nw = {~0u, ~0u, ~0u, ~0u};
        const __amdgpu_buffer_rsrc_t kvr = MKRSRC(ws + OFF_KVP, NH * 2 * 16 * 1024), zr = MKRSRC(ws + OFF_ZP, NH * 2 * 16 * 128);
        const unsigned rec = (unsigned)((2 * nt + pa) * 32 + mt);
        __builtin_amdgcn_raw_buffer_store_b128(nw, kvr, rec * 1024 + lane * 16, 0, 16);
        __builtin_amdgcn_raw_buffer_store_b128(nw, kvr, rec * 1024 + lane * 16 + 16384, 0, 16);
        if (lane < 10) __builtin_amdgcn_raw_buffer_store_b128(nw, zr, lane < 8 ? (rec + (lane >> 2) * 16) * 64 + (lane & 3) * 16 : ZSC + (rec + (lane & 1) * 16) * 16, 0, 16);
      }
#pragma unroll
      for (int t = R_AT + 1; t < R_AT + R_GAP; ++t) BAR();
      float racc[4], gacc[4];
#pragma unroll
      for (int r = 0; r < 4; ++r) {
        BAR();
        if (r == 1) { WAIT_VM(0); SB(); }
        racc[r] = 0.f; gacc[r] = 0.f;
#pragma unroll
        for (int i = 0; i < 2; ++i)
#pragma unroll
          for (int e = 0; e < 4; ++e) {
            const float x0 = blo(xr[r][i][e]), x1 = bhi(xr[r][i][e]);
            racc[r] += x0 * blo(rv[i][e]); racc[r] += x1 * bhi(rv[i][e]);
            const unsigned pr = cvt_pk(x0 * blo(gv[i][e]), x1 * bhi(gv[i][e]));
            gacc[r] += blo(pr); gacc[r] += bhi(pr);
          }
        PIN(racc[r]); PIN(gacc[r]);
        if (r == 3) { SB(); pf = *(const unsigned*)(wo + (size_t)(128 * ml + 64 * pa + lane) * DM + 64 * nt3); cpf2 = CODE_PF(w == 7); }
      }
      BAR();
      float rs = merge16(merge32(racc[0], racc[1]), merge32(racc[2], racc[3]));
      float gs = merge16(merge32(gacc[0], gacc[1]), merge32(gacc[2], gacc[3]));
      rs += dppf<0x128>(rs); gs += dppf<0x128>(gs); rs += dppf<0x124>(rs); gs += dppf<0x124>(gs);
      rs += dppf<0x122>(rs); gs += dppf<0x122>(gs); rs += dppf<0x121>(rs); gs += dppf<0x121>(gs);
      const int rr = r0 + ((lane >> 5) | ((lane >> 3) & 2));
      const unsigned rb = cvt_pk(rs, rs) & 0xFFFFu;
      unsigned key = (((rb & 0x8000u) ? (~rb & 0xFFFFu) : (rb | 0x8000u)) << 16) | (0xFFFFu - (unsigned)rr);
      { const auto t_ = __builtin_amdgcn_permlane16_swap(key, key, false, false); key = max(t_[0], t_[1]); }
      { const auto t_ = __builtin_amdgcn_permlane32_swap(key, key, false, false); key = max(t_[0], t_[1]); }
      const unsigned tg = rfl(ep) + 1u;
      if (lane == 0) misc[C_TAG + pa] = pa ? key : tg;
      WAIT_LGKM0();
      BAR();
      if (pa == 0 && lane == 0) __hip_atomic_store((gu64*)(ws + OFF_RG) + bid, ((u64)tg << 32) | max(key, (unsigned)misc[C_TAG + 1]), RLX_AGENT);
      if ((lane & 15) == 0) __hip_atomic_store((gu64*)(ws + OFF_GG) + rr, ((u64)tg << 32) | f2u(bfr(sigmoid_f(bfr(gs)))), RLX_AGENT);
      DN_LD(0, DN_E);
#pragma unroll
      for (int t = R_AT + R_GAP + 6; t < 13; ++t) BAR();
    }
    unsigned sel[2][4];
#pragma unroll
    for (int i = 0; i < 8; ++i) {
      const unsigned pick = (i >> 2) ? 0x0302u : 0x0100u;
      sel[i >> 2][i & 3] = ((lane & 7) >> 1) == (i & 3) ? ((lane & 1) ? (pick << 16) | 0x0C0Cu : 0x0C0C0000u | pick) : 0x0C0C0C0Cu;
      PIN(sel[i >> 2][i & 3]);
    }
    u32x4 rg[2];
#pragma unroll
    for (int i = 0; i < 2; ++i) rg[i] = __builtin_amdgcn_raw_buffer_load_b128(MKRSRC(ws + OFF_RG, 2048), lane * 16 + i * 1024, 0, 16);
    const gu64* ggp = (const gu64*)(ws + OFF_GG) + m0 + 64 * pa + lane;
    u64 gg = __hip_atomic_load(ggp, RLX_AGENT);
    BAR();
    const unsigned tag = rfl(misc[C_TAG]);
    unsigned idx;
    unsigned bestk;
    bool late;
    {
      unsigned best = 0;
      bool ok = (unsigned)(gg >> 32) == tag;
#pragma unroll
      for (int i = 0; i < 4; ++i) { ok = ok && rg[i >> 1][2 * (i & 1) + 1] == tag; best = max(best, rg[i >> 1][2 * (i & 1)]); }
      late = !all_true(ok);
      if (__builtin_expect(late, 0)) {
        BAR();
#pragma unroll 1
        for (unsigned spins = 0;; ++spins) {
          u64 rh[4];
#pragma unroll
          for (int i = 0; i < 4; ++i) rh[i] = __hip_atomic_load((gu64*)(ws + OFF_RG) + i * 64 + lane, RLX_AGENT);
          gg = __hip_atomic_load(ggp, RLX_AGENT);
          ok = (unsigned)(gg >> 32) == tag; best = 0;
#pragma unroll
          for (int i = 0; i < 4; ++i) { ok = ok && (unsigned)(rh[i] >> 32) == tag; best = max(best, (unsigned)rh[i]); }
          if (all_true(ok) || spins > SPIN_CAP) break;
          __builtin_amdgcn_s_sleep(8);
        }
      }
      best = wave_max(best);
      idx = (0xFFFFu - (best & 0xFFFFu)) & (SEQ - 1);
      bestk = best;
    }
    asm volatile("" ::"v"(pf), "v"(cpf), "v"(cpf2));
    u32x4 xs[4];
#pragma unroll
    for (int i = 0; i < 4; ++i) xs[i] = *(const u32x4*)(x + (size_t)idx * DM + gd0 + 8 * i);
    if (!late) BAR();
    __builtin_amdgcn_s_sleep(2);
    GU_LD(GU_E, 32);
    ((unsigned*)(smem + L_GATE))[64 * pa + lane] = (unsigned)gg;
    CFENCE();
    if (lane == 0) misc[C_IDX + pa] = idx + 1u;
    if (bid == 0 && pa == 0 && lane == 0) __hip_atomic_store((gu32*)(ws + OFF_EPOCH), tag, RLX_AGENT);
#define AFR(pr, i) FRAG(((u32x4){__builtin_amdgcn_perm(0u, pr, sel[(i) & 1][0]), __builtin_amdgcn_perm(0u, pr, sel[(i) & 1][1]), \
                                 __builtin_amdgcn_perm(0u, pr, sel[(i) & 1][2]), __builtin_amdgcn_perm(0u, pr, sel[(i) & 1][3])}))
    f32x4* rdw = (f32x4*)(red + pa * 128 + (lane >> 5) * 64 + (lane & 7) * 8 + ((lane >> 4) & 1) * 4);
    const bool rvalid = (lane >> 5) == ((lane >> 3) & 1);
    const auto rv = LP(volatile float, L_RED, lane * 4);
    {
      bf16x8 af[32];
      f32x4 a = {0.f, 0.f, 0.f, 0.f};
#pragma unroll
      for (int i = 0; i < GU_AH; ++i) { af[i] = AFR(xs[i >> 3][(i >> 1) & 3], i); PIN(af[i]); }
      SB();
#pragma unroll
      for (int i = 0; i < 32; ++i) {
        a = MFMA(af[i], FRAG(wv[i]), a);
        PIN(a);
        if (i + GU_AH < 32) { af[(i + GU_AH) & 31] = AFR(xs[((i + GU_AH) >> 3) & 3][((i + GU_AH) >> 1) & 3], i + GU_AH); PIN(af[(i + GU_AH) & 31]); }
        SB();
      }
      if (rvalid) *rdw = a;
      CFENCE();
      if (pa == 1) misc[C_GU] = 1u;
      else {
        float v;
#pragma unroll 1
        for (unsigned spins = 0; spins < (1u << 22); ++spins) {
          const unsigned f_ = misc[C_GU];
          CFENCE();
          v = (rv[0] + rv[64]) + (rv[128] + rv[192]);
          if (rfl(f_) != 0u) break;
          __builtin_amdgcn_s_sleep(1);
        }
        if (gemv) __hip_atomic_store((gu64*)(ws + OFF_GU) + ((help ? 4 : 0) + gdh * 2 + gmat) * FF + gfs * 64 + lane, ((u64)tag << 32) | f2u(v), RLX_AGENT);
      }
    }
    {
#pragma unroll 1
      for (unsigned spins = 0; spins < (1u << 22) && !all_true(WFLAGS(W_X2) != 0u); ++spins) __builtin_amdgcn_s_sleep(1);
      __builtin_amdgcn_s_sleep(10);
      DN_LD(DN_E, 14);
      __builtin_amdgcn_s_sleep(8);
      float hv[2];
      const int fl = f0 + 2 * (lane < 56 ? lane : 55);
      const __amdgpu_buffer_rsrc_t gr = MKRSRC(ws + OFF_GU, 8 * FF * 8);
      const bool ex[2] = {(((unsigned)((uintptr_t)w_gate >> 7) + (fl >> 6)) & 7u) == 3u, (((unsigned)((uintptr_t)w_up >> 7) + (fl >> 6)) & 7u) == 3u};
#pragma unroll 1
      for (unsigned spins = 0;; ++spins) {
        u32x4 g[8];
        CFENCE();
#pragma unroll
        for (int i = 0; i < 8; ++i) {
          g[i] = (u32x4){0u, tag, 0u, tag};
          if (i < 4 || ex[i & 1]) g[i] = __builtin_amdgcn_raw_buffer_load_b128(gr, fl * 8, i * (FF * 8), 16);
        }
        bool ok = true;
#pragma unroll
        for (int j = 0; j < 2; ++j) {
#pragma unroll
          for (int i = 0; i < 8; ++i) ok = ok && g[i][2 * j + 1] == tag;
          const float gt = bfr((u2f(g[0][2 * j]) + u2f(g[4][2 * j])) + (u2f(g[2][2 * j]) + u2f(g[6][2 * j])));
          const float up = bfr((u2f(g[1][2 * j]) + u2f(g[5][2 * j])) + (u2f(g[3][2 * j]) + u2f(g[7][2 * j])));
          hv[j] = bfr(gt * __builtin_amdgcn_rcpf(1.0f + __expf(-gt))) * up;
        }
        if (all_true(ok) || spins > SPIN_CAP) break;
        __builtin_amdgcn_s_sleep(4);
      }
      u32x4 hp[2];
      {
        char* hb = smem + L_H + pa * 256;
        if (lane < 56) *(unsigned*)(hb + (lane / 7) * 32 + (lane % 7) * 4) = cvt_pk(hv[0], hv[1]);
        CFENCE();
        hp[0] = *(volatile lu32x4*)(hb + (lane >> 3) * 32); hp[1] = *(volatile lu32x4*)(hb + (lane >> 3) * 32 + 16);
      }
      bf16x8 af[14];
#pragma unroll
      for (int i = 0; i < 14; ++i) { af[i] = AFR(hp[i >> 3][(i >> 1) & 3], i); PIN(af[i]); }
      SB();
      f32x4 a = {0.f, 0.f, 0.f, 0.f};
#pragma unroll
      for (int i = 0; i < 14; ++i) a = MFMA(af[i], FRAG(wd[i]), a);
      if (rvalid) rdw[64] = a;
      CFENCE();
      if (pa == 1) misc[C_DN] = 1u;
      else {
        float y;
#pragma unroll 1
        for (unsigned spins = 0; spins < (1u << 22); ++spins) {
          const unsigned f_ = misc[C_DN];
          CFENCE();
          y = (rv[256] + rv[320]) + (rv[384] + rv[448]);
          if (rfl(f_) != 0u) break;
          __builtin_amdgcn_s_sleep(1);
        }
        __hip_atomic_store((gu64*)(ws + OFF_YG) + fg * 1024 + cg * 64 + lane, ((u64)tag << 32) | f2u(y), RLX_AGENT);
      }
    }
    if (pa == 1 && (int)(idx >> 7) == mt3) {
      const float xe = u2f((unsigned)x[(size_t)idx * DM + 64 * nt3 + lane] << 16);
      const unsigned ob = bestk >> 16;
      const float rsel = bfr(sigmoid_f(u2f(((ob & 0x8000u) ? (ob & 0x7FFFu) : (~ob & 0xFFFFu)) << 16)));
      float y = 0.f, gt = 0.f;
      __builtin_amdgcn_s_sleep(30);
#pragma unroll 1
      for (unsigned spins = 0;; ++spins) {
        u64 g[17];
#pragma unroll
        for (int i = 0; i < 16; ++i) g[i] = __hip_atomic_load((gu64*)(ws + OFF_YG) + i * 1024 + 64 * nt3 + lane, RLX_AGENT);
        g[16] = __hip_atomic_load((gu64*)(ws + OFF_GG) + idx, RLX_AGENT);
        bool ok = (unsigned)(g[16] >> 32) == tag;
        gt = u2f((unsigned)g[16]); y = 0.f;
#pragma unroll
        for (int i = 0; i < 16; ++i) { ok = ok && (unsigned)(g[i] >> 32) == tag; y += u2f((unsigned)g[i]); }
        if (all_true(ok) || spins > SPIN_CAP) break;
        __builtin_amdgcn_s_sleep(8);
      }
      red[512 + lane] = bfr(bfr(1.0f - gt) * bfr(xe + bfr(bfr(y) * rsel))) - bfr(bfr(1.0f - gt) * xe);
      CFENCE();
      if (lane == 0) misc[C_Y] = 1u;
    }
    return;
  }

  if (w == 0) misc[lane & 31] = 0u;
  if (w == 1) *(u32x2*)(smem + L_CW + lane * 8) = (u32x2){0u, 0u};
  const int hh = w & 1, wr = w >> 1;
  const int c16 = lane & 15, g = lane >> 4;
  const int fsw = (c16 >> 3) | ((g & 1) << 1);
  const int head = 2 * nt + hh;
  int aoff[2], boff[2];
#pragma unroll
  for (int i = 0; i < 2; ++i) {
    aoff[i] = (wr * 64 + c16) * 128 + (((4 * i + g) ^ (c16 >> 1)) * 16);
    boff[i] = 16384 + (8 * g + (c16 >> 2)) * 128 + (((2 * hh + (i ^ wr)) ^ fsw) * 32) + (c16 & 3) * 8;
  }
  f32x4 acc[3][4][2];
#pragma unroll
  for (int i = 0; i < 24; ++i) { const float s0 = i < 16 ? 1.f : 0.f; acc[i / 8][(i >> 1) & 3][i & 1] = (f32x4){s0, s0, s0, s0}; PIN(acc[i / 8][(i >> 1) & 3][i & 1]); }
  bf16x8 a[2][4], b[2][6];
  s16x4 tlo;
#define A_AT(sl, h, mi) (*(const bf16x8*)(smem + (sl) * SLOT + aoff[h] + (mi) * 2048))
#define B_AT(sl, h, s, n) (smem + (sl) * SLOT + boff[n] + (s) * 8192 + (h) * 4096)
#define MM(s, mi, n, A, B) do { if ((s) == 0) AMFMA(acc[0][mi][n], B, A); else AMFMA(acc[s][mi][n], A, B); } while (0)
#define RD(sl, h, u)                                                                                       \
  do {                                                                                                     \
    if ((u) == 0) a[h][0] = A_AT(sl, h, 0);                                                                \
    else if ((u) >= 13) a[h][(u) - 12] = A_AT(sl, h, (u) - 12);                                            \
    else {                                                                                                 \
      const int j_ = (((u) - 1) / 2 + 2) % 6;                                                              \
      if ((u) & 1) tlo = TRD(B_AT(sl, h, j_ >> 1, j_ & 1));                                                \
      else b[h][j_] = CAT(tlo, TRD(B_AT(sl, h, j_ >> 1, j_ & 1) + 512));                                   \
    }                                                                                                      \
  } while (0)
#define O_M(n) ((((n) / 4) & 1) ? 3 - (n) % 4 : (n) % 4)
#define O_J(n) ((n) / 4)
#define STEP(h, rs)                                                                                        \
  _Pragma("unroll") for (int n_ = 0; n_ < 24; ++n_) {                                                      \
    const int jj_ = (O_J(n_) + 2) % 6, mi_ = O_M(n_);                                                      \
    MM(jj_ >> 1, mi_, jj_ & 1, a[h][mi_], b[h][jj_]); SB();                                                \
    if (n_ < 16) { RD(rs, 1 - (h), n_); }                                                                 \
    SB();                                                                                                  \
  }
#define LGKM0() __builtin_amdgcn_s_waitcnt(0xC07F)
#define STAGE(sl, nx) do { STEP(0, sl); LGKM0(); BAR(); STEP(1, nx); LGKM0(); SB(); } while (0)

  __amdgpu_buffer_rsrc_t wsr = MKRSRC(ws, OFF_OF + SEQ * DM * 2);
  asm volatile("" : "+s"(wsr));
  const unsigned rec = (unsigned)((head * 2 + wr) * 16 + mt);
  unsigned x1w = L_X1 + w * 2048 + lane * 16, cw = L_CW + w * 128 + g * 16, one = 1u;
  unsigned kvo = (unsigned)OFF_KVP + rec * 1024 + lane * 16;
  unsigned zo = c16 == 0 ? (unsigned)OFF_ZP + rec * 64 + g * 16 : lane == 1 ? (unsigned)OFF_ZP + ZSC + rec * 16 : 0x80000000u;
  PIN(x1w); PIN(cw); PIN(one); PIN(kvo); PIN(zo);
  WAIT_LGKM0();
  BAR();
#pragma unroll
  for (int u = 0; u < 16; ++u) RD(0, 0, u);
  LGKM0();
  SB();
#pragma unroll 1
  for (int it = 0; it < 4; ++it) { STAGE(0, 1); STAGE(1, 2); STAGE(2, 0); }
  STAGE(0, 1);
  STEP(0, 1); LGKM0(); BAR();
  bf16x8 ta0_0, ta0_1, ta0_2, ta0_3, ta1_0, ta1_1, ta1_2, ta1_3, ta2_0, ta2_1, ta2_2, ta2_3;
  bf16x8 ta3_0, ta3_1, ta3_2, ta3_3, tb0_0_0, tb0_0_1, tb0_1_0, tb0_1_1, tb0_2_0, tb0_2_1, tb0_3_0, tb0_3_1;
  bf16x8 tb1_0_0, tb1_0_1, tb1_1_0, tb1_1_1, tb1_2_0, tb1_2_1, tb1_3_0, tb1_3_1, tb2_0_0, tb2_0_1, tb2_1_0, tb2_1_1;
  bf16x8 tb2_2_0, tb2_2_1, tb2_3_0, tb2_3_1;
  s16x4 tb0_0_0_l, tb0_0_1_l, tb0_1_0_l, tb0_1_1_l, tb0_2_0_l, tb0_2_1_l, tb0_3_0_l, tb0_3_1_l, tb1_0_0_l, tb1_0_1_l, tb1_1_0_l, tb1_1_1_l;
  s16x4 tb1_2_0_l, tb1_2_1_l, tb1_3_0_l, tb1_3_1_l, tb2_0_0_l, tb2_0_1_l, tb2_1_0_l, tb2_1_1_l, tb2_2_0_l, tb2_2_1_l, tb2_3_0_l, tb2_3_1_l;
  unsigned kp[16], vp[16], x1r, cr;
  u32x2 lf15;
  float kmin = 0.f, qmin = 0.f;
  u32x4 ones;
  float ek[4];
#define PHI(src, dst, mn, k, st)                                                                           \
  do {                                                                                                     \
    float& e_ = ek[2 * (k) + ((st) & 1)];                                                                  \
    if ((st) < 2) e_ = __builtin_fmaf((src)[2 * (k) + ((st) & 1)], 1.44269504088896341f, -1.44269504088896341f); \
    else if ((st) < 4) e_ = __builtin_amdgcn_fmed3f(__builtin_amdgcn_exp2f(e_), 0.f, 1.f);                 \
    else if ((st) < 6) e_ = fmax_raw((src)[2 * (k) + ((st) & 1)], e_);                                     \
    else if ((st) == 6) dst = cvt_pk(ek[2 * (k)], ek[2 * (k) + 1]);                                        \
    else mn = __builtin_fminf(__builtin_fminf(mn, (src)[2 * (k)]), (src)[2 * (k) + 1]);                     \
    SB();                                                                                                  \
  } while (0)
#define PK(mi, n, k, st) PHI(acc[1][mi][n], kp[8 * (n) + 2 * (mi) + (k)], kmin, k, st)
#define CV(mi, n, k) do { vp[8 * (n) + 2 * (mi) + (k)] = cvt_pk(acc[2][mi][n][2 * (k)], acc[2][mi][n][2 * (k) + 1]); PIN(vp[8 * (n) + 2 * (mi) + (k)]); SB(); } while (0)
#define F15_RD lf15 = *(volatile lu32x2*)(smem + L_CTL + 4 * C_F15)
#define F15_CHK do { if (__builtin_expect(!all_true((lf15[0] & lf15[1]) != 0u), 0)) { (void)wait_ge<1>(misc + C_F15, 1u); (void)wait_ge<1>(misc + C_F15 + 1, 1u); } CFENCE(); } while (0)
#define PRE(i) do { if ((i) == 30) { x1r = x1w ^ 4096u; PIN(x1r); } if ((i) == 31) { cr = cw ^ 256u; PIN(cr); } \
                    if ((i) == 32) { ones = (u32x4){0x3F803F80u, 0x3F803F80u, 0x3F803F80u, 0x3F803F80u}; PIN(ones); } } while (0)
  MM(1, 0, 0, a[1][0], b[1][2]); SB(); tb1_0_1_l = TRD(B_AT(2, 0, 1, 1)); PRE(0); SB();
  MM(1, 0, 1, a[1][0], b[1][3]); SB(); tb1_0_1 = CAT(tb1_0_1_l, TRD(B_AT(2, 0, 1, 1) + 512)); PRE(1); SB();
  MM(2, 0, 0, a[1][0], b[1][4]); SB(); ta0_0 = A_AT(2, 0, 0); PRE(2); SB();
  MM(2, 0, 1, a[1][0], b[1][5]); SB(); ta0_1 = A_AT(2, 0, 1); PRE(3); SB();
  MM(0, 0, 0, a[1][0], b[1][0]); SB(); ta0_2 = A_AT(2, 0, 2); PRE(4); SB();
  MM(0, 0, 1, a[1][0], b[1][1]); SB(); ta0_3 = A_AT(2, 0, 3); PRE(5); SB();
  MM(1, 1, 0, a[1][1], b[1][2]); SB(); tb1_0_0_l = TRD(B_AT(2, 0, 1, 0)); PRE(6); SB();
  MM(1, 1, 1, a[1][1], b[1][3]); SB(); tb1_0_0 = CAT(tb1_0_0_l, TRD(B_AT(2, 0, 1, 0) + 512)); PRE(7); SB();
  MM(2, 1, 0, a[1][1], b[1][4]); SB(); tb1_1_1_l = TRD(B_AT(2, 1, 1, 1)); PRE(8); SB();
  MM(2, 1, 1, a[1][1], b[1][5]); SB(); tb1_1_1 = CAT(tb1_1_1_l, TRD(B_AT(2, 1, 1, 1) + 512)); PRE(9); SB();
  MM(0, 1, 0, a[1][1], b[1][0]); SB(); ta1_0 = A_AT(2, 1, 0); PRE(10); SB();
  MM(0, 1, 1, a[1][1], b[1][1]); SB(); ta1_1 = A_AT(2, 1, 1); PRE(11); SB();
  MM(1, 2, 0, a[1][2], b[1][2]); SB(); ta1_2 = A_AT(2, 1, 2); PRE(12); SB();
  MM(1, 2, 1, a[1][2], b[1][3]); SB(); ta1_3 = A_AT(2, 1, 3); PRE(13); SB();
  MM(2, 2, 0, a[1][2], b[1][4]); SB(); tb1_1_0_l = TRD(B_AT(2, 1, 1, 0)); PRE(14); SB();
  MM(2, 2, 1, a[1][2], b[1][5]); SB(); tb1_1_0 = CAT(tb1_1_0_l, TRD(B_AT(2, 1, 1, 0) + 512)); PRE(15); SB();
  MM(0, 2, 0, a[1][2], b[1][0]); SB(); PRE(16); SB();
  MM(0, 2, 1, a[1][2], b[1][1]); SB(); PRE(17); SB();
  MM(1, 3, 0, a[1][3], b[1][2]); SB(); PRE(18); SB();
  MM(1, 3, 1, a[1][3], b[1][3]); SB(); PRE(19); SB();
  MM(2, 3, 0, a[1][3], b[1][4]); SB(); F15_RD; PRE(20); SB();
  MM(2, 3, 1, a[1][3], b[1][5]); SB(); PRE(21); SB();
  MM(0, 3, 0, a[1][3], b[1][0]); SB(); tb2_0_1_l = TRD(B_AT(2, 0, 2, 1)); PRE(22); SB();
  MM(0, 3, 1, a[1][3], b[1][1]); SB(); tb2_0_1 = CAT(tb2_0_1_l, TRD(B_AT(2, 0, 2, 1) + 512)); PRE(23); SB();
  MM(1, 0, 1, ta0_0, tb1_0_1); SB(); PRE(24); SB();
  MM(1, 1, 1, ta0_1, tb1_0_1); SB(); PRE(25); SB();
  MM(1, 2, 1, ta0_2, tb1_0_1); SB(); F15_CHK; tb2_1_1_l = TRD(B_AT(2, 1, 2, 1)); PRE(26); SB();
  MM(1, 3, 1, ta0_3, tb1_0_1); SB(); tb2_1_1 = CAT(tb2_1_1_l, TRD(B_AT(2, 1, 2, 1) + 512)); PRE(27); SB();
  MM(1, 0, 0, ta0_0, tb1_0_0); SB(); tb1_2_1_l = TRD(B_AT(0, 0, 1, 1)); PRE(28); SB();
  MM(1, 1, 0, ta0_1, tb1_0_0); SB(); tb1_2_1 = CAT(tb1_2_1_l, TRD(B_AT(0, 0, 1, 1) + 512)); PRE(29); SB();
  MM(1, 2, 0, ta0_2, tb1_0_0); SB(); ta2_0 = A_AT(0, 0, 0); PRE(30); SB();
  MM(1, 3, 0, ta0_3, tb1_0_0); SB(); ta2_1 = A_AT(0, 0, 1); PRE(31); SB();
  MM(1, 0, 1, ta1_0, tb1_1_1); SB(); ta2_2 = A_AT(0, 0, 2); PRE(32); SB();
  MM(1, 1, 1, ta1_1, tb1_1_1); SB(); ta2_3 = A_AT(0, 0, 3); PRE(33); SB();
  MM(1, 2, 1, ta1_2, tb1_1_1); SB(); tb1_3_1_l = TRD(B_AT(0, 1, 1, 1)); PRE(34); SB();
  MM(1, 3, 1, ta1_3, tb1_1_1); SB(); tb1_3_1 = CAT(tb1_3_1_l, TRD(B_AT(0, 1, 1, 1) + 512)); PRE(35); SB();
  MM(1, 0, 0, ta1_0, tb1_1_0); SB(); ta3_0 = A_AT(0, 1, 0); PRE(36); SB();
  MM(1, 1, 0, ta1_1, tb1_1_0); SB(); ta3_1 = A_AT(0, 1, 1); PRE(37); SB();
  MM(1, 2, 0, ta1_2, tb1_1_0); SB(); ta3_2 = A_AT(0, 1, 2); PRE(38); SB();
  MM(1, 3, 0, ta1_3, tb1_1_0); SB(); ta3_3 = A_AT(0, 1, 3); PRE(39); SB();
  MM(1, 0, 1, ta2_0, tb1_2_1); SB(); tb1_2_0_l = TRD(B_AT(0, 0, 1, 0)); PRE(40); SB();
  MM(1, 1, 1, ta2_1, tb1_2_1); SB(); tb1_2_0 = CAT(tb1_2_0_l, TRD(B_AT(0, 0, 1, 0) + 512)); PRE(41); SB();
  MM(1, 2, 1, ta2_2, tb1_2_1); SB(); tb1_3_0_l = TRD(B_AT(0, 1, 1, 0)); PRE(42); SB();
  MM(1, 3, 1, ta2_3, tb1_2_1); SB(); tb1_3_0 = CAT(tb1_3_0_l, TRD(B_AT(0, 1, 1, 0) + 512)); PRE(43); SB();
  MM(1, 0, 1, ta3_0, tb1_3_1); SB(); tb2_2_1_l = TRD(B_AT(0, 0, 2, 1)); PRE(44); SB();
  MM(1, 1, 1, ta3_1, tb1_3_1); SB(); tb2_2_1 = CAT(tb2_2_1_l, TRD(B_AT(0, 0, 2, 1) + 512)); PRE(45); SB();
  MM(1, 2, 1, ta3_2, tb1_3_1); SB(); tb2_3_1_l = TRD(B_AT(0, 1, 2, 1)); PRE(46); SB();
  MM(1, 3, 1, ta3_3, tb1_3_1); SB(); tb2_3_1 = CAT(tb2_3_1_l, TRD(B_AT(0, 1, 2, 1) + 512)); PRE(47); SB();
  MM(1, 0, 0, ta2_0, tb1_2_0); SB(); tb2_0_0_l = TRD(B_AT(2, 0, 2, 0)); PK(0, 1, 0, 0); PK(0, 1, 0, 1); PK(0, 1, 1, 0); PK(0, 1, 1, 1); PK(0, 1, 0, 2); SB();
  MM(1, 1, 0, ta2_1, tb1_2_0); SB(); tb2_0_0 = CAT(tb2_0_0_l, TRD(B_AT(2, 0, 2, 0) + 512)); PK(0, 1, 0, 3); PK(0, 1, 0, 7); PK(0, 1, 1, 2); PK(0, 1, 1, 3); SB();
  MM(1, 2, 0, ta2_2, tb1_2_0); SB(); tb2_1_0_l = TRD(B_AT(2, 1, 2, 0)); PK(0, 1, 1, 7); PK(0, 1, 0, 4); PK(0, 1, 0, 5); PK(0, 1, 1, 4); PK(0, 1, 1, 5); PK(0, 1, 0, 6); SB();
  MM(1, 3, 0, ta2_3, tb1_2_0); SB(); tb2_1_0 = CAT(tb2_1_0_l, TRD(B_AT(2, 1, 2, 0) + 512)); PK(0, 1, 1, 6); PK(1, 1, 0, 0); PK(1, 1, 0, 1); PK(1, 1, 1, 0); PK(1, 1, 1, 1); PK(1, 1, 0, 2); SB();
  MM(1, 0, 0, ta3_0, tb1_3_0); SB(); tb2_2_0_l = TRD(B_AT(0, 0, 2, 0)); PK(1, 1, 0, 3); PK(1, 1, 0, 7); PK(1, 1, 1, 2); PK(1, 1, 1, 3); SB();
  MM(1, 1, 0, ta3_1, tb1_3_0); SB(); tb2_2_0 = CAT(tb2_2_0_l, TRD(B_AT(0, 0, 2, 0) + 512)); PK(1, 1, 1, 7); PK(1, 1, 0, 4); PK(1, 1, 0, 5); PK(1, 1, 1, 4); PK(1, 1, 1, 5); SB();
  MM(1, 2, 0, ta3_2, tb1_3_0); SB(); tb2_3_0_l = TRD(B_AT(0, 1, 2, 0)); PK(1, 1, 0, 6); PK(1, 1, 1, 6); PK(2, 1, 0, 0); PK(2, 1, 0, 1); PK(2, 1, 1, 0); SB();
  MM(1, 3, 0, ta3_3, tb1_3_0); SB(); tb2_3_0 = CAT(tb2_3_0_l, TRD(B_AT(0, 1, 2, 0) + 512)); PK(2, 1, 1, 1); PK(2, 1, 0, 2); PK(2, 1, 0, 3); SB();
  MM(2, 0, 1, ta0_0, tb2_0_1); SB(); PK(2, 1, 0, 7); PK(2, 1, 1, 2); PK(2, 1, 1, 3); SB();
  MM(2, 1, 1, ta0_1, tb2_0_1); SB(); PK(2, 1, 1, 7); PK(2, 1, 0, 4); PK(2, 1, 0, 5); PK(2, 1, 1, 4); PK(2, 1, 1, 5); SB();
  MM(2, 2, 1, ta0_2, tb2_0_1); SB(); PK(2, 1, 0, 6); PK(2, 1, 1, 6); PK(3, 1, 0, 0); PK(3, 1, 0, 1); PK(3, 1, 1, 0); SB();
  MM(2, 3, 1, ta0_3, tb2_0_1); SB(); PK(3, 1, 1, 1); PK(3, 1, 0, 2); PK(3, 1, 0, 3); SB();
  MM(2, 0, 1, ta1_0, tb2_1_1); SB(); PK(3, 1, 0, 7); PK(3, 1, 1, 2); PK(3, 1, 1, 3); SB();
  MM(2, 1, 1, ta1_1, tb2_1_1); SB(); PK(3, 1, 1, 7); PK(3, 1, 0, 4); PK(3, 1, 0, 5); PK(3, 1, 1, 4); PK(3, 1, 1, 5); SB();
  MM(2, 2, 1, ta1_2, tb2_1_1); SB(); PK(3, 1, 0, 6); PK(3, 1, 1, 6); PK(0, 0, 0, 0); PK(0, 0, 0, 1); PK(0, 0, 1, 0); SB();
  MM(2, 3, 1, ta1_3, tb2_1_1); SB(); PK(0, 0, 1, 1); PK(0, 0, 0, 2); PK(0, 0, 0, 3); SB();
  MM(2, 0, 1, ta2_0, tb2_2_1); SB(); PK(0, 0, 0, 7); PK(0, 0, 1, 2); PK(0, 0, 1, 3); SB();
  MM(2, 1, 1, ta2_1, tb2_2_1); SB(); PK(0, 0, 1, 7); PK(0, 0, 0, 4); PK(0, 0, 0, 5); PK(0, 0, 1, 4); PK(0, 0, 1, 5); SB();
  MM(2, 2, 1, ta2_2, tb2_2_1); SB(); PK(0, 0, 0, 6); PK(0, 0, 1, 6); PK(1, 0, 0, 0); PK(1, 0, 0, 1); PK(1, 0, 1, 0); SB();
  MM(2, 3, 1, ta2_3, tb2_2_1); SB(); PK(1, 0, 1, 1); PK(1, 0, 0, 2); PK(1, 0, 0, 3); SB();
  MM(2, 0, 1, ta3_0, tb2_3_1); SB(); PK(1, 0, 0, 7); PK(1, 0, 1, 2); PK(1, 0, 1, 3); SB();
  MM(2, 1, 1, ta3_1, tb2_3_1); SB(); PK(1, 0, 1, 7); PK(1, 0, 0, 4); PK(1, 0, 0, 5); PK(1, 0, 1, 4); PK(1, 0, 1, 5); SB();
  MM(2, 2, 1, ta3_2, tb2_3_1); SB(); PK(1, 0, 0, 6); PK(1, 0, 1, 6); PK(2, 0, 0, 0); PK(2, 0, 0, 1); PK(2, 0, 1, 0); SB();
  MM(2, 3, 1, ta3_3, tb2_3_1); SB(); PK(2, 0, 1, 1); PK(2, 0, 0, 2); PK(2, 0, 0, 3); SB();
  MM(2, 0, 0, ta0_0, tb2_0_0); SB(); PK(2, 0, 0, 7); PK(2, 0, 1, 2); PK(2, 0, 1, 3); SB();
  MM(2, 1, 0, ta0_1, tb2_0_0); SB(); PK(2, 0, 1, 7); PK(2, 0, 0, 4); PK(2, 0, 0, 5); PK(2, 0, 1, 4); PK(2, 0, 1, 5); SB();
  MM(2, 2, 0, ta0_2, tb2_0_0); SB(); PK(2, 0, 0, 6); PK(2, 0, 1, 6); PK(3, 0, 0, 0); PK(3, 0, 0, 1); PK(3, 0, 1, 0); SB();
  MM(2, 3, 0, ta0_3, tb2_0_0); SB(); PK(3, 0, 1, 1); PK(3, 0, 0, 2); PK(3, 0, 0, 3); SB();
  MM(2, 0, 0, ta1_0, tb2_1_0); SB(); PK(3, 0, 0, 7); PK(3, 0, 1, 2); PK(3, 0, 1, 3); SB();
  MM(2, 1, 0, ta1_1, tb2_1_0); SB(); PK(3, 0, 1, 7); PK(3, 0, 0, 4); PK(3, 0, 0, 5); PK(3, 0, 1, 4); PK(3, 0, 1, 5); SB();
  MM(2, 2, 0, ta1_2, tb2_1_0); SB(); PK(3, 0, 0, 6); PK(3, 0, 1, 6); CV(0, 1, 0); CV(0, 1, 1); CV(1, 1, 0); SB();
  MM(2, 3, 0, ta1_3, tb2_1_0); SB(); CV(1, 1, 1); CV(2, 1, 0); CV(2, 1, 1); CV(3, 1, 0); CV(3, 1, 1); SB();
  const f32x4 zero = {0.f, 0.f, 0.f, 0.f};
#define TAKE(flag, READS)                                                                                  \
  _Pragma("unroll 1") for (unsigned spins = 0; spins < (1u << 22); ++spins) {                              \
    const unsigned f_ = (flag);                                                                            \
    CFENCE();                                                                                              \
    READS;                                                                                                 \
    if (__builtin_expect(rfl(f_) != 0u, 1)) break;                                                         \
  }
  unsigned lidx, ltag;
  {
    phi_fix(kp, kmin);
#define KF(j, n) FRAG(((u32x4){kp[8 * (n) + 4 * (j)], kp[8 * (n) + 4 * (j) + 1], kp[8 * (n) + 4 * (j) + 2], kp[8 * (n) + 4 * (j) + 3]}))
#define VF(j, n) FRAG(((u32x4){vp[8 * (n) + 4 * (j)], vp[8 * (n) + 4 * (j) + 1], vp[8 * (n) + 4 * (j) + 2], vp[8 * (n) + 4 * (j) + 3]}))
    f32x4 s1 = MFMA(KF(0, 1), VF(0, 1), zero), s0 = MFMA(KF(0, 0), VF(0, 1), zero), zs = MFMA(KF(0, 1), FRAG(ones), zero);
    s1 = MFMA(KF(1, 1), VF(1, 1), s1); s0 = MFMA(KF(1, 0), VF(1, 1), s0); zs = MFMA(KF(1, 1), FRAG(ones), zs);
#define QR(u, n) do { tb0_##u##_##n##_l = TRD(B_AT((u) < 2 ? 2 : 0, (u) & 1, 0, n)); tb0_##u##_##n = CAT(tb0_##u##_##n##_l, TRD(B_AT((u) < 2 ? 2 : 0, (u) & 1, 0, n) + 512)); SB(); } while (0)
#define X1_RD { p0 = *LP(volatile f32x4, x1r, 0); p1 = *LP(volatile f32x4, x1r, 1024); pz = *LP(volatile f32x4, cr, 0); }
    SB();
    MM(2, 0, 0, ta2_0, tb2_2_0); SB(); QR(0, 0); MM(2, 1, 0, ta2_1, tb2_2_0); SB(); QR(0, 1);
    *LP(f32x4, x1w, 0) = s1; *LP(f32x4, x1w, 1024) = s0; *LP(f32x4, cw, 0) = zs;
    CFENCE();
    *LP(volatile unsigned, cw, W_X1) = one;
    SB();
    MM(2, 0, 0, ta3_0, tb2_3_0); SB(); QR(1, 0); MM(2, 1, 0, ta3_1, tb2_3_0); SB(); QR(1, 1);
    MM(2, 2, 0, ta2_2, tb2_2_0); SB(); QR(2, 0); MM(2, 3, 0, ta2_3, tb2_2_0); SB(); CV(0, 0, 0); CV(0, 0, 1); QR(2, 1);
    MM(2, 2, 0, ta3_2, tb2_3_0); SB(); CV(1, 0, 0); CV(1, 0, 1); QR(3, 0); MM(2, 3, 0, ta3_3, tb2_3_0); SB(); QR(3, 1);
    f32x4 p0, p1, pz;
    const unsigned f1 = *LP(volatile unsigned, cr, W_X1);
    CFENCE();
    X1_RD
    SB();
    f32x4 kv0 = MFMA(KF(0, 0), VF(0, 0), zero), kv1 = MFMA(KF(0, 1), VF(0, 0), zero);
    SB();
    CV(2, 0, 0); CV(2, 0, 1);
    f32x4 z = MFMA(KF(0, 0), FRAG(ones), zero);
    SB();
    CV(3, 0, 0); CV(3, 0, 1);
    kv0 = MFMA(KF(1, 0), VF(1, 0), kv0); kv1 = MFMA(KF(1, 1), VF(1, 0), kv1);
    z = MFMA(KF(1, 0), FRAG(ones), z);
    SB();
    if (__builtin_expect(!all_true(f1 != 0u), 0)) { TAKE(*LP(volatile unsigned, cr, W_X1), X1_RD); LGKM0(); }
    const f32x4 m0_ = kv0 + p0, m1_ = kv1 + p1;
    float am = fmax_raw(fabsf(m0_[0]), fabsf(m1_[0]));
#pragma unroll
    for (int e = 1; e < 4; ++e) am = fmax_raw(fmax_raw(am, fabsf(m0_[e])), fabsf(m1_[e]));
#define PKH(a_, b_) __builtin_bit_cast(unsigned, __builtin_convertvector((f32x2){(a_), (b_)}, f16x2_t))
    u32x4 hv = {PKH(m0_[0], m0_[1]), PKH(m0_[2], m0_[3]), PKH(m1_[0], m1_[1]), PKH(m1_[2], m1_[3])};
#define Z_STORE(scl) do { unsigned z0_ = zv[0]; asm("v_writelane_b32 %0, %1, 1" : "+v"(z0_) : "s"(scl)); zv[0] = z0_; __builtin_amdgcn_raw_buffer_store_b128(zv, wsr, zo, 0, 16); } while (0)
    PIN(hv);
    if (__builtin_expect(all_true(f2u(am) - 0x3C800000u <= 0x47000000u - 0x3C800000u), 1)) {
      __builtin_amdgcn_raw_buffer_store_b128(hv, wsr, kvo, 0, 16);
      SB();
      u32x4 zv = __builtin_bit_cast(u32x4, z + pz);
      Z_STORE(0x3F800000u);
    } else {
      unsigned ex = max(wave_max(f2u(am) & 0x7FFFFFFFu) >> 23, 15u);
      if (ex >= 121u && ex < 142u) ex = 141u;
      const float inv = u2f((268u - ex) << 23);
      u32x4 hs = {PKH(m0_[0] * inv, m0_[1] * inv), PKH(m0_[2] * inv, m0_[3] * inv), PKH(m1_[0] * inv, m1_[1] * inv), PKH(m1_[2] * inv, m1_[3] * inv)};
      u32x4 zv = __builtin_bit_cast(u32x4, z + pz);
#pragma unroll
      for (int e = 0; e < 4; ++e) { hs[e] = min(hs[e], ~1u); zv[e] = min(zv[e] | (ex != 141u ? 0x80000000u : 0u), ~1u); }
      CFENCE();
      __builtin_amdgcn_raw_buffer_store_b128(hs, wsr, kvo, 0, 16);
      Z_STORE((ex - 14u) << 23);
      CFENCE();
    }
  }
  *LP(volatile unsigned, cw, W_KS) = one;
  unsigned ko, oo, x2w, zfa, zfb, zfp, gta;
  u32x2 eye, ov[4][2];
  u32x4 t[16];
  float gq[4];
  unsigned pq[16];
#define PQ(mi, n, k, st) PHI(acc[0][mi][n], pq[4 * (mi) + 2 * (n) + (k)], qmin, k, st)
#define NOPQ() asm volatile("s_nop 4" : "+v"(acc[0][3][0]), "+v"(acc[0][3][1]))
#define PRQ(i)                                                                                             \
  do {                                                                                                     \
    if ((i) == 0) { lidx = misc[C_IDX + wr]; ltag = misc[C_TAG]; }                                         \
    if ((i) == 2) { zfp = L_CTL + 4 * (C_ZF + hh); PIN(zfp); }                                             \
    if ((i) == 3) { ko = kvo - mt * 1024; PIN(ko); }                                                       \
    if ((i) == 4) { gta = L_GATE + wr * 256 + c16 * 4; PIN(gta); }                                         \
    if ((i) == 5) { zfa = L_ZF + hh * 64 + wr * 8 + g * 16; PIN(zfa); }                                    \
    if ((i) == 6) { zfb = zfa ^ 8u; PIN(zfb); }                                                            \
    if ((i) == 7) { const unsigned o_ = 0x3C00u << (16 * (lane & 1)); eye = (u32x2){(lane & 2) ? 0u : o_, (lane & 2) ? o_ : 0u}; PIN(eye); } \
    if ((i) == 8) { x2w = L_X2 + w * 1024 + lane * 16; PIN(x2w); }                                         \
    if ((i) == 9) { oo = (unsigned)OFF_OF + (unsigned)(((mt * 8 + wr * 4) * NH + head) * 1024) + lane * 16; PIN(oo); } \
  } while (0)
#define SHOT_GATE                                                                                          \
  do {                                                                                                     \
    phi_fix(pq, qmin);                                                                                     \
    _Pragma("unroll") for (int i = 0; i < 16; ++i) PIN(pq[i]);                                             \
    if (__builtin_expect(!all_true(lidx != 0u), 0)) (void)wait_ge<1>(misc + C_IDX + wr, 1u);        \
    (void)wait_ge<1>(misc + C_GO + hh, 1u);                                                                \
    CFENCE();                                                                                              \
    _Pragma("unroll") for (int i = 0; i < 4; ++i) gq[i] = *LP(volatile float, gta, i * 64);                \
  } while (0)
#define T_LD(i) t[i] = __builtin_amdgcn_raw_buffer_load_b128(wsr, ko + ((i) & 3) * 1024, ((i) >> 2) * 4096, 16)
  SB();
  MM(0, 0, 0, ta0_0, tb0_0_0); SB(); PRQ(0); SB();
  MM(0, 0, 1, ta0_0, tb0_0_1); SB(); PRQ(1); SB();
  MM(0, 0, 0, ta1_0, tb0_1_0); SB(); PRQ(2); SB();
  MM(0, 0, 1, ta1_0, tb0_1_1); SB(); PRQ(3); SB();
  MM(0, 0, 0, ta2_0, tb0_2_0); SB(); PRQ(4); SB();
  MM(0, 0, 1, ta2_0, tb0_2_1); SB(); PRQ(5); SB();
  MM(0, 0, 0, ta3_0, tb0_3_0); SB(); PRQ(6); SB();
  MM(0, 0, 1, ta3_0, tb0_3_1); SB(); PRQ(7); SB();
  MM(0, 1, 0, ta0_1, tb0_0_0); SB(); PRQ(8); SB();
  MM(0, 1, 1, ta0_1, tb0_0_1); SB(); PRQ(9); SB();
  MM(0, 1, 0, ta1_1, tb0_1_0); SB(); PQ(0, 0, 0, 0); PQ(0, 0, 0, 1); PQ(0, 0, 1, 0); SB();
  MM(0, 1, 1, ta1_1, tb0_1_1); SB(); PQ(0, 0, 1, 1); PQ(0, 0, 0, 2); SB();
  MM(0, 1, 0, ta2_1, tb0_2_0); SB(); PQ(0, 0, 0, 3); PQ(0, 0, 0, 7); SB();
  MM(0, 1, 1, ta2_1, tb0_2_1); SB(); PQ(0, 0, 1, 2); PQ(0, 0, 1, 3); SB();
  MM(0, 1, 0, ta3_1, tb0_3_0); SB(); PQ(0, 0, 1, 7); PQ(0, 0, 0, 4); PQ(0, 0, 0, 5); SB();
  MM(0, 1, 1, ta3_1, tb0_3_1); SB(); PQ(0, 0, 1, 4); PQ(0, 0, 1, 5); PQ(0, 0, 0, 6); SB();
  MM(0, 2, 0, ta0_2, tb0_0_0); SB(); PQ(0, 0, 1, 6); PQ(0, 1, 0, 0); PQ(0, 1, 0, 1); SB();
  MM(0, 2, 1, ta0_2, tb0_0_1); SB(); PQ(0, 1, 1, 0); PQ(0, 1, 1, 1); PQ(0, 1, 0, 2); SB();
  MM(0, 2, 0, ta1_2, tb0_1_0); SB(); PQ(0, 1, 0, 3); PQ(0, 1, 0, 7); SB();
  MM(0, 2, 1, ta1_2, tb0_1_1); SB(); PQ(0, 1, 1, 2); PQ(0, 1, 1, 3); SB();
  MM(0, 2, 0, ta2_2, tb0_2_0); SB(); PQ(0, 1, 1, 7); PQ(0, 1, 0, 4); PQ(0, 1, 0, 5); SB();
  MM(0, 2, 1, ta2_2, tb0_2_1); SB(); PQ(0, 1, 1, 4); PQ(0, 1, 1, 5); PQ(0, 1, 0, 6); SB();
  MM(0, 2, 0, ta3_2, tb0_3_0); SB(); PQ(0, 1, 1, 6); PQ(1, 0, 0, 0); PQ(1, 0, 0, 1); SB();
  MM(0, 2, 1, ta3_2, tb0_3_1); SB(); PQ(1, 0, 1, 0); PQ(1, 0, 1, 1); PQ(1, 0, 0, 2); SB();
  MM(0, 3, 0, ta0_3, tb0_0_0); SB(); PQ(1, 0, 0, 3); PQ(1, 0, 0, 7); SB();
  MM(0, 3, 1, ta0_3, tb0_0_1); SB(); PQ(1, 0, 1, 2); PQ(1, 0, 1, 3); SB();
  MM(0, 3, 0, ta1_3, tb0_1_0); SB(); PQ(1, 0, 1, 7); PQ(1, 0, 0, 4); PQ(1, 0, 0, 5); SB();
  MM(0, 3, 1, ta1_3, tb0_1_1); SB(); PQ(1, 0, 1, 4); PQ(1, 0, 1, 5); PQ(1, 0, 0, 6); SB();
  MM(0, 3, 0, ta2_3, tb0_2_0); SB(); PQ(1, 0, 1, 6); PQ(1, 1, 0, 0); PQ(1, 1, 0, 1); SB();
  MM(0, 3, 1, ta2_3, tb0_2_1); SB(); PQ(1, 1, 1, 0); PQ(1, 1, 1, 1); PQ(1, 1, 0, 2); SB();
  MM(0, 3, 0, ta3_3, tb0_3_0); SB(); PQ(1, 1, 0, 3); PQ(1, 1, 0, 7); SB();
  MM(0, 3, 1, ta3_3, tb0_3_1); SB(); PQ(1, 1, 1, 2); PQ(1, 1, 1, 3); SB();
  PQ(1, 1, 1, 7); SB();
  PQ(1, 1, 0, 4); SB();
  PQ(1, 1, 0, 5); SB();
  PQ(1, 1, 1, 4); SB();
  PQ(1, 1, 1, 5); SB();
  PQ(1, 1, 0, 6); SB();
  PQ(1, 1, 1, 6); SB();
  PQ(2, 0, 0, 0); SB();
  PQ(2, 0, 0, 1); SB();
  PQ(2, 0, 1, 0); SB();
  PQ(2, 0, 1, 1); SB();
  PQ(2, 0, 0, 2); SB();
  PQ(2, 0, 0, 3); SB();
  PQ(2, 0, 0, 7); SB();
  PQ(2, 0, 1, 2); SB();
  PQ(2, 0, 1, 3); SB();
  PQ(2, 0, 1, 7); SB();
  PQ(2, 0, 0, 4); SB();
  PQ(2, 0, 0, 5); SB();
  PQ(2, 0, 1, 4); SB();
  PQ(2, 0, 1, 5); SB();
  PQ(2, 0, 0, 6); SB();
  PQ(2, 0, 1, 6); SB();
  PQ(2, 1, 0, 0); SB();
  PQ(2, 1, 0, 1); SB();
  PQ(2, 1, 1, 0); SB();
  PQ(2, 1, 1, 1); SB();
  PQ(2, 1, 0, 2); SB();
  PQ(2, 1, 0, 3); SB();
  PQ(2, 1, 0, 7); SB();
  PQ(2, 1, 1, 2); SB();
  PQ(2, 1, 1, 3); SB();
  PQ(2, 1, 1, 7); SB();
  PQ(2, 1, 0, 4); SB();
  PQ(2, 1, 0, 5); SB();
  PQ(2, 1, 1, 4); SB();
  PQ(2, 1, 1, 5); SB();
  PQ(2, 1, 0, 6); SB();
  PQ(2, 1, 1, 6); SB();
  NOPQ();
  PQ(3, 0, 0, 0); SB();
  PQ(3, 0, 0, 1); SB();
  PQ(3, 0, 1, 0); SB();
  PQ(3, 0, 1, 1); SB();
  PQ(3, 0, 0, 2); SB();
  PQ(3, 0, 0, 3); SB();
  PQ(3, 0, 0, 7); SB();
  PQ(3, 0, 1, 2); SB();
  PQ(3, 0, 1, 3); SB();
  PQ(3, 0, 1, 7); SB();
  PQ(3, 0, 0, 4); SB();
  PQ(3, 0, 0, 5); SB();
  PQ(3, 0, 1, 4); SB();
  PQ(3, 0, 1, 5); SB();
  PQ(3, 0, 0, 6); SB();
  PQ(3, 0, 1, 6); SB();
  PQ(3, 1, 0, 0); SB();
  PQ(3, 1, 0, 1); SB();
  PQ(3, 1, 1, 0); SB();
  PQ(3, 1, 1, 1); SB();
  PQ(3, 1, 0, 2); SB();
  PQ(3, 1, 0, 3); SB();
  PQ(3, 1, 0, 7); SB();
  PQ(3, 1, 1, 2); SB();
  PQ(3, 1, 1, 3); SB();
  PQ(3, 1, 1, 7); SB();
  PQ(3, 1, 0, 4); SB();
  PQ(3, 1, 0, 5); SB();
  PQ(3, 1, 1, 4); SB();
  PQ(3, 1, 1, 5); SB();
  PQ(3, 1, 0, 6); SB();
  PQ(3, 1, 1, 6); SB();
  SHOT_GATE;
  T_LD(0);
  T_LD(1);
  T_LD(2);
  T_LD(3);
  T_LD(4);
  T_LD(5);
  T_LD(6);
  T_LD(7);
  T_LD(8);
  T_LD(9);
  T_LD(10);
  T_LD(11);
  T_LD(12);
  T_LD(13);
  T_LD(14);
  T_LD(15);
  *LP(volatile unsigned, cw, W_AG) = one;
  SB();
  {
#define PF(mi) FRAG(((u32x4){pq[4 * (mi)], pq[4 * (mi) + 1], pq[4 * (mi) + 2], pq[4 * (mi) + 3]}))
#define H4(v, k) __builtin_bit_cast(f16x4_t, (u32x2){(v)[2 * (k)], (v)[2 * (k) + 1]})
    f32x4 s0 = zero, s1 = zero;
#define KV_MM(i0, i1)                                                                                      \
  _Pragma("unroll") for (int i = (i0); i < (i1); ++i) {                                                    \
    s0 = __builtin_amdgcn_mfma_f32_4x4x4f16(__builtin_bit_cast(f16x4_t, eye), H4(t[i], 0), s0, 0, 0, 0);   \
    s1 = __builtin_amdgcn_mfma_f32_4x4x4f16(__builtin_bit_cast(f16x4_t, eye), H4(t[i], 1), s1, 0, 0, 0);   \
  }
    float rc[4];
    u32x2 z0, z1;
#define Z_RD { z0 = *LP(volatile u32x2, zfa, 0); z1 = *LP(volatile u32x2, zfb, 0); }
    f32x4 d[4];
#define DEN1() do { const u32x4 zf = {z0[0], z0[1], z1[0], z1[1]}; _Pragma("unroll") for (int mi = 0; mi < 4; ++mi) d[mi] = MFMA(FRAG(zf), PF(mi), zero); } while (0)
#define DEN2()                                                                                             \
  do {                                                                                                     \
    _Pragma("unroll") for (int mi = 0; mi < 4; ++mi) rc[mi] = __builtin_amdgcn_rcpf(d[mi][0] + 1e-6f);     \
    _Pragma("unroll") for (int mi = 0; mi < 4; ++mi) { rc[mi] *= gq[mi]; PIN(rc[mi]); }                    \
  } while (0)
    KV_MM(0, DENAT - 4);
    SB();
    unsigned zfl = *LP(volatile unsigned, zfp, 0);
    CFENCE();
    Z_RD
    SB();
    KV_MM(DENAT - 4, DENAT);
    SB();
    DEN1();
    SB();
    KV_MM(DENAT, DENAT + 2);
    SB();
    DEN2();
    const u64 zbad = __builtin_amdgcn_ballot_w64(zfl != 1u);
    SB();
    KV_MM(DENAT + 2, 16);
    const u64 kbad = __builtin_amdgcn_ballot_w64(__builtin_isunordered(s0[0], s1[0]));
    if (__builtin_expect((kbad | zbad) != 0ull, 0)) {
      if (kbad != 0ull) {
        unsigned todo = 15u;
#pragma unroll 1
        for (unsigned spins = 0; spins <= SPIN_CAP; ++spins) {
          unsigned miss = 0;
#pragma unroll
          for (int q = 0; q < 4; ++q)
            if (todo & (1u << q)) { if (!all_true(max(max(MX4(t[4 * q]), MX4(t[4 * q + 1])), max(MX4(t[4 * q + 2]), MX4(t[4 * q + 3]))) != ~0u)) miss |= 1u << q; }
          todo = miss;
          if (todo == 0u) break;
          __builtin_amdgcn_s_sleep(2);
          CFENCE();
#pragma unroll
          for (int q = 0; q < 4; ++q)
            if (todo & (1u << q)) { T_LD(4 * q); T_LD(4 * q + 1); T_LD(4 * q + 2); T_LD(4 * q + 3); }
        }
        s0 = zero; s1 = zero;
        KV_MM(0, 16);
      }
      zfl = rfl(zfl);
      if (zfl == 0u) { TAKE(zfl = misc[C_ZF + hh], Z_RD); zfl = rfl(zfl); DEN1(); DEN2(); }
      if (zfl != 1u) {
        unsigned sc;
#pragma unroll 1
        for (unsigned spins = 0; spins <= SPIN_CAP; ++spins) {
          CFENCE();
          sc = __builtin_amdgcn_raw_buffer_load_b32(wsr, (unsigned)OFF_ZP + ZSC + (rec - mt + c16) * 16, 0, 16);
          if (all_true(sc != ~0u)) break;
        }
        s0 = zero; s1 = zero;
#pragma unroll
        for (int i = 0; i < 16; ++i) {
          const float s_ = u2f((unsigned)__builtin_amdgcn_readlane((int)sc, i));
#pragma unroll
          for (int e = 0; e < 4; ++e) {
            const unsigned tw = t[i][e];
            const f16x2_t h = __builtin_bit_cast(f16x2_t, tw);
            f32x4& d = e < 2 ? s0 : s1;
            d[2 * (e & 1)] = __builtin_fmaf((float)h[0], s_, d[2 * (e & 1)]); d[2 * (e & 1) + 1] = __builtin_fmaf((float)h[1], s_, d[2 * (e & 1) + 1]);
          }
        }
      }
    }
    const u32x4 kown = {cvt_pk(s0[0], s0[1]), cvt_pk(s0[2], s0[3]), cvt_pk(s1[0], s1[1]), cvt_pk(s1[2], s1[3])};
    *LP(u32x2, x2w, 8) = (u32x2){kown[0], kown[1]}; *LP(u32x2, x2w, 0) = (u32x2){kown[2], kown[3]};
    CFENCE();
    *LP(volatile unsigned, cw, W_X2) = one;
    SB();
    f32x4 o[4];
    u32x4 koth;
#pragma unroll
    for (int mi = 0; mi < 4; ++mi) o[mi] = MFMA(FRAG(kown), PF(mi), zero);
    const unsigned x2r = x2w ^ 2048u;
    SB();
#define O_CVT(mi, h) do { ov[mi][h] = (u32x2){cvt_pk(o[mi][0] * rc[mi], o[mi][1] * rc[mi]), cvt_pk(o[mi][2] * rc[mi], o[mi][3] * rc[mi])}; if ((h) == 0) PIN(ov[mi][0]); } while (0)
    O_CVT(0, 0); SB();
    const unsigned f2 = *LP(volatile unsigned, cr, W_X2);
    CFENCE();
    koth = *LP(volatile u32x4, x2r, 0);
    SB();
    O_CVT(1, 0); O_CVT(2, 0); O_CVT(3, 0);
    SB();
    if (__builtin_expect(!all_true(f2 != 0u), 0)) TAKE(*LP(volatile unsigned, cr, W_X2), koth = *LP(volatile u32x4, x2r, 0));
#pragma unroll
    for (int mi = 0; mi < 4; ++mi) o[mi] = MFMA(FRAG(koth), PF(mi), zero);
    SB();
    unsigned oom[4];
#pragma unroll
    for (int mi = 0; mi < 4; ++mi) { oom[mi] = oo + mi * (NH * 1024); PIN(oom[mi]); }
    SB();
#pragma unroll
    for (int mi = 0; mi < 4; ++mi) { O_CVT(mi, 1); __builtin_amdgcn_raw_buffer_store_b128((u32x4){ov[mi][0][0], ov[mi][0][1], ov[mi][1][0], ov[mi][1][1]}, wsr, oom[mi], 0, 16); SB(); }
  }
  SB();
  f32x4 oc[4][2];
  unsigned xa[4], xb[2], so[4];
  unsigned rlo, rhi;
  {
    u32x2 xl[8];
#pragma unroll
    for (int n = 0; n < 4; ++n) { xa[n] = (unsigned)(((32 * w + c16) * 128 + (((g >> 1) ^ (c16 >> 1)) * 16) + (g & 1) * 8) ^ (n * 32)); PIN(xa[n]); }
#pragma unroll
    for (int i = 0; i < 8; ++i) xl[i] = *LP(volatile u32x2, xa[i & 3], (i >> 2) * 2048);
    const unsigned kw = w * 4096 + lane * 16, wlo = hh ? x1w : kw, whi = hh ? kw : x1w;
    rlo = hh ? x1w ^ 2048u : kw; rhi = hh ? kw : x1w ^ 2048u;
#pragma unroll
    for (int mi = 0; mi < 4; ++mi) *LP(volatile u32x4, mi < 2 ? wlo : whi, (mi & 1) * 1024) = (u32x4){ov[mi][0][0], ov[mi][0][1], ov[mi][1][0], ov[mi][1][1]};
    *LP(volatile unsigned, cw, W_X1) = 2u;
    unsigned fo = lane == 0 ? (unsigned)OFF_OFLAG + (unsigned)(mt * 16 + nt) * 128u : 0x80000000u, fa = L_CW + (lane & 3) * 128, two = 2u;
    PIN(fo); PIN(fa); PIN(two); PIN(ltag);
    SB();
    WAIT_VM(0);
    *LP(volatile unsigned, cw, W_KS) = two;
    if (all_true(*LP(volatile unsigned, fa, W_KS) == 2u)) __builtin_amdgcn_raw_buffer_store_b32(ltag, wsr, fo, 0, 16);
    CFENCE();
    SB();
#pragma unroll
    for (int i = 0; i < 8; ++i) {
      const int tt = i >> 2;
      const float omg = bfr(1.0f - (tt ? (hh ? gq[3] : gq[1]) : (hh ? gq[2] : gq[0])));
#pragma unroll
      for (int k = 0; k < 2; ++k) {
        const unsigned pr = cvt_pk(omg * blo(xl[i][k]), omg * bhi(xl[i][k]));
        oc[i & 3][tt][2 * k] = blo(pr); oc[i & 3][tt][2 * k + 1] = bhi(pr);
      }
      PIN(oc[i & 3][tt]);
    }
#pragma unroll
    for (int i = 0; i < 2; ++i) { xb[i] = (unsigned)((32 * w + 8 * i + (lane >> 3)) * 128 + (((lane & 7) ^ (4 * i + g)) * 16)); PIN(xb[i]); }
#pragma unroll
    for (int i = 0; i < 4; ++i) { so[i] = (unsigned)(((m0 + 32 * w + 8 * i + (lane >> 3)) * DM + 64 * nt3) * 2 + (lane & 7) * 16); PIN(so[i]); }
  }
  int wof[2][2][4];
#pragma unroll
  for (int n = 0; n < 16; ++n) {
    wof[n >> 3][(n >> 2) & 1][n & 3] = 16384 + ((n >> 2) & 1) * 65536 + (((n >> 3) ^ wr) * 16 + 4 * g + (c16 >> 2)) * 128 + (((n & 3) ^ fsw) * 32) + (c16 & 3) * 8;
    PIN(wof[n >> 3][(n >> 2) & 1][n & 3]);
  }
  unsigned ob0 = (unsigned)OFF_OF + (unsigned)(((mt3 * 8 + 2 * w) * NH) * 1024 + lane * 16), ob1 = ob0 + NH * 1024;
  int cs1 = CH(1) * 2048, cs2 = CH(2) * 2048, cs3 = CH(3) * 2048;
  bf16x8 wf[2][4], wl[4];
  s16x4 tl2;
#define W_AT(hd, n, r) (smem + wof[r][WOPOS((hd) >> 1) >> 3][n] + (WOPOS((hd) >> 1) & 7) * 8192 + ((hd) & 1) * 4096)
  const unsigned lw = min(misc[C_WO], misc[C_WO + 1]);
  PIN(ob0); PIN(ob1); asm volatile("" : "+s"(cs1), "+s"(cs2), "+s"(cs3));
  SB();

  if (__builtin_expect(rfl(lw) == 0u, 0)) { (void)wait_ge<1>(misc + C_WO, 1u); (void)wait_ge<1>(misc + C_WO + 1, 1u); }
  CFENCE();
#pragma unroll
  for (int n = 0; n < 4; ++n) wf[0][n] = CAT(TRD(W_AT(0, n, 0)), TRD(W_AT(0, n, 1)));
  u32x4 of[OPF + 1][4];
  unsigned rdy = 0;
#define O_READY(c) do { if (__builtin_expect(rdy <= (unsigned)(c), 0)) rdy = wait_ge<0>(misc + C_ORDY, (unsigned)(c) + 1u); CFENCE(); } while (0)
#define O_LD1(c, k4) of[(c) % (OPF + 1)][k4] = __builtin_amdgcn_raw_buffer_load_b128(wsr, (((k4) & 1) ? ob1 : ob0) + ((k4) >> 1) * 1024, (c) == 1 ? cs1 : (c) == 2 ? cs2 : (c) == 3 ? cs3 : CH(c) * 2048, 16)
  TAKE(*LP(volatile unsigned, cw ^ 128u, W_X1) >> 1, { _Pragma("unroll") for (int k = 0; k < 4; ++k) of[0][k] = *LP(volatile u32x4, k < 2 ? rlo : rhi, (k & 1) * 1024); });
  unsigned li = 0;
#define P3_MM(c, j)                                                                                        \
  do {                                                                                                     \
    constexpr int hd = 2 * (c) + ((j) >> 3), n = ((j) >> 1) & 3;                                           \
    AMFMA(oc[n][(j) & 1], wf[(j) >> 3][n], of[(c) % (OPF + 1)][2 * ((j) >> 3) + ((j) & 1)]); SB();         \
    if (((j) & 1) == 0) tlo = TRD(W_AT(hd + 1, n, 0)); else wf[1 - ((j) >> 3)][n] = CAT(tlo, TRD(W_AT(hd + 1, n, 1))); \
    if ((c) > 0 && (c) + OPF < 16) {                                                                       \
      if ((j) == 0) O_READY((c) + OPF);                                                                    \
      if (((j) & 3) == 1) O_LD1((c) + OPF, (c) + OPF == 15 ? (((j) >> 3) | (((j) >> 1) & 2)) : (j) >> 2);  \
    }                                                                                                      \
    if ((c) == 14 && (j) < 8) { if (((j) & 1) == 0) tl2 = TRD(W_AT(31, n, 0)); else wl[n] = CAT(tl2, TRD(W_AT(31, n, 1))); } \
    if ((c) == 14 && (j) == 8) li = misc[C_IDX];                                                           \
    SB();                                                                                                  \
  } while (0)
#define P3_4(c, j) P3_MM(c, j); P3_MM(c, (j) + 1); P3_MM(c, (j) + 2); P3_MM(c, (j) + 3)
#define P3_C(c) P3_4(c, 0); P3_4(c, 4); P3_4(c, 8); P3_4(c, 12)
  P3_C(0);
#pragma unroll
  for (int c = 1; c <= OPF; ++c) { O_READY(c); O_LD1(c, 0); SB(); O_LD1(c, 1); SB(); O_LD1(c, 2); SB(); O_LD1(c, 3); SB(); }
  P3_C(1); P3_C(2); P3_C(3); P3_C(4); P3_C(5); P3_C(6); P3_C(7);
  P3_C(8); P3_C(9); P3_C(10); P3_C(11); P3_C(12); P3_C(13); P3_C(14);
  li = rfl(li);
  if (__builtin_expect(li == 0u, 0)) li = wait_ge<1>(misc + C_IDX, 1u);
  const int idx = (int)li - 1;
  if ((idx >> 5) == mt3 * 4 + w) {
    f32x4 dl[4];
    NOP8(oc);
    TAKE(misc[C_Y], { _Pragma("unroll") for (int n = 0; n < 4; ++n) dl[n] = *(volatile lf32x4*)(smem + L_RED + 2048 + n * 64 + g * 16); });
    const bool mine = c16 == (idx & 15);
    if (idx & 16) {
#pragma unroll
      for (int n = 0; n < 4; ++n) oc[n][1] += mine ? dl[n] : zero;
    } else {
#pragma unroll
      for (int n = 0; n < 4; ++n) oc[n][0] += mine ? dl[n] : zero;
    }
    NOP8(oc);
  }
  u32x4 ot[4];
  const __amdgpu_buffer_rsrc_t outr = MKRSRC(out, SEQ * DM * 2);
#define E_MM(tt, h, n) do { AMFMA(oc[n][tt], (h) ? wl[n] : wf[0][n], of[15 % (OPF + 1)][2 * (h) + (tt)]); SB(); } while (0)
#define E_WR(tt, n) do { *LP(volatile u32x2, xa[n], (tt) * 2048) = (u32x2){cvt_pk(oc[n][tt][0], oc[n][tt][1]), cvt_pk(oc[n][tt][2], oc[n][tt][3])}; SB(); } while (0)
#define E_RD(tt) do { ot[2 * (tt)] = *LP(volatile u32x4, xb[0], (tt) * 2048); ot[2 * (tt) + 1] = *LP(volatile u32x4, xb[1], (tt) * 2048); SB(); } while (0)
#define E_ST(i) do { __builtin_amdgcn_raw_buffer_store_b128(ot[i], outr, so[i], 0, 17); SB(); } while (0)
#define E_NOP(tt) asm volatile("s_nop 4" : "+v"(oc[0][tt]))
  SB();
  E_MM(0, 0, 0); E_MM(0, 0, 1); E_MM(0, 0, 2); E_MM(0, 0, 3); E_MM(0, 1, 0); E_MM(0, 1, 1); E_MM(0, 1, 2); E_MM(0, 1, 3);
  E_NOP(0); E_WR(0, 0); E_WR(0, 1);
  E_MM(1, 0, 0); E_WR(0, 2); E_MM(1, 0, 1); E_WR(0, 3); E_MM(1, 0, 2); E_RD(0); E_MM(1, 0, 3);
  E_MM(1, 1, 0); E_MM(1, 1, 1); E_MM(1, 1, 2); E_MM(1, 1, 3);
  E_ST(0); E_ST(1);
  E_NOP(1); E_WR(1, 0); E_WR(1, 1); E_WR(1, 2); E_WR(1, 3); E_RD(1);
  E_ST(2); E_ST(3);
}

extern "C" void kernel_launch(void* const* d_in, const int* in_sizes, int n_in,
                              void* d_out, int out_size, void* d_ws, size_t ws_size,
                              hipStream_t stream) {
  char* ws = (char*)d_ws;
  (void)hipFuncSetAttribute((const void*)fused_kernel, hipFuncAttributeMaxDynamicSharedMemorySize, L_TOTAL);
  fused_kernel<<<256, 512, L_TOTAL, stream>>>((const u16*)d_in[0], (const u16*)d_in[1], (const u16*)d_in[2], ws, (u16*)d_out,
                                              (const u16*)d_in[4], (const u16*)d_in[8], (const u16*)d_in[5], (const u16*)d_in[6],
                                              (const u16*)d_in[7]);
}
```

```cpp
#include <hip/hip_runtime.h>
#include <hip/hip_bf16.h>
#include <stdint.h>

typedef unsigned short u16;
typedef unsigned long long u64;
typedef __attribute__((ext_vector_type(8))) short bf16x8;
typedef __attribute__((ext_vector_type(4))) short s16x4;
typedef __attribute__((ext_vector_type(4))) float f32x4;
typedef __attribute__((ext_vector_type(2))) float f32x2;
typedef __attribute__((ext_vector_type(2))) __bf16 bf16x2_t;
typedef __attribute__((ext_vector_type(2))) _Float16 f16x2_t;
typedef __attribute__((ext_vector_type(4))) _Float16 f16x4_t;
typedef __attribute__((ext_vector_type(4))) unsigned int u32x4;
typedef __attribute__((ext_vector_type(2))) unsigned int u32x2;
typedef __attribute__((ext_vector_type(8))) unsigned int u32x8;
typedef __attribute__((address_space(3))) s16x4* lds_s16x4_ptr;
typedef __attribute__((address_space(1))) unsigned gu32;
typedef __attribute__((address_space(1))) u64 gu64;
typedef __attribute__((address_space(3))) unsigned lu32;
typedef __attribute__((address_space(3))) u32x2 lu32x2;
typedef __attribute__((address_space(3))) u32x4 lu32x4;
typedef __attribute__((address_space(3))) f32x4 lf32x4;
#define RLX_AGENT __ATOMIC_RELAXED, __HIP_MEMORY_SCOPE_AGENT

#if defined(__HIP_DEVICE_COMPILE__)
#define NOPK __attribute__((target("no-packed-fp32-ops")))
#else
#define NOPK
#endif

#define SEQ 2048
#define DM 1024
#define N1 3072
#define FF 3584
#define NH 32

#define OFF_EPOCH  ((size_t)0)
#define OFF_RG     ((size_t)4096)
#define OFF_GG     ((size_t)16384)
#define OFF_GU     ((size_t)262144)
#define OFF_YG     ((size_t)524288)
#define OFF_KVP    ((size_t)1 << 20)
#define OFF_ZP     ((size_t)2 << 20)
#define ZSC        65536u
#define OFF_OFLAG  (((size_t)3 << 20) + 65536)
#define OFF_OF     ((size_t)4 << 20)

#define SPIN_CAP (1u << 18)

__device__ __forceinline__ float u2f(unsigned u) { return __builtin_bit_cast(float, u); }
__device__ __forceinline__ unsigned f2u(float f) { return __builtin_bit_cast(unsigned, f); }
__device__ __forceinline__ bool all_true(bool ok) { return __builtin_amdgcn_ballot_w64(!ok) == 0ull; }
__device__ __forceinline__ unsigned cvt_pk(float a, float b) {
  f32x2 v = {a, b};
  return __builtin_bit_cast(unsigned, __builtin_convertvector(v, bf16x2_t));
}
__device__ __forceinline__ float blo(unsigned u) { return u2f(u << 16); }
__device__ __forceinline__ float bhi(unsigned u) { return u2f(u & 0xFFFF0000u); }
__device__ __forceinline__ float bfr(float f) { return blo(cvt_pk(f, f)); }
__device__ __forceinline__ float fmax_raw(float a, float b) { return __builtin_elementwise_maximum(a, b); }
__device__ __forceinline__ unsigned eps2(unsigned p) { return cvt_pk(blo(p) + 1e-6f, bhi(p) + 1e-6f); }
template <int N>
__device__ __forceinline__ void phi_fix(unsigned (&p)[N], float umin) {
  if (__builtin_expect(__builtin_amdgcn_ballot_w64(umin < -5.9f) != 0ull, 0)) {
#pragma unroll
    for (int i = 0; i < N; ++i) p[i] = eps2(p[i]);
  }
}
__device__ __forceinline__ float sigmoid_f(float x) { return 1.0f / (1.0f + __expf(-x)); }

template <int CTRL, int RM = 0xf> __device__ __forceinline__ int dppi(int old, int v) {
  return __builtin_amdgcn_update_dpp(old, v, CTRL, RM, 0xf, false);
}
template <int CTRL, int RM = 0xf> __device__ __forceinline__ float dppf(float v) {
  return __builtin_bit_cast(float, dppi<CTRL, RM>(0, __builtin_bit_cast(int, v)));
}
__device__ __forceinline__ unsigned wave_max(unsigned v) {
  v = max(v, (unsigned)dppi<0x111>(0, (int)v)); v = max(v, (unsigned)dppi<0x112>(0, (int)v));
  v = max(v, (unsigned)dppi<0x114>(0, (int)v)); v = max(v, (unsigned)dppi<0x118>(0, (int)v));
  v = max(v, (unsigned)dppi<0x142, 0xa>(0, (int)v)); v = max(v, (unsigned)dppi<0x143, 0xc>(0, (int)v));
  return (unsigned)__builtin_amdgcn_readlane((int)v, 63);
}
__device__ __forceinline__ float merge32(float a, float b) {
  const auto r = __builtin_amdgcn_permlane32_swap(f2u(a), f2u(b), false, false);
  return u2f(r[0]) + u2f(r[1]);
}
__device__ __forceinline__ float merge16(float a, float b) {
  const auto r = __builtin_amdgcn_permlane16_swap(f2u(a), f2u(b), false, false);
  return u2f(r[0]) + u2f(r[1]);
}
typedef __attribute__((address_space(3))) void* ldsp;
template <int IMM>
__device__ __forceinline__ void bufl16(__amdgpu_buffer_rsrc_t r, unsigned l, unsigned voff, unsigned soff) {
#if defined(__HIP_DEVICE_COMPILE__)
  __builtin_amdgcn_raw_ptr_buffer_load_lds(r, (ldsp)(uintptr_t)l, 16, voff, soff, IMM, 0);
#endif
}
#define LP(T, base, imm) ((__attribute__((address_space(3))) T*)(uintptr_t)(unsigned)((base) + (imm)))
#define MKRSRC(p, n) __builtin_amdgcn_make_buffer_rsrc((void*)(p), 0, (n), 0x00020000)
#define WAIT_VM(n) asm volatile("s_waitcnt vmcnt(%0)" ::"n"(n) : "memory")
#define WAIT_LGKM0() asm volatile("s_waitcnt lgkmcnt(0)" ::: "memory")
#define BAR() do { __builtin_amdgcn_sched_barrier(0); asm volatile("s_barrier" ::: "memory"); __builtin_amdgcn_sched_barrier(0); } while (0)
#define CFENCE() asm volatile("" ::: "memory")
#define MFMA(a, b, c) __builtin_amdgcn_mfma_f32_16x16x32_bf16((a), (b), (c), 0, 0, 0)
#define PIN(v) asm volatile("" : "+v"(v))
#define AMFMA(acc, a, b) asm volatile("v_mfma_f32_16x16x32_bf16 %0, %1, %2, %0" : "+v"(acc) : "v"(a), "v"(b))
#define NOP8(t) asm volatile("s_nop 7" : "+v"(t[0][0]), "+v"(t[0][1]), "+v"(t[1][0]), "+v"(t[1][1]), "+v"(t[2][0]), "+v"(t[2][1]), "+v"(t[3][0]), "+v"(t[3][1]))
#define SB() __builtin_amdgcn_sched_barrier(0)
#define TRD(p) ((s16x4)__builtin_amdgcn_ds_read_tr16_b64_v4i16((lds_s16x4_ptr)(p)))
#define CAT(lo, hi) __builtin_shufflevector(lo, hi, 0, 1, 2, 3, 4, 5, 6, 7)
#define FRAG(x) __builtin_bit_cast(bf16x8, x)

#define PACE   57
#define BURST  30
#define R_AT   2
#define R_GAP  2
#define CPF_N  20
#define WO_E   6
#define WO_ES  4
#define WO_M   9
#define G_T    35
#define P5_T   30
#define GU_AH  8
#define DN_E   8
#define GU_E   4
#define DENAT  12
#define OPF    3

__device__ __forceinline__ unsigned rfl(unsigned v) { return (unsigned)__builtin_amdgcn_readfirstlane((int)v); }
template <int SLEEP>
__device__ __forceinline__ unsigned wait_ge(volatile lu32* p, unsigned need) {
  unsigned v = 0;
#pragma unroll 1
  for (unsigned spins = 0; spins < (1u << 22); ++spins) {
    v = rfl(*p);
    if (v >= need) break;
    if (SLEEP) __builtin_amdgcn_s_sleep(SLEEP);
  }
  CFENCE();
  return v;
}

#define SLOT     40960
#define L_X1     147456
#define L_X2     155648
#define L_CW     159744
#define L_ZF     160256
#define L_GATE   160384
#define L_RED    160896
#define L_H      163200
#define L_CTL    163712
#define L_TOTAL  163840
#define W_X1   64
#define W_X2   68
#define W_AG   72
#define W_KS   76
#define WFLAGS(f) (*(volatile lu32*)(smem + L_CW + (lane & 3) * 128 + (f)))
#define WOPOS(c) ((c) < 3 ? 13 + (c) : (c) < 8 ? (c) : (c) < 11 ? (c) - 8 : (c) - 3)
#define C_F15  0
#define C_WO   2
#define C_ZF   4
#define C_TAG  8
#define C_IDX  10
#define C_GO   12
#define C_ORDY 18
#define C_GU   19
#define C_DN   20
#define C_Y    23

NOPK __global__ __launch_bounds__(512, 2) void fused_kernel(const u16* __restrict__ x, const u16* __restrict__ wqkv,
                                                            const u16* __restrict__ wo, char* ws, u16* __restrict__ out,
                                                            const u16* __restrict__ w_r, const u16* __restrict__ gate_w,
                                                            const u16* __restrict__ w_gate, const u16* __restrict__ w_up,
                                                            const u16* __restrict__ w_down) {
  extern __shared__ __attribute__((aligned(16))) char smem[];
  asm volatile("s_load_dwordx2 s[100:101], %0, 60" :: "s"(__builtin_amdgcn_kernarg_segment_ptr()) : "s100", "s101");
  SB();
  const int tid = threadIdx.x, lane = tid & 63;
  const int w = __builtin_amdgcn_readfirstlane(tid >> 6);
  u64 pc0;
  asm volatile("s_getpc_b64 %0" : "=s"(pc0));
  const int bid = blockIdx.x, xcd = bid & 7, ix = bid >> 3;
  const int ml = ix >> 2, nl = ix & 3;
  const int mt = (xcd >> 2) * 8 + ml, nt = (xcd & 3) * 4 + nl;
  const int m0 = mt * 128;
  const int mt3 = mt, nt3 = nt ^ 1;
#define CH(c) (((4 * (xcd & 3) + (c)) & 15) ^ ((c) < 4 ? nl : 0))
  volatile lu32* misc = (volatile lu32*)(smem + L_CTL);

  if (__builtin_expect((w >> 1) == 2, 1)) {
    u32x8 ka;
    asm volatile("s_load_dwordx8 %0, %1, 0x0" : "=s"(ka) : "s"(__builtin_amdgcn_kernarg_segment_ptr()));
    SB();
    const int p = w - 4;
    unsigned va[4], vb[4], vw[4];
#pragma unroll
    for (int j = 0; j < 4; ++j) {
      const int row = 32 * (w & 1) + 8 * j + (lane >> 3);
      va[j] = (unsigned)((m0 + row) * (DM * 2) + (((lane & 7) ^ ((row >> 1) & 7)) * 16) - 1024 * j);
      vb[j] = (unsigned)(row * (N1 * 2) + 128 * nt + (((lane & 7) ^ ((row & 2) | ((j & 1) << 2))) * 16) - 1024 * j);
      PIN(va[j]); PIN(vb[j]);
    }
    unsigned l0 = (unsigned)(uintptr_t)(ldsp)smem + p * 4096;
    int kf = (4 * ml + 4 * (xcd & 3)) & 15, ks = (4 * ml - 2 * (ml >> 2) + 4 * (xcd & 3)) & 15, sres = (3 - nt) & 7;
    asm volatile("" : "+s"(l0), "+s"(kf), "+s"(ks), "+s"(sres));
    SB();
    asm volatile("s_waitcnt lgkmcnt(0)" : "+s"(ka));
    const u16* x = (const u16*)(((u64)ka[1] << 32) | ka[0]);
    const u16* wqkv = (const u16*)(((u64)ka[3] << 32) | ka[2]);
    const u16* wo = (const u16*)(((u64)ka[5] << 32) | ka[4]);
    char* ws = (char*)(((u64)ka[7] << 32) | ka[6]);
    int ko_ = (int)((ka[2] >> 7) & 7u) == sres ? ks : kf;
    const __amdgpu_buffer_rsrc_t ra = MKRSRC(x, SEQ * DM * 2), rb = MKRSRC(wqkv, DM * N1 * 2), rw = MKRSRC(wo, DM * DM * 2);
#define UNIT(r, v, off, so) do { bufl16<0>(r, l0 + (off), v[0], so); bufl16<1024>(r, l0 + (off), v[1], so); bufl16<2048>(r, l0 + (off), v[2], so); bufl16<3072>(r, l0 + (off), v[3], so); } while (0)
#define ISSUE(t)                                                                                           \
  do {                                                                                                     \
    const int k_ = (ko_ + (t)) & 15;                                                                       \
    UNIT(ra, va, ((t) % 3) * SLOT, k_ * 128); UNIT(ra, va, ((t) % 3) * SLOT + 8192, k_ * 128 + 64 * DM * 2); \
    _Pragma("unroll") for (int s_ = 0; s_ < 3; ++s_) UNIT(rb, vb, ((t) % 3) * SLOT + 16384 + s_ * 8192, k_ * (64 * N1 * 2) + s_ * (DM * 2)); \
  } while (0)
#define PACED_BAR()                                                                                        \
  do {                                                                                                     \
    if (p == 0) {                                                                                          \
      unsigned now;                                                                                        \
      do { now = (unsigned)__builtin_amdgcn_s_memrealtime(); } while ((int)(now - tp) < 0);                \
      tp = ((int)(now - tp) > BURST ? now - BURST : tp) + PACE;                                            \
    }                                                                                                      \
    BAR();                                                                                                 \
  } while (0)
    ISSUE(0); ISSUE(1);
    WAIT_VM(20);
    BAR();
    ISSUE(2);
    unsigned tp = (unsigned)__builtin_amdgcn_s_memrealtime() - PACE;
#pragma unroll
    for (int j = 0; j < 4; ++j) vw[j] = (unsigned)((32 * p + 8 * j + (lane >> 3)) * (DM * 2) + 128 * nt3 + (((lane & 7) ^ ((lane >> 3) & 6)) * 16) - 1024 * j);
#define WO_CHUNK(c) UNIT(rw, vw, 16384 + WOPOS(c) * 8192, CH(c) * (64 * DM * 2))
#pragma unroll
    for (int t = 0; t < 13; ++t) {
      WAIT_VM(20);
      PACED_BAR();
      asm volatile("" : "+s"(l0), "+s"(ko_));
      ISSUE(t + 3);
    }
    WO_CHUNK(0); WO_CHUNK(1); WO_CHUNK(2);
    SB();
    const bool wslow = ((((uintptr_t)wo >> 7) + nt3) & 7) == 3;
    const int woe = wslow ? WO_ES : WO_E, wom = wslow ? WO_M + 1 : WO_M;
    WAIT_VM(32);
    PACED_BAR();
    WAIT_VM(12);
    if (lane == 0) misc[C_F15 + p] = 1u;
#pragma unroll 1
    for (int c = 3; c < woe; ++c) WO_CHUNK(c);
    {
      const __amdgpu_buffer_rsrc_t zr = MKRSRC(ws + OFF_ZP, NH * 2 * 16 * 128);
      const unsigned zo = (unsigned)((2 * nt + p) * 2048 + lane * 16);
      u32x4 zw[2];
      unsigned zm;
      (void)wait_ge<1>(misc + C_IDX + p, 1u);
#pragma unroll 1
      for (unsigned spins = 0; spins < (1u << 22) && !all_true(WFLAGS(W_KS) != 0u); ++spins) __builtin_amdgcn_s_sleep(1);
      CFENCE();
      const unsigned tk = (unsigned)__builtin_amdgcn_s_memrealtime();
#pragma unroll 1
      for (int c = woe; c < wom; ++c) WO_CHUNK(c);
      UNIT(ra, va, 0, nt3 * 128); UNIT(ra, va, 8192, nt3 * 128 + 64 * DM * 2);
      while ((unsigned)__builtin_amdgcn_s_memrealtime() - tk < (unsigned)G_T) __builtin_amdgcn_s_sleep(1);
#define MX4(v) max(max((v)[0], (v)[1]), max((v)[2], (v)[3]))
#pragma unroll 1
      for (unsigned spins = 0; spins <= SPIN_CAP; ++spins) {
        CFENCE();
        zw[0] = __builtin_amdgcn_raw_buffer_load_b128(zr, zo, 0, 16); zw[1] = __builtin_amdgcn_raw_buffer_load_b128(zr, zo + 1024, 0, 16);
        SB(); misc[C_GO + p] = 1u; SB();
        zm = max(MX4(zw[0]), MX4(zw[1]));
        if (all_true(zm != ~0u)) break;
        __builtin_amdgcn_s_sleep(2);
      }
      const bool plain = all_true((int)zm >= 0);
      if (__builtin_expect(!plain, 0)) { zw[0] &= 0x7FFFFFFFu; zw[1] &= 0x7FFFFFFFu; }
      float z0 = merge16(merge32(u2f(zw[0][0]), u2f(zw[0][2])), merge32(u2f(zw[1][0]), u2f(zw[1][2])));
      float z1 = merge16(merge32(u2f(zw[0][1]), u2f(zw[0][3])), merge32(u2f(zw[1][1]), u2f(zw[1][3])));
      z0 += dppf<0x128>(z0); z1 += dppf<0x128>(z1); z0 += dppf<0x124>(z0); z1 += dppf<0x124>(z1);
      *(unsigned*)(smem + L_ZF + p * 64 + (lane & 3) * 16 + ((lane >> 4) & 1) * 8 + (lane >> 5) * 4) = cvt_pk(z0, z1);
      CFENCE();
      misc[C_ZF + p] = plain ? 1u : 2u;
    }
#pragma unroll 1
    for (unsigned spins = 0; spins < (1u << 22) && !all_true(WFLAGS(W_AG) != 0u); ++spins) __builtin_amdgcn_s_sleep(2);
    CFENCE();
#pragma unroll 1
    for (int c = wom; c < 16; ++c) WO_CHUNK(c);
    WAIT_VM(0);
    if (lane == 0) misc[C_WO + p] = 1u;
#undef ISSUE
#undef PACED_BAR
    if (p == 0) return;
    const unsigned tag = rfl(misc[C_TAG]);
    gu32* of = (gu32*)(ws + OFF_OFLAG + (mt3 * 16 + CH(lane & 15)) * 128);
#pragma unroll 1
    for (unsigned spins = 0; spins < (1u << 22) && !all_true(WFLAGS(W_KS) == 2u); ++spins) __builtin_amdgcn_s_sleep(1);
    const unsigned t6 = (unsigned)__builtin_amdgcn_s_memrealtime();
    while ((unsigned)__builtin_amdgcn_s_memrealtime() - t6 < (unsigned)P5_T) __builtin_amdgcn_s_sleep(1);
#pragma unroll 1
    for (unsigned spins = 0;; ++spins) {
      const unsigned f = __hip_atomic_load(of, RLX_AGENT);
      const unsigned miss = (unsigned)__builtin_amdgcn_ballot_w64(f != tag) & 0xFFFEu;
      const unsigned m = (unsigned)__builtin_ctz((spins > SPIN_CAP ? 0u : miss) | 0x10000u);
      misc[C_ORDY] = m;
      if (m == 16u) break;
      __builtin_amdgcn_s_sleep(2);
    }
    return;
  }

  if (__builtin_expect(w >= 6, 0)) {
    const int pa = w - 6;
#define CODE_PF(on) __builtin_amdgcn_raw_buffer_load_b32(MKRSRC((pc0 & ~255ull) + (blockIdx.x >> 3) * (CPF_N * 64u), (on) ? CPF_N * 64 : 0), lane * 64, 0, 0)
    unsigned cpf = CODE_PF(w == 7), cpf2;
    SB();
    float* red = (float*)(smem + L_RED);
    const bool help = bid >= 224, gemv = bid < 252;
    const int gmat = bid & 1;
    const u16* wgu = gmat ? w_up : w_gate;
    const int sres = (int)(3u - (unsigned)((uintptr_t)wgu >> 7)) & 7;
    const int gfs = help ? sres + 8 * (((bid - 224) >> 1) % 7) : (bid >> 1) % 56;
    const int gdh = (help ? ((bid - 224) >> 1) / 7 : (bid >> 1) / 56) & 1;
    const bool act0 = !help, act1 = gemv && (help || (gfs & 7) != sres);
    const int gd0 = gdh * 512 + 256 * pa + 32 * (lane >> 3);
    const __amdgpu_buffer_rsrc_t rgu0 = MKRSRC(wgu, act0 ? DM * FF * 2 : 0), rgu1 = MKRSRC(wgu, act1 ? DM * FF * 2 : 0);
    const unsigned wpo = (unsigned)((gd0 * FF + gfs * 64 + 8 * (lane & 7)) * 2);
    u32x4 wv[32], wd[14];
    const int fg = bid >> 4, cg = bid & 15;
    const int f0 = fg * 224 + pa * 112;
    const __amdgpu_buffer_rsrc_t rdn = MKRSRC(w_down, DM * FF * 2);
    const unsigned wqo = (unsigned)(((f0 + 14 * (lane >> 3)) * DM + cg * 64 + 8 * (lane & 7)) * 2);
    asm volatile("" ::"s"(w_r), "s"(gate_w), "s"(ws), "s"(w_gate), "s"(w_up), "s"(w_down));
    unsigned pf;
#define GU_LD(i0, i1) do { SB(); _Pragma("unroll") for (int i = (i0); i < (i1); ++i) wv[i] = __builtin_amdgcn_raw_buffer_load_b128(i < 16 ? rgu0 : rgu1, wpo, i * (FF * 2), 2); SB(); } while (0)
#define DN_LD(i0, i1) do { SB(); _Pragma("unroll") for (int i = (i0); i < (i1); ++i) wd[i] = __builtin_amdgcn_raw_buffer_load_b128(rdn, wqo, i * (DM * 2), 2); SB(); } while (0)
    DN_LD(0, DN_E);
    {
#pragma unroll
      for (int t = 0; t <= R_AT; ++t) BAR();
      const int r0 = m0 + nt * 8 + 4 * pa;
      u32x4 xr[4][2], rv[2], gv[2];
#pragma unroll
      for (int i = 0; i < 2; ++i) {
        rv[i] = *(const u32x4*)(w_r + i * 512 + lane * 8);
        gv[i] = *(const u32x4*)(gate_w + i * 512 + lane * 8);
      }
#pragma unroll
      for (int r = 0; r < 4; ++r)
#pragma unroll
        for (int i = 0; i < 2; ++i) xr[r][i] = *(const u32x4*)(x + (size_t)(r0 + r) * DM + i * 512 + lane * 8);
      const unsigned ep = __hip_atomic_load((gu32*)(ws + OFF_EPOCH), RLX_AGENT);
      {
        const u32x4 nw = {~0u, ~0u, ~0u, ~0u};
        const __amdgpu_buffer_rsrc_t kvr = MKRSRC(ws + OFF_KVP, NH * 2 * 16 * 1024), zr = MKRSRC(ws + OFF_ZP, NH * 2 * 16 * 128);
        const unsigned rec = (unsigned)((2 * nt + pa) * 32 + mt);
        __builtin_amdgcn_raw_buffer_store_b128(nw, kvr, rec * 1024 + lane * 16, 0, 16);
        __builtin_amdgcn_raw_buffer_store_b128(nw, kvr, rec * 1024 + lane * 16 + 16384, 0, 16);
        if (lane < 10) __builtin_amdgcn_raw_buffer_store_b128(nw, zr, lane < 8 ? (rec + (lane >> 2) * 16) * 64 + (lane & 3) * 16 : ZSC + (rec + (lane & 1) * 16) * 16, 0, 16);
      }
#pragma unroll
      for (int t = R_AT + 1; t < R_AT + R_GAP; ++t) BAR();
      float racc[4], gacc[4];
#pragma unroll
      for (int r = 0; r < 4; ++r) {
        BAR();
        if (r == 1) { WAIT_VM(0); SB(); }
        racc[r] = 0.f; gacc[r] = 0.f;
#pragma unroll
        for (int i = 0; i < 2; ++i)
#pragma unroll
          for (int e = 0; e < 4; ++e) {
            const float x0 = blo(xr[r][i][e]), x1 = bhi(xr[r][i][e]);
            racc[r] += x0 * blo(rv[i][e]); racc[r] += x1 * bhi(rv[i][e]);
            const unsigned pr = cvt_pk(x0 * blo(gv[i][e]), x1 * bhi(gv[i][e]));
            gacc[r] += blo(pr); gacc[r] += bhi(pr);
          }
        PIN(racc[r]); PIN(gacc[r]);
        if (r == 3) { SB(); pf = *(const unsigned*)(wo + (size_t)(128 * ml + 64 * pa + lane) * DM + 64 * nt3); cpf2 = CODE_PF(w == 7); }
      }
      BAR();
      float rs = merge16(merge32(racc[0], racc[1]), merge32(racc[2], racc[3]));
      float gs = merge16(merge32(gacc[0], gacc[1]), merge32(gacc[2], gacc[3]));
      rs += dppf<0x128>(rs); gs += dppf<0x128>(gs); rs += dppf<0x124>(rs); gs += dppf<0x124>(gs);
      rs += dppf<0x122>(rs); gs += dppf<0x122>(gs); rs += dppf<0x121>(rs); gs += dppf<0x121>(gs);
      const int rr = r0 + ((lane >> 5) | ((lane >> 3) & 2));
      const unsigned rb = cvt_pk(rs, rs) & 0xFFFFu;
      unsigned key = (((rb & 0x8000u) ? (~rb & 0xFFFFu) : (rb | 0x8000u)) << 16) | (0xFFFFu - (unsigned)rr);
      { const auto t_ = __builtin_amdgcn_permlane16_swap(key, key, false, false); key = max(t_[0], t_[1]); }
      { const auto t_ = __builtin_amdgcn_permlane32_swap(key, key, false, false); key = max(t_[0], t_[1]); }
      const unsigned tg = rfl(ep) + 1u;
      if (lane == 0) misc[C_TAG + pa] = pa ? key : tg;
      WAIT_LGKM0();
      BAR();
      if (pa == 0 && lane == 0) __hip_atomic_store((gu64*)(ws + OFF_RG) + bid, ((u64)tg << 32) | max(key, (unsigned)misc[C_TAG + 1]), RLX_AGENT);
      if (pa == 1 && lane < 32) __hip_atomic_store((gu32*)(ws + OFF_OFLAG + (mt * 16 + nt) * 128) + lane, ~tg, RLX_AGENT);
      if ((lane & 15) == 0) __hip_atomic_store((gu64*)(ws + OFF_GG) + rr, ((u64)tg << 32) | f2u(bfr(sigmoid_f(bfr(gs)))), RLX_AGENT);
      GU_LD(0, GU_E);
#pragma unroll
      for (int t = R_AT + R_GAP + 6; t < 13; ++t) BAR();
    }
    unsigned sel[2][4];
#pragma unroll
    for (int i = 0; i < 8; ++i) {
      const unsigned pick = (i >> 2) ? 0x0302u : 0x0100u;
      sel[i >> 2][i & 3] = ((lane & 7) >> 1) == (i & 3) ? ((lane & 1) ? (pick << 16) | 0x0C0Cu : 0x0C0C0000u | pick) : 0x0C0C0C0Cu;
      PIN(sel[i >> 2][i & 3]);
    }
    u32x4 rg[2];
#pragma unroll
    for (int i = 0; i < 2; ++i) rg[i] = __builtin_amdgcn_raw_buffer_load_b128(MKRSRC(ws + OFF_RG, 2048), lane * 16 + i * 1024, 0, 16);
    const gu64* ggp = (const gu64*)(ws + OFF_GG) + m0 + 64 * pa + lane;
    u64 gg = __hip_atomic_load(ggp, RLX_AGENT);
    BAR();
    const unsigned tag = rfl(misc[C_TAG]);
    unsigned idx;
    unsigned bestk;
    bool late;
    {
      unsigned best = 0;
      bool ok = (unsigned)(gg >> 32) == tag;
#pragma unroll
      for (int i = 0; i < 4; ++i) { ok = ok && rg[i >> 1][2 * (i & 1) + 1] == tag; best = max(best, rg[i >> 1][2 * (i & 1)]); }
      late = !all_true(ok);
      if (__builtin_expect(late, 0)) {
        BAR();
#pragma unroll 1
        for (unsigned spins = 0;; ++spins) {
          u64 rh[4];
#pragma unroll
          for (int i = 0; i < 4; ++i) rh[i] = __hip_atomic_load((gu64*)(ws + OFF_RG) + i * 64 + lane, RLX_AGENT);
          gg = __hip_atomic_load(ggp, RLX_AGENT);
          ok = (unsigned)(gg >> 32) == tag; best = 0;
#pragma unroll
          for (int i = 0; i < 4; ++i) { ok = ok && (unsigned)(rh[i] >> 32) == tag; best = max(best, (unsigned)rh[i]); }
          if (all_true(ok) || spins > SPIN_CAP) break;
          __builtin_amdgcn_s_sleep(8);
        }
      }
      best = wave_max(best);
      idx = (0xFFFFu - (best & 0xFFFFu)) & (SEQ - 1);
      bestk = best;
    }
    WAIT_VM(0);
    asm volatile("" ::"v"(pf), "v"(cpf), "v"(cpf2));
    u32x4 xs[4];
#pragma unroll
    for (int i = 0; i < 4; ++i) xs[i] = *(const u32x4*)(x + (size_t)idx * DM + gd0 + 8 * i);
    if (!late) BAR();
    __builtin_amdgcn_s_sleep(2);
    GU_LD(GU_E, 32);
    ((unsigned*)(smem + L_GATE))[64 * pa + lane] = (unsigned)gg;
    CFENCE();
    if (lane == 0) misc[C_IDX + pa] = idx + 1u;
    if (bid == 0 && pa == 0 && lane == 0) __hip_atomic_store((gu32*)(ws + OFF_EPOCH), tag, RLX_AGENT);
#define AFR(pr, i) FRAG(((u32x4){__builtin_amdgcn_perm(0u, pr, sel[(i) & 1][0]), __builtin_amdgcn_perm(0u, pr, sel[(i) & 1][1]), \
                                 __builtin_amdgcn_perm(0u, pr, sel[(i) & 1][2]), __builtin_amdgcn_perm(0u, pr, sel[(i) & 1][3])}))
    f32x4* rdw = (f32x4*)(red + pa * 128 + (lane >> 5) * 64 + (lane & 7) * 8 + ((lane >> 4) & 1) * 4);
    const bool rvalid = (lane >> 5) == ((lane >> 3) & 1);
    const auto rv = LP(volatile float, L_RED, lane * 4);
    {
      bf16x8 af[32];
      f32x4 a = {0.f, 0.f, 0.f, 0.f};
#pragma unroll
      for (int i = 0; i < GU_AH; ++i) { af[i] = AFR(xs[i >> 3][(i >> 1) & 3], i); PIN(af[i]); }
      SB();
#pragma unroll
      for (int i = 0; i < 32; ++i) {
        a = MFMA(af[i], FRAG(wv[i]), a);
        PIN(a);
        if (i + GU_AH < 32) { af[(i + GU_AH) & 31] = AFR(xs[((i + GU_AH) >> 3) & 3][((i + GU_AH) >> 1) & 3], i + GU_AH); PIN(af[(i + GU_AH) & 31]); }
        SB();
      }
      if (rvalid) *rdw = a;
      CFENCE();
      if (pa == 1) misc[C_GU] = 1u;
      else {
        float v;
#pragma unroll 1
        for (unsigned spins = 0; spins < (1u << 22); ++spins) {
          const unsigned f_ = misc[C_GU];
          CFENCE();
          v = (rv[0] + rv[64]) + (rv[128] + rv[192]);
          if (rfl(f_) != 0u) break;
          __builtin_amdgcn_s_sleep(1);
        }
        if (gemv) __hip_atomic_store((gu64*)(ws + OFF_GU) + ((help ? 4 : 0) + gdh * 2 + gmat) * FF + gfs * 64 + lane, ((u64)tag << 32) | f2u(v), RLX_AGENT);
      }
    }
    {
#pragma unroll 1
      for (unsigned spins = 0; spins < (1u << 22) && !all_true(WFLAGS(W_AG) == 2u); ++spins) __builtin_amdgcn_s_sleep(1);
      DN_LD(DN_E, 14);
      __builtin_amdgcn_s_sleep(8);
      float hv[2];
      const int fl = f0 + 2 * (lane < 56 ? lane : 55);
      const __amdgpu_buffer_rsrc_t gr = MKRSRC(ws + OFF_GU, 8 * FF * 8);
      const bool ex[2] = {(((unsigned)((uintptr_t)w_gate >> 7) + (fl >> 6)) & 7u) == 3u, (((unsigned)((uintptr_t)w_up >> 7) + (fl >> 6)) & 7u) == 3u};
#pragma unroll 1
      for (unsigned spins = 0;; ++spins) {
        u32x4 g[8];
        CFENCE();
#pragma unroll
        for (int i = 0; i < 8; ++i) {
          g[i] = (u32x4){0u, tag, 0u, tag};
          if (i < 4 || ex[i & 1]) g[i] = __builtin_amdgcn_raw_buffer_load_b128(gr, fl * 8, i * (FF * 8), 16);
        }
        bool ok = true;
#pragma unroll
        for (int j = 0; j < 2; ++j) {
#pragma unroll
          for (int i = 0; i < 8; ++i) ok = ok && g[i][2 * j + 1] == tag;
          const float gt = bfr((u2f(g[0][2 * j]) + u2f(g[4][2 * j])) + (u2f(g[2][2 * j]) + u2f(g[6][2 * j])));
          const float up = bfr((u2f(g[1][2 * j]) + u2f(g[5][2 * j])) + (u2f(g[3][2 * j]) + u2f(g[7][2 * j])));
          hv[j] = bfr(gt * __builtin_amdgcn_rcpf(1.0f + __expf(-gt))) * up;
        }
        if (all_true(ok) || spins > SPIN_CAP) break;
        __builtin_amdgcn_s_sleep(4);
      }
      u32x4 hp[2];
      {
        char* hb = smem + L_H + pa * 256;
        if (lane < 56) *(unsigned*)(hb + (lane / 7) * 32 + (lane % 7) * 4) = cvt_pk(hv[0], hv[1]);
        CFENCE();
        hp[0] = *(volatile lu32x4*)(hb + (lane >> 3) * 32); hp[1] = *(volatile lu32x4*)(hb + (lane >> 3) * 32 + 16);
      }
      bf16x8 af[14];
#pragma unroll
      for (int i = 0; i < 14; ++i) { af[i] = AFR(hp[i >> 3][(i >> 1) & 3], i); PIN(af[i]); }
      SB();
      f32x4 a = {0.f, 0.f, 0.f, 0.f};
#pragma unroll
      for (int i = 0; i < 14; ++i) a = MFMA(af[i], FRAG(wd[i]), a);
      if (rvalid) rdw[64] = a;
      CFENCE();
      if (pa == 1) misc[C_DN] = 1u;
      else {
        float y;
#pragma unroll 1
        for (unsigned spins = 0; spins < (1u << 22); ++spins) {
          const unsigned f_ = misc[C_DN];
          CFENCE();
          y = (rv[256] + rv[320]) + (rv[384] + rv[448]);
          if (rfl(f_) != 0u) break;
          __builtin_amdgcn_s_sleep(1);
        }
        __hip_atomic_store((gu64*)(ws + OFF_YG) + fg * 1024 + cg * 64 + lane, ((u64)tag << 32) | f2u(y), RLX_AGENT);
      }
    }
    if (pa == 1 && (int)(idx >> 7) == mt3) {
      const float xe = u2f((unsigned)x[(size_t)idx * DM + 64 * nt3 + lane] << 16);
      const unsigned ob = bestk >> 16;
      const float rsel = bfr(sigmoid_f(u2f(((ob & 0x8000u) ? (ob & 0x7FFFu) : (~ob & 0xFFFFu)) << 16)));
      float y = 0.f, gt = 0.f;
      __builtin_amdgcn_s_sleep(30);
#pragma unroll 1
      for (unsigned spins = 0;; ++spins) {
        u64 g[17];
#pragma unroll
        for (int i = 0; i < 16; ++i) g[i] = __hip_atomic_load((gu64*)(ws + OFF_YG) + i * 1024 + 64 * nt3 + lane, RLX_AGENT);
        g[16] = __hip_atomic_load((gu64*)(ws + OFF_GG) + idx, RLX_AGENT);
        bool ok = (unsigned)(g[16] >> 32) == tag;
        gt = u2f((unsigned)g[16]); y = 0.f;
#pragma unroll
        for (int i = 0; i < 16; ++i) { ok = ok && (unsigned)(g[i] >> 32) == tag; y += u2f((unsigned)g[i]); }
        if (all_true(ok) || spins > SPIN_CAP) break;
        __builtin_amdgcn_s_sleep(8);
      }
      red[512 + lane] = bfr(bfr(1.0f - gt) * bfr(xe + bfr(bfr(y) * rsel))) - bfr(bfr(1.0f - gt) * xe);
      CFENCE();
      if (lane == 0) misc[C_Y] = 1u;
    }
    return;
  }

  if (w == 0) misc[lane & 31] = 0u;
  if (w == 1) *(u32x2*)(smem + L_CW + lane * 8) = (u32x2){0u, 0u};
  const int hh = w & 1, wr = w >> 1;
  const int c16 = lane & 15, g = lane >> 4;
  const int fsw = (c16 >> 3) | ((g & 1) << 1);
  const int head = 2 * nt + hh;
  int aoff[2], boff[2];
#pragma unroll
  for (int i = 0; i < 2; ++i) {
    aoff[i] = (wr * 64 + c16) * 128 + (((4 * i + g) ^ (c16 >> 1)) * 16);
    boff[i] = 16384 + (8 * g + (c16 >> 2)) * 128 + (((2 * hh + (i ^ wr)) ^ fsw) * 32) + (c16 & 3) * 8;
  }
  f32x4 acc[3][4][2];
#pragma unroll
  for (int i = 0; i < 24; ++i) { const float s0 = i < 16 ? 1.f : 0.f; acc[i / 8][(i >> 1) & 3][i & 1] = (f32x4){s0, s0, s0, s0}; PIN(acc[i / 8][(i >> 1) & 3][i & 1]); }
  bf16x8 a[2][4], b[2][6];
  s16x4 tlo;
#define A_AT(sl, h, mi) (*(const bf16x8*)(smem + (sl) * SLOT + aoff[h] + (mi) * 2048))
#define B_AT(sl, h, s, n) (smem + (sl) * SLOT + boff[n] + (s) * 8192 + (h) * 4096)
#define MM(s, mi, n, A, B) do { if ((s) == 0) AMFMA(acc[0][mi][n], B, A); else AMFMA(acc[s][mi][n], A, B); } while (0)
#define RD(sl, h, u)                                                                                       \
  do {                                                                                                     \
    if ((u) == 0) a[h][0] = A_AT(sl, h, 0);                                                                \
    else if ((u) >= 13) a[h][(u) - 12] = A_AT(sl, h, (u) - 12);                                            \
    else {                                                                                                 \
      const int j_ = (((u) - 1) / 2 + 2) % 6;                                                              \
      if ((u) & 1) tlo = TRD(B_AT(sl, h, j_ >> 1, j_ & 1));                                                \
      else b[h][j_] = CAT(tlo, TRD(B_AT(sl, h, j_ >> 1, j_ & 1) + 512));                                   \
    }                                                                                                      \
  } while (0)
#define O_M(n) ((((n) / 4) & 1) ? 3 - (n) % 4 : (n) % 4)
#define O_J(n) ((n) / 4)
#define STEP(h, rs)                                                                                        \
  _Pragma("unroll") for (int n_ = 0; n_ < 24; ++n_) {                                                      \
    const int jj_ = (O_J(n_) + 2) % 6, mi_ = O_M(n_);                                                      \
    MM(jj_ >> 1, mi_, jj_ & 1, a[h][mi_], b[h][jj_]); SB();                                                \
    if (n_ < 16) { RD(rs, 1 - (h), n_); }                                                                 \
    SB();                                                                                                  \
  }
#define LGKM0() __builtin_amdgcn_s_waitcnt(0xC07F)
#define STAGE(sl, nx) do { STEP(0, sl); LGKM0(); BAR(); STEP(1, nx); LGKM0(); SB(); } while (0)

  __amdgpu_buffer_rsrc_t wsr = MKRSRC(ws, OFF_OF + SEQ * DM * 2);
  asm volatile("" : "+s"(wsr));
  const unsigned rec = (unsigned)((head * 2 + wr) * 16 + mt);
  unsigned x1w = L_X1 + w * 2048 + lane * 16, cw = L_CW + w * 128 + g * 16, one = 1u;
  unsigned kvo = (unsigned)OFF_KVP + rec * 1024 + lane * 16;
  unsigned zo = c16 == 0 ? (unsigned)OFF_ZP + rec * 64 + g * 16 : lane == 1 ? (unsigned)OFF_ZP + ZSC + rec * 16 : 0x80000000u;
  PIN(x1w); PIN(cw); PIN(one); PIN(kvo); PIN(zo);
  WAIT_LGKM0();
  BAR();
#pragma unroll
  for (int u = 0; u < 16; ++u) RD(0, 0, u);
  LGKM0();
  SB();
#pragma unroll 1
  for (int it = 0; it < 4; ++it) { STAGE(0, 1); STAGE(1, 2); STAGE(2, 0); }
  STAGE(0, 1);
  STEP(0, 1); LGKM0(); BAR();
  bf16x8 ta0_0, ta0_1, ta0_2, ta0_3, ta1_0, ta1_1, ta1_2, ta1_3, ta2_0, ta2_1, ta2_2, ta2_3;
  bf16x8 ta3_0, ta3_1, ta3_2, ta3_3, tb0_0_0, tb0_0_1, tb0_1_0, tb0_1_1, tb0_2_0, tb0_2_1, tb0_3_0, tb0_3_1;
  bf16x8 tb1_0_0, tb1_0_1, tb1_1_0, tb1_1_1, tb1_2_0, tb1_2_1, tb1_3_0, tb1_3_1, tb2_0_0, tb2_0_1, tb2_1_0, tb2_1_1;
  bf16x8 tb2_2_0, tb2_2_1, tb2_3_0, tb2_3_1;
  s16x4 tb0_0_0_l, tb0_0_1_l, tb0_1_0_l, tb0_1_1_l, tb0_2_0_l, tb0_2_1_l, tb0_3_0_l, tb0_3_1_l, tb1_0_0_l, tb1_0_1_l, tb1_1_0_l, tb1_1_1_l;
  s16x4 tb1_2_0_l, tb1_2_1_l, tb1_3_0_l, tb1_3_1_l, tb2_0_0_l, tb2_0_1_l, tb2_1_0_l, tb2_1_1_l, tb2_2_0_l, tb2_2_1_l, tb2_3_0_l, tb2_3_1_l;
  unsigned kp[16], vp[16], x1r, cr;
  u32x2 lf15;
  float kmin = 0.f, qmin = 0.f;
  u32x4 ones;
  float ek[4];
#define PHI(src, dst, mn, k, st)                                                                           \
  do {                                                                                                     \
    float& e_ = ek[2 * (k) + ((st) & 1)];                                                                  \
    if ((st) < 2) e_ = __builtin_fmaf((src)[2 * (k) + ((st) & 1)], 1.44269504088896341f, -1.44269504088896341f); \
    else if ((st) < 4) e_ = __builtin_amdgcn_fmed3f(__builtin_amdgcn_exp2f(e_), 0.f, 1.f);                 \
    else if ((st) < 6) e_ = fmax_raw((src)[2 * (k) + ((st) & 1)], e_);                                     \
    else if ((st) == 6) dst = cvt_pk(ek[2 * (k)], ek[2 * (k) + 1]);                                        \
    else mn = __builtin_fminf(__builtin_fminf(mn, (src)[2 * (k)]), (src)[2 * (k) + 1]);                     \
    SB();                                                                                                  \
  } while (0)
#define PK(mi, n, k, st) PHI(acc[1][mi][n], kp[8 * (n) + 2 * (mi) + (k)], kmin, k, st)
#define CV(mi, n, k) do { vp[8 * (n) + 2 * (mi) + (k)] = cvt_pk(acc[2][mi][n][2 * (k)], acc[2][mi][n][2 * (k) + 1]); PIN(vp[8 * (n) + 2 * (mi) + (k)]); SB(); } while (0)
#define F15_RD lf15 = *(volatile lu32x2*)(smem + L_CTL + 4 * C_F15)
#define F15_CHK do { if (__builtin_expect(!all_true((lf15[0] & lf15[1]) != 0u), 0)) { (void)wait_ge<1>(misc + C_F15, 1u); (void)wait_ge<1>(misc + C_F15 + 1, 1u); } CFENCE(); } while (0)
#define PRE(i) do { if ((i) == 30) { x1r = x1w ^ 4096u; PIN(x1r); } if ((i) == 31) { cr = cw ^ 256u; PIN(cr); } \
                    if ((i) == 32) { ones = (u32x4){0x3F803F80u, 0x3F803F80u, 0x3F803F80u, 0x3F803F80u}; PIN(ones); } } while (0)
  MM(1, 0, 0, a[1][0], b[1][2]); SB(); tb1_0_1_l = TRD(B_AT(2, 0, 1, 1)); PRE(0); SB();
  MM(1, 0, 1, a[1][0], b[1][3]); SB(); tb1_0_1 = CAT(tb1_0_1_l, TRD(B_AT(2, 0, 1, 1) + 512)); PRE(1); SB();
  MM(2, 0, 0, a[1][0], b[1][4]); SB(); ta0_0 = A_AT(2, 0, 0); PRE(2); SB();
  MM(2, 0, 1, a[1][0], b[1][5]); SB(); ta0_1 = A_AT(2, 0, 1); PRE(3); SB();
  MM(0, 0, 0, a[1][0], b[1][0]); SB(); ta0_2 = A_AT(2, 0, 2); PRE(4); SB();
  MM(0, 0, 1, a[1][0], b[1][1]); SB(); ta0_3 = A_AT(2, 0, 3); PRE(5); SB();
  MM(1, 1, 0, a[1][1], b[1][2]); SB(); tb1_0_0_l = TRD(B_AT(2, 0, 1, 0)); PRE(6); SB();
  MM(1, 1, 1, a[1][1], b[1][3]); SB(); tb1_0_0 = CAT(tb1_0_0_l, TRD(B_AT(2, 0, 1, 0) + 512)); PRE(7); SB();
  MM(2, 1, 0, a[1][1], b[1][4]); SB(); tb1_1_1_l = TRD(B_AT(2, 1, 1, 1)); PRE(8); SB();
  MM(2, 1, 1, a[1][1], b[1][5]); SB(); tb1_1_1 = CAT(tb1_1_1_l, TRD(B_AT(2, 1, 1, 1) + 512)); PRE(9); SB();
  MM(0, 1, 0, a[1][1], b[1][0]); SB(); ta1_0 = A_AT(2, 1, 0); PRE(10); SB();
  MM(0, 1, 1, a[1][1], b[1][1]); SB(); ta1_1 = A_AT(2, 1, 1); PRE(11); SB();
  MM(1, 2, 0, a[1][2], b[1][2]); SB(); ta1_2 = A_AT(2, 1, 2); PRE(12); SB();
  MM(1, 2, 1, a[1][2], b[1][3]); SB(); ta1_3 = A_AT(2, 1, 3); PRE(13); SB();
  MM(2, 2, 0, a[1][2], b[1][4]); SB(); tb1_1_0_l = TRD(B_AT(2, 1, 1, 0)); PRE(14); SB();
  MM(2, 2, 1, a[1][2], b[1][5]); SB(); tb1_1_0 = CAT(tb1_1_0_l, TRD(B_AT(2, 1, 1, 0) + 512)); PRE(15); SB();
  MM(0, 2, 0, a[1][2], b[1][0]); SB(); PRE(16); SB();
  MM(0, 2, 1, a[1][2], b[1][1]); SB(); PRE(17); SB();
  MM(1, 3, 0, a[1][3], b[1][2]); SB(); PRE(18); SB();
  MM(1, 3, 1, a[1][3], b[1][3]); SB(); PRE(19); SB();
  MM(2, 3, 0, a[1][3], b[1][4]); SB(); F15_RD; PRE(20); SB();
  MM(2, 3, 1, a[1][3], b[1][5]); SB(); PRE(21); SB();
  MM(0, 3, 0, a[1][3], b[1][0]); SB(); tb2_0_1_l = TRD(B_AT(2, 0, 2, 1)); PRE(22); SB();
  MM(0, 3, 1, a[1][3], b[1][1]); SB(); tb2_0_1 = CAT(tb2_0_1_l, TRD(B_AT(2, 0, 2, 1) + 512)); PRE(23); SB();
  MM(1, 0, 1, ta0_0, tb1_0_1); SB(); PRE(24); SB();
  MM(1, 1, 1, ta0_1, tb1_0_1); SB(); PRE(25); SB();
  MM(1, 2, 1, ta0_2, tb1_0_1); SB(); F15_CHK; tb2_1_1_l = TRD(B_AT(2, 1, 2, 1)); PRE(26); SB();
  MM(1, 3, 1, ta0_3, tb1_0_1); SB(); tb2_1_1 = CAT(tb2_1_1_l, TRD(B_AT(2, 1, 2, 1) + 512)); PRE(27); SB();
  MM(1, 0, 0, ta0_0, tb1_0_0); SB(); tb1_2_1_l = TRD(B_AT(0, 0, 1, 1)); PRE(28); SB();
  MM(1, 1, 0, ta0_1, tb1_0_0); SB(); tb1_2_1 = CAT(tb1_2_1_l, TRD(B_AT(0, 0, 1, 1) + 512)); PRE(29); SB();
  MM(1, 2, 0, ta0_2, tb1_0_0); SB(); ta2_0 = A_AT(0, 0, 0); PRE(30); SB();
  MM(1, 3, 0, ta0_3, tb1_0_0); SB(); ta2_1 = A_AT(0, 0, 1); PRE(31); SB();
  MM(1, 0, 1, ta1_0, tb1_1_1); SB(); ta2_2 = A_AT(0, 0, 2); PRE(32); SB();
  MM(1, 1, 1, ta1_1, tb1_1_1); SB(); ta2_3 = A_AT(0, 0, 3); PRE(33); SB();
  MM(1, 2, 1, ta1_2, tb1_1_1); SB(); tb1_3_1_l = TRD(B_AT(0, 1, 1, 1)); PRE(34); SB();
  MM(1, 3, 1, ta1_3, tb1_1_1); SB(); tb1_3_1 = CAT(tb1_3_1_l, TRD(B_AT(0, 1, 1, 1) + 512)); PRE(35); SB();
  MM(1, 0, 0, ta1_0, tb1_1_0); SB(); ta3_0 = A_AT(0, 1, 0); PRE(36); SB();
  MM(1, 1, 0, ta1_1, tb1_1_0); SB(); ta3_1 = A_AT(0, 1, 1); PRE(37); SB();
  MM(1, 2, 0, ta1_2, tb1_1_0); SB(); ta3_2 = A_AT(0, 1, 2); PRE(38); SB();
  MM(1, 3, 0, ta1_3, tb1_1_0); SB(); ta3_3 = A_AT(0, 1, 3); PRE(39); SB();
  MM(1, 0, 1, ta2_0, tb1_2_1); SB(); tb1_2_0_l = TRD(B_AT(0, 0, 1, 0)); PRE(40); SB();
  MM(1, 1, 1, ta2_1, tb1_2_1); SB(); tb1_2_0 = CAT(tb1_2_0_l, TRD(B_AT(0, 0, 1, 0) + 512)); PRE(41); SB();
  MM(1, 2, 1, ta2_2, tb1_2_1); SB(); tb1_3_0_l = TRD(B_AT(0, 1, 1, 0)); PRE(42); SB();
  MM(1, 3, 1, ta2_3, tb1_2_1); SB(); tb1_3_0 = CAT(tb1_3_0_l, TRD(B_AT(0, 1, 1, 0) + 512)); PRE(43); SB();
  MM(1, 0, 1, ta3_0, tb1_3_1); SB(); tb2_2_1_l = TRD(B_AT(0, 0, 2, 1)); PRE(44); SB();
  MM(1, 1, 1, ta3_1, tb1_3_1); SB(); tb2_2_1 = CAT(tb2_2_1_l, TRD(B_AT(0, 0, 2, 1) + 512)); PRE(45); SB();
  MM(1, 2, 1, ta3_2, tb1_3_1); SB(); tb2_3_1_l = TRD(B_AT(0, 1, 2, 1)); PRE(46); SB();
  MM(1, 3, 1, ta3_3, tb1_3_1); SB(); tb2_3_1 = CAT(tb2_3_1_l, TRD(B_AT(0, 1, 2, 1) + 512)); PRE(47); SB();
  MM(1, 0, 0, ta2_0, tb1_2_0); SB(); tb2_0_0_l = TRD(B_AT(2, 0, 2, 0)); PK(0, 1, 0, 0); PK(0, 1, 0, 1); PK(0, 1, 1, 0); PK(0, 1, 1, 1); PK(0, 1, 0, 2); SB();
  MM(1, 1, 0, ta2_1, tb1_2_0); SB(); tb2_0_0 = CAT(tb2_0_0_l, TRD(B_AT(2, 0, 2, 0) + 512)); PK(0, 1, 0, 3); PK(0, 1, 0, 7); PK(0, 1, 1, 2); PK(0, 1, 1, 3); SB();
  MM(1, 2, 0, ta2_2, tb1_2_0); SB(); tb2_1_0_l = TRD(B_AT(2, 1, 2, 0)); PK(0, 1, 1, 7); PK(0, 1, 0, 4); PK(0, 1, 0, 5); PK(0, 1, 1, 4); PK(0, 1, 1, 5); PK(0, 1, 0, 6); SB();
  MM(1, 3, 0, ta2_3, tb1_2_0); SB(); tb2_1_0 = CAT(tb2_1_0_l, TRD(B_AT(2, 1, 2, 0) + 512)); PK(0, 1, 1, 6); PK(1, 1, 0, 0); PK(1, 1, 0, 1); PK(1, 1, 1, 0); PK(1, 1, 1, 1); PK(1, 1, 0, 2); SB();
  MM(1, 0, 0, ta3_0, tb1_3_0); SB(); tb2_2_0_l = TRD(B_AT(0, 0, 2, 0)); PK(1, 1, 0, 3); PK(1, 1, 0, 7); PK(1, 1, 1, 2); PK(1, 1, 1, 3); SB();
  MM(1, 1, 0, ta3_1, tb1_3_0); SB(); tb2_2_0 = CAT(tb2_2_0_l, TRD(B_AT(0, 0, 2, 0) + 512)); PK(1, 1, 1, 7); PK(1, 1, 0, 4); PK(1, 1, 0, 5); PK(1, 1, 1, 4); PK(1, 1, 1, 5); SB();
  MM(1, 2, 0, ta3_2, tb1_3_0); SB(); tb2_3_0_l = TRD(B_AT(0, 1, 2, 0)); PK(1, 1, 0, 6); PK(1, 1, 1, 6); PK(2, 1, 0, 0); PK(2, 1, 0, 1); PK(2, 1, 1, 0); SB();
  MM(1, 3, 0, ta3_3, tb1_3_0); SB(); tb2_3_0 = CAT(tb2_3_0_l, TRD(B_AT(0, 1, 2, 0) + 512)); PK(2, 1, 1, 1); PK(2, 1, 0, 2); PK(2, 1, 0, 3); SB();
  MM(2, 0, 1, ta0_0, tb2_0_1); SB(); PK(2, 1, 0, 7); PK(2, 1, 1, 2); PK(2, 1, 1, 3); SB();
  MM(2, 1, 1, ta0_1, tb2_0_1); SB(); PK(2, 1, 1, 7); PK(2, 1, 0, 4); PK(2, 1, 0, 5); PK(2, 1, 1, 4); PK(2, 1, 1, 5); SB();
  MM(2, 2, 1, ta0_2, tb2_0_1); SB(); PK(2, 1, 0, 6); PK(2, 1, 1, 6); PK(3, 1, 0, 0); PK(3, 1, 0, 1); PK(3, 1, 1, 0); SB();
  MM(2, 3, 1, ta0_3, tb2_0_1); SB(); PK(3, 1, 1, 1); PK(3, 1, 0, 2); PK(3, 1, 0, 3); SB();
  MM(2, 0, 1, ta1_0, tb2_1_1); SB(); PK(3, 1, 0, 7); PK(3, 1, 1, 2); PK(3, 1, 1, 3); SB();
  MM(2, 1, 1, ta1_1, tb2_1_1); SB(); PK(3, 1, 1, 7); PK(3, 1, 0, 4); PK(3, 1, 0, 5); PK(3, 1, 1, 4); PK(3, 1, 1, 5); SB();
  MM(2, 2, 1, ta1_2, tb2_1_1); SB(); PK(3, 1, 0, 6); PK(3, 1, 1, 6); PK(0, 0, 0, 0); PK(0, 0, 0, 1); PK(0, 0, 1, 0); SB();
  MM(2, 3, 1, ta1_3, tb2_1_1); SB(); PK(0, 0, 1, 1); PK(0, 0, 0, 2); PK(0, 0, 0, 3); SB();
  MM(2, 0, 1, ta2_0, tb2_2_1); SB(); PK(0, 0, 0, 7); PK(0, 0, 1, 2); PK(0, 0, 1, 3); SB();
  MM(2, 1, 1, ta2_1, tb2_2_1); SB(); PK(0, 0, 1, 7); PK(0, 0, 0, 4); PK(0, 0, 0, 5); PK(0, 0, 1, 4); PK(0, 0, 1, 5); SB();
  MM(2, 2, 1, ta2_2, tb2_2_1); SB(); PK(0, 0, 0, 6); PK(0, 0, 1, 6); PK(1, 0, 0, 0); PK(1, 0, 0, 1); PK(1, 0, 1, 0); SB();
  MM(2, 3, 1, ta2_3, tb2_2_1); SB(); PK(1, 0, 1, 1); PK(1, 0, 0, 2); PK(1, 0, 0, 3); SB();
  MM(2, 0, 1, ta3_0, tb2_3_1); SB(); PK(1, 0, 0, 7); PK(1, 0, 1, 2); PK(1, 0, 1, 3); SB();
  MM(2, 1, 1, ta3_1, tb2_3_1); SB(); PK(1, 0, 1, 7); PK(1, 0, 0, 4); PK(1, 0, 0, 5); PK(1, 0, 1, 4); PK(1, 0, 1, 5); SB();
  MM(2, 2, 1, ta3_2, tb2_3_1); SB(); PK(1, 0, 0, 6); PK(1, 0, 1, 6); PK(2, 0, 0, 0); PK(2, 0, 0, 1); PK(2, 0, 1, 0); SB();
  MM(2, 3, 1, ta3_3, tb2_3_1); SB(); PK(2, 0, 1, 1); PK(2, 0, 0, 2); PK(2, 0, 0, 3); SB();
  MM(2, 0, 0, ta0_0, tb2_0_0); SB(); PK(2, 0, 0, 7); PK(2, 0, 1, 2); PK(2, 0, 1, 3); SB();
  MM(2, 1, 0, ta0_1, tb2_0_0); SB(); PK(2, 0, 1, 7); PK(2, 0, 0, 4); PK(2, 0, 0, 5); PK(2, 0, 1, 4); PK(2, 0, 1, 5); SB();
  MM(2, 2, 0, ta0_2, tb2_0_0); SB(); PK(2, 0, 0, 6); PK(2, 0, 1, 6); PK(3, 0, 0, 0); PK(3, 0, 0, 1); PK(3, 0, 1, 0); SB();
  MM(2, 3, 0, ta0_3, tb2_0_0); SB(); PK(3, 0, 1, 1); PK(3, 0, 0, 2); PK(3, 0, 0, 3); SB();
  MM(2, 0, 0, ta1_0, tb2_1_0); SB(); PK(3, 0, 0, 7); PK(3, 0, 1, 2); PK(3, 0, 1, 3); SB();
  MM(2, 1, 0, ta1_1, tb2_1_0); SB(); PK(3, 0, 1, 7); PK(3, 0, 0, 4); PK(3, 0, 0, 5); PK(3, 0, 1, 4); PK(3, 0, 1, 5); SB();
  MM(2, 2, 0, ta1_2, tb2_1_0); SB(); PK(3, 0, 0, 6); PK(3, 0, 1, 6); CV(0, 1, 0); CV(0, 1, 1); CV(1, 1, 0); SB();
  MM(2, 3, 0, ta1_3, tb2_1_0); SB(); CV(1, 1, 1); CV(2, 1, 0); CV(2, 1, 1); CV(3, 1, 0); CV(3, 1, 1); SB();
  const f32x4 zero = {0.f, 0.f, 0.f, 0.f};
#define TAKE(flag, READS)                                                                                  \
  _Pragma("unroll 1") for (unsigned spins = 0; spins < (1u << 22); ++spins) {                              \
    const unsigned f_ = (flag);                                                                            \
    CFENCE();                                                                                              \
    READS;                                                                                                 \
    if (__builtin_expect(rfl(f_) != 0u, 1)) break;                                                         \
  }
  unsigned lidx, ltag;
  {
    phi_fix(kp, kmin);
#define KF(j, n) FRAG(((u32x4){kp[8 * (n) + 4 * (j)], kp[8 * (n) + 4 * (j) + 1], kp[8 * (n) + 4 * (j) + 2], kp[8 * (n) + 4 * (j) + 3]}))
#define VF(j, n) FRAG(((u32x4){vp[8 * (n) + 4 * (j)], vp[8 * (n) + 4 * (j) + 1], vp[8 * (n) + 4 * (j) + 2], vp[8 * (n) + 4 * (j) + 3]}))
    f32x4 s1 = MFMA(KF(0, 1), VF(0, 1), zero), s0 = MFMA(KF(0, 0), VF(0, 1), zero), zs = MFMA(KF(0, 1), FRAG(ones), zero);
    s1 = MFMA(KF(1, 1), VF(1, 1), s1); s0 = MFMA(KF(1, 0), VF(1, 1), s0); zs = MFMA(KF(1, 1), FRAG(ones), zs);
#define QR(u, n) do { tb0_##u##_##n##_l = TRD(B_AT((u) < 2 ? 2 : 0, (u) & 1, 0, n)); tb0_##u##_##n = CAT(tb0_##u##_##n##_l, TRD(B_AT((u) < 2 ? 2 : 0, (u) & 1, 0, n) + 512)); SB(); } while (0)
#define X1_RD { p0 = *LP(volatile f32x4, x1r, 0); p1 = *LP(volatile f32x4, x1r, 1024); pz = *LP(volatile f32x4, cr, 0); }
    SB();
    MM(2, 0, 0, ta2_0, tb2_2_0); SB(); QR(0, 0); MM(2, 1, 0, ta2_1, tb2_2_0); SB(); QR(0, 1);
    *LP(f32x4, x1w, 0) = s1; *LP(f32x4, x1w, 1024) = s0; *LP(f32x4, cw, 0) = zs;
    CFENCE();
    *LP(volatile unsigned, cw, W_X1) = one;
    SB();
    MM(2, 0, 0, ta3_0, tb2_3_0); SB(); QR(1, 0); MM(2, 1, 0, ta3_1, tb2_3_0); SB(); QR(1, 1);
    MM(2, 2, 0, ta2_2, tb2_2_0); SB(); QR(2, 0); MM(2, 3, 0, ta2_3, tb2_2_0); SB(); CV(0, 0, 0); CV(0, 0, 1); QR(2, 1);
    MM(2, 2, 0, ta3_2, tb2_3_0); SB(); CV(1, 0, 0); CV(1, 0, 1); QR(3, 0); MM(2, 3, 0, ta3_3, tb2_3_0); SB(); QR(3, 1);
    f32x4 p0, p1, pz;
    const unsigned f1 = *LP(volatile unsigned, cr, W_X1);
    CFENCE();
    X1_RD
    SB();
    f32x4 kv0 = MFMA(KF(0, 0), VF(0, 0), zero), kv1 = MFMA(KF(0, 1), VF(0, 0), zero);
    SB();
    CV(2, 0, 0); CV(2, 0, 1);
    f32x4 z = MFMA(KF(0, 0), FRAG(ones), zero);
    SB();
    CV(3, 0, 0); CV(3, 0, 1);
    kv0 = MFMA(KF(1, 0), VF(1, 0), kv0); kv1 = MFMA(KF(1, 1), VF(1, 0), kv1);
    z = MFMA(KF(1, 0), FRAG(ones), z);
    SB();
    if (__builtin_expect(!all_true(f1 != 0u), 0)) { TAKE(*LP(volatile unsigned, cr, W_X1), X1_RD); LGKM0(); }
    const f32x4 m0_ = kv0 + p0, m1_ = kv1 + p1;
    float am = fmax_raw(fabsf(m0_[0]), fabsf(m1_[0]));
#pragma unroll
    for (int e = 1; e < 4; ++e) am = fmax_raw(fmax_raw(am, fabsf(m0_[e])), fabsf(m1_[e]));
#define PKH(a_, b_) __builtin_bit_cast(unsigned, __builtin_convertvector((f32x2){(a_), (b_)}, f16x2_t))
    u32x4 hv = {PKH(m0_[0], m0_[1]), PKH(m0_[2], m0_[3]), PKH(m1_[0], m1_[1]), PKH(m1_[2], m1_[3])};
#define Z_STORE(scl) do { unsigned z0_ = zv[0]; asm("v_writelane_b32 %0, %1, 1" : "+v"(z0_) : "s"(scl)); zv[0] = z0_; __builtin_amdgcn_raw_buffer_store_b128(zv, wsr, zo, 0, 16); } while (0)
    PIN(hv);
    if (__builtin_expect(all_true(f2u(am) - 0x3C800000u <= 0x47000000u - 0x3C800000u), 1)) {
      __builtin_amdgcn_raw_buffer_store_b128(hv, wsr, kvo, 0, 16);
      SB();
      u32x4 zv = __builtin_bit_cast(u32x4, z + pz);
      Z_STORE(0x3F800000u);
    } else {
      unsigned ex = max(wave_max(f2u(am) & 0x7FFFFFFFu) >> 23, 15u);
      if (ex >= 121u && ex < 142u) ex = 141u;
      const float inv = u2f((268u - ex) << 23);
      u32x4 hs = {PKH(m0_[0] * inv, m0_[1] * inv), PKH(m0_[2] * inv, m0_[3] * inv), PKH(m1_[0] * inv, m1_[1] * inv), PKH(m1_[2] * inv, m1_[3] * inv)};
      u32x4 zv = __builtin_bit_cast(u32x4, z + pz);
#pragma unroll
      for (int e = 0; e < 4; ++e) { hs[e] = min(hs[e], ~1u); zv[e] = min(zv[e] | (ex != 141u ? 0x80000000u : 0u), ~1u); }
      CFENCE();
      __builtin_amdgcn_raw_buffer_store_b128(hs, wsr, kvo, 0, 16);
      Z_STORE((ex - 14u) << 23);
      CFENCE();
    }
  }
  *LP(volatile unsigned, cw, W_KS) = one;
  unsigned ko, oo, x2w, zfa, zfb, zfp, gta;
  u32x2 eye, ov[4][2];
  u32x4 t[16];
  float gq[4];
  unsigned pq[16];
#define PQ(mi, n, k, st) PHI(acc[0][mi][n], pq[4 * (mi) + 2 * (n) + (k)], qmin, k, st)
#define NOPQ() asm volatile("s_nop 4" : "+v"(acc[0][3][0]), "+v"(acc[0][3][1]))
#define PRQ(i)                                                                                             \
  do {                                                                                                     \
    if ((i) == 0) { lidx = misc[C_IDX + wr]; ltag = misc[C_TAG]; }                                         \
    if ((i) == 2) { zfp = L_CTL + 4 * (C_ZF + hh); PIN(zfp); }                                             \
    if ((i) == 3) { ko = kvo - mt * 1024; PIN(ko); }                                                       \
    if ((i) == 4) { gta = L_GATE + wr * 256 + c16 * 4; PIN(gta); }                                         \
    if ((i) == 5) { zfa = L_ZF + hh * 64 + wr * 8 + g * 16; PIN(zfa); }                                    \
    if ((i) == 6) { zfb = zfa ^ 8u; PIN(zfb); }                                                            \
    if ((i) == 7) { const unsigned o_ = 0x3C00u << (16 * (lane & 1)); eye = (u32x2){(lane & 2) ? 0u : o_, (lane & 2) ? o_ : 0u}; PIN(eye); } \
    if ((i) == 8) { x2w = L_X2 + w * 1024 + lane * 16; PIN(x2w); }                                         \
    if ((i) == 9) { oo = (unsigned)OFF_OF + (unsigned)(((mt * 8 + wr * 4) * NH + head) * 1024) + lane * 16; PIN(oo); } \
  } while (0)
#define SHOT_GATE                                                                                          \
  do {                                                                                                     \
    phi_fix(pq, qmin);                                                                                     \
    _Pragma("unroll") for (int i = 0; i < 16; ++i) PIN(pq[i]);                                             \
    if (__builtin_expect(!all_true(lidx != 0u), 0)) (void)wait_ge<1>(misc + C_IDX + wr, 1u);        \
    (void)wait_ge<1>(misc + C_GO + hh, 1u);                                                                \
    CFENCE();                                                                                              \
    _Pragma("unroll") for (int i = 0; i < 4; ++i) gq[i] = *LP(volatile float, gta, i * 64);                \
  } while (0)
#define T_LD(i) t[i] = __builtin_amdgcn_raw_buffer_load_b128(wsr, ko + ((i) & 3) * 1024, ((i) >> 2) * 4096, 16)
  SB();
  MM(0, 0, 0, ta0_0, tb0_0_0); SB(); PRQ(0); SB();
  MM(0, 0, 1, ta0_0, tb0_0_1); SB(); PRQ(1); SB();
  MM(0, 0, 0, ta1_0, tb0_1_0); SB(); PRQ(2); SB();
  MM(0, 0, 1, ta1_0, tb0_1_1); SB(); PRQ(3); SB();
  MM(0, 0, 0, ta2_0, tb0_2_0); SB(); PRQ(4); SB();
  MM(0, 0, 1, ta2_0, tb0_2_1); SB(); PRQ(5); SB();
  MM(0, 0, 0, ta3_0, tb0_3_0); SB(); PRQ(6); SB();
  MM(0, 0, 1, ta3_0, tb0_3_1); SB(); PRQ(7); SB();
  MM(0, 1, 0, ta0_1, tb0_0_0); SB(); PRQ(8); SB();
  MM(0, 1, 1, ta0_1, tb0_0_1); SB(); PRQ(9); SB();
  MM(0, 1, 0, ta1_1, tb0_1_0); SB(); PQ(0, 0, 0, 0); PQ(0, 0, 0, 1); PQ(0, 0, 1, 0); SB();
  MM(0, 1, 1, ta1_1, tb0_1_1); SB(); PQ(0, 0, 1, 1); PQ(0, 0, 0, 2); SB();
  MM(0, 1, 0, ta2_1, tb0_2_0); SB(); PQ(0, 0, 0, 3); PQ(0, 0, 0, 7); SB();
  MM(0, 1, 1, ta2_1, tb0_2_1); SB(); PQ(0, 0, 1, 2); PQ(0, 0, 1, 3); SB();
  MM(0, 1, 0, ta3_1, tb0_3_0); SB(); PQ(0, 0, 1, 7); PQ(0, 0, 0, 4); PQ(0, 0, 0, 5); SB();
  MM(0, 1, 1, ta3_1, tb0_3_1); SB(); PQ(0, 0, 1, 4); PQ(0, 0, 1, 5); PQ(0, 0, 0, 6); SB();
  MM(0, 2, 0, ta0_2, tb0_0_0); SB(); PQ(0, 0, 1, 6); PQ(0, 1, 0, 0); PQ(0, 1, 0, 1); SB();
  MM(0, 2, 1, ta0_2, tb0_0_1); SB(); PQ(0, 1, 1, 0); PQ(0, 1, 1, 1); PQ(0, 1, 0, 2); SB();
  MM(0, 2, 0, ta1_2, tb0_1_0); SB(); PQ(0, 1, 0, 3); PQ(0, 1, 0, 7); SB();
  MM(0, 2, 1, ta1_2, tb0_1_1); SB(); PQ(0, 1, 1, 2); PQ(0, 1, 1, 3); SB();
  MM(0, 2, 0, ta2_2, tb0_2_0); SB(); PQ(0, 1, 1, 7); PQ(0, 1, 0, 4); PQ(0, 1, 0, 5); SB();
  MM(0, 2, 1, ta2_2, tb0_2_1); SB(); PQ(0, 1, 1, 4); PQ(0, 1, 1, 5); PQ(0, 1, 0, 6); SB();
  MM(0, 2, 0, ta3_2, tb0_3_0); SB(); PQ(0, 1, 1, 6); PQ(1, 0, 0, 0); PQ(1, 0, 0, 1); SB();
  MM(0, 2, 1, ta3_2, tb0_3_1); SB(); PQ(1, 0, 1, 0); PQ(1, 0, 1, 1); PQ(1, 0, 0, 2); SB();
  MM(0, 3, 0, ta0_3, tb0_0_0); SB(); PQ(1, 0, 0, 3); PQ(1, 0, 0, 7); SB();
  MM(0, 3, 1, ta0_3, tb0_0_1); SB(); PQ(1, 0, 1, 2); PQ(1, 0, 1, 3); SB();
  MM(0, 3, 0, ta1_3, tb0_1_0); SB(); PQ(1, 0, 1, 7); PQ(1, 0, 0, 4); PQ(1, 0, 0, 5); SB();
  MM(0, 3, 1, ta1_3, tb0_1_1); SB(); PQ(1, 0, 1, 4); PQ(1, 0, 1, 5); PQ(1, 0, 0, 6); SB();
  MM(0, 3, 0, ta2_3, tb0_2_0); SB(); PQ(1, 0, 1, 6); PQ(1, 1, 0, 0); PQ(1, 1, 0, 1); SB();
  MM(0, 3, 1, ta2_3, tb0_2_1); SB(); PQ(1, 1, 1, 0); PQ(1, 1, 1, 1); PQ(1, 1, 0, 2); SB();
  MM(0, 3, 0, ta3_3, tb0_3_0); SB(); PQ(1, 1, 0, 3); PQ(1, 1, 0, 7); SB();
  MM(0, 3, 1, ta3_3, tb0_3_1); SB(); PQ(1, 1, 1, 2); PQ(1, 1, 1, 3); SB();
  PQ(1, 1, 1, 7); SB();
  PQ(1, 1, 0, 4); SB();
  PQ(1, 1, 0, 5); SB();
  PQ(1, 1, 1, 4); SB();
  PQ(1, 1, 1, 5); SB();
  PQ(1, 1, 0, 6); SB();
  PQ(1, 1, 1, 6); SB();
  PQ(2, 0, 0, 0); SB();
  PQ(2, 0, 0, 1); SB();
  PQ(2, 0, 1, 0); SB();
  PQ(2, 0, 1, 1); SB();
  PQ(2, 0, 0, 2); SB();
  PQ(2, 0, 0, 3); SB();
  PQ(2, 0, 0, 7); SB();
  PQ(2, 0, 1, 2); SB();
  PQ(2, 0, 1, 3); SB();
  PQ(2, 0, 1, 7); SB();
  PQ(2, 0, 0, 4); SB();
  PQ(2, 0, 0, 5); SB();
  PQ(2, 0, 1, 4); SB();
  PQ(2, 0, 1, 5); SB();
  PQ(2, 0, 0, 6); SB();
  PQ(2, 0, 1, 6); SB();
  PQ(2, 1, 0, 0); SB();
  PQ(2, 1, 0, 1); SB();
  PQ(2, 1, 1, 0); SB();
  PQ(2, 1, 1, 1); SB();
  PQ(2, 1, 0, 2); SB();
  PQ(2, 1, 0, 3); SB();
  PQ(2, 1, 0, 7); SB();
  PQ(2, 1, 1, 2); SB();
  PQ(2, 1, 1, 3); SB();
  PQ(2, 1, 1, 7); SB();
  PQ(2, 1, 0, 4); SB();
  PQ(2, 1, 0, 5); SB();
  PQ(2, 1, 1, 4); SB();
  PQ(2, 1, 1, 5); SB();
  PQ(2, 1, 0, 6); SB();
  PQ(2, 1, 1, 6); SB();
  NOPQ();
  PQ(3, 0, 0, 0); SB();
  PQ(3, 0, 0, 1); SB();
  PQ(3, 0, 1, 0); SB();
  PQ(3, 0, 1, 1); SB();
  PQ(3, 0, 0, 2); SB();
  PQ(3, 0, 0, 3); SB();
  PQ(3, 0, 0, 7); SB();
  PQ(3, 0, 1, 2); SB();
  PQ(3, 0, 1, 3); SB();
  PQ(3, 0, 1, 7); SB();
  PQ(3, 0, 0, 4); SB();
  PQ(3, 0, 0, 5); SB();
  PQ(3, 0, 1, 4); SB();
  PQ(3, 0, 1, 5); SB();
  PQ(3, 0, 0, 6); SB();
  PQ(3, 0, 1, 6); SB();
  PQ(3, 1, 0, 0); SB();
  PQ(3, 1, 0, 1); SB();
  PQ(3, 1, 1, 0); SB();
  PQ(3, 1, 1, 1); SB();
  PQ(3, 1, 0, 2); SB();
  PQ(3, 1, 0, 3); SB();
  PQ(3, 1, 0, 7); SB();
  PQ(3, 1, 1, 2); SB();
  PQ(3, 1, 1, 3); SB();
  PQ(3, 1, 1, 7); SB();
  PQ(3, 1, 0, 4); SB();
  PQ(3, 1, 0, 5); SB();
  PQ(3, 1, 1, 4); SB();
  PQ(3, 1, 1, 5); SB();
  PQ(3, 1, 0, 6); SB();
  PQ(3, 1, 1, 6); SB();
  SHOT_GATE;
  T_LD(0);
  T_LD(1);
  T_LD(2);
  T_LD(3);
  T_LD(4);
  T_LD(5);
  T_LD(6);
  T_LD(7);
  T_LD(8);
  T_LD(9);
  T_LD(10);
  T_LD(11);
  T_LD(12);
  T_LD(13);
  T_LD(14);
  T_LD(15);
  *LP(volatile unsigned, cw, W_AG) = one;
  SB();
  {
#define PF(mi) FRAG(((u32x4){pq[4 * (mi)], pq[4 * (mi) + 1], pq[4 * (mi) + 2], pq[4 * (mi) + 3]}))
#define H4(v, k) __builtin_bit_cast(f16x4_t, (u32x2){(v)[2 * (k)], (v)[2 * (k) + 1]})
    f32x4 s0 = zero, s1 = zero;
#define KV_MM(i0, i1)                                                                                      \
  _Pragma("unroll") for (int i = (i0); i < (i1); ++i) {                                                    \
    s0 = __builtin_amdgcn_mfma_f32_4x4x4f16(__builtin_bit_cast(f16x4_t, eye), H4(t[i], 0), s0, 0, 0, 0);   \
    s1 = __builtin_amdgcn_mfma_f32_4x4x4f16(__builtin_bit_cast(f16x4_t, eye), H4(t[i], 1), s1, 0, 0, 0);   \
  }
    float rc[4];
    u32x2 z0, z1;
#define Z_RD { z0 = *LP(volatile u32x2, zfa, 0); z1 = *LP(volatile u32x2, zfb, 0); }
    f32x4 d[4];
#define DEN1() do { const u32x4 zf = {z0[0], z0[1], z1[0], z1[1]}; _Pragma("unroll") for (int mi = 0; mi < 4; ++mi) d[mi] = MFMA(FRAG(zf), PF(mi), zero); } while (0)
#define DEN2()                                                                                             \
  do {                                                                                                     \
    _Pragma("unroll") for (int mi = 0; mi < 4; ++mi) rc[mi] = __builtin_amdgcn_rcpf(d[mi][0] + 1e-6f);     \
    _Pragma("unroll") for (int mi = 0; mi < 4; ++mi) { rc[mi] *= gq[mi]; PIN(rc[mi]); }                    \
  } while (0)
    KV_MM(0, DENAT - 4);
    SB();
    unsigned zfl = *LP(volatile unsigned, zfp, 0);
    CFENCE();
    Z_RD
    SB();
    KV_MM(DENAT - 4, DENAT);
    SB();
    DEN1();
    SB();
    KV_MM(DENAT, DENAT + 2);
    SB();
    DEN2();
    const u64 zbad = __builtin_amdgcn_ballot_w64(zfl != 1u);
    SB();
    KV_MM(DENAT + 2, 16);
    const u64 kbad = __builtin_amdgcn_ballot_w64(__builtin_isunordered(s0[0], s1[0]));
    if (__builtin_expect((kbad | zbad) != 0ull, 0)) {
      if (kbad != 0ull) {
        unsigned todo = 15u;
#pragma unroll 1
        for (unsigned spins = 0; spins <= SPIN_CAP; ++spins) {
          unsigned miss = 0;
#pragma unroll
          for (int q = 0; q < 4; ++q)
            if (todo & (1u << q)) { if (!all_true(max(max(MX4(t[4 * q]), MX4(t[4 * q + 1])), max(MX4(t[4 * q + 2]), MX4(t[4 * q + 3]))) != ~0u)) miss |= 1u << q; }
          todo = miss;
          if (todo == 0u) break;
          __builtin_amdgcn_s_sleep(2);
          CFENCE();
#pragma unroll
          for (int q = 0; q < 4; ++q)
            if (todo & (1u << q)) { T_LD(4 * q); T_LD(4 * q + 1); T_LD(4 * q + 2); T_LD(4 * q + 3); }
        }
        s0 = zero; s1 = zero;
        KV_MM(0, 16);
      }
      zfl = rfl(zfl);
      if (zfl == 0u) { TAKE(zfl = misc[C_ZF + hh], Z_RD); zfl = rfl(zfl); DEN1(); DEN2(); }
      if (zfl != 1u) {
        unsigned sc;
#pragma unroll 1
        for (unsigned spins = 0; spins <= SPIN_CAP; ++spins) {
          CFENCE();
          sc = __builtin_amdgcn_raw_buffer_load_b32(wsr, (unsigned)OFF_ZP + ZSC + (rec - mt + c16) * 16, 0, 16);
          if (all_true(sc != ~0u)) break;
        }
        s0 = zero; s1 = zero;
#pragma unroll
        for (int i = 0; i < 16; ++i) {
          const float s_ = u2f((unsigned)__builtin_amdgcn_readlane((int)sc, i));
#pragma unroll
          for (int e = 0; e < 4; ++e) {
            const unsigned tw = t[i][e];
            const f16x2_t h = __builtin_bit_cast(f16x2_t, tw);
            f32x4& d = e < 2 ? s0 : s1;
            d[2 * (e & 1)] = __builtin_fmaf((float)h[0], s_, d[2 * (e & 1)]); d[2 * (e & 1) + 1] = __builtin_fmaf((float)h[1], s_, d[2 * (e & 1) + 1]);
          }
        }
      }
    }
    const u32x4 kown = {cvt_pk(s0[0], s0[1]), cvt_pk(s0[2], s0[3]), cvt_pk(s1[0], s1[1]), cvt_pk(s1[2], s1[3])};
    *LP(u32x2, x2w, 8) = (u32x2){kown[0], kown[1]}; *LP(u32x2, x2w, 0) = (u32x2){kown[2], kown[3]};
    CFENCE();
    *LP(volatile unsigned, cw, W_X2) = one;
    SB();
    f32x4 o[4];
    u32x4 koth;
#pragma unroll
    for (int mi = 0; mi < 4; ++mi) o[mi] = MFMA(FRAG(kown), PF(mi), zero);
    const unsigned x2r = x2w ^ 2048u;
    SB();
#define O_CVT(mi, h) do { ov[mi][h] = (u32x2){cvt_pk(o[mi][0] * rc[mi], o[mi][1] * rc[mi]), cvt_pk(o[mi][2] * rc[mi], o[mi][3] * rc[mi])}; if ((h) == 0) PIN(ov[mi][0]); } while (0)
    O_CVT(0, 0); SB();
    const unsigned f2 = *LP(volatile unsigned, cr, W_X2);
    CFENCE();
    koth = *LP(volatile u32x4, x2r, 0);
    SB();
    O_CVT(1, 0); O_CVT(2, 0); O_CVT(3, 0);
    SB();
    if (__builtin_expect(!all_true(f2 != 0u), 0)) TAKE(*LP(volatile unsigned, cr, W_X2), koth = *LP(volatile u32x4, x2r, 0));
#pragma unroll
    for (int mi = 0; mi < 4; ++mi) o[mi] = MFMA(FRAG(koth), PF(mi), zero);
    SB();
    unsigned oom[4];
#pragma unroll
    for (int mi = 0; mi < 4; ++mi) { oom[mi] = oo + mi * (NH * 1024); PIN(oom[mi]); }
    SB();
#pragma unroll
    for (int mi = 0; mi < 4; ++mi) { O_CVT(mi, 1); __builtin_amdgcn_raw_buffer_store_b128((u32x4){ov[mi][0][0], ov[mi][0][1], ov[mi][1][0], ov[mi][1][1]}, wsr, oom[mi], 0, 16); SB(); }
    *LP(volatile unsigned, cw, W_AG) = 2u;
  }
  SB();
  f32x4 oc[4][2];
  unsigned xa[4], xb[2], so[4];
  unsigned rlo, rhi;
  {
    u32x2 xl[8];
#pragma unroll
    for (int n = 0; n < 4; ++n) { xa[n] = (unsigned)(((32 * w + c16) * 128 + (((g >> 1) ^ (c16 >> 1)) * 16) + (g & 1) * 8) ^ (n * 32)); PIN(xa[n]); }
#pragma unroll
    for (int i = 0; i < 8; ++i) xl[i] = *LP(volatile u32x2, xa[i & 3], (i >> 2) * 2048);
    const unsigned kw = w * 4096 + lane * 16, wlo = hh ? x1w : kw, whi = hh ? kw : x1w;
    rlo = hh ? x1w ^ 2048u : kw; rhi = hh ? kw : x1w ^ 2048u;
#pragma unroll
    for (int mi = 0; mi < 4; ++mi) *LP(volatile u32x4, mi < 2 ? wlo : whi, (mi & 1) * 1024) = (u32x4){ov[mi][0][0], ov[mi][0][1], ov[mi][1][0], ov[mi][1][1]};
    *LP(volatile unsigned, cw, W_X1) = 2u;
    unsigned fo = lane == 0 ? (unsigned)OFF_OFLAG + (unsigned)(mt * 16 + nt) * 128u : 0x80000000u, fa = L_CW + (lane & 3) * 128, two = 2u;
    PIN(fo); PIN(fa); PIN(two); PIN(ltag);
    SB();
    WAIT_VM(0);
    *LP(volatile unsigned, cw, W_KS) = two;
    if (all_true(*LP(volatile unsigned, fa, W_KS) == 2u)) __builtin_amdgcn_raw_buffer_store_b32(ltag, wsr, fo, 0, 16);
    CFENCE();
    SB();
#pragma unroll
    for (int i = 0; i < 8; ++i) {
      const int tt = i >> 2;
      const float omg = bfr(1.0f - (tt ? (hh ? gq[3] : gq[1]) : (hh ? gq[2] : gq[0])));
#pragma unroll
      for (int k = 0; k < 2; ++k) {
        const unsigned pr = cvt_pk(omg * blo(xl[i][k]), omg * bhi(xl[i][k]));
        oc[i & 3][tt][2 * k] = blo(pr); oc[i & 3][tt][2 * k + 1] = bhi(pr);
      }
      PIN(oc[i & 3][tt]);
    }
#pragma unroll
    for (int i = 0; i < 2; ++i) { xb[i] = (unsigned)((32 * w + 8 * i + (lane >> 3)) * 128 + (((lane & 7) ^ (4 * i + g)) * 16)); PIN(xb[i]); }
#pragma unroll
    for (int i = 0; i < 4; ++i) { so[i] = (unsigned)(((m0 + 32 * w + 8 * i + (lane >> 3)) * DM + 64 * nt3) * 2 + (lane & 7) * 16); PIN(so[i]); }
  }
  int wof[2][2][4];
#pragma unroll
  for (int n = 0; n < 16; ++n) {
    wof[n >> 3][(n >> 2) & 1][n & 3] = 16384 + ((n >> 2) & 1) * 65536 + (((n >> 3) ^ wr) * 16 + 4 * g + (c16 >> 2)) * 128 + (((n & 3) ^ fsw) * 32) + (c16 & 3) * 8;
    PIN(wof[n >> 3][(n >> 2) & 1][n & 3]);
  }
  unsigned ob0 = (unsigned)OFF_OF + (unsigned)(((mt3 * 8 + 2 * w) * NH) * 1024 + lane * 16), ob1 = ob0 + NH * 1024;
  int cs1 = CH(1) * 2048, cs2 = CH(2) * 2048, cs3 = CH(3) * 2048;
  bf16x8 wf[2][4], wl[4];
  s16x4 tl2;
#define W_AT(hd, n, r) (smem + wof[r][WOPOS((hd) >> 1) >> 3][n] + (WOPOS((hd) >> 1) & 7) * 8192 + ((hd) & 1) * 4096)
  const unsigned lw = min(misc[C_WO], misc[C_WO + 1]);
  PIN(ob0); PIN(ob1); asm volatile("" : "+s"(cs1), "+s"(cs2), "+s"(cs3));
  SB();

  if (__builtin_expect(rfl(lw) == 0u, 0)) { (void)wait_ge<1>(misc + C_WO, 1u); (void)wait_ge<1>(misc + C_WO + 1, 1u); }
  CFENCE();
#pragma unroll
  for (int n = 0; n < 4; ++n) wf[0][n] = CAT(TRD(W_AT(0, n, 0)), TRD(W_AT(0, n, 1)));
  u32x4 of[OPF + 1][4];
  unsigned rdy = 0;
#define O_READY(c) do { if (__builtin_expect(rdy <= (unsigned)(c), 0)) rdy = wait_ge<0>(misc + C_ORDY, (unsigned)(c) + 1u); CFENCE(); } while (0)
#define O_LD1(c, k4) of[(c) % (OPF + 1)][k4] = __builtin_amdgcn_raw_buffer_load_b128(wsr, (((k4) & 1) ? ob1 : ob0) + ((k4) >> 1) * 1024, (c) == 1 ? cs1 : (c) == 2 ? cs2 : (c) == 3 ? cs3 : CH(c) * 2048, 16)
  TAKE(*LP(volatile unsigned, cw ^ 128u, W_X1) >> 1, { _Pragma("unroll") for (int k = 0; k < 4; ++k) of[0][k] = *LP(volatile u32x4, k < 2 ? rlo : rhi, (k & 1) * 1024); });
  unsigned li = 0;
#define P3_MM(c, j)                                                                                        \
  do {                                                                                                     \
    constexpr int hd = 2 * (c) + ((j) >> 3), n = ((j) >> 1) & 3;                                           \
    AMFMA(oc[n][(j) & 1], wf[(j) >> 3][n], of[(c) % (OPF + 1)][2 * ((j) >> 3) + ((j) & 1)]); SB();         \
    if (((j) & 1) == 0) tlo = TRD(W_AT(hd + 1, n, 0)); else wf[1 - ((j) >> 3)][n] = CAT(tlo, TRD(W_AT(hd + 1, n, 1))); \
    if ((c) > 0 && (c) + OPF < 16) {                                                                       \
      if ((j) == 0) O_READY((c) + OPF);                                                                    \
      if (((j) & 3) == 1) O_LD1((c) + OPF, (c) + OPF == 15 ? (((j) >> 3) | (((j) >> 1) & 2)) : (j) >> 2);  \
    }                                                                                                      \
    if ((c) == 14 && (j) < 8) { if (((j) & 1) == 0) tl2 = TRD(W_AT(31, n, 0)); else wl[n] = CAT(tl2, TRD(W_AT(31, n, 1))); } \
    if ((c) == 14 && (j) == 8) li = misc[C_IDX];                                                           \
    SB();                                                                                                  \
  } while (0)
#define P3_4(c, j) P3_MM(c, j); P3_MM(c, (j) + 1); P3_MM(c, (j) + 2); P3_MM(c, (j) + 3)
#define P3_C(c) P3_4(c, 0); P3_4(c, 4); P3_4(c, 8); P3_4(c, 12)
  P3_C(0);
#pragma unroll
  for (int c = 1; c <= OPF; ++c) { O_READY(c); O_LD1(c, 0); SB(); O_LD1(c, 1); SB(); O_LD1(c, 2); SB(); O_LD1(c, 3); SB(); }
  P3_C(1); P3_C(2); P3_C(3); P3_C(4); P3_C(5); P3_C(6); P3_C(7);
  P3_C(8); P3_C(9); P3_C(10); P3_C(11); P3_C(12); P3_C(13); P3_C(14);
  li = rfl(li);
  if (__builtin_expect(li == 0u, 0)) li = wait_ge<1>(misc + C_IDX, 1u);
  const int idx = (int)li - 1;
  if ((idx >> 5) == mt3 * 4 + w) {
    f32x4 dl[4];
    NOP8(oc);
    TAKE(misc[C_Y], { _Pragma("unroll") for (int n = 0; n < 4; ++n) dl[n] = *(volatile lf32x4*)(smem + L_RED + 2048 + n * 64 + g * 16); });
    const bool mine = c16 == (idx & 15);
    if (idx & 16) {
#pragma unroll
      for (int n = 0; n < 4; ++n) oc[n][1] += mine ? dl[n] : zero;
    } else {
#pragma unroll
      for (int n = 0; n < 4; ++n) oc[n][0] += mine ? dl[n] : zero;
    }
    NOP8(oc);
  }
  u32x4 ot[4];
  const __amdgpu_buffer_rsrc_t outr = MKRSRC(out, SEQ * DM * 2);
#define E_MM(tt, h, n) do { AMFMA(oc[n][tt], (h) ? wl[n] : wf[0][n], of[15 % (OPF + 1)][2 * (h) + (tt)]); SB(); } while (0)
#define E_WR(tt, n) do { *LP(volatile u32x2, xa[n], (tt) * 2048) = (u32x2){cvt_pk(oc[n][tt][0], oc[n][tt][1]), cvt_pk(oc[n][tt][2], oc[n][tt][3])}; SB(); } while (0)
#define E_RD(tt) do { ot[2 * (tt)] = *LP(volatile u32x4, xb[0], (tt) * 2048); ot[2 * (tt) + 1] = *LP(volatile u32x4, xb[1], (tt) * 2048); SB(); } while (0)
#define E_ST(i) do { __builtin_amdgcn_raw_buffer_store_b128(ot[i], outr, so[i], 0, 17); SB(); } while (0)
#define E_NOP(tt) asm volatile("s_nop 4" : "+v"(oc[0][tt]))
  SB();
  E_MM(0, 0, 0); E_MM(0, 0, 1); E_MM(0, 0, 2); E_MM(0, 0, 3); E_MM(0, 1, 0); E_MM(0, 1, 1); E_MM(0, 1, 2); E_MM(0, 1, 3);
  E_NOP(0); E_WR(0, 0); E_WR(0, 1);
  E_MM(1, 0, 0); E_WR(0, 2); E_MM(1, 0, 1); E_WR(0, 3); E_MM(1, 0, 2); E_RD(0); E_MM(1, 0, 3);
  E_MM(1, 1, 0); E_MM(1, 1, 1); E_MM(1, 1, 2); E_MM(1, 1, 3);
  E_ST(0); E_ST(1);
  E_NOP(1); E_WR(1, 0); E_WR(1, 1); E_WR(1, 2); E_WR(1, 3); E_RD(1);
  E_ST(2); E_ST(3);
}

extern "C" void kernel_launch(void* const* d_in, const int* in_sizes, int n_in,
                              void* d_out, int out_size, void* d_ws, size_t ws_size,
                              hipStream_t stream) {
  char* ws = (char*)d_ws;
  (void)hipFuncSetAttribute((const void*)fused_kernel, hipFuncAttributeMaxDynamicSharedMemorySize, L_TOTAL);
  fused_kernel<<<256, 512, L_TOTAL, stream>>>((const u16*)d_in[0], (const u16*)d_in[1], (const u16*)d_in[2], ws, (u16*)d_out,
                                              (const u16*)d_in[4], (const u16*)d_in[8], (const u16*)d_in[5], (const u16*)d_in[6],
                                              (const u16*)d_in[7]);
}
```
